```python
import torch

def _softmax(x, axis=-1):
    return torch.softmax(x, dim=axis)
def _gelu(x):
    return torch.nn.functional.gelu(x, approximate="tanh")
def _silu(x):
    return torch.nn.functional.silu(x)
def _sigmoid(x):
    return torch.sigmoid(x)
def _layernorm(x, axis=-1, eps=1e-5):
    m = x.mean(dim=axis, keepdim=True)
    v = x.var(dim=axis, keepdim=True, unbiased=False)
    return (x - m) / torch.sqrt(v + eps)
def _scatter_add(tgt, idx, src, axis=0):
    return tgt.clone().index_add_(axis, idx, src)
def _segment_sum(src, seg, n):
    out = torch.zeros((n,) + src.shape[1:], dtype=src.dtype)
    return out.index_add_(0, seg, src)
def _pad(x, pw):
    flat = tuple(v for pair in reversed(pw) for v in pair)
    return torch.nn.functional.pad(x, flat)

kernel_name = 'sequential_preLN__softmax_full_mha__dense_activation_relu__prompt'

def setup_inputs():

    _w = (lambda a: (lambda t: t.to(torch.bfloat16) if t.is_floating_point() else t)(torch.as_tensor(a)))
    def _A_setup():
        B, S, D = 1, 2048, 448
        return {
            "x": _w((lambda *s: torch.randn(*s, dtype=torch.bfloat16) * 0.05)(B, S, D)),
            "wqkv": _w((lambda *s: torch.randn(*s, dtype=torch.bfloat16) * 0.05)(D, 3 * D)),
            "wo": _w((lambda *s: torch.randn(*s, dtype=torch.bfloat16) * 0.05)(D, D)),
        }
    _A_in = _A_setup()

    def _B_setup():
        B, S, D, Dff = 1, 2048, 448, 1792
        return {
            "x": _w((lambda *s: torch.randn(*s, dtype=torch.bfloat16) * 0.05)(B, S, D)),
            "w1": _w((lambda *s: torch.randn(*s, dtype=torch.bfloat16) * 0.05)(D, Dff)),
            "w2": _w((lambda *s: torch.randn(*s, dtype=torch.bfloat16) * 0.05)(Dff, D)),
        }
    _B_in = _B_setup()

    return {
        "A_x": _A_in["x"],
        "A_wqkv": _A_in["wqkv"],
        "A_wo": _A_in["wo"],
        "B_x": _B_in["x"],
        "B_w1": _B_in["w1"],
        "B_w2": _B_in["w2"],
    }

def reference(A_x, A_wqkv, A_wo, B_x, B_w1, B_w2):
    def _A_ref(x, wqkv, wo):
        B, S, D = x.shape
        H = 16; Dh = D // H
        qkv = (lambda x, *d: x.permute(*d))((x @ wqkv).reshape(B, S, 3, H, Dh), 2, 0, 3, 1, 4)
        q, k, v = qkv[0], qkv[1], qkv[2]
        scale = Dh ** -0.5
        scores = torch.einsum("bhqd,bhkd->bhqk", q, k) * scale
        p = _softmax(scores, axis=-1)
        o = torch.einsum("bhqk,bhkd->bhqd", p, v)
        return (lambda x, *d: x.permute(*d))(o, 0, 2, 1, 3).reshape(B, S, D) @ wo

    def _B_ref(x, w1, w2):
        h = torch.einsum("bsd,df->bsf", x, w1)
        zero = torch.zeros_like(h)
        h = torch.maximum(h, zero)
        return torch.einsum("bsf,fd->bsd", h, w2)

    x = A_x
    h = x + _A_ref(_layernorm(x, axis=-1), A_wqkv, A_wo)
    h2 = h + _B_ref(_layernorm(h, axis=-1), B_w1, B_w2)
    return h2
```

```cpp
#include <hip/hip_runtime.h>
#include <hip/hip_bf16.h>
typedef unsigned short u16;
static __device__ __forceinline__ float b2f(u16 v){ return __uint_as_float((unsigned)v << 16); }
static __device__ __forceinline__ u16 f2b(float f){ unsigned u = __float_as_uint(f); u += 0x7fffu + ((u >> 16) & 1u); return (u16)(u >> 16); }
constexpr int S = 2048, D = 448, H = 16, DH = 28, FF = 1792;

__global__ void k_ln(const u16* x, const float* xf, float* o) {
  int r = blockIdx.x; __shared__ float red[64];
  float v[7]; float s = 0;
  for (int i = 0; i < 7; ++i) { int c = threadIdx.x + 64 * i; v[i] = x ? b2f(x[r * D + c]) : xf[r * D + c]; s += v[i]; }
  for (int o2 = 32; o2; o2 >>= 1) s += __shfl_xor(s, o2);
  float m = s / D; float q = 0;
  for (int i = 0; i < 7; ++i) { float d = v[i] - m; q += d * d; }
  for (int o2 = 32; o2; o2 >>= 1) q += __shfl_xor(q, o2);
  float rs = rsqrtf(q / D + 1e-5f);
  for (int i = 0; i < 7; ++i) o[r * D + threadIdx.x + 64 * i] = (v[i] - m) * rs;
}
__global__ void k_gemm(const float* A, const u16* W, float* C, u16* Cb, const u16* resb, const float* resf, int N, int K, int mode) {
  int n = blockIdx.x * 64 + threadIdx.x; int m0 = blockIdx.y * 8;
  float acc[8] = {};
  for (int k = 0; k < K; ++k) { float w = b2f(W[k * N + n]); for (int i = 0; i < 8; ++i) acc[i] += A[(m0 + i) * K + k] * w; }
  for (int i = 0; i < 8; ++i) {
    int idx = (m0 + i) * N + n; float v = acc[i];
    if (mode == 1) v = fmaxf(v, 0.f);
    if (mode == 2) v += b2f(resb[idx]);
    if (mode == 3) { Cb[idx] = f2b(v + resf[idx]); continue; }
    C[idx] = v;
  }
}
__global__ void k_attn(const float* qkv, float* o) {
  int q = blockIdx.x * 64 + threadIdx.x, h = blockIdx.y;
  float qv[DH], acc[DH]; float m = -1e30f, l = 0;
  for (int d = 0; d < DH; ++d) { qv[d] = qkv[q * 3 * D + h * DH + d] * 0.18898223650461362f; acc[d] = 0; }
  for (int k = 0; k < S; ++k) {
    float s = 0; for (int d = 0; d < DH; ++d) s += qv[d] * qkv[k * 3 * D + D + h * DH + d];
    float mn = fmaxf(m, s); float a = __expf(m - mn), p = __expf(s - mn); l = l * a + p; m = mn;
    for (int d = 0; d < DH; ++d) acc[d] = acc[d] * a + p * qkv[k * 3 * D + 2 * D + h * DH + d];
  }
  for (int d = 0; d < DH; ++d) o[q * D + h * DH + d] = acc[d] / l;
}
extern "C" void kernel_launch(void* const* d_in, const int* in_sizes, int n_in, void* d_out, int out_size, void* d_ws, size_t ws_size, hipStream_t stream) {
  const u16* x = (const u16*)d_in[0]; const u16* wqkv = (const u16*)d_in[1]; const u16* wo = (const u16*)d_in[2];
  const u16* w1 = (const u16*)d_in[4]; const u16* w2 = (const u16*)d_in[5];
  float* ws = (float*)d_ws; float* xn = ws; float* qkv = xn + S * D; float* o = qkv + S * 3 * D; float* h = o + S * D; float* hn = h + S * D; float* hid = hn + S * D;
  k_ln<<<S, 64, 0, stream>>>(x, nullptr, xn);
  k_gemm<<<dim3(3 * D / 64, S / 8), 64, 0, stream>>>(xn, wqkv, qkv, nullptr, nullptr, nullptr, 3 * D, D, 0);
  k_attn<<<dim3(S / 64, H), 64, 0, stream>>>(qkv, o);
  k_gemm<<<dim3(D / 64, S / 8), 64, 0, stream>>>(o, wo, h, nullptr, x, nullptr, D, D, 2);
  k_ln<<<S, 64, 0, stream>>>(nullptr, h, hn);
  k_gemm<<<dim3(FF / 64, S / 8), 64, 0, stream>>>(hn, w1, hid, nullptr, nullptr, nullptr, FF, D, 1);
  k_gemm<<<dim3(D / 64, S / 8), 64, 0, stream>>>(hid, w2, nullptr, (u16*)d_out, nullptr, h, D, FF, 3);
}
```

```cpp
#include <hip/hip_runtime.h>
#include <stdint.h>

typedef unsigned u32;
typedef u32 u32x2 __attribute__((ext_vector_type(2)));
typedef u32 u32x4 __attribute__((ext_vector_type(4)));
typedef float f32x2 __attribute__((ext_vector_type(2)));
typedef float f32x4 __attribute__((ext_vector_type(4)));
typedef float f32x16 __attribute__((ext_vector_type(16)));
typedef __bf16 bf16x2 __attribute__((ext_vector_type(2)));
typedef short s16x4 __attribute__((ext_vector_type(4)));
typedef __amdgpu_buffer_rsrc_t rsrc_t;
#define DI __device__ __forceinline__
#define LAS __attribute__((address_space(3)))

constexpr u32 WS_GEN = 0, WS_TMO = 128;
constexpr u32 WS_F1 = 4096, WS_F3 = 32768, WS_F4 = 40960, WS_F5 = 49152, WS_F6 = 57344, WS_F2 = 65536;
constexpr u32 MB = 1u << 20;
constexpr u32 WS_Q = 1 * MB, WS_KV = 3 * MB, WS_O = 7 * MB, WS_WP = 9 * MB, WS_W2F = 11 * MB, WS_H = 13 * MB, WS_HA = 20 * MB, WS_ST = 24 * MB;
constexpr u32 WS_END = 26 * MB;
constexpr u32 OOB = 0x80000000u;
constexpr u32 LDS_BYTES = 163840, SCR = 131072, CMB = 114688;
constexpr u32 XR = 0, WR = 49152, ZR = 65536, OR_ = 131072, OS = 4352, XCH = 148480;
constexpr float QSCALE = 0.18898223650461362f * 1.4426950408889634f;
constexpr u32 CODE_BYTES = 48 * 1024;
constexpr u32 SPIN_CAP = 1u << 16;
#define STAMP(n) do {} while (0)

DI u32 cvtpk(float lo, float hi) { f32x2 v = {lo, hi}; return __builtin_bit_cast(u32, __builtin_convertvector(v, bf16x2)); }
DI float dot2(u32 a, u32 b, float c) { return __builtin_amdgcn_fdot2_f32_bf16(__builtin_bit_cast(bf16x2, a), __builtin_bit_cast(bf16x2, b), c, false); }
DI float hsum(float v) { auto r = __builtin_amdgcn_permlane32_swap(__float_as_uint(v), __float_as_uint(v), false, false); return __uint_as_float(r[0]) + __uint_as_float(r[1]); }
DI u32 hi_half(u32 v) { auto r = __builtin_amdgcn_permlane32_swap(v, v, false, false); return r[1]; }
DI u32 fresh_lane() { u32 l; asm volatile("v_mbcnt_lo_u32_b32 %0, -1, 0\n\tv_mbcnt_hi_u32_b32 %0, -1, %0" : "=v"(l)); return l; }
#define RELANE() do { lane = fresh_lane(); k.lane = lane; k.r = lane & 31; k.hh = lane >> 5; } while (0)
DI u32 rfl(u32 v) { return __builtin_amdgcn_readfirstlane(v); }

template <int D> DI void mfa(u32x4 a, u32x4 b) { asm volatile("s_nop 1\n\tv_mfma_f32_32x32x16_bf16 a[%c0:%c1], %2, %3, a[%c0:%c1]" ::"i"(D), "i"(D + 15), "v"(a), "v"(b)); }
template <int D> DI void mfa0(u32x4 a, u32x4 b) { asm volatile("s_nop 1\n\tv_mfma_f32_32x32x16_bf16 a[%c0:%c1], %2, %3, 0" ::"i"(D), "i"(D + 15), "v"(a), "v"(b)); }
template <int D, bool Z> DI void mfaz(u32x4 a, u32x4 b) { if constexpr (Z) mfa0<D>(a, b); else mfa<D>(a, b); }
template <int D, bool Z> DI void mf(u32x4 a, u32x4 b) {
  if constexpr (Z) asm volatile("v_mfma_f32_32x32x16_bf16 a[%c0:%c1], %2, %3, 0" ::"i"(D), "i"(D + 15), "v"(a), "v"(b));
  else asm volatile("v_mfma_f32_32x32x16_bf16 a[%c0:%c1], %2, %3, a[%c0:%c1]" ::"i"(D), "i"(D + 15), "v"(a), "v"(b));
}
#define SB() __builtin_amdgcn_sched_barrier(0)
template <int D, int A, bool Z> DI void mfaa(u32x4 b) {
  if constexpr (Z) asm volatile("v_mfma_f32_32x32x16_bf16 a[%c0:%c1], a[%c2:%c3], %4, 0" ::"i"(D), "i"(D + 15), "i"(A), "i"(A + 3), "v"(b));
  else asm volatile("v_mfma_f32_32x32x16_bf16 a[%c0:%c1], a[%c2:%c3], %4, a[%c0:%c1]" ::"i"(D), "i"(D + 15), "i"(A), "i"(A + 3), "v"(b));
}
template <bool Z> DI void mfv(f32x16& d, u32x4 a, u32x4 b) {
  if constexpr (Z) asm volatile("v_mfma_f32_32x32x16_bf16 %0, %1, %2, 0" : "=&v"(d) : "v"(a), "v"(b));
  else asm volatile("v_mfma_f32_32x32x16_bf16 %0, %1, %2, %0" : "+v"(d) : "v"(a), "v"(b));
}
DI void mfvn(f32x16& d, u32x4 a, u32x4 b) { asm volatile("s_nop 1\n\tv_mfma_f32_32x32x16_bf16 %0, %1, %2, %0" : "+v"(d) : "v"(a), "v"(b)); }
DI void mf_drain() { asm volatile("s_nop 7\n\ts_nop 7"); }
template <int R> DI float accr() { float v; asm volatile("v_accvgpr_read_b32 %0, a[%c1]" : "=v"(v) : "i"(R)); return v; }
template <int R> DI void park(u32 v) { asm volatile("v_accvgpr_write_b32 a[%c0], %1" ::"i"(R), "v"(v)); }
template <int R> DI u32 unpark() { return __float_as_uint(accr<R>()); }
template <int D> DI void acc16(float (&v)[16]) {
  v[0] = accr<D>(); v[1] = accr<D + 1>(); v[2] = accr<D + 2>(); v[3] = accr<D + 3>(); v[4] = accr<D + 4>(); v[5] = accr<D + 5>(); v[6] = accr<D + 6>(); v[7] = accr<D + 7>();
  v[8] = accr<D + 8>(); v[9] = accr<D + 9>(); v[10] = accr<D + 10>(); v[11] = accr<D + 11>(); v[12] = accr<D + 12>(); v[13] = accr<D + 13>(); v[14] = accr<D + 14>(); v[15] = accr<D + 15>();
}
template <int A, int OFF> DI void dsw_acc(u32 addr) { asm volatile("ds_write_b128 %0, a[%c1:%c2] offset:%c3" ::"v"(addr), "i"(A), "i"(A + 3), "i"(OFF) : "memory"); }
template <int A, int OFF> DI void ld_acc(rsrc_t r, u32 voff, u32 so) { asm volatile("buffer_load_dwordx4 a[%c0:%c1], %2, %3, %4 offen offset:%c5 sc1" ::"i"(A), "i"(A + 3), "v"(voff), "s"(r), "s"(so), "i"(OFF) : "memory"); }

DI void glds(rsrc_t r, u32 voff, u32 so, u32 lds) { asm volatile("s_mov_b32 m0, %3\n\ts_nop 0\n\tbuffer_load_dwordx4 %0, %1, %2 offen lds" ::"v"(voff), "s"(r), "s"(so), "s"(lds) : "memory"); }
DI void glds_c(rsrc_t r, u32 voff, u32 so, u32 lds) { asm volatile("s_mov_b32 m0, %3\n\ts_nop 0\n\tbuffer_load_dwordx4 %0, %1, %2 offen sc1 lds" ::"v"(voff), "s"(r), "s"(so), "s"(lds) : "memory"); }
template <int N> DI void wait_bar() { asm volatile("s_waitcnt vmcnt(%c0) lgkmcnt(0)\n\ts_barrier" ::"i"(N) : "memory"); }
template <int N> DI void vm_bar() { asm volatile("s_waitcnt vmcnt(%c0)\n\ts_barrier" ::"i"(N) : "memory"); }
template <int N> DI void wait_vm() { asm volatile("s_waitcnt vmcnt(%c0)" ::"i"(N) : "memory"); }
DI void lds_bar() { asm volatile("s_waitcnt lgkmcnt(0)\n\ts_barrier" ::: "memory"); }
DI u32x4 lds128(u32 a) { return *(const LAS u32x4*)a; }
DI u32x4 ldstr(u32 a0, u32 a1) {
  s16x4 lo = __builtin_amdgcn_ds_read_tr16_b64_v4i16((LAS s16x4*)a0), hi = __builtin_amdgcn_ds_read_tr16_b64_v4i16((LAS s16x4*)a1);
  return __builtin_bit_cast(u32x4, __builtin_shufflevector(lo, hi, 0, 1, 2, 3, 4, 5, 6, 7));
}
DI u32x4 ldc(rsrc_t r, u32 voff, u32 so) { return __builtin_amdgcn_raw_buffer_load_b128(r, voff, so, 16); }
DI void stc(rsrc_t r, u32x4 v, u32 voff, u32 so) { __builtin_amdgcn_raw_buffer_store_b128(v, r, voff, so, 16); }
DI void stc1(rsrc_t r, u32 v, u32 voff) { __builtin_amdgcn_raw_buffer_store_b32(v, r, voff, 0, 16); }
DI void poll(rsrc_t ws, u32 voff, u32 tag) {
#pragma nounroll
  for (u32 spin = 0;; ++spin) {
    asm volatile("" ::: "memory");
    u32 v = __builtin_amdgcn_raw_buffer_load_b32(ws, voff, 0, 16);
    if (__builtin_amdgcn_ballot_w64(v != tag) == 0) break;
    if (spin > SPIN_CAP) { stc1(ws, 1u, WS_TMO); break; }
    __builtin_amdgcn_s_sleep(1);
  }
  asm volatile("" ::: "memory");
}
DI u32x4 corr_w(float cs, int hh) { u32 h = cvtpk(cs, cs); float lo = cs - __uint_as_float(h & 0xffff0000u); u32 d0 = cvtpk(cs, lo); u32x4 r = {hh ? 0u : d0, hh ? 0u : (h & 0xffffu), 0u, 0u}; return r; }
DI u32x4 corr_x(float nm, int hh) { u32 h = cvtpk(nm, nm); float lo = nm - __uint_as_float(h & 0xffff0000u); u32x4 r = {hh ? 0u : h, hh ? 0u : (cvtpk(lo, lo) & 0xffffu), 0u, 0u}; return r; }
constexpr u32 ONES = 0x3f803f80u;
DI void sum8(u32x4 f, float& s, u32 one) { s = dot2(f[0], one, s); s = dot2(f[1], one, s); s = dot2(f[2], one, s); s = dot2(f[3], one, s); }
DI void sq8(u32x4 f, float& s) { s = dot2(f[0], f[0], s); s = dot2(f[1], f[1], s); s = dot2(f[2], f[2], s); s = dot2(f[3], f[3], s); }

#define PIN1(a) asm volatile("" : "+v"(a))
#define PIN2(a, b) asm volatile("" : "+v"(a), "+v"(b))
struct Ctx {
  rsrc_t rx, rqkv, rwo, rw1, rw2, ws;
  u32 lane, r, hh, w, g, c, tag;
  u32 vx0, vx1, vwo, vw[3], vw1;
  u32 sx, sw, s1, so, w4k, w3k, w1k;
  u32 xb[4], ob[4], wa, wc, zb[4], od, one;
  rsrc_t rkv; u32 qso, l16, f1, f1p;
  unsigned long long t_start, c_start;
};
template <int LI, int SI> DI void gp(rsrc_t r, u32 voff, u32 sb, u32 lb) {
  u32 t;
  asm volatile("s_add_u32 m0, %4, %c5\n\ts_add_u32 %0, %3, %c6\n\tbuffer_load_dwordx4 %1, %2, %0 offen lds" : "=&s"(t) : "v"(voff), "s"(r), "s"(sb), "s"(lb), "i"(LI), "i"(SI) : "memory", "scc");
}
template <int N, int I> DI void piece(const Ctx& k) {
  if constexpr (N < 7) {
    if constexpr (I < 4) gp<XR + (N % 3) * 16384 + I * 1024, N * 128 + I * 8 * 896>(k.rx, I < 2 ? k.vx0 : k.vx1, k.sx, k.w4k);
    else if constexpr (I < 7) gp<WR + (N & 3) * 12288 + (I - 4) * 1024, N * 64 * 2688>(k.rqkv, k.vw[I - 4], k.sw, k.w3k);
  } else if constexpr (N < 14) {
    constexpr int kc = N - 7;
    if constexpr (I < 4) gp<ZR + ((kc + 2) & 3) * 16384 + I * 1024, (64 * kc + 4 * I) * 3584>(k.rw1, k.vw1, k.s1, k.w4k);
    else if constexpr (I == 4) gp<OR_ + (kc & 3) * OS, kc * 128>(k.rwo, k.vwo, k.so, k.w1k);
  }
}
template <int N> DI void stage(const Ctx& k) { piece<N, 0>(k); piece<N, 4>(k); piece<N, 1>(k); piece<N, 5>(k); piece<N, 2>(k); piece<N, 6>(k); piece<N, 3>(k); }

struct QF { u32x4 x, w0, w1, w2; };
struct QA { f32x16 a0, a1, a2; };
template <int KS> DI u32x4 qx(const Ctx& k) { return lds128(k.xb[KS & 3] + ((KS >> 2) % 3) * 16384); }
template <int KS, int J> DI u32x4 qw(const Ctx& k) { constexpr u32 wb = ((KS >> 2) & 3) * 12288 + (KS & 3) * 3072 + J * 64; return ldstr(k.wa + wb, k.wa + wb + 768); }
template <int NCH, int J> DI u32x4 qc(const Ctx& k) { constexpr u32 wb = (NCH & 3) * 12288 + J * 64; return ldstr(k.wc + wb, k.wc + wb + 768); }
template <int KS> DI QF qkv_read(const Ctx& k) { QF f; f.x = qx<KS>(k); f.w0 = qw<KS, 0>(k); f.w1 = qw<KS, 1>(k); f.w2 = qw<KS, 2>(k); return f; }
template <bool ISV, bool Z> DI void qm(f32x16& d, u32x4 x, u32x4 w) { if constexpr (ISV) mfv<Z>(d, x, w); else mfv<Z>(d, w, x); }
template <bool ISV, int KS> DI void qkv_step(const Ctx& k, QF cur, QA& A, u32x4 (&xf)[28], float& sx, float& sxx, float (&cs)[3]) {
  QF nxt;
  constexpr int S = KS & 3, NCH = KS >> 2, NP = NCH + 2;
  constexpr bool L = S == 3; constexpr int KN = KS + 1;
  SB();
  qm<ISV, KS == 0>(A.a0, cur.x, cur.w0);
  if constexpr (!L) { nxt.x = qx<KN>(k); nxt.w0 = qw<KN, 0>(k); } else nxt.w0 = qc<NCH, 0>(k);
  SB();
  qm<ISV, KS == 0>(A.a1, cur.x, cur.w1);
  if constexpr (!L) nxt.w1 = qw<KN, 1>(k); else nxt.w1 = qc<NCH, 1>(k);
  piece<NP, S>(k);
  SB();
  qm<ISV, KS == 0>(A.a2, cur.x, cur.w2);
  if constexpr (!L) nxt.w2 = qw<KN, 2>(k); else nxt.w2 = qc<NCH, 2>(k);
  piece<NP, 4 + S>(k);
  SB();
  xf[KS] = cur.x;
  if constexpr (L) {
#pragma unroll
    for (int i = KS - 3; i <= KS; ++i) { sum8(xf[i], sx, k.one); sq8(xf[i], sxx); }
    PIN2(sx, sxx); SB();
    if constexpr (NCH < 5) {
      vm_bar<7>();
      QF q2 = qkv_read<KN>(k); SB();
      sum8(nxt.w0, cs[0], k.one); sum8(nxt.w1, cs[1], k.one); sum8(nxt.w2, cs[2], k.one); PIN2(cs[0], cs[1]); PIN1(cs[2]); SB();
      qkv_step<ISV, KN>(k, q2, A, xf, sx, sxx, cs);
    } else { sum8(nxt.w0, cs[0], k.one); sum8(nxt.w1, cs[1], k.one); sum8(nxt.w2, cs[2], k.one); PIN2(cs[0], cs[1]); PIN1(cs[2]); SB(); }
  } else qkv_step<ISV, KN>(k, nxt, A, xf, sx, sxx, cs);
}
struct SP { u32 oa, ob, oc, c0, c1; unsigned long long ma, mb; float qs; };
DI SP qk_plan(const Ctx& k, u32 T, bool live) {
  SP p; const bool isq = T < 14; const u32 tp = isq ? T : T - 14, hs = isq ? 17 : 18;
  const u32 r16 = (live ? k.r * 16 : OOB) + (isq ? WS_Q + (4 * k.g + k.w) * 2048 : WS_KV + (4 * k.g + k.w) * 4096);
  const u32 va = 4 * tp + k.hh, pa = (va * 37) >> 8, ra = va - 7 * pa, vb = va + 2, pb = (vb * 37) >> 8, rb = vb - 7 * pb;
  p.oa = r16 + ((2 * pa + (ra > 3)) << hs) + (ra > 3 ? ra - 3 : ra) * 512; p.ob = r16 + ((2 * pb + (rb > 3)) << hs) + (rb > 3 ? rb - 3 : rb) * 512;
  p.oc = (ra == 3 || rb == 3) ? r16 + ((2 * (ra == 3 ? pa : pb) + 1) << hs) : OOB;
  p.ma = __builtin_amdgcn_ballot_w64(ra == 3); p.mb = __builtin_amdgcn_ballot_w64(rb == 3);
  p.c0 = isq ? 0u : ONES; p.c1 = isq ? 0u : 0x3f80u; p.qs = isq ? QSCALE : 1.f;
  return p;
}
DI SP v_plan(const Ctx& k, u32 T, bool live) {
  SP p; const u32 col = 32 * (T - 28) + k.r, head = (col * 2341u) >> 16, d = col - 28 * head;
  p.oa = live ? WS_KV + (4 * k.g + k.w) * 4096 + 2048 + head * 262144u + (k.hh * 32 + d) * 16 : OOB;
  p.ob = d >= 24 ? p.oa + 64 : OOB; p.oc = d == 24 ? ONES : 0u; p.ma = p.mb = 0; p.c0 = p.c1 = 0; p.qs = 1.f;
  return p;
}
DI u32 sel(unsigned long long m, u32 t, u32 f) { u32 r; asm("v_cndmask_b32 %0, %1, %2, %3" : "=v"(r) : "v"(f), "v"(t), "s"(m)); return r; }
DI void qk_store(const Ctx& k, const SP& p, const f32x16& a, float rstd) {
  const float sc = rstd * p.qs;
  u32 d[8];
#pragma unroll
  for (int i = 0; i < 8; ++i) d[i] = cvtpk(a[2 * i] * sc, a[2 * i + 1] * sc);
  u32x4 da = {d[0], d[1], sel(p.ma, p.c0, d[2]), sel(p.ma, p.c1, d[3])}, db = {d[4], d[5], sel(p.mb, p.c0, d[6]), sel(p.mb, p.c1, d[7])};
  u32x4 dc = {p.c0, p.c1, sel(p.ma, d[2], d[6]), sel(p.ma, d[3], d[7])};
  stc(k.ws, da, p.oa, 0); stc(k.ws, db, p.ob, 0); stc(k.ws, dc, p.oc, 0);
}
DI void v_store(const Ctx& k, const SP& p, const f32x16& a, const f32x16& rb) {
#pragma unroll
  for (int s = 0; s < 2; ++s) {
    u32x4 dd = {cvtpk(a[8 * s] * rb[8 * s], a[8 * s + 1] * rb[8 * s + 1]), cvtpk(a[8 * s + 2] * rb[8 * s + 2], a[8 * s + 3] * rb[8 * s + 3]),
                cvtpk(a[8 * s + 4] * rb[8 * s + 4], a[8 * s + 5] * rb[8 * s + 5]), cvtpk(a[8 * s + 6] * rb[8 * s + 6], a[8 * s + 7] * rb[8 * s + 7])};
    stc(k.ws, dd, p.oa + s * 1024, 0);
  }
}
template <bool ISV> DI void qkv_phase(const Ctx& k, u32 T0, u32 first, u32x4 (&xf)[28], u32x4& xc) {
  SP p0, p1, p2;
  if constexpr (ISV) {
    p0 = v_plan(k, T0, first <= 0); p1 = v_plan(k, T0 + 1, first <= 1); p2 = v_plan(k, T0 + 2, first <= 2);
  } else { p0 = qk_plan(k, T0, first <= 0); p1 = qk_plan(k, T0 + 1, first <= 1); p2 = qk_plan(k, T0 + 2, true); }
  asm volatile("" : "+v"(p0.oa), "+v"(p0.ob), "+v"(p0.oc), "+v"(p1.oa), "+v"(p1.ob), "+v"(p1.oc), "+v"(p2.oa), "+v"(p2.ob), "+v"(p2.oc));
  u32 xw = XCH + k.lane * 16 + k.w * 4, xr = XCH + k.r * 16; PIN2(xw, xr);
  float sx = 0.f, sxx = 0.f, cs[3] = {0.f, 0.f, 0.f};
  QA A;
  wait_bar<7>();
  STAMP(14);
  qkv_step<ISV, 0>(k, qkv_read<0>(k), A, xf, sx, sxx, cs);
  vm_bar<5>();
  STAMP(20);
  {
    const u32x4 c0 = qc<6, 0>(k), c1 = qc<6, 1>(k), c2 = qc<6, 2>(k);
    xf[24] = qx<24>(k); xf[25] = qx<25>(k); xf[26] = qx<26>(k); xf[27] = qx<27>(k);
    SB();
    sum8(c0, cs[0], k.one); sum8(c1, cs[1], k.one); sum8(c2, cs[2], k.one);
    *(LAS float*)(xw) = cs[0]; *(LAS float*)(xw + 1024) = cs[1]; *(LAS float*)(xw + 2048) = cs[2];
    SB();
  }
  u32x4 w0 = qw<24, 0>(k), w1 = qw<24, 1>(k), w2 = qw<24, 2>(k);
#pragma unroll
  for (int i = 24; i < 28; ++i) { sum8(xf[i], sx, k.one); sq8(xf[i], sxx); }
  PIN2(sx, sxx);
  lds_bar();
  f32x4 e[6];
  SB(); qm<ISV, false>(A.a0, xf[24], w0); w0 = qw<25, 0>(k); e[0] = *(const LAS f32x4*)(xr); e[1] = *(const LAS f32x4*)(xr + 512);
  SB(); qm<ISV, false>(A.a1, xf[24], w1); w1 = qw<25, 1>(k); e[2] = *(const LAS f32x4*)(xr + 1024); e[3] = *(const LAS f32x4*)(xr + 1536);
  SB(); qm<ISV, false>(A.a2, xf[24], w2); w2 = qw<25, 2>(k); e[4] = *(const LAS f32x4*)(xr + 2048); e[5] = *(const LAS f32x4*)(xr + 2560);
  SB(); qm<ISV, false>(A.a0, xf[25], w0); w0 = qw<26, 0>(k); sx = hsum(sx); PIN1(sx); piece<8, 0>(k);
  SB(); qm<ISV, false>(A.a1, xf[25], w1); w1 = qw<26, 1>(k); sxx = hsum(sxx); PIN1(sxx); piece<8, 1>(k);
  SB(); qm<ISV, false>(A.a2, xf[25], w2); w2 = qw<26, 2>(k);
  const float mu = sx * (1.f / 448.f); float rstd = __builtin_amdgcn_rsqf(fmaxf(sxx * (1.f / 448.f) - mu * mu, 0.f) + 1e-5f); PIN1(rstd); piece<8, 2>(k);
  SB(); qm<ISV, false>(A.a0, xf[26], w0); w0 = qw<27, 0>(k); xc = corr_x(-mu, k.hh); PIN1(xc);
#define ESUM(J) (((e[2 * J][0] + e[2 * J][1]) + (e[2 * J][2] + e[2 * J][3])) + ((e[2 * J + 1][0] + e[2 * J + 1][1]) + (e[2 * J + 1][2] + e[2 * J + 1][3])))
  SB(); qm<ISV, false>(A.a1, xf[26], w1); w1 = qw<27, 1>(k); cs[0] = ESUM(0); PIN1(cs[0]); piece<8, 3>(k);
  SB(); qm<ISV, false>(A.a2, xf[26], w2); w2 = qw<27, 2>(k); cs[1] = ESUM(1); PIN1(cs[1]); piece<8, 4>(k);
  SB(); qm<ISV, false>(A.a0, xf[27], w0); cs[2] = ESUM(2); u32x4 k0 = corr_w(cs[0], k.hh); PIN2(cs[2], k0);
  SB(); qm<ISV, false>(A.a1, xf[27], w1); u32x4 k1 = corr_w(cs[1], k.hh); PIN1(k1);
  SB(); qm<ISV, false>(A.a2, xf[27], w2); u32x4 k2 = corr_w(cs[2], k.hh); PIN1(k2);
  SB();
  STAMP(21);
  if constexpr (ISV) {
    const u32 h = cvtpk(rstd, rstd); const float r1 = rstd - __uint_as_float(h & 0xffff0000u);
    const u32 d0 = cvtpk(rstd, r1); const float r2 = r1 - __uint_as_float(d0 & 0xffff0000u);
    const u32x4 ra = {k.hh ? 0u : d0, k.hh ? 0u : (cvtpk(r2, r2) & 0xffffu), 0u, 0u}, one = {ONES, ONES, 0u, 0u};
    f32x16 rb; asm volatile("s_nop 1\n\tv_mfma_f32_32x32x16_bf16 %0, %1, %2, 0" : "=&v"(rb) : "v"(ra), "v"(one));
    mfvn(A.a0, xc, k0); mfvn(A.a1, xc, k1); mfvn(A.a2, xc, k2);
    { u32x4 cc = {p0.oc, p0.oc, p0.oc, p0.oc}; stc(k.ws, cc, p0.ob, 0); stc(k.ws, cc, p0.ob + 1024, 0); }
    { u32x4 cc = {p1.oc, p1.oc, p1.oc, p1.oc}; stc(k.ws, cc, p1.ob, 0); stc(k.ws, cc, p1.ob + 1024, 0); }
    { u32x4 cc = {p2.oc, p2.oc, p2.oc, p2.oc}; stc(k.ws, cc, p2.ob, 0); stc(k.ws, cc, p2.ob + 1024, 0); }
    asm volatile("s_nop 7\n\ts_nop 3" : "+v"(A.a0), "+v"(A.a1), "+v"(A.a2), "+v"(rb));
    v_store(k, p0, A.a0, rb); v_store(k, p1, A.a1, rb); v_store(k, p2, A.a2, rb);
  } else {
    mfvn(A.a0, k0, xc); mfvn(A.a1, k1, xc); mfvn(A.a2, k2, xc);
    asm volatile("s_nop 5" : "+v"(A.a0));
    qk_store(k, p0, A.a0, rstd); SB();
    asm volatile("" : "+v"(A.a1), "+v"(A.a2));
    qk_store(k, p1, A.a1, rstd); qk_store(k, p2, A.a2, rstd);
  }
}

template <int I> DI void apf(const Ctx& k) {
  if constexpr (I < 2) ld_acc<96 + 4 * I, I * 1024>(k.rkv, k.l16, 0u);
  else if constexpr (I < 10) ld_acc<64 + 4 * (I - 2), ((I - 2) & 1) * 1024>(k.ws, k.l16, k.qso ^ (u32)(((I - 2) >> 1) * 2048));
  else if constexpr (I < 23) ld_acc<96 + 4 * (I - 8), ((I - 8) & 3) * 1024>(k.rkv, k.l16, (u32)(((I - 8) >> 2) * 4096));
}
template <int I> DI void apf_all(const Ctx& k) { apf<I>(k); if constexpr (I < 22) apf_all<I + 1>(k); }

struct ZF { u32x4 o, w[4]; };
template <int NCH> DI u32x4 zd(const Ctx& k) { return lds128(k.od + (NCH & 3) * OS); }
template <int KS> DI u32x4 zo(const Ctx& k) { return lds128(k.ob[KS & 3] + ((KS >> 2) & 3) * OS); }
template <int KS, int J> DI u32x4 zw(const Ctx& k) { constexpr u32 wb = (((KS >> 2) + 2) & 3) * 16384 + (KS & 3) * 4096; return ldstr(k.zb[J] + wb, k.zb[J] + wb + 1024); }
template <int KS> DI ZF z_read(const Ctx& k) { ZF f; f.w[0] = zw<KS, 0>(k); f.w[1] = zw<KS, 1>(k); f.w[2] = zw<KS, 2>(k); f.w[3] = zw<KS, 3>(k); f.o = zo<KS>(k); return f; }
DI void vsum2(u32 d, float& s0, float& s1) { s0 += __uint_as_float(d << 16); s1 += __uint_as_float(d & 0xffff0000u); PIN2(s0, s1); }
template <int KS> DI void z_step(const Ctx& k, ZF cur, u32x4 dd, f32x16& wp, const u32x4 (&xf)[28], float (&so)[2], u32& fs) {
  ZF nxt;
  constexpr bool N = KS < 27; constexpr int KN = N ? KS + 1 : KS;
  constexpr int NP = 7 + ((KS + 1) >> 2) + 2, S = (KS + 1) & 3, AP = KS >= 19 ? 3 * (KS - 19) : 99;
  if constexpr (N && (KN & 3) == 0) {
    if constexpr (KN == 4) vm_bar<14>();
    else if constexpr (KN == 20) { asm volatile("s_waitcnt vmcnt(5)\n\ts_barrier" : "+v"(fs) :: "memory"); if (__builtin_amdgcn_ballot_w64(fs != k.tag) != 0) poll(k.ws, k.f1, k.tag); }
    else if constexpr (KN == 24) vm_bar<12>();
    else vm_bar<5>();
  }
  SB();
  mf<0, KS == 0>(cur.w[0], xf[KS]); if constexpr (N) nxt.w[0] = zw<KN, 0>(k);
  if constexpr (KS == 0) piece<9, 0>(k);
  if constexpr (KS == 7) stc1(k.ws, k.tag, k.f1p);
  if constexpr (KS == 15) asm volatile("buffer_load_dword %0, %1, %2, 0 offen sc1" : "=v"(fs) : "v"(k.f1), "s"(k.ws) : "memory");
  apf<AP>(k); SB();
  mf<16, KS == 0>(cur.w[1], xf[KS]); if constexpr (N) nxt.w[1] = zw<KN, 1>(k); if constexpr (S == 0 || KS == 0) piece<NP, 4>(k); SB();
  mf<32, KS == 0>(cur.w[2], xf[KS]); if constexpr (N) nxt.w[2] = zw<KN, 2>(k); apf<AP + 1>(k); SB();
  mf<48, KS == 0>(cur.w[3], xf[KS]); if constexpr (N) nxt.w[3] = zw<KN, 3>(k); piece<NP, S>(k); apf<AP + 2>(k); SB();
  mfv<KS == 0>(wp, cur.o, cur.w[0]); if constexpr (N) nxt.o = zo<KN>(k); vsum2(dd[KS & 3], so[0], so[1]);
  if constexpr (N && S == 0) dd = zd<(KN >> 2)>(k);
  SB();
  if constexpr (N) z_step<KN>(k, nxt, dd, wp, xf, so, fs);
}

template <int J> DI void hs_read(float (&a)[16]) { acc16<16 * J>(a); }
template <int J, int s> DI void hs_put(const Ctx& k, const float (&a)[16]) {
  const u32 so = WS_H + ((4 * k.g + k.w) * 112 + 2 * (4 * k.c + ((k.w + J) & 3))) * 1024;
  u32x4 d = {cvtpk(fmaxf(a[8 * s], 0.f), fmaxf(a[8 * s + 1], 0.f)), cvtpk(fmaxf(a[8 * s + 2], 0.f), fmaxf(a[8 * s + 3], 0.f)),
             cvtpk(fmaxf(a[8 * s + 4], 0.f), fmaxf(a[8 * s + 5], 0.f)), cvtpk(fmaxf(a[8 * s + 6], 0.f), fmaxf(a[8 * s + 7], 0.f))};
  stc(k.ws, d, k.lane * 16, so + s * 1024);
}
template <int A> DI void tile_send(u32 w, u32 lane) {
  const u32 a1 = CMB + ((w ^ 1) * 3 + 0) * 4096 + lane * 16, a2 = CMB + ((w ^ 2) * 3 + 1) * 4096 + lane * 16, a3 = CMB + ((w ^ 3) * 3 + 2) * 4096 + lane * 16;
  dsw_acc<A + 16, 0>(a1); dsw_acc<A + 20, 1024>(a1); dsw_acc<A + 24, 2048>(a1); dsw_acc<A + 28, 3072>(a1);
  dsw_acc<A + 32, 0>(a2); dsw_acc<A + 36, 1024>(a2); dsw_acc<A + 40, 2048>(a2); dsw_acc<A + 44, 3072>(a2);
  dsw_acc<A + 48, 0>(a3); dsw_acc<A + 52, 1024>(a3); dsw_acc<A + 56, 2048>(a3); dsw_acc<A + 60, 3072>(a3);
}
DI void tile_put(u32 a, const f32x16& o) {
#pragma unroll
  for (int q = 0; q < 4; ++q) { f32x4 v = {o[4 * q], o[4 * q + 1], o[4 * q + 2], o[4 * q + 3]}; *(LAS f32x4*)(a + q * 1024) = v; }
}
DI void tile_recv(u32 w, u32 lane, float (&o)[16]) {
  const u32 a = CMB + w * 12288 + lane * 16;
  f32x4 v[12];
#pragma unroll
  for (int p = 0; p < 12; ++p) v[p] = *(const LAS f32x4*)(a + p * 1024);
#pragma unroll
  for (int p = 0; p < 12; ++p) { const int q = p & 3; o[4 * q] += v[p][0]; o[4 * q + 1] += v[p][1]; o[4 * q + 2] += v[p][2]; o[4 * q + 3] += v[p][3]; }
}

__global__ __launch_bounds__(256, 1) void fused_block(const char* __restrict__ x, const char* __restrict__ wqkv, const char* __restrict__ wo, const char* __restrict__ w1,
                                                       const char* __restrict__ w2, char* __restrict__ out, char* ws) {
  asm volatile("" ::: "a0", "a255");
  Ctx k;
  const u32 b = blockIdx.x, xcd = b & 7, bi = b >> 3, loc = 2 * xcd + (bi >> 4), oth = bi & 15;
  k.lane = threadIdx.x & 63; k.r = k.lane & 31; k.hh = k.lane >> 5; k.w = rfl(threadIdx.x >> 6); k.g = loc; k.c = oth;
  asm volatile("" : "+s"(k.w));
  u32 lane = k.lane; const u32 w = k.w, c = k.c, g = k.g;
  k.rx = __builtin_amdgcn_make_buffer_rsrc((void*)x, 0, 2048 * 896, 0x00020000);
  k.rqkv = __builtin_amdgcn_make_buffer_rsrc((void*)wqkv, 0, 448 * 2688, 0x00020000);
  k.rwo = __builtin_amdgcn_make_buffer_rsrc((void*)wo, 0, 448 * 896, 0x00020000);
  k.rw1 = __builtin_amdgcn_make_buffer_rsrc((void*)w1, 0, 448 * 3584, 0x00020000);
  k.rw2 = __builtin_amdgcn_make_buffer_rsrc((void*)w2, 0, 1792 * 896, 0x00020000);
  k.ws = __builtin_amdgcn_make_buffer_rsrc((void*)ws, 0, WS_END, 0x00020000);
  const u32 Tn = 3 * c - (c >= 10 ? 2 : 0), Tm = c < 10 ? 25 : 39, T0 = Tn < Tm ? Tn : Tm, first = Tn - T0;
  u32 gen;
  {
    const u32 rr = lane >> 3, cp = lane & 7;
    k.vx0 = rr * 896 + ((cp ^ (rr >> 1)) << 4); k.vx1 = k.vx0 ^ 64;
#pragma unroll
    for (int j = 0; j < 3; ++j) { const u32 e = (3 * w + j) * 64 + lane, row = (e * 5462u) >> 16; k.vw[j] = row * (2688 - 192) + e * 16; }
    k.sx = (128 * g + 32 * w) * 896; k.sw = T0 * 64; k.s1 = 16 * w * 3584 + 256 * c; k.so = 28 * g * 896; k.w4k = w * 4096; k.w3k = w * 3072; k.w1k = w * 1024;
    stage<0>(k);
    asm volatile("s_load_dword %0, %1, 0" : "=s"(gen) : "s"(ws) : "memory");
    const u32 wr = 8 * w + rr; k.vwo = (wr < 27 ? wr : 27) * 896 + ((cp ^ ((rr >> 1) | ((w >> 1) << 2))) << 4);
    k.vw1 = (lane >> 4) * 3584 + (((lane & 15) ^ ((lane >> 4) << 2)) << 4);
    const u32 f = ((k.r & 7) >> 1) | ((k.r >> 4) << 2);
#pragma unroll
    for (int s = 0; s < 4; ++s) { const u32 xa = k.r * 128 + (((2 * s + k.hh) ^ f) << 4); k.xb[s] = XR + w * 4096 + xa; k.ob[s] = OR_ + (k.r < 28 ? xa : 4096u); PIN1(k.xb[s]); }
    piece<1, 0>(k); piece<1, 4>(k);
    const u32 q = (lane & 15) >> 2, p = lane & 3, cb = ((lane >> 4) & 1) * 32 + p * 8, pp = c < 10 ? ((p & 1) * 2 + (p >> 1)) : p;
    k.od = OR_ + k.r * 128 + (((k.hh * 4 + w) ^ f) << 4);
    if (lane < 16) *(LAS u32*)(OR_ + (lane >> 2) * OS + 4096 + (lane & 3) * 4) = ONES;
    k.wa = WR + (8 * k.hh + q) * 192 + ((lane >> 4) & 1) * 32 + pp * 8; k.wc = k.wa + w * 3072; k.one = ONES;
    PIN2(k.wa, k.wc); PIN1(k.one);
#pragma unroll
    for (int j = 0; j < 4; ++j) { k.zb[j] = ZR + (8 * k.hh + q) * 256 + ((((w + j) & 3) ^ q) << 6) + cb; }
    piece<1, 1>(k); piece<1, 5>(k);
    const u32 sl = lane & 3, k0 = (448 + 28 * loc + (sl & 1) * 27) >> 5, v0 = (28 * loc + (sl & 1) * 27) >> 5;
    const u32 ck = k0 >= 27 ? 9 : (k0 * 43) >> 7, cq = (v0 * 43) >> 7, cv = v0 >= 12 ? 14 : 10 + cq;
    const u32 gi = lane < 16 ? 4 * w + (lane >> 2) : oth, ci = lane < 16 ? (sl < 2 ? ck : cv) : cq;
    k.f1 = WS_F1 + (xcd * 256 + gi * 16 + ci) * 4; k.l16 = lane * 16;
    park<240>(k.ob[0]); park<241>(k.ob[1]); park<242>(k.ob[2]); park<243>(k.ob[3]); park<244>(k.zb[0]); park<245>(k.zb[1]); park<246>(k.zb[2]); park<247>(k.zb[3]);
    park<248>(k.od); park<249>(k.f1); park<250>(k.l16); park<251>((w == 0 && lane < 8) ? WS_F1 + (lane * 256 + g * 16 + c) * 4 : OOB);
    piece<1, 2>(k); piece<1, 6>(k); piece<1, 3>(k);
    k.rkv = __builtin_amdgcn_make_buffer_rsrc((void*)(ws + WS_KV + (loc * 64 + w * 16) * 4096), 0, 65536, 0x00020000);
    k.qso = WS_Q + (loc * 64 + oth * 4 + w) * 2048;
  }
  asm volatile("s_waitcnt lgkmcnt(0)" : "+s"(gen) :: "memory"); k.tag = gen + 1;
  const u32 tag = k.tag;
  u32x4 xf[28], xc;
  if (c < 10) qkv_phase<false>(k, T0, first, xf, xc); else qkv_phase<true>(k, T0, first, xf, xc);
  STAMP(1);
  k.zb[0] = unpark<244>(); k.zb[1] = unpark<245>(); k.zb[2] = unpark<246>(); k.zb[3] = unpark<247>(); k.ob[0] = unpark<240>(); k.od = unpark<248>();
  k.ob[1] = unpark<241>(); k.ob[2] = unpark<242>(); k.ob[3] = unpark<243>(); k.f1 = unpark<249>(); k.l16 = unpark<250>(); k.f1p = unpark<251>();
  RELANE();
  if (c < 14) {
    wait_bar<14>();
    STAMP(2);
    float so[2] = {0.f, 0.f}; f32x16 wp; u32 fs;
    z_step<0>(k, z_read<0>(k), zd<0>(k), wp, xf, so, fs);
    STAMP(28);
    asm volatile("s_nop 7" : "+v"(wp));
    const float cs0 = __uint_as_float(hi_half(__float_as_uint(wp[12])));
    *(LAS float*)(XCH + 4096 + w * 128 + k.r * 4) = cs0;
    *(LAS float*)(XCH + 4608 + lane * 16 + w * 4) = so[0] + so[1];
    lds_bar();
    const f32x4 sv = *(const LAS f32x4*)(XCH + 4608 + k.r * 16), sw = *(const LAS f32x4*)(XCH + 4608 + 512 + k.r * 16);
    const float cs1 = *(const LAS float*)(XCH + 4096 + ((w + 1) & 3) * 128 + k.r * 4), cs2 = *(const LAS float*)(XCH + 4096 + ((w + 2) & 3) * 128 + k.r * 4), cs3 = *(const LAS float*)(XCH + 4096 + ((w + 3) & 3) * 128 + k.r * 4);
    const u32x4 c0 = corr_w(cs0, k.hh);
    mfa<0>(c0, xc);
    mfvn(wp, corr_x((((sv[0] + sv[1]) + (sv[2] + sv[3])) + ((sw[0] + sw[1]) + (sw[2] + sw[3]))) * (-1.f / 448.f), k.hh), c0);
    mfa<16>(corr_w(cs1, k.hh), xc); mfa<32>(corr_w(cs2, k.hh), xc); mfa<48>(corr_w(cs3, k.hh), xc);
    asm volatile("s_nop 3" : "+v"(wp));
#pragma unroll
    for (int s = 0; s < 2; ++s) {
      u32x4 d = {cvtpk(wp[8 * s], wp[8 * s + 1]), cvtpk(wp[8 * s + 2], wp[8 * s + 3]), cvtpk(wp[8 * s + 4], wp[8 * s + 5]), cvtpk(wp[8 * s + 6], wp[8 * s + 7])};
      stc(k.ws, d, k.l16, WS_WP + ((c * 32 + 2 * g + s) * 4 + w) * 1024);
    }
  } else {
    wait_bar<0>();
    stc1(k.ws, tag, k.f1p);
    const u32 e0 = k.r - 8 * k.hh, e1 = e0 - 16;
    u32x4 i0, i1;
#pragma unroll
    for (int j = 0; j < 4; ++j) { i0[j] = e0 == 2 * j ? 0x3f80u : (e0 == 2 * j + 1 ? 0x3f800000u : 0u); i1[j] = e1 == 2 * j ? 0x3f80u : (e1 == 2 * j + 1 ? 0x3f800000u : 0u); }
    if (c == 14) {
      mfa0<0>(i0, xf[0]); mfa<0>(i1, xf[1]); mfa0<16>(i0, xf[2]); mfa<16>(i1, xf[3]); mfa0<32>(i0, xf[4]); mfa<32>(i1, xf[5]); mfa0<48>(i0, xf[6]); mfa<48>(i1, xf[7]);
      mfa0<160>(i0, xf[8]); mfa<160>(i1, xf[9]); mfa0<176>(i0, xf[10]); mfa<176>(i1, xf[11]); mfa0<192>(i0, xf[12]); mfa<192>(i1, xf[13]);
    } else {
      mfa0<0>(i0, xf[14]); mfa<0>(i1, xf[15]); mfa0<16>(i0, xf[16]); mfa<16>(i1, xf[17]); mfa0<32>(i0, xf[18]); mfa<32>(i1, xf[19]); mfa0<48>(i0, xf[20]); mfa<48>(i1, xf[21]);
      mfa0<160>(i0, xf[22]); mfa<160>(i1, xf[23]); mfa0<176>(i0, xf[24]); mfa<176>(i1, xf[25]); mfa0<192>(i0, xf[26]); mfa<192>(i1, xf[27]);
    }
    const u32 e = 2 * g + (c - 14);
    if (e < 28) {
      for (u32 i = 0; i < 14; ++i) glds(k.rw2, lane * 16, e * 57344 + (w * 14 + i) * 1024, (w * 14 + i) * 1024);
      wait_vm<0>();
      const u32 q = (lane & 15) >> 2, ta = (16 * w + 4 * k.hh + q) * 896 + ((lane >> 4) & 1) * 32 + (lane & 3) * 8;
      for (u32 nt = 0; nt < 14; ++nt) stc(k.ws, ldstr(ta + nt * 64, ta + nt * 64 + 8 * 896), lane * 16, WS_W2F + (((nt >> 1) * 112 + 4 * e + w) * 2 + (nt & 1)) * 1024);
      wait_bar<0>();
      stc1(k.ws, tag, (w == 0 && lane < 8) ? WS_F6 + lane * 128 + e * 4 : OOB);
    }
    for (u32 i = w; i < 112; i += 4) { const u32 ee = i * 64 + lane, row = (ee * 9363u) >> 18; glds(k.rwo, row * 896 + (ee - 28 * row) * 16, (c - 14) * 448, i * 1024); }
    poll(k.ws, k.f1, tag);
    apf_all<0>(k);
  }
  STAMP(3);
  {
    const u32 hd = loc, rb = oth;
    const unsigned long long mke = (hd & 1) ? 0ull : 0xffffffff00000000ull, mko = (hd & 1) ? 0x00000000ffffffffull : 0ull;
    float o[16];
#pragma nounroll
    for (u32 attempt = 0; attempt < 2; ++attempt) {
      u32 so = 3 * 4096, mo, mc = attempt ? 17 : 1, c1 = (c < 14 && !attempt) ? 2 : 4, c2 = 4 - c1, gm0 = w * 1024, gso = WS_WP + c * 131072 + w * 1024;
      unsigned long long ms;
      f32x16 o0 = {}, o1 = {}, o2 = {}, o3 = {};
      asm volatile(
      "s_mov_b32 %[mo], 0\n\t"
      "v_mov_b32 v188, 0\n\t"
      "v_accvgpr_write_b32 a156, 0\n\t"
      "v_mov_b32 v180, 0xff800000\n\t"
      "v_mov_b32 v189, 0\n\t"
      "v_accvgpr_write_b32 a157, 0\n\t"
      "v_mov_b32 v181, 0xff800000\n\t"
      "v_mov_b32 v190, 0\n\t"
      "v_accvgpr_write_b32 a158, 0\n\t"
      "v_mov_b32 v182, 0xff800000\n\t"
      "v_mov_b32 v191, 0\n\t"
      "v_accvgpr_write_b32 a159, 0\n\t"
      "v_mov_b32 v183, 0xff800000\n\t"
      "s_waitcnt vmcnt(13)\n\t"
      "mx_%=:\n\t"
      "v_mfma_f32_32x32x16_bf16 v[192:207], a[96:99], a[64:67], 0\n\t"
      "v_mfma_f32_32x32x16_bf16 v[208:223], a[96:99], a[72:75], 0\n\t"
      "v_mfma_f32_32x32x16_bf16 v[224:239], a[96:99], a[80:83], 0\n\t"
      "v_mfma_f32_32x32x16_bf16 v[240:255], a[96:99], a[88:91], 0\n\t"
      "v_mfma_f32_32x32x16_bf16 v[192:207], a[100:103], a[68:71], v[192:207]\n\t"
      "v_mfma_f32_32x32x16_bf16 v[208:223], a[100:103], a[76:79], v[208:223]\n\t"
      "v_mfma_f32_32x32x16_bf16 v[224:239], a[100:103], a[84:87], v[224:239]\n\t"
      "v_mfma_f32_32x32x16_bf16 v[240:255], a[100:103], a[92:95], v[240:255]\n\t"
      "v_xor_b32 v184, 0, %[mxa]\n\t"
      "v_xor_b32 v185, 512, %[mxa]\n\t"
      "v_xor_b32 v186, 1024, %[mxa]\n\t"
      "v_xor_b32 v187, 1536, %[mxa]\n\t"
      "s_nop 5\n\t"
      "v_max3_f32 v180, v192, v193, v180\n\t"
      "v_max3_f32 v180, v194, v195, v180\n\t"
      "v_max3_f32 v180, v196, v197, v180\n\t"
      "v_max3_f32 v180, v198, v199, v180\n\t"
      "v_max3_f32 v180, v200, v201, v180\n\t"
      "v_max3_f32 v180, v202, v203, v180\n\t"
      "v_max3_f32 v180, v204, v205, v180\n\t"
      "v_max3_f32 v180, v206, v207, v180\n\t"
      "v_max3_f32 v181, v208, v209, v181\n\t"
      "v_max3_f32 v181, v210, v211, v181\n\t"
      "v_max3_f32 v181, v212, v213, v181\n\t"
      "v_max3_f32 v181, v214, v215, v181\n\t"
      "v_max3_f32 v181, v216, v217, v181\n\t"
      "v_max3_f32 v181, v218, v219, v181\n\t"
      "v_max3_f32 v181, v220, v221, v181\n\t"
      "v_max3_f32 v181, v222, v223, v181\n\t"
      "v_max3_f32 v182, v224, v225, v182\n\t"
      "v_max3_f32 v182, v226, v227, v182\n\t"
      "v_max3_f32 v182, v228, v229, v182\n\t"
      "v_max3_f32 v182, v230, v231, v182\n\t"
      "v_max3_f32 v182, v232, v233, v182\n\t"
      "v_max3_f32 v182, v234, v235, v182\n\t"
      "v_max3_f32 v182, v236, v237, v182\n\t"
      "v_max3_f32 v182, v238, v239, v182\n\t"
      "v_max3_f32 v183, v240, v241, v183\n\t"
      "v_max3_f32 v183, v242, v243, v183\n\t"
      "v_max3_f32 v183, v244, v245, v183\n\t"
      "v_max3_f32 v183, v246, v247, v183\n\t"
      "v_max3_f32 v183, v248, v249, v183\n\t"
      "v_max3_f32 v183, v250, v251, v183\n\t"
      "v_max3_f32 v183, v252, v253, v183\n\t"
      "v_max3_f32 v183, v254, v255, v183\n\t"
      "s_sub_u32 %[mc], %[mc], 1\n\t"
      "s_cmp_eq_u32 %[mc], 0\n\t"
      "s_cbranch_scc1 mxd_%=\n\t"
      "s_add_u32 %[mo], %[mo], 0x1000\n\t"
      "s_and_b32 %[mo], %[mo], 0xffff\n\t"
      "buffer_load_dwordx4 a[96:99], %[voff], %[srd], %[mo] offen offset:0 sc1\n\t"
      "buffer_load_dwordx4 a[100:103], %[voff], %[srd], %[mo] offen offset:1024 sc1\n\t"
      "s_waitcnt vmcnt(0)\n\t"
      "s_branch mx_%=\n\t"
      "mxd_%=:\n\t"
      "v_add_u32 v240, %[w4], v184\n\t"
      "v_add_u32 v241, %[w4], v185\n\t"
      "v_add_u32 v242, %[w4], v186\n\t"
      "v_add_u32 v243, %[w4], v187\n\t"
      "ds_write_b32 v240, v180\n\t"
      "ds_write_b32 v241, v181\n\t"
      "ds_write_b32 v242, v182\n\t"
      "ds_write_b32 v243, v183\n\t"
      "s_waitcnt lgkmcnt(0)\n\t"
      "s_barrier\n\t"
      "ds_read_b128 v[224:227], v184\n\t"
      "ds_read_b128 v[228:231], v184 offset:2048\n\t"
      "ds_read_b128 v[232:235], v185\n\t"
      "ds_read_b128 v[236:239], v185 offset:2048\n\t"
      "ds_read_b128 v[240:243], v186\n\t"
      "ds_read_b128 v[244:247], v186 offset:2048\n\t"
      "ds_read_b128 v[248:251], v187\n\t"
      "ds_read_b128 v[252:255], v187 offset:2048\n\t"
      "s_waitcnt lgkmcnt(6)\n\t"
      "v_max3_f32 v180, v224, v225, v226\n\t"
      "v_max3_f32 v228, v228, v229, v230\n\t"
      "v_max3_f32 v180, v180, v227, v231\n\t"
      "v_max_f32 v180, v180, v228\n\t"
      "v_cvt_pk_bf16_f32 v224, -v180, -v180\n\t"
      "v_and_b32 v224, 0xffff0000, v224\n\t"
      "v_sub_f32 v225, -v180, v224\n\t"
      "v_cvt_pk_bf16_f32 v224, -v180, v225\n\t"
      "v_and_b32 v226, 0xffff0000, v224\n\t"
      "v_sub_f32 v225, v225, v226\n\t"
      "v_cvt_pk_bf16_f32 v225, v225, 0\n\t"
      "s_mov_b64 exec, %[mko]\n\t"
      "v_accvgpr_write_b32 a64, v224\n\t"
      "v_accvgpr_write_b32 a65, v225\n\t"
      "s_mov_b64 exec, %[mke]\n\t"
      "v_accvgpr_write_b32 a70, v224\n\t"
      "v_accvgpr_write_b32 a71, v225\n\t"
      "s_mov_b64 exec, -1\n\t"
      "s_waitcnt lgkmcnt(4)\n\t"
      "v_max3_f32 v181, v232, v233, v234\n\t"
      "v_max3_f32 v236, v236, v237, v238\n\t"
      "v_max3_f32 v181, v181, v235, v239\n\t"
      "v_max_f32 v181, v181, v236\n\t"
      "v_cvt_pk_bf16_f32 v232, -v181, -v181\n\t"
      "v_and_b32 v232, 0xffff0000, v232\n\t"
      "v_sub_f32 v233, -v181, v232\n\t"
      "v_cvt_pk_bf16_f32 v232, -v181, v233\n\t"
      "v_and_b32 v234, 0xffff0000, v232\n\t"
      "v_sub_f32 v233, v233, v234\n\t"
      "v_cvt_pk_bf16_f32 v233, v233, 0\n\t"
      "s_mov_b64 exec, %[mko]\n\t"
      "v_accvgpr_write_b32 a72, v232\n\t"
      "v_accvgpr_write_b32 a73, v233\n\t"
      "s_mov_b64 exec, %[mke]\n\t"
      "v_accvgpr_write_b32 a78, v232\n\t"
      "v_accvgpr_write_b32 a79, v233\n\t"
      "s_mov_b64 exec, -1\n\t"
      "s_nop 0\n\t"
      "v_mfma_f32_32x32x16_bf16 v[192:207], a[96:99], a[64:67], 0\n\t"
      "v_mfma_f32_32x32x16_bf16 v[192:207], a[100:103], a[68:71], v[192:207]\n\t"
      "s_waitcnt lgkmcnt(2)\n\t"
      "v_max3_f32 v182, v240, v241, v242\n\t"
      "v_max3_f32 v244, v244, v245, v246\n\t"
      "v_max3_f32 v182, v182, v243, v247\n\t"
      "v_max_f32 v182, v182, v244\n\t"
      "v_cvt_pk_bf16_f32 v240, -v182, -v182\n\t"
      "v_and_b32 v240, 0xffff0000, v240\n\t"
      "v_sub_f32 v241, -v182, v240\n\t"
      "v_cvt_pk_bf16_f32 v240, -v182, v241\n\t"
      "v_and_b32 v242, 0xffff0000, v240\n\t"
      "v_sub_f32 v241, v241, v242\n\t"
      "v_cvt_pk_bf16_f32 v241, v241, 0\n\t"
      "s_mov_b64 exec, %[mko]\n\t"
      "v_accvgpr_write_b32 a80, v240\n\t"
      "v_accvgpr_write_b32 a81, v241\n\t"
      "s_mov_b64 exec, %[mke]\n\t"
      "v_accvgpr_write_b32 a86, v240\n\t"
      "v_accvgpr_write_b32 a87, v241\n\t"
      "s_mov_b64 exec, -1\n\t"
      "s_nop 0\n\t"
      "v_mfma_f32_32x32x16_bf16 v[208:223], a[96:99], a[72:75], 0\n\t"
      "v_mfma_f32_32x32x16_bf16 v[208:223], a[100:103], a[76:79], v[208:223]\n\t"
      "s_waitcnt lgkmcnt(0)\n\t"
      "v_max3_f32 v183, v248, v249, v250\n\t"
      "v_max3_f32 v252, v252, v253, v254\n\t"
      "v_max3_f32 v183, v183, v251, v255\n\t"
      "v_max_f32 v183, v183, v252\n\t"
      "v_cvt_pk_bf16_f32 v248, -v183, -v183\n\t"
      "v_and_b32 v248, 0xffff0000, v248\n\t"
      "v_sub_f32 v249, -v183, v248\n\t"
      "v_cvt_pk_bf16_f32 v248, -v183, v249\n\t"
      "v_and_b32 v250, 0xffff0000, v248\n\t"
      "v_sub_f32 v249, v249, v250\n\t"
      "v_cvt_pk_bf16_f32 v249, v249, 0\n\t"
      "s_mov_b64 exec, %[mko]\n\t"
      "v_accvgpr_write_b32 a88, v248\n\t"
      "v_accvgpr_write_b32 a89, v249\n\t"
      "s_mov_b64 exec, %[mke]\n\t"
      "v_accvgpr_write_b32 a94, v248\n\t"
      "v_accvgpr_write_b32 a95, v249\n\t"
      "s_mov_b64 exec, -1\n\t"
      "v_mov_b32 v182, %[tag]\n\t"
      "s_waitcnt vmcnt(11)\n\t"
      "la_%=:\n\t"
      "v_exp_f32 v192, v192\n\t"
      "v_exp_f32 v193, v193\n\t"
      "v_mfma_f32_32x32x16_bf16 %[o3], a[156:159], v[188:191], %[o3]\n\t"
      "v_exp_f32 v194, v194\n\t"
      "v_exp_f32 v195, v195\n\t"
      "v_cvt_pk_bf16_f32 v184, v192, v193\n\t"
      "v_cvt_pk_bf16_f32 v185, v194, v195\n\t"
      "v_exp_f32 v196, v196\n\t"
      "v_exp_f32 v197, v197\n\t"
      "v_mfma_f32_32x32x16_bf16 v[224:239], a[96:99], a[80:83], 0\n\t"
      "buffer_load_dwordx4 a[156:159], %[voff], %[srd], %[so] offen offset:3072 sc1\n\t"
      "s_add_u32 %[so], %[so], 0x1000\n\t"
      "v_exp_f32 v198, v198\n\t"
      "v_exp_f32 v199, v199\n\t"
      "v_cvt_pk_bf16_f32 v186, v196, v197\n\t"
      "v_cvt_pk_bf16_f32 v187, v198, v199\n\t"
      "v_exp_f32 v200, v200\n\t"
      "v_exp_f32 v201, v201\n\t"
      "v_mfma_f32_32x32x16_bf16 %[o0], a[104:107], v[184:187], %[o0]\n\t"
      "v_exp_f32 v202, v202\n\t"
      "v_exp_f32 v203, v203\n\t"
      "v_cvt_pk_bf16_f32 v188, v200, v201\n\t"
      "v_cvt_pk_bf16_f32 v189, v202, v203\n\t"
      "v_exp_f32 v204, v204\n\t"
      "v_exp_f32 v205, v205\n\t"
      "v_mfma_f32_32x32x16_bf16 v[224:239], a[100:103], a[84:87], v[224:239]\n\t"
      "v_exp_f32 v206, v206\n\t"
      "v_exp_f32 v207, v207\n\t"
      "v_cvt_pk_bf16_f32 v190, v204, v205\n\t"
      "v_cvt_pk_bf16_f32 v191, v206, v207\n\t"
      "v_exp_f32 v208, v208\n\t"
      "v_exp_f32 v209, v209\n\t"
      "v_mfma_f32_32x32x16_bf16 %[o0], a[108:111], v[188:191], %[o0]\n\t"
      "v_exp_f32 v210, v210\n\t"
      "v_exp_f32 v211, v211\n\t"
      "v_cvt_pk_bf16_f32 v184, v208, v209\n\t"
      "v_cvt_pk_bf16_f32 v185, v210, v211\n\t"
      "v_exp_f32 v212, v212\n\t"
      "v_exp_f32 v213, v213\n\t"
      "v_mfma_f32_32x32x16_bf16 v[240:255], a[96:99], a[88:91], 0\n\t"
      "v_exp_f32 v214, v214\n\t"
      "v_exp_f32 v215, v215\n\t"
      "v_cvt_pk_bf16_f32 v186, v212, v213\n\t"
      "v_cvt_pk_bf16_f32 v187, v214, v215\n\t"
      "v_exp_f32 v216, v216\n\t"
      "v_exp_f32 v217, v217\n\t"
      "v_mfma_f32_32x32x16_bf16 %[o1], a[104:107], v[184:187], %[o1]\n\t"
      "buffer_load_dwordx4 a[96:99], %[voff], %[srd], %[so] offen offset:0 sc1\n\t"
      "v_exp_f32 v218, v218\n\t"
      "v_exp_f32 v219, v219\n\t"
      "v_cvt_pk_bf16_f32 v188, v216, v217\n\t"
      "v_cvt_pk_bf16_f32 v189, v218, v219\n\t"
      "v_exp_f32 v220, v220\n\t"
      "v_exp_f32 v221, v221\n\t"
      "v_mfma_f32_32x32x16_bf16 v[240:255], a[100:103], a[92:95], v[240:255]\n\t"
      "v_exp_f32 v222, v222\n\t"
      "v_exp_f32 v223, v223\n\t"
      "v_cvt_pk_bf16_f32 v190, v220, v221\n\t"
      "v_cvt_pk_bf16_f32 v191, v222, v223\n\t"
      "v_exp_f32 v224, v224\n\t"
      "v_exp_f32 v225, v225\n\t"
      "v_mfma_f32_32x32x16_bf16 %[o1], a[108:111], v[188:191], %[o1]\n\t"
      "buffer_load_dwordx4 a[100:103], %[voff], %[srd], %[so] offen offset:1024 sc1\n\t"
      "s_waitcnt vmcnt(10)\n\t"
      "v_exp_f32 v226, v226\n\t"
      "v_exp_f32 v227, v227\n\t"
      "v_cvt_pk_bf16_f32 v184, v224, v225\n\t"
      "v_cvt_pk_bf16_f32 v185, v226, v227\n\t"
      "v_exp_f32 v228, v228\n\t"
      "v_exp_f32 v229, v229\n\t"
      "v_mfma_f32_32x32x16_bf16 v[192:207], a[112:115], a[64:67], 0\n\t"
      "v_exp_f32 v230, v230\n\t"
      "v_exp_f32 v231, v231\n\t"
      "v_cvt_pk_bf16_f32 v186, v228, v229\n\t"
      "v_cvt_pk_bf16_f32 v187, v230, v231\n\t"
      "v_exp_f32 v232, v232\n\t"
      "v_exp_f32 v233, v233\n\t"
      "v_mfma_f32_32x32x16_bf16 %[o2], a[104:107], v[184:187], %[o2]\n\t"
      "v_exp_f32 v234, v234\n\t"
      "v_exp_f32 v235, v235\n\t"
      "v_cvt_pk_bf16_f32 v188, v232, v233\n\t"
      "v_cvt_pk_bf16_f32 v189, v234, v235\n\t"
      "v_exp_f32 v236, v236\n\t"
      "v_exp_f32 v237, v237\n\t"
      "v_mfma_f32_32x32x16_bf16 v[192:207], a[116:119], a[68:71], v[192:207]\n\t"
      "v_exp_f32 v238, v238\n\t"
      "v_exp_f32 v239, v239\n\t"
      "v_cvt_pk_bf16_f32 v190, v236, v237\n\t"
      "v_cvt_pk_bf16_f32 v191, v238, v239\n\t"
      "v_exp_f32 v240, v240\n\t"
      "v_exp_f32 v241, v241\n\t"
      "v_mfma_f32_32x32x16_bf16 %[o2], a[108:111], v[188:191], %[o2]\n\t"
      "v_exp_f32 v242, v242\n\t"
      "v_exp_f32 v243, v243\n\t"
      "v_cvt_pk_bf16_f32 v184, v240, v241\n\t"
      "v_cvt_pk_bf16_f32 v185, v242, v243\n\t"
      "v_exp_f32 v244, v244\n\t"
      "v_exp_f32 v245, v245\n\t"
      "v_mfma_f32_32x32x16_bf16 v[208:223], a[112:115], a[72:75], 0\n\t"
      "v_exp_f32 v246, v246\n\t"
      "v_exp_f32 v247, v247\n\t"
      "v_cvt_pk_bf16_f32 v186, v244, v245\n\t"
      "v_cvt_pk_bf16_f32 v187, v246, v247\n\t"
      "v_exp_f32 v248, v248\n\t"
      "v_exp_f32 v249, v249\n\t"
      "v_mfma_f32_32x32x16_bf16 %[o3], a[104:107], v[184:187], %[o3]\n\t"
      "v_exp_f32 v250, v250\n\t"
      "v_exp_f32 v251, v251\n\t"
      "v_cvt_pk_bf16_f32 v188, v248, v249\n\t"
      "v_cvt_pk_bf16_f32 v189, v250, v251\n\t"
      "v_exp_f32 v252, v252\n\t"
      "v_exp_f32 v253, v253\n\t"
      "v_mfma_f32_32x32x16_bf16 v[208:223], a[116:119], a[76:79], v[208:223]\n\t"
      "buffer_load_dwordx4 a[104:107], %[voff], %[srd], %[so] offen offset:2048 sc1\n\t"
      "v_exp_f32 v254, v254\n\t"
      "v_exp_f32 v255, v255\n\t"
      "v_cvt_pk_bf16_f32 v190, v252, v253\n\t"
      "v_cvt_pk_bf16_f32 v191, v254, v255\n\t"
      "v_exp_f32 v192, v192\n\t"
      "v_exp_f32 v193, v193\n\t"
      "v_mfma_f32_32x32x16_bf16 %[o3], a[108:111], v[188:191], %[o3]\n\t"
      "v_exp_f32 v194, v194\n\t"
      "v_exp_f32 v195, v195\n\t"
      "v_cvt_pk_bf16_f32 v184, v192, v193\n\t"
      "v_cvt_pk_bf16_f32 v185, v194, v195\n\t"
      "v_exp_f32 v196, v196\n\t"
      "v_exp_f32 v197, v197\n\t"
      "v_mfma_f32_32x32x16_bf16 v[224:239], a[112:115], a[80:83], 0\n\t"
      "buffer_load_dwordx4 a[108:111], %[voff], %[srd], %[so] offen offset:3072 sc1\n\t"
      "s_add_u32 %[so], %[so], 0x1000\n\t"
      "v_exp_f32 v198, v198\n\t"
      "v_exp_f32 v199, v199\n\t"
      "v_cvt_pk_bf16_f32 v186, v196, v197\n\t"
      "v_cvt_pk_bf16_f32 v187, v198, v199\n\t"
      "v_exp_f32 v200, v200\n\t"
      "v_exp_f32 v201, v201\n\t"
      "v_mfma_f32_32x32x16_bf16 %[o0], a[120:123], v[184:187], %[o0]\n\t"
      "v_exp_f32 v202, v202\n\t"
      "v_exp_f32 v203, v203\n\t"
      "v_cvt_pk_bf16_f32 v188, v200, v201\n\t"
      "v_cvt_pk_bf16_f32 v189, v202, v203\n\t"
      "v_exp_f32 v204, v204\n\t"
      "v_exp_f32 v205, v205\n\t"
      "v_mfma_f32_32x32x16_bf16 v[224:239], a[116:119], a[84:87], v[224:239]\n\t"
      "v_exp_f32 v206, v206\n\t"
      "v_exp_f32 v207, v207\n\t"
      "v_cvt_pk_bf16_f32 v190, v204, v205\n\t"
      "v_cvt_pk_bf16_f32 v191, v206, v207\n\t"
      "v_exp_f32 v208, v208\n\t"
      "v_exp_f32 v209, v209\n\t"
      "v_mfma_f32_32x32x16_bf16 %[o0], a[124:127], v[188:191], %[o0]\n\t"
      "v_exp_f32 v210, v210\n\t"
      "v_exp_f32 v211, v211\n\t"
      "v_cvt_pk_bf16_f32 v184, v208, v209\n\t"
      "v_cvt_pk_bf16_f32 v185, v210, v211\n\t"
      "v_exp_f32 v212, v212\n\t"
      "v_exp_f32 v213, v213\n\t"
      "v_mfma_f32_32x32x16_bf16 v[240:255], a[112:115], a[88:91], 0\n\t"
      "v_exp_f32 v214, v214\n\t"
      "v_exp_f32 v215, v215\n\t"
      "v_cvt_pk_bf16_f32 v186, v212, v213\n\t"
      "v_cvt_pk_bf16_f32 v187, v214, v215\n\t"
      "v_exp_f32 v216, v216\n\t"
      "v_exp_f32 v217, v217\n\t"
      "v_mfma_f32_32x32x16_bf16 %[o1], a[120:123], v[184:187], %[o1]\n\t"
      "buffer_load_dwordx4 a[112:115], %[voff], %[srd], %[so] offen offset:0 sc1\n\t"
      "v_exp_f32 v218, v218\n\t"
      "v_exp_f32 v219, v219\n\t"
      "v_cvt_pk_bf16_f32 v188, v216, v217\n\t"
      "v_cvt_pk_bf16_f32 v189, v218, v219\n\t"
      "v_exp_f32 v220, v220\n\t"
      "v_exp_f32 v221, v221\n\t"
      "v_mfma_f32_32x32x16_bf16 v[240:255], a[116:119], a[92:95], v[240:255]\n\t"
      "v_exp_f32 v222, v222\n\t"
      "v_exp_f32 v223, v223\n\t"
      "v_cvt_pk_bf16_f32 v190, v220, v221\n\t"
      "v_cvt_pk_bf16_f32 v191, v222, v223\n\t"
      "v_exp_f32 v224, v224\n\t"
      "v_exp_f32 v225, v225\n\t"
      "v_mfma_f32_32x32x16_bf16 %[o1], a[124:127], v[188:191], %[o1]\n\t"
      "buffer_load_dwordx4 a[116:119], %[voff], %[srd], %[so] offen offset:1024 sc1\n\t"
      "s_waitcnt vmcnt(10)\n\t"
      "v_exp_f32 v226, v226\n\t"
      "v_exp_f32 v227, v227\n\t"
      "v_cvt_pk_bf16_f32 v184, v224, v225\n\t"
      "v_cvt_pk_bf16_f32 v185, v226, v227\n\t"
      "v_exp_f32 v228, v228\n\t"
      "v_exp_f32 v229, v229\n\t"
      "v_mfma_f32_32x32x16_bf16 v[192:207], a[128:131], a[64:67], 0\n\t"
      "v_exp_f32 v230, v230\n\t"
      "v_exp_f32 v231, v231\n\t"
      "v_cvt_pk_bf16_f32 v186, v228, v229\n\t"
      "v_cvt_pk_bf16_f32 v187, v230, v231\n\t"
      "v_exp_f32 v232, v232\n\t"
      "v_exp_f32 v233, v233\n\t"
      "v_mfma_f32_32x32x16_bf16 %[o2], a[120:123], v[184:187], %[o2]\n\t"
      "v_exp_f32 v234, v234\n\t"
      "v_exp_f32 v235, v235\n\t"
      "v_cvt_pk_bf16_f32 v188, v232, v233\n\t"
      "v_cvt_pk_bf16_f32 v189, v234, v235\n\t"
      "v_exp_f32 v236, v236\n\t"
      "v_exp_f32 v237, v237\n\t"
      "v_mfma_f32_32x32x16_bf16 v[192:207], a[132:135], a[68:71], v[192:207]\n\t"
      "v_exp_f32 v238, v238\n\t"
      "v_exp_f32 v239, v239\n\t"
      "v_cvt_pk_bf16_f32 v190, v236, v237\n\t"
      "v_cvt_pk_bf16_f32 v191, v238, v239\n\t"
      "v_exp_f32 v240, v240\n\t"
      "v_exp_f32 v241, v241\n\t"
      "v_mfma_f32_32x32x16_bf16 %[o2], a[124:127], v[188:191], %[o2]\n\t"
      "v_exp_f32 v242, v242\n\t"
      "v_exp_f32 v243, v243\n\t"
      "v_cvt_pk_bf16_f32 v184, v240, v241\n\t"
      "v_cvt_pk_bf16_f32 v185, v242, v243\n\t"
      "v_exp_f32 v244, v244\n\t"
      "v_exp_f32 v245, v245\n\t"
      "v_mfma_f32_32x32x16_bf16 v[208:223], a[128:131], a[72:75], 0\n\t"
      "v_exp_f32 v246, v246\n\t"
      "v_exp_f32 v247, v247\n\t"
      "v_cvt_pk_bf16_f32 v186, v244, v245\n\t"
      "v_cvt_pk_bf16_f32 v187, v246, v247\n\t"
      "v_exp_f32 v248, v248\n\t"
      "v_exp_f32 v249, v249\n\t"
      "v_mfma_f32_32x32x16_bf16 %[o3], a[120:123], v[184:187], %[o3]\n\t"
      "v_exp_f32 v250, v250\n\t"
      "v_exp_f32 v251, v251\n\t"
      "v_cvt_pk_bf16_f32 v188, v248, v249\n\t"
      "v_cvt_pk_bf16_f32 v189, v250, v251\n\t"
      "v_exp_f32 v252, v252\n\t"
      "v_exp_f32 v253, v253\n\t"
      "v_mfma_f32_32x32x16_bf16 v[208:223], a[132:135], a[76:79], v[208:223]\n\t"
      "buffer_load_dwordx4 a[120:123], %[voff], %[srd], %[so] offen offset:2048 sc1\n\t"
      "v_exp_f32 v254, v254\n\t"
      "v_exp_f32 v255, v255\n\t"
      "v_cvt_pk_bf16_f32 v190, v252, v253\n\t"
      "v_cvt_pk_bf16_f32 v191, v254, v255\n\t"
      "v_exp_f32 v192, v192\n\t"
      "v_exp_f32 v193, v193\n\t"
      "v_mfma_f32_32x32x16_bf16 %[o3], a[124:127], v[188:191], %[o3]\n\t"
      "v_exp_f32 v194, v194\n\t"
      "v_exp_f32 v195, v195\n\t"
      "v_cvt_pk_bf16_f32 v184, v192, v193\n\t"
      "v_cvt_pk_bf16_f32 v185, v194, v195\n\t"
      "v_exp_f32 v196, v196\n\t"
      "v_exp_f32 v197, v197\n\t"
      "v_mfma_f32_32x32x16_bf16 v[224:239], a[128:131], a[80:83], 0\n\t"
      "buffer_load_dwordx4 a[124:127], %[voff], %[srd], %[so] offen offset:3072 sc1\n\t"
      "s_add_u32 %[so], %[so], 0x1000\n\t"
      "v_exp_f32 v198, v198\n\t"
      "v_exp_f32 v199, v199\n\t"
      "v_cvt_pk_bf16_f32 v186, v196, v197\n\t"
      "v_cvt_pk_bf16_f32 v187, v198, v199\n\t"
      "v_exp_f32 v200, v200\n\t"
      "v_exp_f32 v201, v201\n\t"
      "v_mfma_f32_32x32x16_bf16 %[o0], a[136:139], v[184:187], %[o0]\n\t"
      "v_exp_f32 v202, v202\n\t"
      "v_exp_f32 v203, v203\n\t"
      "v_cvt_pk_bf16_f32 v188, v200, v201\n\t"
      "v_cvt_pk_bf16_f32 v189, v202, v203\n\t"
      "v_exp_f32 v204, v204\n\t"
      "v_exp_f32 v205, v205\n\t"
      "v_mfma_f32_32x32x16_bf16 v[224:239], a[132:135], a[84:87], v[224:239]\n\t"
      "v_exp_f32 v206, v206\n\t"
      "v_exp_f32 v207, v207\n\t"
      "v_cvt_pk_bf16_f32 v190, v204, v205\n\t"
      "v_cvt_pk_bf16_f32 v191, v206, v207\n\t"
      "v_exp_f32 v208, v208\n\t"
      "v_exp_f32 v209, v209\n\t"
      "v_mfma_f32_32x32x16_bf16 %[o0], a[140:143], v[188:191], %[o0]\n\t"
      "v_exp_f32 v210, v210\n\t"
      "v_exp_f32 v211, v211\n\t"
      "v_cvt_pk_bf16_f32 v184, v208, v209\n\t"
      "v_cvt_pk_bf16_f32 v185, v210, v211\n\t"
      "v_exp_f32 v212, v212\n\t"
      "v_exp_f32 v213, v213\n\t"
      "v_mfma_f32_32x32x16_bf16 v[240:255], a[128:131], a[88:91], 0\n\t"
      "v_exp_f32 v214, v214\n\t"
      "v_exp_f32 v215, v215\n\t"
      "v_cvt_pk_bf16_f32 v186, v212, v213\n\t"
      "v_cvt_pk_bf16_f32 v187, v214, v215\n\t"
      "v_exp_f32 v216, v216\n\t"
      "v_exp_f32 v217, v217\n\t"
      "v_mfma_f32_32x32x16_bf16 %[o1], a[136:139], v[184:187], %[o1]\n\t"
      "buffer_load_dwordx4 a[128:131], %[voff], %[srd], %[so] offen offset:0 sc1\n\t"
      "v_exp_f32 v218, v218\n\t"
      "v_exp_f32 v219, v219\n\t"
      "v_cvt_pk_bf16_f32 v188, v216, v217\n\t"
      "v_cvt_pk_bf16_f32 v189, v218, v219\n\t"
      "v_exp_f32 v220, v220\n\t"
      "v_exp_f32 v221, v221\n\t"
      "v_mfma_f32_32x32x16_bf16 v[240:255], a[132:135], a[92:95], v[240:255]\n\t"
      "v_exp_f32 v222, v222\n\t"
      "v_exp_f32 v223, v223\n\t"
      "v_cvt_pk_bf16_f32 v190, v220, v221\n\t"
      "v_cvt_pk_bf16_f32 v191, v222, v223\n\t"
      "v_exp_f32 v224, v224\n\t"
      "v_exp_f32 v225, v225\n\t"
      "v_mfma_f32_32x32x16_bf16 %[o1], a[140:143], v[188:191], %[o1]\n\t"
      "buffer_load_dwordx4 a[132:135], %[voff], %[srd], %[so] offen offset:1024 sc1\n\t"
      "s_waitcnt vmcnt(10)\n\t"
      "v_exp_f32 v226, v226\n\t"
      "v_exp_f32 v227, v227\n\t"
      "v_cvt_pk_bf16_f32 v184, v224, v225\n\t"
      "v_cvt_pk_bf16_f32 v185, v226, v227\n\t"
      "v_exp_f32 v228, v228\n\t"
      "v_exp_f32 v229, v229\n\t"
      "v_mfma_f32_32x32x16_bf16 v[192:207], a[144:147], a[64:67], 0\n\t"
      "v_exp_f32 v230, v230\n\t"
      "v_exp_f32 v231, v231\n\t"
      "v_cvt_pk_bf16_f32 v186, v228, v229\n\t"
      "v_cvt_pk_bf16_f32 v187, v230, v231\n\t"
      "v_exp_f32 v232, v232\n\t"
      "v_exp_f32 v233, v233\n\t"
      "v_mfma_f32_32x32x16_bf16 %[o2], a[136:139], v[184:187], %[o2]\n\t"
      "buffer_store_dword v182, %[fpub], %[wsrd], 0 offen sc1\n\t"
      "v_exp_f32 v234, v234\n\t"
      "v_exp_f32 v235, v235\n\t"
      "v_cvt_pk_bf16_f32 v188, v232, v233\n\t"
      "v_cvt_pk_bf16_f32 v189, v234, v235\n\t"
      "v_exp_f32 v236, v236\n\t"
      "v_exp_f32 v237, v237\n\t"
      "v_mfma_f32_32x32x16_bf16 v[192:207], a[148:151], a[68:71], v[192:207]\n\t"
      "v_exp_f32 v238, v238\n\t"
      "v_exp_f32 v239, v239\n\t"
      "v_cvt_pk_bf16_f32 v190, v236, v237\n\t"
      "v_cvt_pk_bf16_f32 v191, v238, v239\n\t"
      "v_exp_f32 v240, v240\n\t"
      "v_exp_f32 v241, v241\n\t"
      "v_mfma_f32_32x32x16_bf16 %[o2], a[140:143], v[188:191], %[o2]\n\t"
      "buffer_load_dword v180, %[foff], %[wsrd], 0 offen sc1\n\t"
      "v_exp_f32 v242, v242\n\t"
      "v_exp_f32 v243, v243\n\t"
      "v_cvt_pk_bf16_f32 v184, v240, v241\n\t"
      "v_cvt_pk_bf16_f32 v185, v242, v243\n\t"
      "v_exp_f32 v244, v244\n\t"
      "v_exp_f32 v245, v245\n\t"
      "v_mfma_f32_32x32x16_bf16 v[208:223], a[144:147], a[72:75], 0\n\t"
      "buffer_load_dword v181, %[fof6], %[wsrd], 0 offen sc1\n\t"
      "v_exp_f32 v246, v246\n\t"
      "v_exp_f32 v247, v247\n\t"
      "v_cvt_pk_bf16_f32 v186, v244, v245\n\t"
      "v_cvt_pk_bf16_f32 v187, v246, v247\n\t"
      "v_exp_f32 v248, v248\n\t"
      "v_exp_f32 v249, v249\n\t"
      "v_mfma_f32_32x32x16_bf16 %[o3], a[136:139], v[184:187], %[o3]\n\t"
      "v_exp_f32 v250, v250\n\t"
      "v_exp_f32 v251, v251\n\t"
      "v_cvt_pk_bf16_f32 v188, v248, v249\n\t"
      "v_cvt_pk_bf16_f32 v189, v250, v251\n\t"
      "v_exp_f32 v252, v252\n\t"
      "v_exp_f32 v253, v253\n\t"
      "v_mfma_f32_32x32x16_bf16 v[208:223], a[148:151], a[76:79], v[208:223]\n\t"
      "buffer_load_dwordx4 a[136:139], %[voff], %[srd], %[so] offen offset:2048 sc1\n\t"
      "v_exp_f32 v254, v254\n\t"
      "v_exp_f32 v255, v255\n\t"
      "v_cvt_pk_bf16_f32 v190, v252, v253\n\t"
      "v_cvt_pk_bf16_f32 v191, v254, v255\n\t"
      "v_exp_f32 v192, v192\n\t"
      "v_exp_f32 v193, v193\n\t"
      "v_mfma_f32_32x32x16_bf16 %[o3], a[140:143], v[188:191], %[o3]\n\t"
      "v_exp_f32 v194, v194\n\t"
      "v_exp_f32 v195, v195\n\t"
      "v_cvt_pk_bf16_f32 v184, v192, v193\n\t"
      "v_cvt_pk_bf16_f32 v185, v194, v195\n\t"
      "v_exp_f32 v196, v196\n\t"
      "v_exp_f32 v197, v197\n\t"
      "v_mfma_f32_32x32x16_bf16 v[224:239], a[144:147], a[80:83], 0\n\t"
      "buffer_load_dwordx4 a[140:143], %[voff], %[srd], %[so] offen offset:3072 sc1\n\t"
      "s_add_u32 %[so], %[so], 0x1000\n\t"
      "v_exp_f32 v198, v198\n\t"
      "v_exp_f32 v199, v199\n\t"
      "v_cvt_pk_bf16_f32 v186, v196, v197\n\t"
      "v_cvt_pk_bf16_f32 v187, v198, v199\n\t"
      "v_exp_f32 v200, v200\n\t"
      "v_exp_f32 v201, v201\n\t"
      "v_mfma_f32_32x32x16_bf16 %[o0], a[152:155], v[184:187], %[o0]\n\t"
      "v_exp_f32 v202, v202\n\t"
      "v_exp_f32 v203, v203\n\t"
      "v_cvt_pk_bf16_f32 v188, v200, v201\n\t"
      "v_cvt_pk_bf16_f32 v189, v202, v203\n\t"
      "v_exp_f32 v204, v204\n\t"
      "v_exp_f32 v205, v205\n\t"
      "v_mfma_f32_32x32x16_bf16 v[224:239], a[148:151], a[84:87], v[224:239]\n\t"
      "v_exp_f32 v206, v206\n\t"
      "v_exp_f32 v207, v207\n\t"
      "v_cvt_pk_bf16_f32 v190, v204, v205\n\t"
      "v_cvt_pk_bf16_f32 v191, v206, v207\n\t"
      "v_exp_f32 v208, v208\n\t"
      "v_exp_f32 v209, v209\n\t"
      "v_mfma_f32_32x32x16_bf16 %[o0], a[156:159], v[188:191], %[o0]\n\t"
      "v_exp_f32 v210, v210\n\t"
      "v_exp_f32 v211, v211\n\t"
      "v_cvt_pk_bf16_f32 v184, v208, v209\n\t"
      "v_cvt_pk_bf16_f32 v185, v210, v211\n\t"
      "v_exp_f32 v212, v212\n\t"
      "v_exp_f32 v213, v213\n\t"
      "v_mfma_f32_32x32x16_bf16 v[240:255], a[144:147], a[88:91], 0\n\t"
      "v_exp_f32 v214, v214\n\t"
      "v_exp_f32 v215, v215\n\t"
      "v_cvt_pk_bf16_f32 v186, v212, v213\n\t"
      "v_cvt_pk_bf16_f32 v187, v214, v215\n\t"
      "v_exp_f32 v216, v216\n\t"
      "v_exp_f32 v217, v217\n\t"
      "v_mfma_f32_32x32x16_bf16 %[o1], a[152:155], v[184:187], %[o1]\n\t"
      "buffer_load_dwordx4 a[144:147], %[voff], %[srd], %[so] offen offset:0 sc1\n\t"
      "v_exp_f32 v218, v218\n\t"
      "v_exp_f32 v219, v219\n\t"
      "v_cvt_pk_bf16_f32 v188, v216, v217\n\t"
      "v_cvt_pk_bf16_f32 v189, v218, v219\n\t"
      "v_exp_f32 v220, v220\n\t"
      "v_exp_f32 v221, v221\n\t"
      "v_mfma_f32_32x32x16_bf16 v[240:255], a[148:151], a[92:95], v[240:255]\n\t"
      "v_exp_f32 v222, v222\n\t"
      "v_exp_f32 v223, v223\n\t"
      "v_cvt_pk_bf16_f32 v190, v220, v221\n\t"
      "v_cvt_pk_bf16_f32 v191, v222, v223\n\t"
      "v_exp_f32 v224, v224\n\t"
      "v_exp_f32 v225, v225\n\t"
      "v_mfma_f32_32x32x16_bf16 %[o1], a[156:159], v[188:191], %[o1]\n\t"
      "buffer_load_dwordx4 a[148:151], %[voff], %[srd], %[so] offen offset:1024 sc1\n\t"
      "s_waitcnt vmcnt(10)\n\t"
      "v_exp_f32 v226, v226\n\t"
      "v_exp_f32 v227, v227\n\t"
      "v_cvt_pk_bf16_f32 v184, v224, v225\n\t"
      "v_cvt_pk_bf16_f32 v185, v226, v227\n\t"
      "v_exp_f32 v228, v228\n\t"
      "v_exp_f32 v229, v229\n\t"
      "v_mfma_f32_32x32x16_bf16 v[192:207], a[96:99], a[64:67], 0\n\t"
      "v_exp_f32 v230, v230\n\t"
      "v_exp_f32 v231, v231\n\t"
      "v_cvt_pk_bf16_f32 v186, v228, v229\n\t"
      "v_cvt_pk_bf16_f32 v187, v230, v231\n\t"
      "v_exp_f32 v232, v232\n\t"
      "v_exp_f32 v233, v233\n\t"
      "v_mfma_f32_32x32x16_bf16 %[o2], a[152:155], v[184:187], %[o2]\n\t"
      "v_exp_f32 v234, v234\n\t"
      "v_exp_f32 v235, v235\n\t"
      "v_cvt_pk_bf16_f32 v188, v232, v233\n\t"
      "v_cvt_pk_bf16_f32 v189, v234, v235\n\t"
      "v_exp_f32 v236, v236\n\t"
      "v_exp_f32 v237, v237\n\t"
      "v_mfma_f32_32x32x16_bf16 v[192:207], a[100:103], a[68:71], v[192:207]\n\t"
      "v_exp_f32 v238, v238\n\t"
      "v_exp_f32 v239, v239\n\t"
      "v_cvt_pk_bf16_f32 v190, v236, v237\n\t"
      "v_cvt_pk_bf16_f32 v191, v238, v239\n\t"
      "v_exp_f32 v240, v240\n\t"
      "v_exp_f32 v241, v241\n\t"
      "v_mfma_f32_32x32x16_bf16 %[o2], a[156:159], v[188:191], %[o2]\n\t"
      "v_exp_f32 v242, v242\n\t"
      "v_exp_f32 v243, v243\n\t"
      "v_cvt_pk_bf16_f32 v184, v240, v241\n\t"
      "v_cvt_pk_bf16_f32 v185, v242, v243\n\t"
      "v_exp_f32 v244, v244\n\t"
      "v_exp_f32 v245, v245\n\t"
      "v_mfma_f32_32x32x16_bf16 v[208:223], a[96:99], a[72:75], 0\n\t"
      "v_exp_f32 v246, v246\n\t"
      "v_exp_f32 v247, v247\n\t"
      "v_cvt_pk_bf16_f32 v186, v244, v245\n\t"
      "v_cvt_pk_bf16_f32 v187, v246, v247\n\t"
      "v_exp_f32 v248, v248\n\t"
      "v_exp_f32 v249, v249\n\t"
      "v_mfma_f32_32x32x16_bf16 %[o3], a[152:155], v[184:187], %[o3]\n\t"
      "v_exp_f32 v250, v250\n\t"
      "v_exp_f32 v251, v251\n\t"
      "v_cvt_pk_bf16_f32 v188, v248, v249\n\t"
      "v_cvt_pk_bf16_f32 v189, v250, v251\n\t"
      "v_exp_f32 v252, v252\n\t"
      "v_exp_f32 v253, v253\n\t"
      "v_mfma_f32_32x32x16_bf16 v[208:223], a[100:103], a[76:79], v[208:223]\n\t"
      "buffer_load_dwordx4 a[152:155], %[voff], %[srd], %[so] offen offset:2048 sc1\n\t"
      "s_sub_u32 %[c1], %[c1], 1\n\t"
      "s_cmp_lg_u32 %[c1], 0\n\t"
      "v_exp_f32 v254, v254\n\t"
      "v_exp_f32 v255, v255\n\t"
      "v_cvt_pk_bf16_f32 v190, v252, v253\n\t"
      "v_cvt_pk_bf16_f32 v191, v254, v255\n\t"
      "s_cbranch_scc1 la_%=\n\t"
      "s_cmp_eq_u32 %[c2], 0\n\t"
      "s_cbranch_scc1 fin_%=\n\t"
      "s_waitcnt vmcnt(5)\n\t"
      "s_mov_b32 %[mc], 0x10000\n\t"
      "fw_%=:\n\t"
      "v_cmp_ne_u32 vcc, %[tag], v180\n\t"
      "v_cmp_ne_u32 %[ms], %[tag], v181\n\t"
      "s_nop 4\n\t"
      "s_or_b64 vcc, vcc, %[ms]\n\t"
      "s_cbranch_scc0 fok_%=\n\t"
      "s_sub_u32 %[mc], %[mc], 1\n\t"
      "s_cmp_eq_u32 %[mc], 0\n\t"
      "s_cbranch_scc1 fok_%=\n\t"
      "s_sleep 1\n\t"
      "buffer_load_dword v180, %[foff], %[wsrd], 0 offen sc1\n\t"
      "buffer_load_dword v181, %[fof6], %[wsrd], 0 offen sc1\n\t"
      "s_waitcnt vmcnt(0)\n\t"
      "s_branch fw_%=\n\t"
      "fok_%=:\n\t"
      "lb_%=:\n\t"
      "v_exp_f32 v192, v192\n\t"
      "v_exp_f32 v193, v193\n\t"
      "v_mfma_f32_32x32x16_bf16 %[o3], a[156:159], v[188:191], %[o3]\n\t"
      "v_exp_f32 v194, v194\n\t"
      "v_exp_f32 v195, v195\n\t"
      "v_cvt_pk_bf16_f32 v184, v192, v193\n\t"
      "v_cvt_pk_bf16_f32 v185, v194, v195\n\t"
      "v_exp_f32 v196, v196\n\t"
      "v_exp_f32 v197, v197\n\t"
      "v_mfma_f32_32x32x16_bf16 v[224:239], a[96:99], a[80:83], 0\n\t"
      "buffer_load_dwordx4 a[156:159], %[voff], %[srd], %[so] offen offset:3072 sc1\n\t"
      "s_add_u32 %[so], %[so], 0x1000\n\t"
      "v_exp_f32 v198, v198\n\t"
      "v_exp_f32 v199, v199\n\t"
      "v_cvt_pk_bf16_f32 v186, v196, v197\n\t"
      "v_cvt_pk_bf16_f32 v187, v198, v199\n\t"
      "v_exp_f32 v200, v200\n\t"
      "v_exp_f32 v201, v201\n\t"
      "v_mfma_f32_32x32x16_bf16 %[o0], a[104:107], v[184:187], %[o0]\n\t"
      "v_exp_f32 v202, v202\n\t"
      "v_exp_f32 v203, v203\n\t"
      "v_cvt_pk_bf16_f32 v188, v200, v201\n\t"
      "v_cvt_pk_bf16_f32 v189, v202, v203\n\t"
      "v_exp_f32 v204, v204\n\t"
      "v_exp_f32 v205, v205\n\t"
      "v_mfma_f32_32x32x16_bf16 v[224:239], a[100:103], a[84:87], v[224:239]\n\t"
      "v_exp_f32 v206, v206\n\t"
      "v_exp_f32 v207, v207\n\t"
      "v_cvt_pk_bf16_f32 v190, v204, v205\n\t"
      "v_cvt_pk_bf16_f32 v191, v206, v207\n\t"
      "v_exp_f32 v208, v208\n\t"
      "v_exp_f32 v209, v209\n\t"
      "v_mfma_f32_32x32x16_bf16 %[o0], a[108:111], v[188:191], %[o0]\n\t"
      "v_exp_f32 v210, v210\n\t"
      "v_exp_f32 v211, v211\n\t"
      "v_cvt_pk_bf16_f32 v184, v208, v209\n\t"
      "v_cvt_pk_bf16_f32 v185, v210, v211\n\t"
      "v_exp_f32 v212, v212\n\t"
      "v_exp_f32 v213, v213\n\t"
      "v_mfma_f32_32x32x16_bf16 v[240:255], a[96:99], a[88:91], 0\n\t"
      "v_exp_f32 v214, v214\n\t"
      "v_exp_f32 v215, v215\n\t"
      "v_cvt_pk_bf16_f32 v186, v212, v213\n\t"
      "v_cvt_pk_bf16_f32 v187, v214, v215\n\t"
      "v_exp_f32 v216, v216\n\t"
      "v_exp_f32 v217, v217\n\t"
      "v_mfma_f32_32x32x16_bf16 %[o1], a[104:107], v[184:187], %[o1]\n\t"
      "buffer_load_dwordx4 a[96:99], %[voff], %[srd], %[so] offen offset:0 sc1\n\t"
      "v_exp_f32 v218, v218\n\t"
      "v_exp_f32 v219, v219\n\t"
      "v_cvt_pk_bf16_f32 v188, v216, v217\n\t"
      "v_cvt_pk_bf16_f32 v189, v218, v219\n\t"
      "v_exp_f32 v220, v220\n\t"
      "v_exp_f32 v221, v221\n\t"
      "v_mfma_f32_32x32x16_bf16 v[240:255], a[100:103], a[92:95], v[240:255]\n\t"
      "v_exp_f32 v222, v222\n\t"
      "v_exp_f32 v223, v223\n\t"
      "v_cvt_pk_bf16_f32 v190, v220, v221\n\t"
      "v_cvt_pk_bf16_f32 v191, v222, v223\n\t"
      "v_exp_f32 v224, v224\n\t"
      "v_exp_f32 v225, v225\n\t"
      "v_mfma_f32_32x32x16_bf16 %[o1], a[108:111], v[188:191], %[o1]\n\t"
      "buffer_load_dwordx4 a[100:103], %[voff], %[srd], %[so] offen offset:1024 sc1\n\t"
      "s_waitcnt vmcnt(12)\n\t"
      "v_exp_f32 v226, v226\n\t"
      "v_exp_f32 v227, v227\n\t"
      "v_cvt_pk_bf16_f32 v184, v224, v225\n\t"
      "v_cvt_pk_bf16_f32 v185, v226, v227\n\t"
      "v_exp_f32 v228, v228\n\t"
      "v_exp_f32 v229, v229\n\t"
      "v_mfma_f32_32x32x16_bf16 v[192:207], a[112:115], a[64:67], 0\n\t"
      "v_exp_f32 v230, v230\n\t"
      "v_exp_f32 v231, v231\n\t"
      "v_cvt_pk_bf16_f32 v186, v228, v229\n\t"
      "v_cvt_pk_bf16_f32 v187, v230, v231\n\t"
      "v_exp_f32 v232, v232\n\t"
      "v_exp_f32 v233, v233\n\t"
      "v_mfma_f32_32x32x16_bf16 %[o2], a[104:107], v[184:187], %[o2]\n\t"
      "v_exp_f32 v234, v234\n\t"
      "v_exp_f32 v235, v235\n\t"
      "v_cvt_pk_bf16_f32 v188, v232, v233\n\t"
      "v_cvt_pk_bf16_f32 v189, v234, v235\n\t"
      "v_exp_f32 v236, v236\n\t"
      "v_exp_f32 v237, v237\n\t"
      "v_mfma_f32_32x32x16_bf16 v[192:207], a[116:119], a[68:71], v[192:207]\n\t"
      "v_exp_f32 v238, v238\n\t"
      "v_exp_f32 v239, v239\n\t"
      "v_cvt_pk_bf16_f32 v190, v236, v237\n\t"
      "v_cvt_pk_bf16_f32 v191, v238, v239\n\t"
      "v_exp_f32 v240, v240\n\t"
      "v_exp_f32 v241, v241\n\t"
      "v_mfma_f32_32x32x16_bf16 %[o2], a[108:111], v[188:191], %[o2]\n\t"
      "v_exp_f32 v242, v242\n\t"
      "v_exp_f32 v243, v243\n\t"
      "v_cvt_pk_bf16_f32 v184, v240, v241\n\t"
      "v_cvt_pk_bf16_f32 v185, v242, v243\n\t"
      "v_exp_f32 v244, v244\n\t"
      "v_exp_f32 v245, v245\n\t"
      "v_mfma_f32_32x32x16_bf16 v[208:223], a[112:115], a[72:75], 0\n\t"
      "v_exp_f32 v246, v246\n\t"
      "v_exp_f32 v247, v247\n\t"
      "v_cvt_pk_bf16_f32 v186, v244, v245\n\t"
      "v_cvt_pk_bf16_f32 v187, v246, v247\n\t"
      "v_exp_f32 v248, v248\n\t"
      "v_exp_f32 v249, v249\n\t"
      "v_mfma_f32_32x32x16_bf16 %[o3], a[104:107], v[184:187], %[o3]\n\t"
      "v_exp_f32 v250, v250\n\t"
      "v_exp_f32 v251, v251\n\t"
      "v_cvt_pk_bf16_f32 v188, v248, v249\n\t"
      "v_cvt_pk_bf16_f32 v189, v250, v251\n\t"
      "v_exp_f32 v252, v252\n\t"
      "v_exp_f32 v253, v253\n\t"
      "v_mfma_f32_32x32x16_bf16 v[208:223], a[116:119], a[76:79], v[208:223]\n\t"
      "buffer_load_dwordx4 a[104:107], %[voff], %[srd], %[so] offen offset:2048 sc1\n\t"
      "v_exp_f32 v254, v254\n\t"
      "v_exp_f32 v255, v255\n\t"
      "v_cvt_pk_bf16_f32 v190, v252, v253\n\t"
      "v_cvt_pk_bf16_f32 v191, v254, v255\n\t"
      "v_exp_f32 v192, v192\n\t"
      "v_exp_f32 v193, v193\n\t"
      "v_mfma_f32_32x32x16_bf16 %[o3], a[108:111], v[188:191], %[o3]\n\t"
      "v_exp_f32 v194, v194\n\t"
      "v_exp_f32 v195, v195\n\t"
      "v_cvt_pk_bf16_f32 v184, v192, v193\n\t"
      "v_cvt_pk_bf16_f32 v185, v194, v195\n\t"
      "v_exp_f32 v196, v196\n\t"
      "v_exp_f32 v197, v197\n\t"
      "v_mfma_f32_32x32x16_bf16 v[224:239], a[112:115], a[80:83], 0\n\t"
      "buffer_load_dwordx4 a[108:111], %[voff], %[srd], %[so] offen offset:3072 sc1\n\t"
      "s_add_u32 %[so], %[so], 0x1000\n\t"
      "v_exp_f32 v198, v198\n\t"
      "v_exp_f32 v199, v199\n\t"
      "v_cvt_pk_bf16_f32 v186, v196, v197\n\t"
      "v_cvt_pk_bf16_f32 v187, v198, v199\n\t"
      "v_exp_f32 v200, v200\n\t"
      "v_exp_f32 v201, v201\n\t"
      "v_mfma_f32_32x32x16_bf16 %[o0], a[120:123], v[184:187], %[o0]\n\t"
      "v_exp_f32 v202, v202\n\t"
      "v_exp_f32 v203, v203\n\t"
      "v_cvt_pk_bf16_f32 v188, v200, v201\n\t"
      "v_cvt_pk_bf16_f32 v189, v202, v203\n\t"
      "v_exp_f32 v204, v204\n\t"
      "v_exp_f32 v205, v205\n\t"
      "v_mfma_f32_32x32x16_bf16 v[224:239], a[116:119], a[84:87], v[224:239]\n\t"
      "v_exp_f32 v206, v206\n\t"
      "v_exp_f32 v207, v207\n\t"
      "v_cvt_pk_bf16_f32 v190, v204, v205\n\t"
      "v_cvt_pk_bf16_f32 v191, v206, v207\n\t"
      "v_exp_f32 v208, v208\n\t"
      "v_exp_f32 v209, v209\n\t"
      "v_mfma_f32_32x32x16_bf16 %[o0], a[124:127], v[188:191], %[o0]\n\t"
      "v_exp_f32 v210, v210\n\t"
      "v_exp_f32 v211, v211\n\t"
      "v_cvt_pk_bf16_f32 v184, v208, v209\n\t"
      "v_cvt_pk_bf16_f32 v185, v210, v211\n\t"
      "v_exp_f32 v212, v212\n\t"
      "v_exp_f32 v213, v213\n\t"
      "v_mfma_f32_32x32x16_bf16 v[240:255], a[112:115], a[88:91], 0\n\t"
      "v_exp_f32 v214, v214\n\t"
      "v_exp_f32 v215, v215\n\t"
      "v_cvt_pk_bf16_f32 v186, v212, v213\n\t"
      "v_cvt_pk_bf16_f32 v187, v214, v215\n\t"
      "v_exp_f32 v216, v216\n\t"
      "v_exp_f32 v217, v217\n\t"
      "v_mfma_f32_32x32x16_bf16 %[o1], a[120:123], v[184:187], %[o1]\n\t"
      "buffer_load_dwordx4 a[112:115], %[voff], %[srd], %[so] offen offset:0 sc1\n\t"
      "v_exp_f32 v218, v218\n\t"
      "v_exp_f32 v219, v219\n\t"
      "v_cvt_pk_bf16_f32 v188, v216, v217\n\t"
      "v_cvt_pk_bf16_f32 v189, v218, v219\n\t"
      "v_exp_f32 v220, v220\n\t"
      "v_exp_f32 v221, v221\n\t"
      "v_mfma_f32_32x32x16_bf16 v[240:255], a[116:119], a[92:95], v[240:255]\n\t"
      "v_exp_f32 v222, v222\n\t"
      "v_exp_f32 v223, v223\n\t"
      "v_cvt_pk_bf16_f32 v190, v220, v221\n\t"
      "v_cvt_pk_bf16_f32 v191, v222, v223\n\t"
      "v_exp_f32 v224, v224\n\t"
      "v_exp_f32 v225, v225\n\t"
      "v_mfma_f32_32x32x16_bf16 %[o1], a[124:127], v[188:191], %[o1]\n\t"
      "buffer_load_dwordx4 a[116:119], %[voff], %[srd], %[so] offen offset:1024 sc1\n\t"
      "s_waitcnt vmcnt(12)\n\t"
      "v_exp_f32 v226, v226\n\t"
      "v_exp_f32 v227, v227\n\t"
      "v_cvt_pk_bf16_f32 v184, v224, v225\n\t"
      "v_cvt_pk_bf16_f32 v185, v226, v227\n\t"
      "v_exp_f32 v228, v228\n\t"
      "v_exp_f32 v229, v229\n\t"
      "v_mfma_f32_32x32x16_bf16 v[192:207], a[128:131], a[64:67], 0\n\t"
      "v_exp_f32 v230, v230\n\t"
      "v_exp_f32 v231, v231\n\t"
      "v_cvt_pk_bf16_f32 v186, v228, v229\n\t"
      "v_cvt_pk_bf16_f32 v187, v230, v231\n\t"
      "v_exp_f32 v232, v232\n\t"
      "v_exp_f32 v233, v233\n\t"
      "v_mfma_f32_32x32x16_bf16 %[o2], a[120:123], v[184:187], %[o2]\n\t"
      "v_exp_f32 v234, v234\n\t"
      "v_exp_f32 v235, v235\n\t"
      "v_cvt_pk_bf16_f32 v188, v232, v233\n\t"
      "v_cvt_pk_bf16_f32 v189, v234, v235\n\t"
      "v_exp_f32 v236, v236\n\t"
      "v_exp_f32 v237, v237\n\t"
      "v_mfma_f32_32x32x16_bf16 v[192:207], a[132:135], a[68:71], v[192:207]\n\t"
      "v_exp_f32 v238, v238\n\t"
      "v_exp_f32 v239, v239\n\t"
      "v_cvt_pk_bf16_f32 v190, v236, v237\n\t"
      "v_cvt_pk_bf16_f32 v191, v238, v239\n\t"
      "v_exp_f32 v240, v240\n\t"
      "v_exp_f32 v241, v241\n\t"
      "v_mfma_f32_32x32x16_bf16 %[o2], a[124:127], v[188:191], %[o2]\n\t"
      "v_exp_f32 v242, v242\n\t"
      "v_exp_f32 v243, v243\n\t"
      "v_cvt_pk_bf16_f32 v184, v240, v241\n\t"
      "v_cvt_pk_bf16_f32 v185, v242, v243\n\t"
      "v_exp_f32 v244, v244\n\t"
      "v_exp_f32 v245, v245\n\t"
      "v_mfma_f32_32x32x16_bf16 v[208:223], a[128:131], a[72:75], 0\n\t"
      "v_exp_f32 v246, v246\n\t"
      "v_exp_f32 v247, v247\n\t"
      "v_cvt_pk_bf16_f32 v186, v244, v245\n\t"
      "v_cvt_pk_bf16_f32 v187, v246, v247\n\t"
      "v_exp_f32 v248, v248\n\t"
      "v_exp_f32 v249, v249\n\t"
      "v_mfma_f32_32x32x16_bf16 %[o3], a[120:123], v[184:187], %[o3]\n\t"
      "v_exp_f32 v250, v250\n\t"
      "v_exp_f32 v251, v251\n\t"
      "v_cvt_pk_bf16_f32 v188, v248, v249\n\t"
      "v_cvt_pk_bf16_f32 v189, v250, v251\n\t"
      "v_exp_f32 v252, v252\n\t"
      "v_exp_f32 v253, v253\n\t"
      "v_mfma_f32_32x32x16_bf16 v[208:223], a[132:135], a[76:79], v[208:223]\n\t"
      "buffer_load_dwordx4 a[120:123], %[voff], %[srd], %[so] offen offset:2048 sc1\n\t"
      "v_exp_f32 v254, v254\n\t"
      "v_exp_f32 v255, v255\n\t"
      "v_cvt_pk_bf16_f32 v190, v252, v253\n\t"
      "v_cvt_pk_bf16_f32 v191, v254, v255\n\t"
      "v_exp_f32 v192, v192\n\t"
      "v_exp_f32 v193, v193\n\t"
      "v_mfma_f32_32x32x16_bf16 %[o3], a[124:127], v[188:191], %[o3]\n\t"
      "v_exp_f32 v194, v194\n\t"
      "v_exp_f32 v195, v195\n\t"
      "v_cvt_pk_bf16_f32 v184, v192, v193\n\t"
      "v_cvt_pk_bf16_f32 v185, v194, v195\n\t"
      "v_exp_f32 v196, v196\n\t"
      "v_exp_f32 v197, v197\n\t"
      "v_mfma_f32_32x32x16_bf16 v[224:239], a[128:131], a[80:83], 0\n\t"
      "buffer_load_dwordx4 a[124:127], %[voff], %[srd], %[so] offen offset:3072 sc1\n\t"
      "s_add_u32 %[so], %[so], 0x1000\n\t"
      "v_exp_f32 v198, v198\n\t"
      "v_exp_f32 v199, v199\n\t"
      "v_cvt_pk_bf16_f32 v186, v196, v197\n\t"
      "v_cvt_pk_bf16_f32 v187, v198, v199\n\t"
      "v_exp_f32 v200, v200\n\t"
      "v_exp_f32 v201, v201\n\t"
      "v_mfma_f32_32x32x16_bf16 %[o0], a[136:139], v[184:187], %[o0]\n\t"
      "v_exp_f32 v202, v202\n\t"
      "v_exp_f32 v203, v203\n\t"
      "v_cvt_pk_bf16_f32 v188, v200, v201\n\t"
      "v_cvt_pk_bf16_f32 v189, v202, v203\n\t"
      "v_exp_f32 v204, v204\n\t"
      "v_exp_f32 v205, v205\n\t"
      "v_mfma_f32_32x32x16_bf16 v[224:239], a[132:135], a[84:87], v[224:239]\n\t"
      "v_exp_f32 v206, v206\n\t"
      "v_exp_f32 v207, v207\n\t"
      "v_cvt_pk_bf16_f32 v190, v204, v205\n\t"
      "v_cvt_pk_bf16_f32 v191, v206, v207\n\t"
      "v_exp_f32 v208, v208\n\t"
      "v_exp_f32 v209, v209\n\t"
      "v_mfma_f32_32x32x16_bf16 %[o0], a[140:143], v[188:191], %[o0]\n\t"
      "v_exp_f32 v210, v210\n\t"
      "v_exp_f32 v211, v211\n\t"
      "v_cvt_pk_bf16_f32 v184, v208, v209\n\t"
      "v_cvt_pk_bf16_f32 v185, v210, v211\n\t"
      "v_exp_f32 v212, v212\n\t"
      "v_exp_f32 v213, v213\n\t"
      "v_mfma_f32_32x32x16_bf16 v[240:255], a[128:131], a[88:91], 0\n\t"
      "v_exp_f32 v214, v214\n\t"
      "v_exp_f32 v215, v215\n\t"
      "v_cvt_pk_bf16_f32 v186, v212, v213\n\t"
      "v_cvt_pk_bf16_f32 v187, v214, v215\n\t"
      "v_exp_f32 v216, v216\n\t"
      "v_exp_f32 v217, v217\n\t"
      "v_mfma_f32_32x32x16_bf16 %[o1], a[136:139], v[184:187], %[o1]\n\t"
      "buffer_load_dwordx4 a[128:131], %[voff], %[srd], %[so] offen offset:0 sc1\n\t"
      "v_exp_f32 v218, v218\n\t"
      "v_exp_f32 v219, v219\n\t"
      "v_cvt_pk_bf16_f32 v188, v216, v217\n\t"
      "v_cvt_pk_bf16_f32 v189, v218, v219\n\t"
      "v_exp_f32 v220, v220\n\t"
      "v_exp_f32 v221, v221\n\t"
      "v_mfma_f32_32x32x16_bf16 v[240:255], a[132:135], a[92:95], v[240:255]\n\t"
      "v_exp_f32 v222, v222\n\t"
      "v_exp_f32 v223, v223\n\t"
      "v_cvt_pk_bf16_f32 v190, v220, v221\n\t"
      "v_cvt_pk_bf16_f32 v191, v222, v223\n\t"
      "v_exp_f32 v224, v224\n\t"
      "v_exp_f32 v225, v225\n\t"
      "v_mfma_f32_32x32x16_bf16 %[o1], a[140:143], v[188:191], %[o1]\n\t"
      "buffer_load_dwordx4 a[132:135], %[voff], %[srd], %[so] offen offset:1024 sc1\n\t"
      "s_waitcnt vmcnt(12)\n\t"
      "v_exp_f32 v226, v226\n\t"
      "v_exp_f32 v227, v227\n\t"
      "v_cvt_pk_bf16_f32 v184, v224, v225\n\t"
      "v_cvt_pk_bf16_f32 v185, v226, v227\n\t"
      "v_exp_f32 v228, v228\n\t"
      "v_exp_f32 v229, v229\n\t"
      "v_mfma_f32_32x32x16_bf16 v[192:207], a[144:147], a[64:67], 0\n\t"
      "v_exp_f32 v230, v230\n\t"
      "v_exp_f32 v231, v231\n\t"
      "v_cvt_pk_bf16_f32 v186, v228, v229\n\t"
      "v_cvt_pk_bf16_f32 v187, v230, v231\n\t"
      "v_exp_f32 v232, v232\n\t"
      "v_exp_f32 v233, v233\n\t"
      "v_mfma_f32_32x32x16_bf16 %[o2], a[136:139], v[184:187], %[o2]\n\t"
      "v_exp_f32 v234, v234\n\t"
      "v_exp_f32 v235, v235\n\t"
      "v_cvt_pk_bf16_f32 v188, v232, v233\n\t"
      "v_cvt_pk_bf16_f32 v189, v234, v235\n\t"
      "v_exp_f32 v236, v236\n\t"
      "v_exp_f32 v237, v237\n\t"
      "v_mfma_f32_32x32x16_bf16 v[192:207], a[148:151], a[68:71], v[192:207]\n\t"
      "v_exp_f32 v238, v238\n\t"
      "v_exp_f32 v239, v239\n\t"
      "v_cvt_pk_bf16_f32 v190, v236, v237\n\t"
      "v_cvt_pk_bf16_f32 v191, v238, v239\n\t"
      "v_exp_f32 v240, v240\n\t"
      "v_exp_f32 v241, v241\n\t"
      "v_mfma_f32_32x32x16_bf16 %[o2], a[140:143], v[188:191], %[o2]\n\t"
      "v_exp_f32 v242, v242\n\t"
      "v_exp_f32 v243, v243\n\t"
      "v_cvt_pk_bf16_f32 v184, v240, v241\n\t"
      "v_cvt_pk_bf16_f32 v185, v242, v243\n\t"
      "v_exp_f32 v244, v244\n\t"
      "v_exp_f32 v245, v245\n\t"
      "v_mfma_f32_32x32x16_bf16 v[208:223], a[144:147], a[72:75], 0\n\t"
      "v_exp_f32 v246, v246\n\t"
      "v_exp_f32 v247, v247\n\t"
      "v_cvt_pk_bf16_f32 v186, v244, v245\n\t"
      "v_cvt_pk_bf16_f32 v187, v246, v247\n\t"
      "v_exp_f32 v248, v248\n\t"
      "v_exp_f32 v249, v249\n\t"
      "v_mfma_f32_32x32x16_bf16 %[o3], a[136:139], v[184:187], %[o3]\n\t"
      "v_exp_f32 v250, v250\n\t"
      "v_exp_f32 v251, v251\n\t"
      "v_cvt_pk_bf16_f32 v188, v248, v249\n\t"
      "v_cvt_pk_bf16_f32 v189, v250, v251\n\t"
      "v_exp_f32 v252, v252\n\t"
      "v_exp_f32 v253, v253\n\t"
      "v_mfma_f32_32x32x16_bf16 v[208:223], a[148:151], a[76:79], v[208:223]\n\t"
      "buffer_load_dwordx4 a[136:139], %[voff], %[srd], %[so] offen offset:2048 sc1\n\t"
      "v_exp_f32 v254, v254\n\t"
      "v_exp_f32 v255, v255\n\t"
      "v_cvt_pk_bf16_f32 v190, v252, v253\n\t"
      "v_cvt_pk_bf16_f32 v191, v254, v255\n\t"
      "v_exp_f32 v192, v192\n\t"
      "v_exp_f32 v193, v193\n\t"
      "v_mfma_f32_32x32x16_bf16 %[o3], a[140:143], v[188:191], %[o3]\n\t"
      "v_exp_f32 v194, v194\n\t"
      "v_exp_f32 v195, v195\n\t"
      "v_cvt_pk_bf16_f32 v184, v192, v193\n\t"
      "v_cvt_pk_bf16_f32 v185, v194, v195\n\t"
      "v_exp_f32 v196, v196\n\t"
      "v_exp_f32 v197, v197\n\t"
      "v_mfma_f32_32x32x16_bf16 v[224:239], a[144:147], a[80:83], 0\n\t"
      "buffer_load_dwordx4 a[140:143], %[voff], %[srd], %[so] offen offset:3072 sc1\n\t"
      "s_add_u32 %[so], %[so], 0x1000\n\t"
      "v_exp_f32 v198, v198\n\t"
      "v_exp_f32 v199, v199\n\t"
      "v_cvt_pk_bf16_f32 v186, v196, v197\n\t"
      "v_cvt_pk_bf16_f32 v187, v198, v199\n\t"
      "v_exp_f32 v200, v200\n\t"
      "v_exp_f32 v201, v201\n\t"
      "v_mfma_f32_32x32x16_bf16 %[o0], a[152:155], v[184:187], %[o0]\n\t"
      "v_exp_f32 v202, v202\n\t"
      "v_exp_f32 v203, v203\n\t"
      "v_cvt_pk_bf16_f32 v188, v200, v201\n\t"
      "v_cvt_pk_bf16_f32 v189, v202, v203\n\t"
      "v_exp_f32 v204, v204\n\t"
      "v_exp_f32 v205, v205\n\t"
      "v_mfma_f32_32x32x16_bf16 v[224:239], a[148:151], a[84:87], v[224:239]\n\t"
      "v_exp_f32 v206, v206\n\t"
      "v_exp_f32 v207, v207\n\t"
      "v_cvt_pk_bf16_f32 v190, v204, v205\n\t"
      "v_cvt_pk_bf16_f32 v191, v206, v207\n\t"
      "v_exp_f32 v208, v208\n\t"
      "v_exp_f32 v209, v209\n\t"
      "v_mfma_f32_32x32x16_bf16 %[o0], a[156:159], v[188:191], %[o0]\n\t"
      "v_exp_f32 v210, v210\n\t"
      "v_exp_f32 v211, v211\n\t"
      "v_cvt_pk_bf16_f32 v184, v208, v209\n\t"
      "v_cvt_pk_bf16_f32 v185, v210, v211\n\t"
      "v_exp_f32 v212, v212\n\t"
      "v_exp_f32 v213, v213\n\t"
      "v_mfma_f32_32x32x16_bf16 v[240:255], a[144:147], a[88:91], 0\n\t"
      "v_exp_f32 v214, v214\n\t"
      "v_exp_f32 v215, v215\n\t"
      "v_cvt_pk_bf16_f32 v186, v212, v213\n\t"
      "v_cvt_pk_bf16_f32 v187, v214, v215\n\t"
      "v_exp_f32 v216, v216\n\t"
      "v_exp_f32 v217, v217\n\t"
      "v_mfma_f32_32x32x16_bf16 %[o1], a[152:155], v[184:187], %[o1]\n\t"
      "buffer_load_dwordx4 a[144:147], %[voff], %[srd], %[so] offen offset:0 sc1\n\t"
      "v_exp_f32 v218, v218\n\t"
      "v_exp_f32 v219, v219\n\t"
      "v_cvt_pk_bf16_f32 v188, v216, v217\n\t"
      "v_cvt_pk_bf16_f32 v189, v218, v219\n\t"
      "v_exp_f32 v220, v220\n\t"
      "v_exp_f32 v221, v221\n\t"
      "v_mfma_f32_32x32x16_bf16 v[240:255], a[148:151], a[92:95], v[240:255]\n\t"
      "v_exp_f32 v222, v222\n\t"
      "v_exp_f32 v223, v223\n\t"
      "v_cvt_pk_bf16_f32 v190, v220, v221\n\t"
      "v_cvt_pk_bf16_f32 v191, v222, v223\n\t"
      "v_exp_f32 v224, v224\n\t"
      "v_exp_f32 v225, v225\n\t"
      "v_mfma_f32_32x32x16_bf16 %[o1], a[156:159], v[188:191], %[o1]\n\t"
      "buffer_load_dwordx4 a[148:151], %[voff], %[srd], %[so] offen offset:1024 sc1\n\t"
      "s_waitcnt vmcnt(12)\n\t"
      "v_exp_f32 v226, v226\n\t"
      "v_exp_f32 v227, v227\n\t"
      "v_cvt_pk_bf16_f32 v184, v224, v225\n\t"
      "v_cvt_pk_bf16_f32 v185, v226, v227\n\t"
      "v_exp_f32 v228, v228\n\t"
      "v_exp_f32 v229, v229\n\t"
      "v_mfma_f32_32x32x16_bf16 v[192:207], a[96:99], a[64:67], 0\n\t"
      "v_exp_f32 v230, v230\n\t"
      "v_exp_f32 v231, v231\n\t"
      "v_cvt_pk_bf16_f32 v186, v228, v229\n\t"
      "v_cvt_pk_bf16_f32 v187, v230, v231\n\t"
      "v_exp_f32 v232, v232\n\t"
      "v_exp_f32 v233, v233\n\t"
      "v_mfma_f32_32x32x16_bf16 %[o2], a[152:155], v[184:187], %[o2]\n\t"
      "v_exp_f32 v234, v234\n\t"
      "v_exp_f32 v235, v235\n\t"
      "v_cvt_pk_bf16_f32 v188, v232, v233\n\t"
      "v_cvt_pk_bf16_f32 v189, v234, v235\n\t"
      "v_exp_f32 v236, v236\n\t"
      "v_exp_f32 v237, v237\n\t"
      "v_mfma_f32_32x32x16_bf16 v[192:207], a[100:103], a[68:71], v[192:207]\n\t"
      "v_exp_f32 v238, v238\n\t"
      "v_exp_f32 v239, v239\n\t"
      "v_cvt_pk_bf16_f32 v190, v236, v237\n\t"
      "v_cvt_pk_bf16_f32 v191, v238, v239\n\t"
      "v_exp_f32 v240, v240\n\t"
      "v_exp_f32 v241, v241\n\t"
      "v_mfma_f32_32x32x16_bf16 %[o2], a[156:159], v[188:191], %[o2]\n\t"
      "v_exp_f32 v242, v242\n\t"
      "v_exp_f32 v243, v243\n\t"
      "v_cvt_pk_bf16_f32 v184, v240, v241\n\t"
      "v_cvt_pk_bf16_f32 v185, v242, v243\n\t"
      "v_exp_f32 v244, v244\n\t"
      "v_exp_f32 v245, v245\n\t"
      "v_mfma_f32_32x32x16_bf16 v[208:223], a[96:99], a[72:75], 0\n\t"
      "v_exp_f32 v246, v246\n\t"
      "v_exp_f32 v247, v247\n\t"
      "v_cvt_pk_bf16_f32 v186, v244, v245\n\t"
      "v_cvt_pk_bf16_f32 v187, v246, v247\n\t"
      "v_exp_f32 v248, v248\n\t"
      "v_exp_f32 v249, v249\n\t"
      "v_mfma_f32_32x32x16_bf16 %[o3], a[152:155], v[184:187], %[o3]\n\t"
      "v_exp_f32 v250, v250\n\t"
      "v_exp_f32 v251, v251\n\t"
      "v_cvt_pk_bf16_f32 v188, v248, v249\n\t"
      "v_cvt_pk_bf16_f32 v189, v250, v251\n\t"
      "v_exp_f32 v252, v252\n\t"
      "v_exp_f32 v253, v253\n\t"
      "v_mfma_f32_32x32x16_bf16 v[208:223], a[100:103], a[76:79], v[208:223]\n\t"
      "buffer_load_dwordx4 a[152:155], %[voff], %[srd], %[so] offen offset:2048 sc1\n\t"
      "s_sub_u32 %[c2], %[c2], 1\n\t"
      "s_cmp_lg_u32 %[c2], 0\n\t"
      "v_exp_f32 v254, v254\n\t"
      "v_exp_f32 v255, v255\n\t"
      "v_cvt_pk_bf16_f32 v190, v252, v253\n\t"
      "v_cvt_pk_bf16_f32 v191, v254, v255\n\t"
      "s_cbranch_scc1 lb_%=\n\t"
      "fin_%=:\n\t"
      "s_nop 1\n\t"
      "v_mfma_f32_32x32x16_bf16 %[o3], a[156:159], v[188:191], %[o3]\n\t"
      "s_waitcnt vmcnt(0)\n\t"
      "s_barrier\n\t"
      "s_nop 7\n\t"
      "s_nop 3\n\t"
        : [so] "+s"(so), [mo] "=&s"(mo), [mc] "+s"(mc), [c1] "+s"(c1), [c2] "+s"(c2), [gm0] "+s"(gm0), [gso] "+s"(gso), [ms] "=&s"(ms),
          [o0] "+v"(o0), [o1] "+v"(o1), [o2] "+v"(o2), [o3] "+v"(o3)
        : [voff] "v"(k.l16), [srd] "s"(k.rkv), [wsrd] "s"(k.ws), [mxa] "v"(SCR + w * 512 + k.r * 16), [w4] "v"(w * 4 + k.hh * 2048),
          [mke] "s"(mke), [mko] "s"(mko), [foff] "v"(WS_F2 + (xcd * 16 + c) * 256 + lane * 4), [fof6] "v"(WS_F6 + xcd * 128 + (7 * w + (lane < 7 ? lane : 0)) * 4),
          [fpub] "v"((c < 14 && lane < 8) ? WS_F2 + (lane * 16 + c) * 256 + (g * 4 + w) * 4 : OOB), [tag] "s"(tag)
        : "memory", "scc", "vcc",
        "v180", "v181", "v182", "v183", "v184", "v185", "v186", "v187", "v188", "v189", "v190", "v191", "v192", "v193", "v194", "v195", "v196", "v197", "v198", "v199",
        "v200", "v201", "v202", "v203", "v204", "v205", "v206", "v207", "v208", "v209", "v210", "v211", "v212", "v213", "v214", "v215", "v216", "v217", "v218", "v219",
        "v220", "v221", "v222", "v223", "v224", "v225", "v226", "v227", "v228", "v229", "v230", "v231", "v232", "v233", "v234", "v235", "v236", "v237", "v238", "v239",
        "v240", "v241", "v242", "v243", "v244", "v245", "v246", "v247", "v248", "v249", "v250", "v251", "v252", "v253", "v254", "v255");
      STAMP(5);
      const float lm = fmaxf(fmaxf(o0[12], o1[12]), fmaxf(o2[12], o3[12])), ls = (o0[12] + o1[12]) + (o2[12] + o3[12]);
      const u32 badw = __builtin_amdgcn_ballot_w64(k.hh && !(lm < 1073741824.f && ls == ls)) != 0;
      const float bf = badw ? 1.f : 0.f;
      o1[13] = k.hh ? bf : o1[13]; o2[13] = k.hh ? bf : o2[13]; o3[13] = k.hh ? bf : o3[13];
      tile_put(CMB + ((w ^ 1) * 3 + 0) * 4096 + k.l16, o1); tile_put(CMB + ((w ^ 2) * 3 + 1) * 4096 + k.l16, o2); tile_put(CMB + ((w ^ 3) * 3 + 2) * 4096 + k.l16, o3);
#pragma unroll
      for (int i = 0; i < 16; ++i) o[i] = o0[i];
      lds_bar();
      tile_recv(w, lane, o);
      if (!badw && __builtin_amdgcn_readlane(__float_as_uint(o[13]), 63) == 0u) break;
      lds_bar();
      apf_all<0>(k);
    }
    const float rl = __builtin_amdgcn_rcpf(__uint_as_float(hi_half(__float_as_uint(o[12]))));
    const float rz = k.hh ? 0.f : rl;
    u32x4 d0 = {cvtpk(o[0] * rl, o[1] * rl), cvtpk(o[2] * rl, o[3] * rl), cvtpk(o[4] * rl, o[5] * rl), cvtpk(o[6] * rl, o[7] * rl)};
    u32x4 d1 = {cvtpk(o[8] * rl, o[9] * rl), cvtpk(o[10] * rl, o[11] * rl), cvtpk(o[12] * rz, o[13] * rz), cvtpk(o[14] * rz, o[15] * rz)};
    const u32 so = WS_O + ((4 * rb + w) * 32 + 2 * hd) * 1024;
    stc(k.ws, d0, k.l16, so); stc(k.ws, d1, k.l16, so + 1024);
    STAMP(4);
    wait_bar<0>();
    stc1(k.ws, tag, lane == 0 ? WS_F3 + (rb * 4 + w) * 128 + hd * 4 : OOB);
    STAMP(6);
  }
  RELANE();
  const u32 oso = WS_O + (4 * g + w) * 32768;
#define OL(I) of[I] = ldc(k.ws, k.l16 + ((I) & 3) * 1024, oso + ((I) >> 2) * 4096)
  if (c < 14) {
#pragma unroll
    for (int i = 0; i < 4; ++i) glds_c(k.ws, k.l16, WS_WP + c * 131072 + CMB + (i * 4 + w) * 1024, CMB + (i * 4 + w) * 1024);
    const u32 np = c >= 7 ? c - 7 : c, mh = c >= 7;
    const u32 w2s = WS_W2F + (np * 112 + 28 * w) * 2048 - 1024;
    u32 w2e = k.l16 + ((w & 1) ? 2048 : 1024), w2o = k.l16 + ((w & 1) ? 0 : 1024); PIN2(w2e, w2o);
#define W2L(I) ld_acc<64 + 4 * (I), ((I) & 3) * 1024>(k.ws, ((I) & 1) ? w2o : w2e, w2s + ((I) >> 2) * 4096)
#define W2L4(I) W2L(I); W2L(I + 1); W2L(I + 2); W2L(I + 3)
    W2L4(0); W2L4(4); W2L4(8);
    u32 wp[4], wq[4]; float ha[16];
#pragma unroll
    for (int j = 0; j < 4; ++j) { wp[j] = k.l16 + ((w + j) & 3) * 1024; wq[j] = wp[j] + 65536; PIN2(wp[j], wq[j]); }
#define WP(J, I) lds128(((I) < 16 ? wp[J] : wq[J]) + ((I) & 15) * 4096)
    u32x4 a0 = WP(0, 0), a1 = WP(1, 0), a2 = WP(0, 1), a3 = WP(1, 1);
    poll(k.ws, WS_F3 + (g * 4 + w) * 128 + (lane & 15) * 4, tag);
    STAMP(7);
    u32x4 of[32], wt[8];
#pragma unroll
    for (int i = 0; i < 10; ++i) OL(i);
    __builtin_amdgcn_s_barrier();
#pragma unroll
    for (int i = 0; i < 32; ++i) {
      u32x4 n0, n1; SB();
      mf<0, false>(a0, of[i]); n0 = i < 30 ? WP(0, i + 2) : WP(2, 2 * (i - 30)); if (i < 22) OL(i + 10); SB();
      mf<16, false>(a1, of[i]); n1 = i < 30 ? WP(1, i + 2) : WP(2, 2 * (i - 30) + 1);
      a0 = a2; a1 = a3; a2 = n0; a3 = n1;
    }
#define G1B(A, J, JN, I, S0, S1) do { u32x4 n0, n1; SB(); \
      mf<A, false>(a0, of[I]); n0 = (I) < 28 ? WP(J, (I) + 4) : WP(JN, (I) - 28); S0; SB(); \
      mf<A, false>(a1, of[(I) + 1]); n1 = (I) < 28 ? WP(J, (I) + 5) : WP(JN, (I) - 27); S1; \
      a0 = a2; a1 = a3; a2 = n0; a3 = n1; } while (0)
#define WTL(I) wt[I] = ldc(k.ws, (((I) & 1) ? w2o : w2e) + ((I) & 3) * 1024, w2s + (12 + ((I) >> 2)) * 4096)
    G1B(32, 2, 3, 0, W2L(12), W2L(13)); G1B(32, 2, 3, 2, W2L(14), W2L(15)); G1B(32, 2, 3, 4, W2L(16), W2L(17)); G1B(32, 2, 3, 6, W2L(18), hs_read<0>(ha));
    G1B(32, 2, 3, 8, (hs_put<0, 0>(k, ha)), (hs_put<0, 1>(k, ha))); G1B(32, 2, 3, 10, W2L(19), hs_read<1>(ha));
    G1B(32, 2, 3, 12, (hs_put<1, 0>(k, ha)), (hs_put<1, 1>(k, ha))); G1B(32, 2, 3, 14, W2L(20), W2L(21)); G1B(32, 2, 3, 16, W2L(22), W2L(23)); G1B(32, 2, 3, 18, W2L(24), W2L(25));
    G1B(32, 2, 3, 20, W2L(26), W2L(27)); G1B(32, 2, 3, 22, W2L(28), W2L(29));
    G1B(32, 2, 3, 24, W2L(30), W2L(31)); G1B(32, 2, 3, 26, W2L(32), W2L(33)); G1B(32, 2, 3, 28, W2L(34), W2L(35)); G1B(32, 2, 3, 30, W2L(36), W2L(37));
    G1B(48, 3, 3, 0, W2L(38), W2L(39)); G1B(48, 3, 3, 2, W2L(40), W2L(41)); G1B(48, 3, 3, 4, W2L(42), W2L(43)); G1B(48, 3, 3, 6, W2L(44), hs_read<2>(ha));
    G1B(48, 3, 3, 8, (hs_put<2, 0>(k, ha)), (hs_put<2, 1>(k, ha))); G1B(48, 3, 3, 10, W2L(45), W2L(46));
    G1B(48, 3, 3, 12, W2L(47), WTL(0)); G1B(48, 3, 3, 14, WTL(1), WTL(2)); G1B(48, 3, 3, 16, WTL(3), WTL(4)); G1B(48, 3, 3, 18, WTL(5), WTL(6)); G1B(48, 3, 3, 20, WTL(7), ); G1B(48, 3, 3, 22, , );
    G1B(48, 3, 3, 24, , ); G1B(48, 3, 3, 26, , ); G1B(48, 3, 3, 28, , ); G1B(48, 3, 3, 30, , );
    mf_drain();
    hs_read<3>(ha); hs_put<3, 0>(k, ha); hs_put<3, 1>(k, ha);
    STAMP(12);
    wait_vm<0>();
    stc1(k.ws, tag, lane == 0 ? WS_F4 + (g * 4 + w) * 128 + c * 4 : OOB);
    STAMP(8);
    RELANE();
    poll(k.ws, WS_F4 + (g * 4 + 2 * mh + ((lane >> 2) & 1)) * 128 + (((7 * w) >> 1) + (lane & 3)) * 4, tag);
    STAMP(9);
    const u32 h0 = WS_H + ((4 * g + 2 * mh + (w >> 1)) * 112 + 28 * w) * 1024, h1 = WS_H + ((4 * g + 2 * mh + ((w >> 1) ^ 1)) * 112 + 28 * w) * 1024;
    const u32 rt = 4 * g + 2 * mh + (w >> 1), nt = 2 * np + (w & 1);
    u32x4 hf[56];
#define HA(KS) hf[2 * (KS)] = ldc(k.ws, k.l16 + ((KS) & 3) * 1024, h0 + ((KS) >> 2) * 4096)
#define HB(KS) hf[2 * (KS) + 1] = ldc(k.ws, k.l16 + ((KS) & 3) * 1024, h1 + ((KS) >> 2) * 4096)
#define G2M(D, KS, JN, F) do { if constexpr ((KS) < 24) mfaa<D, 64 + 8 * (KS) + 4 * (JN), (KS) == 0>(F); else mf<D, false>(wt[((KS) < 24 ? 0 : 2 * ((KS) - 24)) + (JN)], F); } while (0)
#define G2S(KS) do { SB(); G2M(0, KS, 0, hf[2 * (KS)]); if constexpr ((KS) + 6 < 28) HA(((KS) + 6) % 28); SB(); G2M(16, KS, 1, hf[2 * (KS)]); SB(); \
      G2M(32, KS, 0, hf[2 * (KS) + 1]); if constexpr ((KS) + 6 < 28) HB(((KS) + 6) % 28); SB(); G2M(48, KS, 1, hf[2 * (KS) + 1]); } while (0)
#define G2S4(KS) G2S(KS); G2S(KS + 1); G2S(KS + 2); G2S(KS + 3)
    HA(0); HB(0); HA(1); HB(1); HA(2); HB(2); HA(3); HB(3); HA(4); HB(4); HA(5); HB(5);
    G2S4(0); G2S4(4); G2S4(8); G2S4(12); G2S4(16); G2S(20); G2S(21);
    u32 f5; asm volatile("buffer_load_dword %0, %1, %2, 0 offen sc1" : "=v"(f5) : "v"(WS_F5 + rt * 128 + (lane & 1) * 4), "s"(k.ws) : "memory");
    G2S(22); G2S(23); G2S4(24);
    asm volatile("s_waitcnt vmcnt(0)" : "+v"(f5) :: "memory");
    if (__builtin_amdgcn_ballot_w64(f5 != tag) != 0) poll(k.ws, WS_F5 + rt * 128 + (lane & 1) * 4, tag);
    u32x4 hv[4];
#pragma unroll
    for (int q = 0; q < 4; ++q) hv[q] = ldc(k.ws, k.l16 + q * 1024, WS_HA + (rt * 14 + nt) * 4096);
    const u32x4 st = ldc(k.ws, k.r * 16, WS_ST + rt * 512);
    STAMP(10);
    lds_bar();
    tile_send<0>(w, lane);
    float o[16]; acc16<0>(o);
    lds_bar();
    tile_recv(w, lane, o);
    const float mu = (__uint_as_float(st[0]) + __uint_as_float(st[2])) * (1.f / 448.f);
    const float var = fmaxf((__uint_as_float(st[1]) + __uint_as_float(st[3])) * (1.f / 448.f) - mu * mu, 0.f), rstd = __builtin_amdgcn_rsqf(var + 1e-5f);
    u32 pk[8];
#pragma unroll
    for (int q = 0; q < 4; ++q) {
      pk[2 * q] = cvtpk(fmaf(o[4 * q], rstd, __uint_as_float(hv[q][0])), fmaf(o[4 * q + 1], rstd, __uint_as_float(hv[q][1])));
      pk[2 * q + 1] = cvtpk(fmaf(o[4 * q + 2], rstd, __uint_as_float(hv[q][2])), fmaf(o[4 * q + 3], rstd, __uint_as_float(hv[q][3])));
    }
    char* op = out + (size_t)(rt * 32 + k.r) * 896 + nt * 64 + k.hh * 16;
#pragma unroll
    for (int q = 0; q < 4; q += 2) {
      auto s0 = __builtin_amdgcn_permlane32_swap(pk[2 * q], pk[2 * q + 2], false, false), s1 = __builtin_amdgcn_permlane32_swap(pk[2 * q + 1], pk[2 * q + 3], false, false);
      u32x4 d = {s0[0], s1[0], s0[1], s1[1]};
      *(u32x4*)(op + q * 16) = d;
    }
    STAMP(11);
  } else {
    const u32 p = c - 14;
    for (u32 i = 112 + w; i < 160; i += 4) { const u32 ee = i * 64 + lane, row = (ee * 9363u) >> 18; glds(k.rwo, row * 896 + (ee - 28 * row) * 16, p * 448, i * 1024); }
    u32 ta = (4 * k.hh + ((lane & 15) >> 2)) * 448 + ((lane >> 4) & 1) * 32 + (lane & 3) * 8, tb = ta + 112 * 448, tc = ta + 224 * 448; PIN2(ta, tb); PIN1(tc);
    poll(k.ws, WS_F3 + (g * 4 + w) * 128 + (lane & 15) * 4, tag);
    u32x4 of[32];
#pragma unroll
    for (int i = 0; i < 8; ++i) OL(i);
    __builtin_amdgcn_s_barrier();
    STAMP(7);
#define OPA(I, RB, J) (((RB) < 112 ? ta : ((RB) < 224 ? tb : tc)) + ((RB) % 112) * 448 + ((I) & 1) * 16 * 448 + (J) * 64)
#define OPR(I, RB, J) ldstr(OPA(I, RB, J), OPA(I, RB, J) + 3584)
#define OP1(A, I, F, NXT) do { SB(); mf<A, false>(F, of[I]); F = NXT; } while (0)
#define OPK(I, IN, RBN) do { OP1(0, I, f0, OPR(IN, RBN, 0)); if ((I) < 24) OL(((I) + 8) & 31); OP1(16, I, f1, OPR(IN, RBN, 1)); OP1(32, I, f2, OPR(IN, RBN, 2)); OP1(48, I, f3, OPR(IN, RBN, 3)); \
      OP1(160, I, f4, OPR(IN, RBN, 4)); OP1(176, I, f5, OPR(IN, RBN, 5)); OP1(192, I, f6, OPR(IN, RBN, 6)); } while (0)
    u32x4 f0 = OPR(0, 0, 0), f1 = OPR(0, 0, 1), f2 = OPR(0, 0, 2), f3 = OPR(0, 0, 3), f4 = OPR(0, 0, 4), f5 = OPR(0, 0, 5), f6 = OPR(0, 0, 6);
#pragma unroll
    for (int i = 0; i < 10; ++i) OPK(i, i + 1, 28 * ((i + 1) >> 1));
    lds_bar();
    for (u32 i = w; i < 51; i += 4) { const u32 ee = i * 64 + lane, row = (ee * 9363u) >> 18; glds(k.rwo, row + 336 < 448 ? (row + 336) * 896 + (ee - 28 * row) * 16 : OOB, p * 448, i * 1024); }
#pragma unroll
    for (int i = 10; i < 23; ++i) OPK(i, i + 1, 28 * ((i + 1) >> 1));
    wait_bar<8>();
#pragma unroll
    for (int i = 23; i < 31; ++i) OPK(i, i + 1, 28 * (((i + 1) >> 1) - 12));
    OPK(31, 31, 28 * 3);
    mf_drain();
    STAMP(9);
    float s1 = 0.f, s2 = 0.f;
#define HST(J, A) do { float a_[16]; acc16<A>(a_); _Pragma("unroll") for (int q = 0; q < 4; ++q) { \
      u32x4 d_ = {__float_as_uint(a_[4 * q]), __float_as_uint(a_[4 * q + 1]), __float_as_uint(a_[4 * q + 2]), __float_as_uint(a_[4 * q + 3])}; \
      stc(k.ws, d_, k.l16 + q * 1024, WS_HA + ((4 * g + w) * 14 + 7 * p + (J)) * 4096); } \
      _Pragma("unroll") for (int q = 0; q < 16; ++q) { s1 += a_[q]; s2 = fmaf(a_[q], a_[q], s2); } } while (0)
    HST(0, 0); HST(1, 16); HST(2, 32); HST(3, 48); HST(4, 160); HST(5, 176); HST(6, 192);
    s1 = hsum(s1); s2 = hsum(s2);
    stc1(k.ws, __float_as_uint(k.hh ? s2 : s1), WS_ST + (4 * g + w) * 512 + k.r * 16 + p * 8 + k.hh * 4);
    STAMP(10);
    wait_vm<0>();
    stc1(k.ws, tag, lane == 0 ? WS_F5 + (g * 4 + w) * 128 + p * 4 : OOB);
    STAMP(8);
    if (b == 255) {
      poll(k.ws, WS_F3 + (w * 16 + (lane >> 2)) * 128 + (lane & 3) * 4, tag); poll(k.ws, WS_F3 + (w * 16 + (lane >> 2)) * 128 + (4 + (lane & 3)) * 4, tag);
      poll(k.ws, WS_F3 + (w * 16 + (lane >> 2)) * 128 + (8 + (lane & 3)) * 4, tag); poll(k.ws, WS_F3 + (w * 16 + (lane >> 2)) * 128 + (12 + (lane & 3)) * 4, tag);
      lds_bar();
      stc1(k.ws, tag, (w == 0 && lane == 0) ? WS_GEN : OOB);
    }
  }
}

extern "C" void kernel_launch(void* const* d_in, const int* in_sizes, int n_in, void* d_out, int out_size, void* d_ws, size_t ws_size, hipStream_t stream) {
  (void)hipFuncSetAttribute((const void*)fused_block, hipFuncAttributeMaxDynamicSharedMemorySize, LDS_BYTES);
  fused_block<<<256, 256, LDS_BYTES, stream>>>((const char*)d_in[0], (const char*)d_in[1], (const char*)d_in[2], (const char*)d_in[4], (const char*)d_in[5], (char*)d_out, (char*)d_ws);
}
```

```cpp
#include <hip/hip_runtime.h>
#include <stdint.h>

typedef unsigned u32;
typedef u32 u32x4 __attribute__((ext_vector_type(4)));
typedef __amdgpu_buffer_rsrc_t rsrc_t;
#define DI __device__ __forceinline__

constexpr u32 WS_GEN = 0, WS_TMO = 128, WS_FE = 4096;
constexpr u32 MB = 1u << 20;
constexpr u32 WS_Q = 1 * MB, WS_KV = 3 * MB, WS_O = 7 * MB, WS_WP = 9 * MB, WS_W2F = 11 * MB, WS_H = 13 * MB, WS_XS = 22 * MB, WS_END = 26 * MB;
constexpr u32 HRT = 147456, H_HA = 114688, H_ST = 143360;
constexpr u32 OOB = 0x80000000u, LDS_BYTES = 163840;
constexpr float QSCALE = 0.18898223650461362f * 1.4426950408889634f;
#define STAMP_OUT() do {} while (0)
#define A10(p) p "0", p "1", p "2", p "3", p "4", p "5", p "6", p "7", p "8", p "9"
#define A100(p) A10(p "0"), A10(p "1"), A10(p "2"), A10(p "3"), A10(p "4"), A10(p "5"), A10(p "6"), A10(p "7"), A10(p "8"), A10(p "9")
#define ACLOB A10("a"), A10("a1"), A10("a2"), A10("a3"), A10("a4"), A10("a5"), A10("a6"), A10("a7"), A10("a8"), A10("a9"), A100("a1"), A10("a20"), A10("a21"), A10("a22"), A10("a23"), A10("a24"), "a250", "a251", "a252", "a253", "a254", "a255"
DI u32 rfl(u32 v) { return __builtin_amdgcn_readfirstlane(v); }

__global__ __launch_bounds__(256, 1) void fused_block(const char* x, const char* wqkv, const char* wo, const char* w1, const char* w2, char* out, char* ws) {
  typedef const __attribute__((address_space(4))) unsigned long long* kptr;
  kptr ka = (kptr)__builtin_amdgcn_kernarg_segment_ptr();
  const u32 b = blockIdx.x; u32 sv = threadIdx.x;
  asm volatile(
      "s_load_dwordx2 s[12:13], %[ka], 0\n\t"
      "s_load_dwordx2 s[16:17], %[ka], 8\n\t"
      "s_load_dwordx2 s[28:29], %[ka], 48\n\t"
      "s_getpc_b64 s[32:33]\n\t"
      "pc0_%=:\n\t"
      "s_bfe_u32 s46, %[bid], 0x40003\n\t"
      "s_mul_i32 s48, s46, 192\n\t"
      "s_add_u32 s48, s48, tab_%=-pc0_%=\n\t"
      "s_add_u32 s49, s48, 64\n\t"
      "s_add_u32 s50, s48, 128\n\t"
      "s_load_dwordx16 s[60:75], s[32:33], s48\n\t"
      "s_load_dwordx16 s[76:91], s[32:33], s49\n\t"
      "s_load_dwordx4 s[92:95], s[32:33], s50\n\t"
      "s_load_dwordx2 s[24:25], %[ka], 16\n\t"
      "s_load_dwordx2 s[20:21], %[ka], 24\n\t"
      "s_load_dwordx2 s[36:37], %[ka], 32\n\t"
      "s_load_dwordx2 s[40:41], %[ka], 40\n\t"
      "v_lshrrev_b32 v24, 6, %[tid]\n\t"
      "v_and_b32 v1, 63, %[tid]\n\t"
      "v_readfirstlane_b32 s44, v24\n\t"
      "s_and_b32 s45, %[bid], 7\n\t"
      "s_lshr_b32 s48, %[bid], 7\n\t"
      "s_lshl_b32 s45, s45, 1\n\t"
      "s_add_u32 s45, s45, s48\n\t"
      "s_mov_b32 s14, 1835008\n\t"
      "s_mov_b32 s15, 0x20000\n\t"
      "s_mov_b32 s18, 1204224\n\t"
      "s_mov_b32 s19, 0x20000\n\t"
      "s_mul_i32 s57, s45, 3\n\t"
      "s_add_u32 s57, s57, s46\n\t"
      "s_mul_i32 s58, s57, 37\n\t"
      "s_lshr_b32 s58, s58, 8\n\t"
      "s_mul_i32 s58, s58, 7\n\t"
      "s_sub_u32 s57, s57, s58\n\t"
      "s_lshl_b32 s57, s57, 2\n\t"
      "s_mov_b32 s100, 0x06543210\n\t"
      "s_mov_b32 s101, 0x00654321\n\t"
      "s_lshr_b64 s[100:101], s[100:101], s57\n\t"
      "v_lshrrev_b32 v24, 3, v1\n\t"
      "v_lshrrev_b32 v25, 4, v1\n\t"
      "v_and_b32 v26, 7, v1\n\t"
      "v_xor_b32 v25, v25, v26\n\t"
      "v_mul_u32_u24 v24, 896, v24\n\t"
      "v_lshl_add_u32 v28, v25, 4, v24\n\t"
      "v_xor_b32 v40, 64, v28\n\t"
      "v_add_u32 v29, 6144, v28\n\t"
      "v_add_u32 v41, 18432, v40\n\t"
      "v_add_u32 v40, 12288, v40\n\t"
      "s_lshl_b32 s59, s45, 2\n\t"
      "s_add_u32 s59, s59, s44\n\t"
      "s_mul_i32 s51, s59, 28672\n\t"
      "s_lshl_b32 s54, s44, 12\n\t"
      "s_mul_i32 s55, s44, 3072\n\t"
      "s_mul_i32 s48, s44, 192\n\t"
      "s_movk_i32 s49, 2496\n\t"
      "v_add_u32 v24, s48, v1\n\t"
      "v_lshlrev_b32 v26, 4, v24\n\t"
      "v_mul_u32_u24 v25, 5462, v24\n\t"
      "v_lshrrev_b32 v25, 16, v25\n\t"
      "v_mad_u32_u24 v30, v25, s49, v26\n\t"
      "v_add_u32 v24, 64, v24\n\t"
      "v_mul_u32_u24 v25, 5462, v24\n\t"
      "v_lshrrev_b32 v25, 16, v25\n\t"
      "v_mad_u32_u24 v31, v25, s49, v26\n\t"
      "v_add_u32 v24, 64, v24\n\t"
      "v_mul_u32_u24 v25, 5462, v24\n\t"
      "v_lshrrev_b32 v25, 16, v25\n\t"
      "v_mad_u32_u24 v32, v25, s49, v26\n\t"
      "s_waitcnt lgkmcnt(0)\n\t"
      "s_load_dword s47, s[28:29], 0\n\t"
      "s_cmp_ge_u32 s46, 14\n\t"
      "s_cbranch_scc1 side_%=\n\t"
      "s_cmp_ge_u32 s46, 9\n\t"
      "s_cbranch_scc1 vq_%=\n\t"
      "s_bfe_u32 s48, s100, 262144\n\t"
      "s_add_u32 m0, s54, 0\n\t"
      "s_mul_i32 s48, s48, 128\n\t"
      "s_add_u32 s48, s48, s51\n\t"
      "buffer_load_dwordx4 v28, s[12:15], s48 offen offset:0 lds\n\t"
      "buffer_load_dwordx4 v29, s[12:15], s48 offen offset:1024 lds\n\t"
      "buffer_load_dwordx4 v40, s[12:15], s48 offen offset:2048 lds\n\t"
      "buffer_load_dwordx4 v41, s[12:15], s48 offen offset:3072 lds\n\t"
      "s_bfe_u32 s48, s100, 262144\n\t"
      "s_add_u32 m0, s55, 65536\n\t"
      "s_mul_i32 s48, s48, 172032\n\t"
      "s_add_u32 s48, s48, s93\n\t"
      "buffer_load_dwordx4 v30, s[16:19], s48 offen offset:0 lds\n\t"
      "buffer_load_dwordx4 v31, s[16:19], s48 offen offset:1024 lds\n\t"
      "buffer_load_dwordx4 v32, s[16:19], s48 offen offset:2048 lds\n\t"
      "s_bfe_u32 s48, s100, 262148\n\t"
      "s_add_u32 m0, s54, 16384\n\t"
      "s_mul_i32 s48, s48, 128\n\t"
      "s_add_u32 s48, s48, s51\n\t"
      "buffer_load_dwordx4 v28, s[12:15], s48 offen offset:0 lds\n\t"
      "v_bfe_u32 v24, v1, 1, 2\n\t"
      "v_bfe_u32 v25, v1, 4, 1\n\t"
      "v_lshl_or_b32 v24, v25, 2, v24\n\t"
      "v_lshrrev_b32 v26, 5, v1\n\t"
      "v_xor_b32 v24, v24, v26\n\t"
      "v_and_b32 v59, 31, v1\n\t"
      "v_lshlrev_b32 v24, 4, v24\n\t"
      "v_lshl_add_u32 v24, v59, 7, v24\n\t"
      "buffer_load_dwordx4 v29, s[12:15], s48 offen offset:1024 lds\n\t"
      "v_add_u32 v33, s54, v24\n\t"
      "v_xor_b32 v34, 32, v33\n\t"
      "v_xor_b32 v35, 64, v33\n\t"
      "v_xor_b32 v36, 0x60, v33\n\t"
      "v_lshlrev_b32 v59, 4, v59\n\t"
      "v_lshlrev_b32 v2, 4, v1\n\t"
      "v_bfe_u32 v24, v1, 2, 2\n\t"
      "v_lshrrev_b32 v25, 5, v1\n\t"
      "buffer_load_dwordx4 v40, s[12:15], s48 offen offset:2048 lds\n\t"
      "v_lshl_add_u32 v24, v25, 3, v24\n\t"
      "v_mul_u32_u24 v37, 192, v24\n\t"
      "v_bfe_u32 v25, v1, 4, 1\n\t"
      "v_lshl_add_u32 v37, v25, 5, v37\n\t"
      "v_bfrev_b32 v25, v1\n\t"
      "v_lshrrev_b32 v25, 30, v25\n\t"
      "v_lshl_add_u32 v37, v25, 3, v37\n\t"
      "v_add_u32 v37, 65536, v37\n\t"
      "buffer_load_dwordx4 v41, s[12:15], s48 offen offset:3072 lds\n\t"
      "v_add_u32 v38, s55, v37\n\t"
      "v_mov_b32 v39, 1065369472\n\t"
      "v_mov_b32 v42, 0\n\t"
      "v_mov_b32 v43, 0\n\t"
      "v_mov_b32 v44, 0\n\t"
      "s_mov_b32 s30, 27262976\n\t"
      "s_mov_b32 s31, 0x20000\n\t"
      "s_mov_b32 s34, 64512\n\t"
      "s_bfe_u32 s48, s100, 262148\n\t"
      "s_add_u32 m0, s55, 77824\n\t"
      "s_mul_i32 s48, s48, 172032\n\t"
      "s_add_u32 s48, s48, s93\n\t"
      "buffer_load_dwordx4 v30, s[16:19], s48 offen offset:0 lds\n\t"
      "s_mov_b32 s35, 0x20000\n\t"
      "s_lshr_b32 s50, %[bid], 3\n\t"
      "s_lshl_b32 s50, s50, 2\n\t"
      "s_add_u32 s50, s50, s44\n\t"
      "s_lshl_b32 s50, s50, 9\n\t"
      "v_mov_b32 v26, 2147483648\n\t"
      "buffer_load_dwordx4 v31, s[16:19], s48 offen offset:1024 lds\n\t"
      "s_mov_b64 exec, 15\n\t"
      "v_lshl_add_u32 v26, v1, 7, s50\n\t"
      "s_mov_b64 exec, -1\n\t"
      "buffer_load_dwordx4 v32, s[16:19], s48 offen offset:2048 lds\n\t"
      "buffer_load_dword a253, v26, s[32:35], 0 offen\n\t"
      "s_mov_b32 s22, 1605632\n\t"
      "s_mov_b32 s23, 0x20000\n\t"
      "s_mov_b32 s26, 401408\n\t"
      "s_mov_b32 s27, 0x20000\n\t"
      "s_mov_b32 s38, 1605632\n\t"
      "s_mov_b32 s39, 0x20000\n\t"
      "s_mul_i32 s52, s44, 57344\n\t"
      "s_lshl_b32 s50, s46, 8\n\t"
      "s_add_u32 s52, s52, s50\n\t"
      "s_mul_i32 s53, s45, 25088\n\t"
      "s_lshl_b32 s56, s44, 10\n\t"
      "v_lshrrev_b32 v24, 4, v1\n\t"
      "v_and_b32 v25, 15, v1\n\t"
      "v_lshlrev_b32 v26, 2, v24\n\t"
      "v_xor_b32 v25, v25, v26\n\t"
      "v_mul_u32_u24 v47, 3584, v24\n\t"
      "s_waitcnt vmcnt(8) lgkmcnt(0)\n\t"
      "s_barrier\n\t"
      "ds_read_b128 v[144:147], v33 offset:0\n\t"
      "ds_read_b64_tr_b16 v[60:61], v37 offset:0\n\t"
      "ds_read_b64_tr_b16 v[62:63], v37 offset:768\n\t"
      "ds_read_b64_tr_b16 v[64:65], v37 offset:64\n\t"
      "ds_read_b64_tr_b16 v[66:67], v37 offset:832\n\t"
      "ds_read_b64_tr_b16 v[68:69], v37 offset:128\n\t"
      "ds_read_b64_tr_b16 v[70:71], v37 offset:896\n\t"
      "ds_read_b128 v[148:151], v34 offset:0\n\t"
      "ds_read_b64_tr_b16 v[72:73], v37 offset:3072\n\t"
      "ds_read_b64_tr_b16 v[74:75], v37 offset:3840\n\t"
      "ds_read_b64_tr_b16 v[76:77], v37 offset:3136\n\t"
      "ds_read_b64_tr_b16 v[78:79], v37 offset:3904\n\t"
      "ds_read_b64_tr_b16 v[80:81], v37 offset:3200\n\t"
      "ds_read_b64_tr_b16 v[82:83], v37 offset:3968\n\t"
      "s_add_u32 s47, s47, 1\n\t"
      "s_bfe_u32 s48, s100, 262152\n\t"
      "s_add_u32 m0, s54, 32768\n\t"
      "s_mul_i32 s48, s48, 128\n\t"
      "s_add_u32 s48, s48, s51\n\t"
      "buffer_load_dwordx4 v28, s[12:15], s48 offen offset:0 lds\n\t"
      "buffer_load_dwordx4 v29, s[12:15], s48 offen offset:1024 lds\n\t"
      "v_lshl_add_u32 v47, v25, 4, v47\n\t"
      "s_lshl_b32 s50, s44, 3\n\t"
      "v_lshrrev_b32 v25, 3, v1\n\t"
      "v_add_u32 v25, s50, v25\n\t"
      "v_min_u32 v25, 27, v25\n\t"
      "v_mul_u32_u24 v25, 896, v25\n\t"
      "s_lshr_b32 s50, s44, 1\n\t"
      "s_lshl_b32 s50, s50, 6\n\t"
      "s_waitcnt lgkmcnt(7)\n\t"
      "v_mfma_f32_32x32x16_bf16 v[96:111], v[60:63], v[144:147], 0\n\t"
      "ds_read_b128 v[152:155], v35 offset:0\n\t"
      "ds_read_b64_tr_b16 v[84:85], v37 offset:6144\n\t"
      "ds_read_b64_tr_b16 v[86:87], v37 offset:6912\n\t"
      "v_and_b32 v26, 0x70, v28\n\t"
      "v_xor_b32 v26, s50, v26\n\t"
      "v_mfma_f32_32x32x16_bf16 v[112:127], v[64:67], v[144:147], 0\n\t"
      "ds_read_b64_tr_b16 v[88:89], v37 offset:6208\n\t"
      "ds_read_b64_tr_b16 v[90:91], v37 offset:6976\n\t"
      "ds_read_b64_tr_b16 v[4:5], v38 offset:0\n\t"
      "ds_read_b64_tr_b16 v[6:7], v38 offset:768\n\t"
      "buffer_load_dwordx4 v40, s[12:15], s48 offen offset:2048 lds\n\t"
      "v_add_u32 v48, v25, v26\n\t"
      "v_mfma_f32_32x32x16_bf16 v[128:143], v[68:71], v[144:147], 0\n\t"
      "ds_read_b64_tr_b16 v[92:93], v37 offset:6272\n\t"
      "ds_read_b64_tr_b16 v[94:95], v37 offset:7040\n\t"
      "buffer_load_dwordx4 v41, s[12:15], s48 offen offset:3072 lds\n\t"
      "v_lshrrev_b32 v27, 5, v1\n\t"
      "s_lshl_b32 s50, s44, 2\n\t"
      "s_waitcnt lgkmcnt(9)\n\t"
      "v_mfma_f32_32x32x16_bf16 v[96:111], v[72:75], v[148:151], v[96:111]\n\t"
      "ds_read_b128 v[156:159], v36 offset:0\n\t"
      "ds_read_b64_tr_b16 v[60:61], v37 offset:9216\n\t"
      "ds_read_b64_tr_b16 v[62:63], v37 offset:9984\n\t"
      "s_add_u32 s50, s50, 157696\n\t"
      "v_add_u32 v45, s50, v2\n\t"
      "v_mfma_f32_32x32x16_bf16 v[112:127], v[76:79], v[148:151], v[112:127]\n\t"
      "ds_read_b64_tr_b16 v[64:65], v37 offset:9280\n\t"
      "ds_read_b64_tr_b16 v[66:67], v37 offset:10048\n\t"
      "ds_read_b64_tr_b16 v[8:9], v38 offset:64\n\t"
      "ds_read_b64_tr_b16 v[10:11], v38 offset:832\n\t"
      "s_bfe_u32 s48, s100, 262152\n\t"
      "s_add_u32 m0, s55, 90112\n\t"
      "s_mul_i32 s48, s48, 172032\n\t"
      "s_add_u32 s48, s48, s93\n\t"
      "buffer_load_dwordx4 v30, s[16:19], s48 offen offset:0 lds\n\t"
      "v_mfma_f32_32x32x16_bf16 v[128:143], v[80:83], v[148:151], v[128:143]\n\t"
      "ds_read_b64_tr_b16 v[68:69], v37 offset:9344\n\t"
      "ds_read_b64_tr_b16 v[70:71], v37 offset:10112\n\t"
      "buffer_load_dwordx4 v31, s[16:19], s48 offen offset:1024 lds\n\t"
      "v_add_u32 v46, 157696, v59\n\t"
      "v_sub_u32 v27, 0, v27\n\t"
      "s_waitcnt vmcnt(7)\n\t"
      "s_barrier\n\t"
      "s_waitcnt lgkmcnt(9)\n\t"
      "v_mfma_f32_32x32x16_bf16 v[96:111], v[84:87], v[152:155], v[96:111]\n\t"
      "ds_read_b128 v[160:163], v33 offset:16384\n\t"
      "ds_read_b64_tr_b16 v[72:73], v37 offset:12288\n\t"
      "ds_read_b64_tr_b16 v[74:75], v37 offset:13056\n\t"
      "v_lshrrev_b32 v24, 2, v1\n\t"
      "v_min_u32 v24, 4, v24\n\t"
      "v_mfma_f32_32x32x16_bf16 v[112:127], v[88:91], v[152:155], v[112:127]\n\t"
      "ds_read_b64_tr_b16 v[76:77], v37 offset:12352\n\t"
      "ds_read_b64_tr_b16 v[78:79], v37 offset:13120\n\t"
      "ds_read_b64_tr_b16 v[12:13], v38 offset:128\n\t"
      "ds_read_b64_tr_b16 v[14:15], v38 offset:896\n\t"
      "buffer_load_dwordx4 v32, s[16:19], s48 offen offset:2048 lds\n\t"
      "v_mul_u32_u24 v24, 4352, v24\n\t"
      "v_mfma_f32_32x32x16_bf16 v[128:143], v[92:95], v[152:155], v[128:143]\n\t"
      "ds_read_b64_tr_b16 v[80:81], v37 offset:12416\n\t"
      "ds_read_b64_tr_b16 v[82:83], v37 offset:13184\n\t"
      "s_bfe_u32 s48, s100, 262156\n\t"
      "s_add_u32 m0, s54, 49152\n\t"
      "s_mul_i32 s48, s48, 128\n\t"
      "s_add_u32 s48, s48, s51\n\t"
      "buffer_load_dwordx4 v28, s[12:15], s48 offen offset:0 lds\n\t"
      "s_waitcnt lgkmcnt(9)\n\t"
      "v_mfma_f32_32x32x16_bf16 v[96:111], v[60:63], v[156:159], v[96:111]\n\t"
      "ds_read_b128 v[164:167], v34 offset:16384\n\t"
      "ds_read_b64_tr_b16 v[84:85], v37 offset:15360\n\t"
      "ds_read_b64_tr_b16 v[86:87], v37 offset:16128\n\t"
      "v_and_b32 v25, 3, v1\n\t"
      "v_lshl_add_u32 v24, v25, 2, v24\n\t"
      "v_mfma_f32_32x32x16_bf16 v[112:127], v[64:67], v[156:159], v[112:127]\n\t"
      "ds_read_b64_tr_b16 v[88:89], v37 offset:15424\n\t"
      "ds_read_b64_tr_b16 v[90:91], v37 offset:16192\n\t"
      "buffer_load_dwordx4 v29, s[12:15], s48 offen offset:1024 lds\n\t"
      "v_add_u32 v24, 131072, v24\n\t"
      "ds_write_b32 v24, v39 offset:4096\n\t"
      "s_lshl_b32 s50, s45, 4\n\t"
      "v_mfma_f32_32x32x16_bf16 v[128:143], v[68:71], v[156:159], v[128:143]\n\t"
      "ds_read_b64_tr_b16 v[92:93], v37 offset:15488\n\t"
      "ds_read_b64_tr_b16 v[94:95], v37 offset:16256\n\t"
      "buffer_load_dwordx4 v40, s[12:15], s48 offen offset:2048 lds\n\t"
      "s_add_u32 s50, s50, s46\n\t"
      "s_lshl_b32 s50, s50, 5\n\t"
      "buffer_load_dwordx4 v41, s[12:15], s48 offen offset:3072 lds\n\t"
      "s_waitcnt lgkmcnt(10)\n\t"
      "v_dot2c_f32_bf16 v42, v4, v39\n\t"
      "v_dot2c_f32_bf16 v43, v8, v39\n\t"
      "v_dot2c_f32_bf16 v44, v12, v39\n\t"
      "v_dot2c_f32_bf16 v42, v5, v39\n\t"
      "v_dot2c_f32_bf16 v43, v9, v39\n\t"
      "v_dot2c_f32_bf16 v44, v13, v39\n\t"
      "v_dot2c_f32_bf16 v42, v6, v39\n\t"
      "v_dot2c_f32_bf16 v43, v10, v39\n\t"
      "v_dot2c_f32_bf16 v44, v14, v39\n\t"
      "v_dot2c_f32_bf16 v42, v7, v39\n\t"
      "v_dot2c_f32_bf16 v43, v11, v39\n\t"
      "v_dot2c_f32_bf16 v44, v15, v39\n\t"
      "s_waitcnt lgkmcnt(8)\n\t"
      "v_mfma_f32_32x32x16_bf16 v[96:111], v[72:75], v[160:163], v[96:111]\n\t"
      "ds_read_b128 v[168:171], v35 offset:16384\n\t"
      "ds_read_b64_tr_b16 v[60:61], v37 offset:18432\n\t"
      "ds_read_b64_tr_b16 v[62:63], v37 offset:19200\n\t"
      "s_add_u32 s50, s50, s44\n\t"
      "s_lshl_b32 s50, s50, 2\n\t"
      "v_mfma_f32_32x32x16_bf16 v[112:127], v[76:79], v[160:163], v[112:127]\n\t"
      "ds_read_b64_tr_b16 v[64:65], v37 offset:18496\n\t"
      "ds_read_b64_tr_b16 v[66:67], v37 offset:19264\n\t"
      "ds_read_b64_tr_b16 v[4:5], v38 offset:12288\n\t"
      "ds_read_b64_tr_b16 v[6:7], v38 offset:13056\n\t"
      "s_bfe_u32 s48, s100, 262156\n\t"
      "s_add_u32 m0, s55, 102400\n\t"
      "s_mul_i32 s48, s48, 172032\n\t"
      "s_add_u32 s48, s48, s93\n\t"
      "buffer_load_dwordx4 v30, s[16:19], s48 offen offset:0 lds\n\t"
      "v_mfma_f32_32x32x16_bf16 v[128:143], v[80:83], v[160:163], v[128:143]\n\t"
      "ds_read_b64_tr_b16 v[68:69], v37 offset:18560\n\t"
      "ds_read_b64_tr_b16 v[70:71], v37 offset:19328\n\t"
      "buffer_load_dwordx4 v31, s[16:19], s48 offen offset:1024 lds\n\t"
      "s_add_u32 s50, s50, 262144\n\t"
      "v_lshl_add_u32 v49, v1, 15, s50\n\t"
      "s_waitcnt lgkmcnt(9)\n\t"
      "v_mfma_f32_32x32x16_bf16 v[96:111], v[84:87], v[164:167], v[96:111]\n\t"
      "ds_read_b128 v[172:175], v36 offset:16384\n\t"
      "ds_read_b64_tr_b16 v[72:73], v37 offset:21504\n\t"
      "ds_read_b64_tr_b16 v[74:75], v37 offset:22272\n\t"
      "s_lshl_b32 s57, s59, 10\n\t"
      "s_add_u32 s57, s57, 524288\n\t"
      "v_mfma_f32_32x32x16_bf16 v[112:127], v[88:91], v[164:167], v[112:127]\n\t"
      "ds_read_b64_tr_b16 v[76:77], v37 offset:21568\n\t"
      "ds_read_b64_tr_b16 v[78:79], v37 offset:22336\n\t"
      "ds_read_b64_tr_b16 v[8:9], v38 offset:12352\n\t"
      "ds_read_b64_tr_b16 v[10:11], v38 offset:13120\n\t"
      "buffer_load_dwordx4 v32, s[16:19], s48 offen offset:2048 lds\n\t"
      "v_and_b32 v24, s65, v27\n\t"
      "v_mfma_f32_32x32x16_bf16 v[128:143], v[92:95], v[164:167], v[128:143]\n\t"
      "ds_read_b64_tr_b16 v[80:81], v37 offset:21632\n\t"
      "ds_read_b64_tr_b16 v[82:83], v37 offset:22400\n\t"
      "s_bfe_u32 s48, s100, 262160\n\t"
      "s_add_u32 m0, s54, 0\n\t"
      "s_mul_i32 s48, s48, 128\n\t"
      "s_add_u32 s48, s48, s51\n\t"
      "buffer_load_dwordx4 v28, s[12:15], s48 offen offset:0 lds\n\t"
      "s_waitcnt vmcnt(8)\n\t"
      "s_barrier\n\t"
      "s_waitcnt lgkmcnt(9)\n\t"
      "v_mfma_f32_32x32x16_bf16 v[96:111], v[60:63], v[168:171], v[96:111]\n\t"
      "ds_read_b128 v[176:179], v33 offset:32768\n\t"
      "ds_read_b64_tr_b16 v[84:85], v37 offset:24576\n\t"
      "ds_read_b64_tr_b16 v[86:87], v37 offset:25344\n\t"
      "v_add3_u32 v50, v24, v59, s64\n\t"
      "v_and_b32 v24, s67, v27\n\t"
      "v_mfma_f32_32x32x16_bf16 v[112:127], v[64:67], v[168:171], v[112:127]\n\t"
      "ds_read_b64_tr_b16 v[88:89], v37 offset:24640\n\t"
      "ds_read_b64_tr_b16 v[90:91], v37 offset:25408\n\t"
      "ds_read_b64_tr_b16 v[12:13], v38 offset:12416\n\t"
      "ds_read_b64_tr_b16 v[14:15], v38 offset:13184\n\t"
      "buffer_load_dwordx4 v29, s[12:15], s48 offen offset:1024 lds\n\t"
      "v_add3_u32 v51, v24, v59, s66\n\t"
      "v_mfma_f32_32x32x16_bf16 v[128:143], v[68:71], v[168:171], v[128:143]\n\t"
      "ds_read_b64_tr_b16 v[92:93], v37 offset:24704\n\t"
      "ds_read_b64_tr_b16 v[94:95], v37 offset:25472\n\t"
      "buffer_load_dwordx4 v40, s[12:15], s48 offen offset:2048 lds\n\t"
      "v_and_b32 v24, s69, v27\n\t"
      "v_add3_u32 v52, v24, v59, s68\n\t"
      "s_waitcnt lgkmcnt(9)\n\t"
      "v_mfma_f32_32x32x16_bf16 v[96:111], v[72:75], v[172:175], v[96:111]\n\t"
      "ds_read_b128 v[180:183], v34 offset:32768\n\t"
      "ds_read_b64_tr_b16 v[60:61], v37 offset:27648\n\t"
      "ds_read_b64_tr_b16 v[62:63], v37 offset:28416\n\t"
      "s_cmp_eq_u32 s70, 0\n\t"
      "s_cselect_b32 s65, 11, 12\n\t"
      "s_cselect_b32 s66, 0x3e8b97f6, 1.0\n\t"
      "v_mfma_f32_32x32x16_bf16 v[112:127], v[76:79], v[172:175], v[112:127]\n\t"
      "ds_read_b64_tr_b16 v[64:65], v37 offset:27712\n\t"
      "ds_read_b64_tr_b16 v[66:67], v37 offset:28480\n\t"
      "buffer_load_dwordx4 v41, s[12:15], s48 offen offset:3072 lds\n\t"
      "s_lshl_b32 s65, s59, s65\n\t"
      "v_and_b32 v24, s76, v27\n\t"
      "v_add3_u32 v53, v24, v59, s71\n\t"
      "v_mfma_f32_32x32x16_bf16 v[128:143], v[80:83], v[172:175], v[128:143]\n\t"
      "ds_read_b64_tr_b16 v[68:69], v37 offset:27776\n\t"
      "ds_read_b64_tr_b16 v[70:71], v37 offset:28544\n\t"
      "s_bfe_u32 s48, s100, 262160\n\t"
      "s_add_u32 m0, s55, 114688\n\t"
      "s_mul_i32 s48, s48, 172032\n\t"
      "s_add_u32 s48, s48, s93\n\t"
      "buffer_load_dwordx4 v30, s[16:19], s48 offen offset:0 lds\n\t"
      "buffer_load_dwordx4 v31, s[16:19], s48 offen offset:1024 lds\n\t"
      "s_waitcnt lgkmcnt(9)\n\t"
      "v_dot2c_f32_bf16 v42, v4, v39\n\t"
      "v_dot2c_f32_bf16 v43, v8, v39\n\t"
      "v_dot2c_f32_bf16 v44, v12, v39\n\t"
      "v_dot2c_f32_bf16 v42, v5, v39\n\t"
      "v_dot2c_f32_bf16 v43, v9, v39\n\t"
      "v_dot2c_f32_bf16 v44, v13, v39\n\t"
      "v_dot2c_f32_bf16 v42, v6, v39\n\t"
      "v_dot2c_f32_bf16 v43, v10, v39\n\t"
      "v_dot2c_f32_bf16 v44, v14, v39\n\t"
      "v_dot2c_f32_bf16 v42, v7, v39\n\t"
      "v_dot2c_f32_bf16 v43, v11, v39\n\t"
      "v_dot2c_f32_bf16 v44, v15, v39\n\t"
      "s_waitcnt lgkmcnt(7)\n\t"
      "v_mfma_f32_32x32x16_bf16 v[96:111], v[84:87], v[176:179], v[96:111]\n\t"
      "ds_read_b128 v[184:187], v35 offset:32768\n\t"
      "ds_read_b64_tr_b16 v[72:73], v37 offset:30720\n\t"
      "ds_read_b64_tr_b16 v[74:75], v37 offset:31488\n\t"
      "v_and_b32 v24, s78, v27\n\t"
      "v_add3_u32 v54, v24, v59, s77\n\t"
      "v_mfma_f32_32x32x16_bf16 v[112:127], v[88:91], v[176:179], v[112:127]\n\t"
      "ds_read_b64_tr_b16 v[76:77], v37 offset:30784\n\t"
      "ds_read_b64_tr_b16 v[78:79], v37 offset:31552\n\t"
      "ds_read_b64_tr_b16 v[4:5], v38 offset:24576\n\t"
      "ds_read_b64_tr_b16 v[6:7], v38 offset:25344\n\t"
      "buffer_load_dwordx4 v32, s[16:19], s48 offen offset:2048 lds\n\t"
      "v_and_b32 v24, s80, v27\n\t"
      "v_mfma_f32_32x32x16_bf16 v[128:143], v[92:95], v[176:179], v[128:143]\n\t"
      "ds_read_b64_tr_b16 v[80:81], v37 offset:30848\n\t"
      "ds_read_b64_tr_b16 v[82:83], v37 offset:31616\n\t"
      "s_bfe_u32 s48, s100, 262164\n\t"
      "s_add_u32 m0, s54, 16384\n\t"
      "s_mul_i32 s48, s48, 128\n\t"
      "s_add_u32 s48, s48, s51\n\t"
      "buffer_load_dwordx4 v28, s[12:15], s48 offen offset:0 lds\n\t"
      "s_waitcnt lgkmcnt(9)\n\t"
      "v_mfma_f32_32x32x16_bf16 v[96:111], v[60:63], v[180:183], v[96:111]\n\t"
      "ds_read_b128 v[188:191], v36 offset:32768\n\t"
      "ds_read_b64_tr_b16 v[84:85], v37 offset:33792\n\t"
      "ds_read_b64_tr_b16 v[86:87], v37 offset:34560\n\t"
      "v_add3_u32 v55, v24, v59, s79\n\t"
      "v_mfma_f32_32x32x16_bf16 v[112:127], v[64:67], v[180:183], v[112:127]\n\t"
      "ds_read_b64_tr_b16 v[88:89], v37 offset:33856\n\t"
      "ds_read_b64_tr_b16 v[90:91], v37 offset:34624\n\t"
      "ds_read_b64_tr_b16 v[8:9], v38 offset:24640\n\t"
      "ds_read_b64_tr_b16 v[10:11], v38 offset:25408\n\t"
      "buffer_load_dwordx4 v29, s[12:15], s48 offen offset:1024 lds\n\t"
      "v_mfma_f32_32x32x16_bf16 v[128:143], v[68:71], v[180:183], v[128:143]\n\t"
      "ds_read_b64_tr_b16 v[92:93], v37 offset:33920\n\t"
      "ds_read_b64_tr_b16 v[94:95], v37 offset:34688\n\t"
      "buffer_load_dwordx4 v40, s[12:15], s48 offen offset:2048 lds\n\t"
      "s_cmp_eq_u32 s81, 0\n\t"
      "s_cselect_b32 s76, 11, 12\n\t"
      "s_cselect_b32 s77, 0x3e8b97f6, 1.0\n\t"
      "s_waitcnt vmcnt(10)\n\t"
      "s_barrier\n\t"
      "s_waitcnt lgkmcnt(9)\n\t"
      "v_mfma_f32_32x32x16_bf16 v[96:111], v[72:75], v[184:187], v[96:111]\n\t"
      "ds_read_b128 v[192:195], v33 offset:49152\n\t"
      "ds_read_b64_tr_b16 v[60:61], v37 offset:36864\n\t"
      "ds_read_b64_tr_b16 v[62:63], v37 offset:37632\n\t"
      "s_lshl_b32 s76, s59, s76\n\t"
      "v_and_b32 v24, s87, v27\n\t"
      "v_mfma_f32_32x32x16_bf16 v[112:127], v[76:79], v[184:187], v[112:127]\n\t"
      "ds_read_b64_tr_b16 v[64:65], v37 offset:36928\n\t"
      "ds_read_b64_tr_b16 v[66:67], v37 offset:37696\n\t"
      "ds_read_b64_tr_b16 v[12:13], v38 offset:24704\n\t"
      "ds_read_b64_tr_b16 v[14:15], v38 offset:25472\n\t"
      "buffer_load_dwordx4 v41, s[12:15], s48 offen offset:3072 lds\n\t"
      "v_add3_u32 v56, v24, v59, s86\n\t"
      "v_mfma_f32_32x32x16_bf16 v[128:143], v[80:83], v[184:187], v[128:143]\n\t"
      "ds_read_b64_tr_b16 v[68:69], v37 offset:36992\n\t"
      "ds_read_b64_tr_b16 v[70:71], v37 offset:37760\n\t"
      "s_bfe_u32 s48, s100, 262164\n\t"
      "s_add_u32 m0, s55, 65536\n\t"
      "s_mul_i32 s48, s48, 172032\n\t"
      "s_add_u32 s48, s48, s93\n\t"
      "buffer_load_dwordx4 v30, s[16:19], s48 offen offset:0 lds\n\t"
      "s_waitcnt lgkmcnt(9)\n\t"
      "v_mfma_f32_32x32x16_bf16 v[96:111], v[84:87], v[188:191], v[96:111]\n\t"
      "ds_read_b128 v[196:199], v34 offset:49152\n\t"
      "ds_read_b64_tr_b16 v[72:73], v37 offset:39936\n\t"
      "ds_read_b64_tr_b16 v[74:75], v37 offset:40704\n\t"
      "v_and_b32 v24, s89, v27\n\t"
      "v_add3_u32 v57, v24, v59, s88\n\t"
      "v_mfma_f32_32x32x16_bf16 v[112:127], v[88:91], v[188:191], v[112:127]\n\t"
      "ds_read_b64_tr_b16 v[76:77], v37 offset:40000\n\t"
      "ds_read_b64_tr_b16 v[78:79], v37 offset:40768\n\t"
      "buffer_load_dwordx4 v31, s[16:19], s48 offen offset:1024 lds\n\t"
      "v_and_b32 v24, s91, v27\n\t"
      "v_add3_u32 v58, v24, v59, s90\n\t"
      "v_mfma_f32_32x32x16_bf16 v[128:143], v[92:95], v[188:191], v[128:143]\n\t"
      "ds_read_b64_tr_b16 v[80:81], v37 offset:40064\n\t"
      "ds_read_b64_tr_b16 v[82:83], v37 offset:40832\n\t"
      "buffer_load_dwordx4 v32, s[16:19], s48 offen offset:2048 lds\n\t"
      "s_cmp_eq_u32 s92, 0\n\t"
      "s_cselect_b32 s87, 11, 12\n\t"
      "s_cselect_b32 s88, 0x3e8b97f6, 1.0\n\t"
      "s_waitcnt lgkmcnt(9)\n\t"
      "v_dot2c_f32_bf16 v42, v4, v39\n\t"
      "v_dot2c_f32_bf16 v43, v8, v39\n\t"
      "v_dot2c_f32_bf16 v44, v12, v39\n\t"
      "v_dot2c_f32_bf16 v42, v5, v39\n\t"
      "v_dot2c_f32_bf16 v43, v9, v39\n\t"
      "v_dot2c_f32_bf16 v44, v13, v39\n\t"
      "v_dot2c_f32_bf16 v42, v6, v39\n\t"
      "v_dot2c_f32_bf16 v43, v10, v39\n\t"
      "v_dot2c_f32_bf16 v44, v14, v39\n\t"
      "v_dot2c_f32_bf16 v42, v7, v39\n\t"
      "v_dot2c_f32_bf16 v43, v11, v39\n\t"
      "v_dot2c_f32_bf16 v44, v15, v39\n\t"
      "s_waitcnt lgkmcnt(7)\n\t"
      "v_mfma_f32_32x32x16_bf16 v[96:111], v[60:63], v[192:195], v[96:111]\n\t"
      "ds_read_b128 v[200:203], v35 offset:49152\n\t"
      "ds_read_b64_tr_b16 v[84:85], v37 offset:43008\n\t"
      "ds_read_b64_tr_b16 v[86:87], v37 offset:43776\n\t"
      "s_lshl_b32 s87, s59, s87\n\t"
      "v_mfma_f32_32x32x16_bf16 v[112:127], v[64:67], v[192:195], v[112:127]\n\t"
      "ds_read_b64_tr_b16 v[88:89], v37 offset:43072\n\t"
      "ds_read_b64_tr_b16 v[90:91], v37 offset:43840\n\t"
      "ds_read_b64_tr_b16 v[4:5], v38 offset:36864\n\t"
      "ds_read_b64_tr_b16 v[6:7], v38 offset:37632\n\t"
      "s_bfe_u32 s48, s100, 262168\n\t"
      "s_add_u32 m0, s54, 32768\n\t"
      "s_mul_i32 s48, s48, 128\n\t"
      "s_add_u32 s48, s48, s51\n\t"
      "buffer_load_dwordx4 v28, s[12:15], s48 offen offset:0 lds\n\t"
      "v_mfma_f32_32x32x16_bf16 v[128:143], v[68:71], v[192:195], v[128:143]\n\t"
      "ds_read_b64_tr_b16 v[92:93], v37 offset:43136\n\t"
      "ds_read_b64_tr_b16 v[94:95], v37 offset:43904\n\t"
      "buffer_load_dwordx4 v29, s[12:15], s48 offen offset:1024 lds\n\t"
      "s_waitcnt lgkmcnt(9)\n\t"
      "v_mfma_f32_32x32x16_bf16 v[96:111], v[72:75], v[196:199], v[96:111]\n\t"
      "ds_read_b128 v[204:207], v36 offset:49152\n\t"
      "ds_read_b64_tr_b16 v[60:61], v37 offset:46080\n\t"
      "ds_read_b64_tr_b16 v[62:63], v37 offset:46848\n\t"
      "v_mfma_f32_32x32x16_bf16 v[112:127], v[76:79], v[196:199], v[112:127]\n\t"
      "ds_read_b64_tr_b16 v[64:65], v37 offset:46144\n\t"
      "ds_read_b64_tr_b16 v[66:67], v37 offset:46912\n\t"
      "ds_read_b64_tr_b16 v[8:9], v38 offset:36928\n\t"
      "ds_read_b64_tr_b16 v[10:11], v38 offset:37696\n\t"
      "buffer_load_dwordx4 v40, s[12:15], s48 offen offset:2048 lds\n\t"
      "v_mfma_f32_32x32x16_bf16 v[128:143], v[80:83], v[196:199], v[128:143]\n\t"
      "ds_read_b64_tr_b16 v[68:69], v37 offset:46208\n\t"
      "ds_read_b64_tr_b16 v[70:71], v37 offset:46976\n\t"
      "buffer_load_dwordx4 v41, s[12:15], s48 offen offset:3072 lds\n\t"
      "s_waitcnt vmcnt(11)\n\t"
      "s_barrier\n\t"
      "s_waitcnt lgkmcnt(9)\n\t"
      "v_mfma_f32_32x32x16_bf16 v[96:111], v[84:87], v[200:203], v[96:111]\n\t"
      "ds_read_b128 v[208:211], v33 offset:0\n\t"
      "ds_read_b64_tr_b16 v[72:73], v37 offset:49152\n\t"
      "ds_read_b64_tr_b16 v[74:75], v37 offset:49920\n\t"
      "v_mfma_f32_32x32x16_bf16 v[112:127], v[88:91], v[200:203], v[112:127]\n\t"
      "ds_read_b64_tr_b16 v[76:77], v37 offset:49216\n\t"
      "ds_read_b64_tr_b16 v[78:79], v37 offset:49984\n\t"
      "ds_read_b64_tr_b16 v[12:13], v38 offset:36992\n\t"
      "ds_read_b64_tr_b16 v[14:15], v38 offset:37760\n\t"
      "s_bfe_u32 s48, s100, 262168\n\t"
      "s_add_u32 m0, s55, 77824\n\t"
      "s_mul_i32 s48, s48, 172032\n\t"
      "s_add_u32 s48, s48, s93\n\t"
      "buffer_load_dwordx4 v30, s[16:19], s48 offen offset:0 lds\n\t"
      "v_mfma_f32_32x32x16_bf16 v[128:143], v[92:95], v[200:203], v[128:143]\n\t"
      "ds_read_b64_tr_b16 v[80:81], v37 offset:49280\n\t"
      "ds_read_b64_tr_b16 v[82:83], v37 offset:50048\n\t"
      "buffer_load_dwordx4 v31, s[16:19], s48 offen offset:1024 lds\n\t"
      "s_waitcnt lgkmcnt(9)\n\t"
      "v_mfma_f32_32x32x16_bf16 v[96:111], v[60:63], v[204:207], v[96:111]\n\t"
      "ds_read_b128 v[212:215], v34 offset:0\n\t"
      "ds_read_b64_tr_b16 v[84:85], v37 offset:52224\n\t"
      "ds_read_b64_tr_b16 v[86:87], v37 offset:52992\n\t"
      "v_mfma_f32_32x32x16_bf16 v[112:127], v[64:67], v[204:207], v[112:127]\n\t"
      "ds_read_b64_tr_b16 v[88:89], v37 offset:52288\n\t"
      "ds_read_b64_tr_b16 v[90:91], v37 offset:53056\n\t"
      "buffer_load_dwordx4 v32, s[16:19], s48 offen offset:2048 lds\n\t"
      "v_mfma_f32_32x32x16_bf16 v[128:143], v[68:71], v[204:207], v[128:143]\n\t"
      "ds_read_b64_tr_b16 v[92:93], v37 offset:52352\n\t"
      "ds_read_b64_tr_b16 v[94:95], v37 offset:53120\n\t"
      "s_waitcnt lgkmcnt(9)\n\t"
      "v_dot2c_f32_bf16 v42, v4, v39\n\t"
      "v_dot2c_f32_bf16 v43, v8, v39\n\t"
      "v_dot2c_f32_bf16 v44, v12, v39\n\t"
      "v_dot2c_f32_bf16 v42, v5, v39\n\t"
      "v_dot2c_f32_bf16 v43, v9, v39\n\t"
      "v_dot2c_f32_bf16 v44, v13, v39\n\t"
      "v_dot2c_f32_bf16 v42, v6, v39\n\t"
      "v_dot2c_f32_bf16 v43, v10, v39\n\t"
      "v_dot2c_f32_bf16 v44, v14, v39\n\t"
      "v_dot2c_f32_bf16 v42, v7, v39\n\t"
      "v_dot2c_f32_bf16 v43, v11, v39\n\t"
      "v_dot2c_f32_bf16 v44, v15, v39\n\t"
      "s_waitcnt lgkmcnt(7)\n\t"
      "v_mfma_f32_32x32x16_bf16 v[96:111], v[72:75], v[208:211], v[96:111]\n\t"
      "ds_read_b128 v[216:219], v35 offset:0\n\t"
      "ds_read_b64_tr_b16 v[60:61], v37 offset:55296\n\t"
      "ds_read_b64_tr_b16 v[62:63], v37 offset:56064\n\t"
      "v_bfe_u32 v24, v1, 2, 2\n\t"
      "v_mul_u32_u24 v28, 320, v24\n\t"
      "v_mfma_f32_32x32x16_bf16 v[112:127], v[76:79], v[208:211], v[112:127]\n\t"
      "ds_read_b64_tr_b16 v[64:65], v37 offset:55360\n\t"
      "ds_read_b64_tr_b16 v[66:67], v37 offset:56128\n\t"
      "ds_read_b64_tr_b16 v[4:5], v38 offset:49152\n\t"
      "ds_read_b64_tr_b16 v[6:7], v38 offset:49920\n\t"
      "v_add_u32 v40, 13312, v47\n\t"
      "v_add_u32 v41, 26624, v47\n\t"
      "v_add_u32 %[tid], 39936, v47\n\t"
      "s_bfe_u32 s48, s100, 262144\n\t"
      "s_add_u32 m0, s54, 49152\n\t"
      "s_mul_i32 s48, s48, 229376\n\t"
      "s_add_u32 s48, s48, s52\n\t"
      "buffer_load_dwordx4 v47, s[20:23], s48 offen offset:0 lds\n\t"
      "v_mfma_f32_32x32x16_bf16 v[128:143], v[80:83], v[208:211], v[128:143]\n\t"
      "ds_read_b64_tr_b16 v[68:69], v37 offset:55424\n\t"
      "ds_read_b64_tr_b16 v[70:71], v37 offset:56192\n\t"
      "buffer_load_dwordx4 v40, s[20:23], s48 offen offset:1024 lds\n\t"
      "v_and_b32 v24, 0x800, v27\n\t"
      "v_bfe_u32 v25, v1, 4, 1\n\t"
      "s_waitcnt lgkmcnt(9)\n\t"
      "v_mfma_f32_32x32x16_bf16 v[96:111], v[84:87], v[212:215], v[96:111]\n\t"
      "ds_read_b128 v[220:223], v36 offset:0\n\t"
      "ds_read_b64_tr_b16 v[72:73], v37 offset:58368\n\t"
      "ds_read_b64_tr_b16 v[74:75], v37 offset:59136\n\t"
      "v_lshl_add_u32 v24, v25, 5, v24\n\t"
      "v_and_b32 v25, 3, v1\n\t"
      "v_mfma_f32_32x32x16_bf16 v[112:127], v[88:91], v[212:215], v[112:127]\n\t"
      "ds_read_b64_tr_b16 v[76:77], v37 offset:58432\n\t"
      "ds_read_b64_tr_b16 v[78:79], v37 offset:59200\n\t"
      "ds_read_b64_tr_b16 v[8:9], v38 offset:49216\n\t"
      "ds_read_b64_tr_b16 v[10:11], v38 offset:49984\n\t"
      "buffer_load_dwordx4 v41, s[20:23], s48 offen offset:2048 lds\n\t"
      "v_lshl_add_u32 v24, v25, 3, v24\n\t"
      "v_mfma_f32_32x32x16_bf16 v[128:143], v[92:95], v[212:215], v[128:143]\n\t"
      "ds_read_b64_tr_b16 v[80:81], v37 offset:58496\n\t"
      "ds_read_b64_tr_b16 v[82:83], v37 offset:59264\n\t"
      "buffer_load_dwordx4 %[tid], s[20:23], s48 offen offset:3072 lds\n\t"
      "v_add_u32 v28, v28, v24\n\t"
      "v_xor_b32 v29, 64, v28\n\t"
      "s_waitcnt vmcnt(11)\n\t"
      "s_barrier\n\t"
      "s_waitcnt lgkmcnt(9)\n\t"
      "v_mfma_f32_32x32x16_bf16 v[96:111], v[60:63], v[216:219], v[96:111]\n\t"
      "ds_read_b128 v[224:227], v33 offset:16384\n\t"
      "ds_read_b64_tr_b16 v[84:85], v37 offset:0\n\t"
      "ds_read_b64_tr_b16 v[86:87], v37 offset:768\n\t"
      "v_xor_b32 v30, 0x80, v28\n\t"
      "v_xor_b32 v31, 0xc0, v28\n\t"
      "v_mfma_f32_32x32x16_bf16 v[112:127], v[64:67], v[216:219], v[112:127]\n\t"
      "ds_read_b64_tr_b16 v[88:89], v37 offset:64\n\t"
      "ds_read_b64_tr_b16 v[90:91], v37 offset:832\n\t"
      "ds_read_b64_tr_b16 v[12:13], v38 offset:49280\n\t"
      "ds_read_b64_tr_b16 v[14:15], v38 offset:50048\n\t"
      "s_bfe_u32 s48, s100, 262144\n\t"
      "s_add_u32 m0, s56, 131072\n\t"
      "s_mul_i32 s48, s48, 128\n\t"
      "s_add_u32 s48, s48, s53\n\t"
      "buffer_load_dwordx4 v48, s[24:27], s48 offen offset:0 lds\n\t"
      "v_mfma_f32_32x32x16_bf16 v[128:143], v[68:71], v[216:219], v[128:143]\n\t"
      "ds_read_b64_tr_b16 v[92:93], v37 offset:128\n\t"
      "ds_read_b64_tr_b16 v[94:95], v37 offset:896\n\t"
      "s_lshl_b32 s50, s44, 6\n\t"
      "v_xor_b32 v32, s50, v28\n\t"
      "v_accvgpr_write_b32 a170, 0\n\t"
      "v_accvgpr_write_b32 a171, 0\n\t"
      "s_waitcnt lgkmcnt(9)\n\t"
      "v_mfma_f32_32x32x16_bf16 v[96:111], v[72:75], v[220:223], v[96:111]\n\t"
      "ds_read_b128 v[228:231], v34 offset:16384\n\t"
      "ds_read_b64_tr_b16 v[60:61], v37 offset:3072\n\t"
      "ds_read_b64_tr_b16 v[62:63], v37 offset:3840\n\t"
      "v_mfma_f32_32x32x16_bf16 v[112:127], v[76:79], v[220:223], v[112:127]\n\t"
      "ds_read_b64_tr_b16 v[64:65], v37 offset:3136\n\t"
      "ds_read_b64_tr_b16 v[66:67], v37 offset:3904\n\t"
      "v_mfma_f32_32x32x16_bf16 v[128:143], v[80:83], v[220:223], v[128:143]\n\t"
      "ds_read_b64_tr_b16 v[68:69], v37 offset:3200\n\t"
      "ds_read_b64_tr_b16 v[70:71], v37 offset:3968\n\t"
      "buffer_load_dwordx2 v[20:21], v2, s[28:31], s57 offen sc1\n\t"
      "buffer_load_dwordx2 v[22:23], v2, s[28:31], s57 offen offset:8 sc1\n\t"
      "s_waitcnt lgkmcnt(9)\n\t"
      "v_dot2c_f32_bf16 v42, v4, v39\n\t"
      "v_dot2c_f32_bf16 v43, v8, v39\n\t"
      "v_dot2c_f32_bf16 v44, v12, v39\n\t"
      "v_dot2c_f32_bf16 v42, v5, v39\n\t"
      "v_dot2c_f32_bf16 v43, v9, v39\n\t"
      "v_dot2c_f32_bf16 v44, v13, v39\n\t"
      "v_dot2c_f32_bf16 v42, v6, v39\n\t"
      "v_dot2c_f32_bf16 v43, v10, v39\n\t"
      "v_dot2c_f32_bf16 v44, v14, v39\n\t"
      "v_dot2c_f32_bf16 v42, v7, v39\n\t"
      "v_dot2c_f32_bf16 v43, v11, v39\n\t"
      "v_dot2c_f32_bf16 v44, v15, v39\n\t"
      "s_waitcnt lgkmcnt(7)\n\t"
      "v_mfma_f32_32x32x16_bf16 v[96:111], v[84:87], v[224:227], v[96:111]\n\t"
      "ds_read_b128 v[232:235], v35 offset:16384\n\t"
      "ds_read_b64_tr_b16 v[72:73], v37 offset:6144\n\t"
      "ds_read_b64_tr_b16 v[74:75], v37 offset:6912\n\t"
      "v_mfma_f32_32x32x16_bf16 v[112:127], v[88:91], v[224:227], v[112:127]\n\t"
      "ds_read_b64_tr_b16 v[76:77], v37 offset:6208\n\t"
      "ds_read_b64_tr_b16 v[78:79], v37 offset:6976\n\t"
      "ds_read_b64_tr_b16 v[4:5], v38 offset:0\n\t"
      "ds_read_b64_tr_b16 v[6:7], v38 offset:768\n\t"
      "s_bfe_u32 s48, s100, 262148\n\t"
      "s_add_u32 m0, s54, 0\n\t"
      "s_mul_i32 s48, s48, 229376\n\t"
      "s_add_u32 s48, s48, s52\n\t"
      "buffer_load_dwordx4 v47, s[20:23], s48 offen offset:0 lds\n\t"
      "v_mfma_f32_32x32x16_bf16 v[128:143], v[92:95], v[224:227], v[128:143]\n\t"
      "ds_read_b64_tr_b16 v[80:81], v37 offset:6272\n\t"
      "ds_read_b64_tr_b16 v[82:83], v37 offset:7040\n\t"
      "buffer_load_dwordx4 v40, s[20:23], s48 offen offset:1024 lds\n\t"
      "s_waitcnt lgkmcnt(9)\n\t"
      "v_mfma_f32_32x32x16_bf16 v[96:111], v[60:63], v[228:231], v[96:111]\n\t"
      "ds_read_b128 v[236:239], v36 offset:16384\n\t"
      "ds_read_b64_tr_b16 v[84:85], v37 offset:9216\n\t"
      "ds_read_b64_tr_b16 v[86:87], v37 offset:9984\n\t"
      "v_mfma_f32_32x32x16_bf16 v[112:127], v[64:67], v[228:231], v[112:127]\n\t"
      "ds_read_b64_tr_b16 v[88:89], v37 offset:9280\n\t"
      "ds_read_b64_tr_b16 v[90:91], v37 offset:10048\n\t"
      "ds_read_b64_tr_b16 v[8:9], v38 offset:64\n\t"
      "ds_read_b64_tr_b16 v[10:11], v38 offset:832\n\t"
      "buffer_load_dwordx4 v41, s[20:23], s48 offen offset:2048 lds\n\t"
      "v_mfma_f32_32x32x16_bf16 v[128:143], v[68:71], v[228:231], v[128:143]\n\t"
      "ds_read_b64_tr_b16 v[92:93], v37 offset:9344\n\t"
      "ds_read_b64_tr_b16 v[94:95], v37 offset:10112\n\t"
      "buffer_load_dwordx4 %[tid], s[20:23], s48 offen offset:3072 lds\n\t"
      "s_waitcnt vmcnt(11)\n\t"
      "s_barrier\n\t"
      "s_waitcnt lgkmcnt(9)\n\t"
      "v_mfma_f32_32x32x16_bf16 v[96:111], v[72:75], v[232:235], v[96:111]\n\t"
      "ds_read_b128 v[240:243], v33 offset:32768\n\t"
      "ds_read_b64_tr_b16 v[60:61], v37 offset:12288\n\t"
      "ds_read_b64_tr_b16 v[62:63], v37 offset:13056\n\t"
      "v_mfma_f32_32x32x16_bf16 v[112:127], v[76:79], v[232:235], v[112:127]\n\t"
      "ds_read_b64_tr_b16 v[64:65], v37 offset:12352\n\t"
      "ds_read_b64_tr_b16 v[66:67], v37 offset:13120\n\t"
      "ds_read_b64_tr_b16 v[12:13], v38 offset:128\n\t"
      "ds_read_b64_tr_b16 v[14:15], v38 offset:896\n\t"
      "s_bfe_u32 s48, s100, 262148\n\t"
      "s_add_u32 m0, s56, 135424\n\t"
      "s_mul_i32 s48, s48, 128\n\t"
      "s_add_u32 s48, s48, s53\n\t"
      "buffer_load_dwordx4 v48, s[24:27], s48 offen offset:0 lds\n\t"
      "v_mfma_f32_32x32x16_bf16 v[128:143], v[80:83], v[232:235], v[128:143]\n\t"
      "ds_read_b64_tr_b16 v[68:69], v37 offset:12416\n\t"
      "ds_read_b64_tr_b16 v[70:71], v37 offset:13184\n\t"
      "s_waitcnt lgkmcnt(9)\n\t"
      "v_mfma_f32_32x32x16_bf16 v[96:111], v[84:87], v[236:239], v[96:111]\n\t"
      "ds_read_b128 v[244:247], v34 offset:32768\n\t"
      "ds_read_b64_tr_b16 v[72:73], v37 offset:15360\n\t"
      "ds_read_b64_tr_b16 v[74:75], v37 offset:16128\n\t"
      "v_mfma_f32_32x32x16_bf16 v[112:127], v[88:91], v[236:239], v[112:127]\n\t"
      "ds_read_b64_tr_b16 v[76:77], v37 offset:15424\n\t"
      "ds_read_b64_tr_b16 v[78:79], v37 offset:16192\n\t"
      "v_mfma_f32_32x32x16_bf16 v[128:143], v[92:95], v[236:239], v[128:143]\n\t"
      "ds_read_b64_tr_b16 v[80:81], v37 offset:15488\n\t"
      "ds_read_b64_tr_b16 v[82:83], v37 offset:16256\n\t"
      "s_waitcnt lgkmcnt(9)\n\t"
      "v_dot2c_f32_bf16 v42, v4, v39\n\t"
      "v_dot2c_f32_bf16 v43, v8, v39\n\t"
      "v_dot2c_f32_bf16 v44, v12, v39\n\t"
      "v_dot2c_f32_bf16 v42, v5, v39\n\t"
      "v_dot2c_f32_bf16 v43, v9, v39\n\t"
      "v_dot2c_f32_bf16 v44, v13, v39\n\t"
      "v_dot2c_f32_bf16 v42, v6, v39\n\t"
      "v_dot2c_f32_bf16 v43, v10, v39\n\t"
      "v_dot2c_f32_bf16 v44, v14, v39\n\t"
      "v_dot2c_f32_bf16 v42, v7, v39\n\t"
      "v_dot2c_f32_bf16 v43, v11, v39\n\t"
      "v_dot2c_f32_bf16 v44, v15, v39\n\t"
      "ds_read_b64_tr_b16 v[4:5], v38 offset:12288\n\t"
      "ds_read_b64_tr_b16 v[6:7], v38 offset:13056\n\t"
      "ds_read_b64_tr_b16 v[8:9], v38 offset:12352\n\t"
      "ds_read_b64_tr_b16 v[10:11], v38 offset:13120\n\t"
      "ds_read_b64_tr_b16 v[12:13], v38 offset:12416\n\t"
      "ds_read_b64_tr_b16 v[14:15], v38 offset:13184\n\t"
      "s_waitcnt vmcnt(5)\n\t"
      "v_xor_b32 v21, s47, v21\n\t"
      "v_xor_b32 v23, s47, v23\n\t"
      "v_or_b32 v21, v21, v23\n\t"
      "v_cmp_ne_u32 vcc, 0, v21\n\t"
      "v_permlane32_swap_b32 v20, v22\n\t"
      "s_cbranch_vccnz xpa_%=\n\t"
      "xra_%=:\n\t"
      "v_mov_b32 v25, 0\n\t"
      "v_add_f32 v20, v20, v22\n\t"
      "v_mov_b32 v18, 0\n\t"
      "v_mov_b32 v22, v20\n\t"
      "s_waitcnt lgkmcnt(0)\n\t"
      "v_dot2c_f32_bf16 v42, v4, v39\n\t"
      "v_dot2c_f32_bf16 v43, v8, v39\n\t"
      "v_dot2c_f32_bf16 v44, v12, v39\n\t"
      "v_dot2c_f32_bf16 v42, v5, v39\n\t"
      "v_dot2c_f32_bf16 v43, v9, v39\n\t"
      "v_dot2c_f32_bf16 v44, v13, v39\n\t"
      "v_dot2c_f32_bf16 v42, v6, v39\n\t"
      "v_dot2c_f32_bf16 v43, v10, v39\n\t"
      "v_dot2c_f32_bf16 v44, v14, v39\n\t"
      "v_dot2c_f32_bf16 v42, v7, v39\n\t"
      "v_dot2c_f32_bf16 v43, v11, v39\n\t"
      "v_dot2c_f32_bf16 v44, v15, v39\n\t"
      "s_waitcnt lgkmcnt(13)\n\t"
      "v_mfma_f32_32x32x16_bf16 v[96:111], v[60:63], v[240:243], v[96:111]\n\t"
      "ds_read_b128 v[248:251], v35 offset:32768\n\t"
      "ds_read_b64_tr_b16 v[84:85], v37 offset:18432\n\t"
      "ds_read_b64_tr_b16 v[86:87], v37 offset:19200\n\t"
      "v_permlane32_swap_b32 v20, v22\n\t"
      "v_mov_b32 v19, 0\n\t"
      "v_permlane32_swap_b32 v42, v43\n\t"
      "v_mfma_f32_32x32x16_bf16 v[112:127], v[64:67], v[240:243], v[112:127]\n\t"
      "ds_read_b64_tr_b16 v[88:89], v37 offset:18496\n\t"
      "ds_read_b64_tr_b16 v[90:91], v37 offset:19264\n\t"
      "s_bfe_u32 s48, s100, 262152\n\t"
      "s_add_u32 m0, s54, 16384\n\t"
      "s_mul_i32 s48, s48, 229376\n\t"
      "s_add_u32 s48, s48, s52\n\t"
      "buffer_load_dwordx4 v47, s[20:23], s48 offen offset:0 lds\n\t"
      "v_mfma_f32_32x32x16_bf16 v[128:143], v[68:71], v[240:243], v[128:143]\n\t"
      "ds_read_b64_tr_b16 v[92:93], v37 offset:18560\n\t"
      "ds_read_b64_tr_b16 v[94:95], v37 offset:19328\n\t"
      "v_permlane32_swap_b32 v44, v25\n\t"
      "v_mul_f32 v20, 0xbb124925, v20\n\t"
      "v_add_f32 v42, v42, v43\n\t"
      "v_add_f32 v44, v44, v25\n\t"
      "s_waitcnt lgkmcnt(13)\n\t"
      "v_mfma_f32_32x32x16_bf16 v[96:111], v[72:75], v[244:247], v[96:111]\n\t"
      "ds_read_b128 v[252:255], v36 offset:32768\n\t"
      "ds_read_b64_tr_b16 v[60:61], v37 offset:21504\n\t"
      "ds_read_b64_tr_b16 v[62:63], v37 offset:22272\n\t"
      "buffer_load_dwordx4 v40, s[20:23], s48 offen offset:1024 lds\n\t"
      "ds_write_b32 v45, v42\n\t"
      "ds_write_b32 v45, v44 offset:1024\n\t"
      "v_mfma_f32_32x32x16_bf16 v[112:127], v[76:79], v[244:247], v[112:127]\n\t"
      "ds_read_b64_tr_b16 v[64:65], v37 offset:21568\n\t"
      "ds_read_b64_tr_b16 v[66:67], v37 offset:22336\n\t"
      "v_mul_f32 v22, 0x3b124925, v22\n\t"
      "v_fma_f32 v22, -v20, v20, v22\n\t"
      "v_cvt_pk_bf16_f32 v16, v20, v20\n\t"
      "v_max_f32 v22, 0, v22\n\t"
      "v_mfma_f32_32x32x16_bf16 v[128:143], v[80:83], v[244:247], v[128:143]\n\t"
      "ds_read_b64_tr_b16 v[68:69], v37 offset:21632\n\t"
      "ds_read_b64_tr_b16 v[70:71], v37 offset:22400\n\t"
      "buffer_load_dwordx4 v41, s[20:23], s48 offen offset:2048 lds\n\t"
      "v_and_b32 v17, 0xffff0000, v16\n\t"
      "s_waitcnt lgkmcnt(4)\n\t"
      "s_barrier\n\t"
      "s_waitcnt lgkmcnt(9)\n\t"
      "v_mfma_f32_32x32x16_bf16 v[96:111], v[84:87], v[248:251], v[96:111]\n\t"
      "ds_read_b128 v[4:7], v46 offset:0\n\t"
      "ds_read_b128 v[8:11], v46 offset:512\n\t"
      "ds_read_b128 v[12:15], v46 offset:1024\n\t"
      "v_add_f32 v22, 0x3727c5ac, v22\n\t"
      "v_sub_f32 v17, v20, v17\n\t"
      "v_rsq_f32 v24, v22\n\t"
      "v_mfma_f32_32x32x16_bf16 v[112:127], v[88:91], v[248:251], v[112:127]\n\t"
      "buffer_load_dwordx4 %[tid], s[20:23], s48 offen offset:3072 lds\n\t"
      "v_cvt_pk_bf16_f32 v17, v17, 0\n\t"
      "v_bfi_b32 v16, v27, 0, v16\n\t"
      "v_bfi_b32 v17, v27, 0, v17\n\t"
      "v_mov_b32 v22, 0\n\t"
      "v_mov_b32 v23, 0\n\t"
      "v_mfma_f32_32x32x16_bf16 v[128:143], v[92:95], v[248:251], v[128:143]\n\t"
      "v_accvgpr_write_b32 a168, v16\n\t"
      "v_accvgpr_write_b32 a169, v17\n\t"
      "s_waitcnt lgkmcnt(0)\n\t"
      "v_add_f32 v4, v4, v5\n\t"
      "v_add_f32 v6, v6, v7\n\t"
      "v_add_f32 v42, v4, v6\n\t"
      "s_waitcnt lgkmcnt(3)\n\t"
      "v_mfma_f32_32x32x16_bf16 v[96:111], v[60:63], v[252:255], v[96:111]\n\t"
      "s_bfe_u32 s48, s100, 262152\n\t"
      "s_add_u32 m0, s56, 139776\n\t"
      "s_mul_i32 s48, s48, 128\n\t"
      "s_add_u32 s48, s48, s53\n\t"
      "buffer_load_dwordx4 v48, s[24:27], s48 offen offset:0 lds\n\t"
      "v_cvt_pk_bf16_f32 v21, v42, 0\n\t"
      "v_mfma_f32_32x32x16_bf16 v[112:127], v[64:67], v[252:255], v[112:127]\n\t"
      "v_lshlrev_b32 v20, 16, v21\n\t"
      "v_sub_f32 v20, v42, v20\n\t"
      "v_cvt_pk_bf16_f32 v20, v42, v20\n\t"
      "v_add_f32 v8, v8, v9\n\t"
      "v_add_f32 v10, v10, v11\n\t"
      "v_add_f32 v43, v8, v10\n\t"
      "v_mfma_f32_32x32x16_bf16 v[128:143], v[68:71], v[252:255], v[128:143]\n\t"
      "v_cvt_pk_bf16_f32 v5, v43, 0\n\t"
      "v_lshlrev_b32 v4, 16, v5\n\t"
      "v_sub_f32 v4, v43, v4\n\t"
      "v_cvt_pk_bf16_f32 v4, v43, v4\n\t"
      "v_mov_b32 v6, 0\n\t"
      "v_mov_b32 v7, 0\n\t"
      "v_add_f32 v12, v12, v13\n\t"
      "v_add_f32 v14, v14, v15\n\t"
      "v_add_f32 v44, v12, v14\n\t"
      "v_cvt_pk_bf16_f32 v9, v44, 0\n\t"
      "v_lshlrev_b32 v8, 16, v9\n\t"
      "v_sub_f32 v8, v44, v8\n\t"
      "v_cvt_pk_bf16_f32 v8, v44, v8\n\t"
      "v_mov_b32 v10, 0\n\t"
      "v_mov_b32 v11, 0\n\t"
      "s_nop 1\n\t"
      "v_mfma_f32_32x32x16_bf16 v[96:111], v[20:23], v[16:19], v[96:111]\n\t"
      "v_and_b32 v84, s70, v39\n\t"
      "v_mul_f32 v86, s66, v24\n\t"
      "v_lshrrev_b32 v85, 16, v84\n\t"
      "v_mfma_f32_32x32x16_bf16 v[112:127], v[4:7], v[16:19], v[112:127]\n\t"
      "v_and_b32 v88, s81, v39\n\t"
      "v_mul_f32 v90, s77, v24\n\t"
      "v_lshrrev_b32 v89, 16, v88\n\t"
      "v_mfma_f32_32x32x16_bf16 v[128:143], v[8:11], v[16:19], v[128:143]\n\t"
      "v_and_b32 v92, s92, v39\n\t"
      "v_mul_f32 v94, s88, v24\n\t"
      "v_lshrrev_b32 v93, 16, v92\n\t"
      "s_sub_u32 s50, 131072, s54\n\t"
      "v_add_u32 v33, s50, v33\n\t"
      "v_add_u32 v34, s50, v34\n\t"
      "v_add_u32 v35, s50, v35\n\t"
      "v_add_u32 v36, s50, v36\n\t"
      "v_pk_mul_f32 v[96:97], v[96:97], v[86:87] op_sel_hi:[1,0]\n\t"
      "v_pk_mul_f32 v[98:99], v[98:99], v[86:87] op_sel_hi:[1,0]\n\t"
      "v_cvt_pk_bf16_f32 v96, v96, v97\n\t"
      "v_cvt_pk_bf16_f32 v97, v98, v99\n\t"
      "v_pk_mul_f32 v[100:101], v[100:101], v[86:87] op_sel_hi:[1,0]\n\t"
      "v_pk_mul_f32 v[102:103], v[102:103], v[86:87] op_sel_hi:[1,0]\n\t"
      "v_cvt_pk_bf16_f32 v98, v100, v101\n\t"
      "v_cvt_pk_bf16_f32 v99, v102, v103\n\t"
      "v_pk_mul_f32 v[104:105], v[104:105], v[86:87] op_sel_hi:[1,0]\n\t"
      "v_pk_mul_f32 v[106:107], v[106:107], v[86:87] op_sel_hi:[1,0]\n\t"
      "v_cvt_pk_bf16_f32 v100, v104, v105\n\t"
      "v_cvt_pk_bf16_f32 v101, v106, v107\n\t"
      "v_pk_mul_f32 v[108:109], v[108:109], v[86:87] op_sel_hi:[1,0]\n\t"
      "v_pk_mul_f32 v[110:111], v[110:111], v[86:87] op_sel_hi:[1,0]\n\t"
      "v_cvt_pk_bf16_f32 v102, v108, v109\n\t"
      "v_cvt_pk_bf16_f32 v103, v110, v111\n\t"
      "v_cndmask_b32 v86, v102, v98, s[60:61]\n\t"
      "v_cndmask_b32 v87, v103, v99, s[60:61]\n\t"
      "v_cndmask_b32 v98, v98, v84, s[60:61]\n\t"
      "v_cndmask_b32 v99, v99, v85, s[60:61]\n\t"
      "buffer_store_dwordx4 v[96:99], v50, s[28:31], s65 offen sc1\n\t"
      "v_cndmask_b32 v102, v102, v84, s[62:63]\n\t"
      "v_cndmask_b32 v103, v103, v85, s[62:63]\n\t"
      "buffer_store_dwordx4 v[100:103], v51, s[28:31], s65 offen sc1\n\t"
      "buffer_store_dwordx4 v[84:87], v52, s[28:31], s65 offen sc1\n\t"
      "v_pk_mul_f32 v[112:113], v[112:113], v[90:91] op_sel_hi:[1,0]\n\t"
      "v_pk_mul_f32 v[114:115], v[114:115], v[90:91] op_sel_hi:[1,0]\n\t"
      "v_cvt_pk_bf16_f32 v112, v112, v113\n\t"
      "v_cvt_pk_bf16_f32 v113, v114, v115\n\t"
      "v_pk_mul_f32 v[116:117], v[116:117], v[90:91] op_sel_hi:[1,0]\n\t"
      "v_pk_mul_f32 v[118:119], v[118:119], v[90:91] op_sel_hi:[1,0]\n\t"
      "v_cvt_pk_bf16_f32 v114, v116, v117\n\t"
      "v_cvt_pk_bf16_f32 v115, v118, v119\n\t"
      "v_pk_mul_f32 v[120:121], v[120:121], v[90:91] op_sel_hi:[1,0]\n\t"
      "v_pk_mul_f32 v[122:123], v[122:123], v[90:91] op_sel_hi:[1,0]\n\t"
      "v_cvt_pk_bf16_f32 v116, v120, v121\n\t"
      "v_cvt_pk_bf16_f32 v117, v122, v123\n\t"
      "v_pk_mul_f32 v[124:125], v[124:125], v[90:91] op_sel_hi:[1,0]\n\t"
      "v_pk_mul_f32 v[126:127], v[126:127], v[90:91] op_sel_hi:[1,0]\n\t"
      "v_cvt_pk_bf16_f32 v118, v124, v125\n\t"
      "v_cvt_pk_bf16_f32 v119, v126, v127\n\t"
      "v_cndmask_b32 v90, v118, v114, s[72:73]\n\t"
      "v_cndmask_b32 v91, v119, v115, s[72:73]\n\t"
      "v_cndmask_b32 v114, v114, v88, s[72:73]\n\t"
      "v_cndmask_b32 v115, v115, v89, s[72:73]\n\t"
      "buffer_store_dwordx4 v[112:115], v53, s[28:31], s76 offen sc1\n\t"
      "v_cndmask_b32 v118, v118, v88, s[74:75]\n\t"
      "v_cndmask_b32 v119, v119, v89, s[74:75]\n\t"
      "buffer_store_dwordx4 v[116:119], v54, s[28:31], s76 offen sc1\n\t"
      "buffer_store_dwordx4 v[88:91], v55, s[28:31], s76 offen sc1\n\t"
      "v_pk_mul_f32 v[128:129], v[128:129], v[94:95] op_sel_hi:[1,0]\n\t"
      "v_pk_mul_f32 v[130:131], v[130:131], v[94:95] op_sel_hi:[1,0]\n\t"
      "v_cvt_pk_bf16_f32 v128, v128, v129\n\t"
      "v_cvt_pk_bf16_f32 v129, v130, v131\n\t"
      "v_pk_mul_f32 v[132:133], v[132:133], v[94:95] op_sel_hi:[1,0]\n\t"
      "v_pk_mul_f32 v[134:135], v[134:135], v[94:95] op_sel_hi:[1,0]\n\t"
      "v_cvt_pk_bf16_f32 v130, v132, v133\n\t"
      "v_cvt_pk_bf16_f32 v131, v134, v135\n\t"
      "v_pk_mul_f32 v[136:137], v[136:137], v[94:95] op_sel_hi:[1,0]\n\t"
      "v_pk_mul_f32 v[138:139], v[138:139], v[94:95] op_sel_hi:[1,0]\n\t"
      "v_cvt_pk_bf16_f32 v132, v136, v137\n\t"
      "v_cvt_pk_bf16_f32 v133, v138, v139\n\t"
      "v_pk_mul_f32 v[140:141], v[140:141], v[94:95] op_sel_hi:[1,0]\n\t"
      "v_pk_mul_f32 v[142:143], v[142:143], v[94:95] op_sel_hi:[1,0]\n\t"
      "v_cvt_pk_bf16_f32 v134, v140, v141\n\t"
      "v_cvt_pk_bf16_f32 v135, v142, v143\n\t"
      "v_cndmask_b32 v94, v134, v130, s[82:83]\n\t"
      "v_cndmask_b32 v95, v135, v131, s[82:83]\n\t"
      "v_cndmask_b32 v130, v130, v92, s[82:83]\n\t"
      "v_cndmask_b32 v131, v131, v93, s[82:83]\n\t"
      "buffer_store_dwordx4 v[128:131], v56, s[28:31], s87 offen sc1\n\t"
      "v_cndmask_b32 v134, v134, v92, s[84:85]\n\t"
      "v_cndmask_b32 v135, v135, v93, s[84:85]\n\t"
      "buffer_store_dwordx4 v[132:135], v57, s[28:31], s87 offen sc1\n\t"
      "buffer_store_dwordx4 v[92:95], v58, s[28:31], s87 offen sc1\n\t"
      "s_mov_b32 exec_lo, 0xf0000000\n\t"
      "s_mov_b32 exec_hi, 0xf0000000\n\t"
      "v_mov_b32 v33, 135168\n\t"
      "v_mov_b32 v34, 135168\n\t"
      "v_mov_b32 v35, 135168\n\t"
      "v_mov_b32 v36, 135168\n\t"
      "s_mov_b64 exec, -1\n\t"
      "zph_%=:\n\t"
      "s_lshl_b32 s48, s45, 2\n\t"
      "s_add_u32 s48, s48, gtab_%=-pc0_%=\n\t"
      "s_load_dword s79, s[32:33], s48\n\t"
      "s_waitcnt vmcnt(18) lgkmcnt(0)\n\t"
      "s_barrier\n\t"
      "ds_read_b64_tr_b16 v[60:61], v28 offset:49152\n\t"
      "ds_read_b64_tr_b16 v[62:63], v28 offset:50176\n\t"
      "ds_read_b64_tr_b16 v[64:65], v29 offset:49152\n\t"
      "ds_read_b64_tr_b16 v[66:67], v29 offset:50176\n\t"
      "ds_read_b64_tr_b16 v[68:69], v30 offset:49152\n\t"
      "ds_read_b64_tr_b16 v[70:71], v30 offset:50176\n\t"
      "ds_read_b64_tr_b16 v[72:73], v31 offset:49152\n\t"
      "ds_read_b64_tr_b16 v[74:75], v31 offset:50176\n\t"
      "ds_read_b64_tr_b16 v[76:77], v32 offset:49152\n\t"
      "ds_read_b64_tr_b16 v[78:79], v32 offset:50176\n\t"
      "ds_read_b128 v[80:83], v33 offset:0\n\t"
      "s_waitcnt lgkmcnt(7)\n\t"
      "v_mfma_f32_32x32x16_bf16 a[0:15], v[60:63], v[144:147], 0\n\t"
      "ds_read_b64_tr_b16 v[104:105], v28 offset:53248\n\t"
      "ds_read_b64_tr_b16 v[106:107], v28 offset:54272\n\t"
      "s_and_b32 s93, %[bid], 7\n\t"
      "v_mfma_f32_32x32x16_bf16 a[16:31], v[64:67], v[144:147], 0\n\t"
      "ds_read_b64_tr_b16 v[108:109], v29 offset:53248\n\t"
      "ds_read_b64_tr_b16 v[110:111], v29 offset:54272\n\t"
      "s_bfe_u32 s48, s100, 262156\n\t"
      "s_add_u32 m0, s54, 32768\n\t"
      "s_mul_i32 s48, s48, 229376\n\t"
      "s_add_u32 s48, s48, s52\n\t"
      "buffer_load_dwordx4 v47, s[20:23], s48 offen offset:0 lds\n\t"
      "s_waitcnt lgkmcnt(7)\n\t"
      "v_mfma_f32_32x32x16_bf16 a[32:47], v[68:71], v[144:147], 0\n\t"
      "ds_read_b64_tr_b16 v[112:113], v30 offset:53248\n\t"
      "ds_read_b64_tr_b16 v[114:115], v30 offset:54272\n\t"
      "s_cmp_ge_u32 s46, 7\n\t"
      "s_cselect_b32 s58, 1, 0\n\t"
      "v_mfma_f32_32x32x16_bf16 a[48:63], v[72:75], v[144:147], 0\n\t"
      "ds_read_b64_tr_b16 v[116:117], v31 offset:53248\n\t"
      "ds_read_b64_tr_b16 v[118:119], v31 offset:54272\n\t"
      "buffer_load_dwordx4 v40, s[20:23], s48 offen offset:1024 lds\n\t"
      "s_mul_i32 s57, s58, 7\n\t"
      "s_sub_u32 s57, s46, s57\n\t"
      "s_waitcnt lgkmcnt(8)\n\t"
      "v_mfma_f32_32x32x16_bf16 v[128:143], v[80:83], v[76:79], 0\n\t"
      "ds_read_b64_tr_b16 v[120:121], v32 offset:53248\n\t"
      "ds_read_b64_tr_b16 v[122:123], v32 offset:54272\n\t"
      "ds_read_b128 v[124:127], v34 offset:0\n\t"
      "v_lshrrev_b32 v24, 3, v1\n\t"
      "s_waitcnt lgkmcnt(7)\n\t"
      "v_mfma_f32_32x32x16_bf16 a[0:15], v[104:107], v[148:151], a[0:15]\n\t"
      "ds_read_b64_tr_b16 v[60:61], v28 offset:57344\n\t"
      "ds_read_b64_tr_b16 v[62:63], v28 offset:58368\n\t"
      "v_mul_u32_u24 v50, 896, v24\n\t"
      "v_lshrrev_b32 v24, 2, v1\n\t"
      "v_mfma_f32_32x32x16_bf16 a[16:31], v[108:111], v[148:151], a[16:31]\n\t"
      "ds_read_b64_tr_b16 v[64:65], v29 offset:57344\n\t"
      "ds_read_b64_tr_b16 v[66:67], v29 offset:58368\n\t"
      "buffer_load_dwordx4 v41, s[20:23], s48 offen offset:2048 lds\n\t"
      "v_and_b32 v24, 4, v24\n\t"
      "v_and_b32 v25, 7, v1\n\t"
      "s_waitcnt lgkmcnt(7)\n\t"
      "v_mfma_f32_32x32x16_bf16 a[32:47], v[112:115], v[148:151], a[32:47]\n\t"
      "ds_read_b64_tr_b16 v[68:69], v30 offset:57344\n\t"
      "ds_read_b64_tr_b16 v[70:71], v30 offset:58368\n\t"
      "v_xor_b32 v24, v24, v25\n\t"
      "v_lshl_add_u32 v50, v24, 4, v50\n\t"
      "v_mfma_f32_32x32x16_bf16 a[48:63], v[116:119], v[148:151], a[48:63]\n\t"
      "ds_read_b64_tr_b16 v[72:73], v31 offset:57344\n\t"
      "ds_read_b64_tr_b16 v[74:75], v31 offset:58368\n\t"
      "buffer_load_dwordx4 %[tid], s[20:23], s48 offen offset:3072 lds\n\t"
      "s_lshl_b32 s55, s45, 1\n\t"
      "s_add_u32 s55, s55, s58\n\t"
      "s_waitcnt lgkmcnt(8)\n\t"
      "v_mfma_f32_32x32x16_bf16 v[128:143], v[124:127], v[120:123], v[128:143]\n\t"
      "ds_read_b64_tr_b16 v[76:77], v32 offset:57344\n\t"
      "ds_read_b64_tr_b16 v[78:79], v32 offset:58368\n\t"
      "ds_read_b128 v[80:83], v35 offset:0\n\t"
      "s_lshl_b32 s55, s55, 2\n\t"
      "s_waitcnt lgkmcnt(7)\n\t"
      "v_mfma_f32_32x32x16_bf16 a[0:15], v[60:63], v[152:155], a[0:15]\n\t"
      "ds_read_b64_tr_b16 v[104:105], v28 offset:61440\n\t"
      "ds_read_b64_tr_b16 v[106:107], v28 offset:62464\n\t"
      "s_add_u32 s55, s55, s44\n\t"
      "s_mul_i32 s51, s55, 14336\n\t"
      "v_mfma_f32_32x32x16_bf16 a[16:31], v[64:67], v[152:155], a[16:31]\n\t"
      "ds_read_b64_tr_b16 v[108:109], v29 offset:61440\n\t"
      "ds_read_b64_tr_b16 v[110:111], v29 offset:62464\n\t"
      "s_bfe_u32 s48, s100, 262156\n\t"
      "s_add_u32 m0, s56, 144128\n\t"
      "s_mul_i32 s48, s48, 128\n\t"
      "s_add_u32 s48, s48, s53\n\t"
      "buffer_load_dwordx4 v48, s[24:27], s48 offen offset:0 lds\n\t"
      "s_waitcnt lgkmcnt(7)\n\t"
      "v_mfma_f32_32x32x16_bf16 a[32:47], v[68:71], v[152:155], a[32:47]\n\t"
      "ds_read_b64_tr_b16 v[112:113], v30 offset:61440\n\t"
      "ds_read_b64_tr_b16 v[114:115], v30 offset:62464\n\t"
      "s_lshl_b32 s91, s57, 7\n\t"
      "s_add_u32 s51, s51, s91\n\t"
      "v_mfma_f32_32x32x16_bf16 a[48:63], v[72:75], v[152:155], a[48:63]\n\t"
      "ds_read_b64_tr_b16 v[116:117], v31 offset:61440\n\t"
      "ds_read_b64_tr_b16 v[118:119], v31 offset:62464\n\t"
      "v_bfe_u32 v24, v1, 2, 2\n\t"
      "v_lshlrev_b32 v51, 7, v24\n\t"
      "s_waitcnt lgkmcnt(8)\n\t"
      "v_mfma_f32_32x32x16_bf16 v[128:143], v[80:83], v[76:79], v[128:143]\n\t"
      "ds_read_b64_tr_b16 v[120:121], v32 offset:61440\n\t"
      "ds_read_b64_tr_b16 v[122:123], v32 offset:62464\n\t"
      "ds_read_b128 v[124:127], v36 offset:0\n\t"
      "v_lshrrev_b32 v24, 1, v24\n\t"
      "s_waitcnt vmcnt(16)\n\t"
      "s_barrier\n\t"
      "s_waitcnt lgkmcnt(7)\n\t"
      "v_mfma_f32_32x32x16_bf16 a[0:15], v[104:107], v[156:159], a[0:15]\n\t"
      "ds_read_b64_tr_b16 v[60:61], v28 offset:0\n\t"
      "ds_read_b64_tr_b16 v[62:63], v28 offset:1024\n\t"
      "v_lshl_add_u32 v51, v24, 6, v51\n\t"
      "v_and_b32 v24, 0x200, v27\n\t"
      "v_mfma_f32_32x32x16_bf16 a[16:31], v[108:111], v[156:159], a[16:31]\n\t"
      "ds_read_b64_tr_b16 v[64:65], v29 offset:0\n\t"
      "ds_read_b64_tr_b16 v[66:67], v29 offset:1024\n\t"
      "s_bfe_u32 s48, s100, 262160\n\t"
      "s_add_u32 m0, s54, 65536\n\t"
      "s_mul_i32 s48, s48, 229376\n\t"
      "s_add_u32 s48, s48, s52\n\t"
      "buffer_load_dwordx4 v47, s[20:23], s48 offen offset:0 lds\n\t"
      "s_waitcnt lgkmcnt(7)\n\t"
      "v_mfma_f32_32x32x16_bf16 a[32:47], v[112:115], v[156:159], a[32:47]\n\t"
      "ds_read_b64_tr_b16 v[68:69], v30 offset:0\n\t"
      "ds_read_b64_tr_b16 v[70:71], v30 offset:1024\n\t"
      "v_and_b32 v25, 0x38, v28\n\t"
      "v_add3_u32 v51, v51, v24, v25\n\t"
      "v_mfma_f32_32x32x16_bf16 a[48:63], v[116:119], v[156:159], a[48:63]\n\t"
      "ds_read_b64_tr_b16 v[72:73], v31 offset:0\n\t"
      "ds_read_b64_tr_b16 v[74:75], v31 offset:1024\n\t"
      "buffer_load_dwordx4 v40, s[20:23], s48 offen offset:1024 lds\n\t"
      "s_add_u32 s91, s54, 98304\n\t"
      "v_add_u32 v51, s91, v51\n\t"
      "s_waitcnt lgkmcnt(8)\n\t"
      "v_mfma_f32_32x32x16_bf16 v[128:143], v[124:127], v[120:123], v[128:143]\n\t"
      "ds_read_b64_tr_b16 v[76:77], v32 offset:0\n\t"
      "ds_read_b64_tr_b16 v[78:79], v32 offset:1024\n\t"
      "ds_read_b128 v[80:83], v33 offset:4352\n\t"
      "v_xor_b32 v58, 64, v51\n\t"
      "s_waitcnt lgkmcnt(7)\n\t"
      "v_mfma_f32_32x32x16_bf16 a[0:15], v[60:63], v[160:163], a[0:15]\n\t"
      "ds_read_b64_tr_b16 v[104:105], v28 offset:4096\n\t"
      "ds_read_b64_tr_b16 v[106:107], v28 offset:5120\n\t"
      "s_lshl_b32 s91, s45, 7\n\t"
      "s_add_u32 s91, s91, 147456\n\t"
      "v_mfma_f32_32x32x16_bf16 a[16:31], v[64:67], v[160:163], a[16:31]\n\t"
      "ds_read_b64_tr_b16 v[108:109], v29 offset:4096\n\t"
      "ds_read_b64_tr_b16 v[110:111], v29 offset:5120\n\t"
      "buffer_load_dwordx4 v41, s[20:23], s48 offen offset:2048 lds\n\t"
      "v_mov_b32 v53, 2147483648\n\t"
      "s_waitcnt lgkmcnt(7)\n\t"
      "v_mfma_f32_32x32x16_bf16 a[32:47], v[68:71], v[160:163], a[32:47]\n\t"
      "ds_read_b64_tr_b16 v[112:113], v30 offset:4096\n\t"
      "ds_read_b64_tr_b16 v[114:115], v30 offset:5120\n\t"
      "s_mov_b32 exec_lo, 0x0fffffff\n\t"
      "s_mov_b32 exec_hi, 0\n\t"
      "v_lshl_add_u32 v53, v1, 2, s91\n\t"
      "s_mov_b64 exec, -1\n\t"
      "s_lshl_b32 s91, s59, 16\n\t"
      "v_mfma_f32_32x32x16_bf16 a[48:63], v[72:75], v[160:163], a[48:63]\n\t"
      "ds_read_b64_tr_b16 v[116:117], v31 offset:4096\n\t"
      "ds_read_b64_tr_b16 v[118:119], v31 offset:5120\n\t"
      "buffer_load_dwordx4 %[tid], s[20:23], s48 offen offset:3072 lds\n\t"
      "s_add_u32 s91, s91, 3145728\n\t"
      "s_waitcnt lgkmcnt(8)\n\t"
      "v_mfma_f32_32x32x16_bf16 v[128:143], v[80:83], v[76:79], v[128:143]\n\t"
      "ds_read_b64_tr_b16 v[120:121], v32 offset:4096\n\t"
      "ds_read_b64_tr_b16 v[122:123], v32 offset:5120\n\t"
      "ds_read_b128 v[124:127], v34 offset:4352\n\t"
      "s_waitcnt lgkmcnt(7)\n\t"
      "v_mfma_f32_32x32x16_bf16 a[0:15], v[104:107], v[164:167], a[0:15]\n\t"
      "ds_read_b64_tr_b16 v[60:61], v28 offset:8192\n\t"
      "ds_read_b64_tr_b16 v[62:63], v28 offset:9216\n\t"
      "s_add_u32 s12, s28, s91\n\t"
      "s_addc_u32 s13, s29, 0\n\t"
      "v_mfma_f32_32x32x16_bf16 a[16:31], v[108:111], v[164:167], a[16:31]\n\t"
      "ds_read_b64_tr_b16 v[64:65], v29 offset:8192\n\t"
      "ds_read_b64_tr_b16 v[66:67], v29 offset:9216\n\t"
      "s_bfe_u32 s48, s100, 262160\n\t"
      "s_add_u32 m0, s56, 148480\n\t"
      "s_mul_i32 s48, s48, 128\n\t"
      "s_add_u32 s48, s48, s53\n\t"
      "buffer_load_dwordx4 v48, s[24:27], s48 offen offset:0 lds\n\t"
      "s_waitcnt lgkmcnt(7)\n\t"
      "v_mfma_f32_32x32x16_bf16 a[32:47], v[112:115], v[164:167], a[32:47]\n\t"
      "ds_read_b64_tr_b16 v[68:69], v30 offset:8192\n\t"
      "ds_read_b64_tr_b16 v[70:71], v30 offset:9216\n\t"
      "s_mov_b32 s14, 0x10000\n\t"
      "s_lshl_b32 s92, s46, 2\n\t"
      "v_mfma_f32_32x32x16_bf16 a[48:63], v[116:119], v[164:167], a[48:63]\n\t"
      "ds_read_b64_tr_b16 v[72:73], v31 offset:8192\n\t"
      "ds_read_b64_tr_b16 v[74:75], v31 offset:9216\n\t"
      "s_add_u32 m0, s54, 98304\n\t"
      "s_add_u32 s48, s51, 0\n\t"
      "buffer_load_dwordx4 v50, s[36:39], s48 offen lds\n\t"
      "s_waitcnt lgkmcnt(8)\n\t"
      "v_mfma_f32_32x32x16_bf16 v[128:143], v[124:127], v[120:123], v[128:143]\n\t"
      "ds_read_b64_tr_b16 v[76:77], v32 offset:8192\n\t"
      "ds_read_b64_tr_b16 v[78:79], v32 offset:9216\n\t"
      "ds_read_b128 v[80:83], v35 offset:4352\n\t"
      "s_add_u32 s92, s92, s44\n\t"
      "s_waitcnt lgkmcnt(7)\n\t"
      "v_mfma_f32_32x32x16_bf16 a[0:15], v[60:63], v[168:171], a[0:15]\n\t"
      "ds_read_b64_tr_b16 v[104:105], v28 offset:12288\n\t"
      "ds_read_b64_tr_b16 v[106:107], v28 offset:13312\n\t"
      "s_lshl_b32 s91, s45, 6\n\t"
      "s_add_u32 s91, s91, s92\n\t"
      "v_mfma_f32_32x32x16_bf16 a[16:31], v[64:67], v[168:171], a[16:31]\n\t"
      "ds_read_b64_tr_b16 v[108:109], v29 offset:12288\n\t"
      "ds_read_b64_tr_b16 v[110:111], v29 offset:13312\n\t"
      "s_add_u32 m0, s54, 99328\n\t"
      "s_add_u32 s48, s51, 7168\n\t"
      "buffer_load_dwordx4 v50, s[36:39], s48 offen lds\n\t"
      "s_waitcnt lgkmcnt(7)\n\t"
      "v_mfma_f32_32x32x16_bf16 a[32:47], v[68:71], v[168:171], a[32:47]\n\t"
      "ds_read_b64_tr_b16 v[112:113], v30 offset:12288\n\t"
      "ds_read_b64_tr_b16 v[114:115], v30 offset:13312\n\t"
      "s_lshl_b32 s91, s91, 11\n\t"
      "s_add_u32 s68, s91, 1048576\n\t"
      "v_mfma_f32_32x32x16_bf16 a[48:63], v[72:75], v[168:171], a[48:63]\n\t"
      "ds_read_b64_tr_b16 v[116:117], v31 offset:12288\n\t"
      "ds_read_b64_tr_b16 v[118:119], v31 offset:13312\n\t"
      "s_mul_i32 s67, s92, 147456\n\t"
      "s_mul_i32 s91, s45, 9216\n\t"
      "s_add_u32 s67, s67, s91\n\t"
      "s_waitcnt lgkmcnt(8)\n\t"
      "v_mfma_f32_32x32x16_bf16 v[128:143], v[80:83], v[76:79], v[128:143]\n\t"
      "ds_read_b64_tr_b16 v[120:121], v32 offset:12288\n\t"
      "ds_read_b64_tr_b16 v[122:123], v32 offset:13312\n\t"
      "ds_read_b128 v[124:127], v36 offset:4352\n\t"
      "s_add_u32 s67, s67, 13631488\n\t"
      "s_waitcnt vmcnt(18)\n\t"
      "s_barrier\n\t"
      "s_waitcnt lgkmcnt(7)\n\t"
      "v_mfma_f32_32x32x16_bf16 a[0:15], v[104:107], v[172:175], a[0:15]\n\t"
      "ds_read_b64_tr_b16 v[60:61], v28 offset:16384\n\t"
      "ds_read_b64_tr_b16 v[62:63], v28 offset:17408\n\t"
      "s_lshl_b32 s78, s92, 15\n\t"
      "s_lshr_b32 s91, s45, 2\n\t"
      "v_mfma_f32_32x32x16_bf16 a[16:31], v[108:111], v[172:175], a[16:31]\n\t"
      "ds_read_b64_tr_b16 v[64:65], v29 offset:16384\n\t"
      "ds_read_b64_tr_b16 v[66:67], v29 offset:17408\n\t"
      "s_bfe_u32 s48, s100, 262164\n\t"
      "s_add_u32 m0, s54, 49152\n\t"
      "s_mul_i32 s48, s48, 229376\n\t"
      "s_add_u32 s48, s48, s52\n\t"
      "buffer_load_dwordx4 v47, s[20:23], s48 offen offset:0 lds\n\t"
      "s_waitcnt lgkmcnt(7)\n\t"
      "v_mfma_f32_32x32x16_bf16 a[32:47], v[112:115], v[172:175], a[32:47]\n\t"
      "ds_read_b64_tr_b16 v[68:69], v30 offset:16384\n\t"
      "ds_read_b64_tr_b16 v[70:71], v30 offset:17408\n\t"
      "s_lshl_b32 s91, s91, 13\n\t"
      "s_add_u32 s78, s78, s91\n\t"
      "v_mfma_f32_32x32x16_bf16 a[48:63], v[116:119], v[172:175], a[48:63]\n\t"
      "ds_read_b64_tr_b16 v[72:73], v31 offset:16384\n\t"
      "ds_read_b64_tr_b16 v[74:75], v31 offset:17408\n\t"
      "buffer_load_dwordx4 v40, s[20:23], s48 offen offset:1024 lds\n\t"
      "s_add_u32 s78, s78, 7340032\n\t"
      "v_lshrrev_b32 v27, 5, v1\n\t"
      "s_waitcnt lgkmcnt(8)\n\t"
      "v_mfma_f32_32x32x16_bf16 v[128:143], v[124:127], v[120:123], v[128:143]\n\t"
      "ds_read_b64_tr_b16 v[76:77], v32 offset:16384\n\t"
      "ds_read_b64_tr_b16 v[78:79], v32 offset:17408\n\t"
      "ds_read_b128 v[80:83], v33 offset:8704\n\t"
      "v_and_b32 v24, 1, v1\n\t"
      "s_waitcnt lgkmcnt(7)\n\t"
      "v_mfma_f32_32x32x16_bf16 a[0:15], v[60:63], v[176:179], a[0:15]\n\t"
      "ds_read_b64_tr_b16 v[104:105], v28 offset:20480\n\t"
      "ds_read_b64_tr_b16 v[106:107], v28 offset:21504\n\t"
      "v_lshl_add_u32 v24, v27, 1, v24\n\t"
      "v_add_u32 v24, 4, v24\n\t"
      "v_mfma_f32_32x32x16_bf16 a[16:31], v[64:67], v[176:179], a[16:31]\n\t"
      "ds_read_b64_tr_b16 v[108:109], v29 offset:20480\n\t"
      "ds_read_b64_tr_b16 v[110:111], v29 offset:21504\n\t"
      "buffer_load_dwordx4 v41, s[20:23], s48 offen offset:2048 lds\n\t"
      "v_and_b32 v25, 3, v1\n\t"
      "s_waitcnt lgkmcnt(7)\n\t"
      "v_mfma_f32_32x32x16_bf16 a[32:47], v[68:71], v[176:179], a[32:47]\n\t"
      "ds_read_b64_tr_b16 v[112:113], v30 offset:20480\n\t"
      "ds_read_b64_tr_b16 v[114:115], v30 offset:21504\n\t"
      "v_cmp_gt_u32 vcc, 16, v1\n\t"
      "v_lshrrev_b32 v26, 2, v1\n\t"
      "s_lshl_b32 s66, s44, 2\n\t"
      "v_cndmask_b32 v24, v24, v25, vcc\n\t"
      "v_lshlrev_b32 v24, 2, v24\n\t"
      "v_mfma_f32_32x32x16_bf16 a[48:63], v[72:75], v[176:179], a[48:63]\n\t"
      "ds_read_b64_tr_b16 v[116:117], v31 offset:20480\n\t"
      "ds_read_b64_tr_b16 v[118:119], v31 offset:21504\n\t"
      "buffer_load_dwordx4 %[tid], s[20:23], s48 offen offset:3072 lds\n\t"
      "v_lshrrev_b32 v24, v24, s79\n\t"
      "v_and_b32 v24, 15, v24\n\t"
      "s_waitcnt lgkmcnt(8)\n\t"
      "v_mfma_f32_32x32x16_bf16 v[128:143], v[80:83], v[76:79], v[128:143]\n\t"
      "ds_read_b64_tr_b16 v[120:121], v32 offset:20480\n\t"
      "ds_read_b64_tr_b16 v[122:123], v32 offset:21504\n\t"
      "ds_read_b128 v[124:127], v34 offset:8704\n\t"
      "v_add_u32 v26, s66, v26\n\t"
      "s_waitcnt vmcnt(16)\n\t"
      "v_mov_b32 v92, s47\n\t"
      "s_mov_b64 exec, s[94:95]\n\t"
      "buffer_store_dword v92, v49, s[28:31], 0 offen sc1\n\t"
      "s_mov_b64 exec, -1\n\t"
      "s_waitcnt lgkmcnt(7)\n\t"
      "v_mfma_f32_32x32x16_bf16 a[0:15], v[104:107], v[180:183], a[0:15]\n\t"
      "ds_read_b64_tr_b16 v[60:61], v28 offset:24576\n\t"
      "ds_read_b64_tr_b16 v[62:63], v28 offset:25600\n\t"
      "v_mov_b32 v25, s46\n\t"
      "v_mov_b32 v23, s45\n\t"
      "v_mfma_f32_32x32x16_bf16 a[16:31], v[108:111], v[180:183], a[16:31]\n\t"
      "ds_read_b64_tr_b16 v[64:65], v29 offset:24576\n\t"
      "ds_read_b64_tr_b16 v[66:67], v29 offset:25600\n\t"
      "s_bfe_u32 s48, s100, 262164\n\t"
      "s_add_u32 m0, s56, 131072\n\t"
      "s_mul_i32 s48, s48, 128\n\t"
      "s_add_u32 s48, s48, s53\n\t"
      "buffer_load_dwordx4 v48, s[24:27], s48 offen offset:0 lds\n\t"
      "s_waitcnt lgkmcnt(7)\n\t"
      "v_mfma_f32_32x32x16_bf16 a[32:47], v[112:115], v[180:183], a[32:47]\n\t"
      "ds_read_b64_tr_b16 v[68:69], v30 offset:24576\n\t"
      "ds_read_b64_tr_b16 v[70:71], v30 offset:25600\n\t"
      "v_cmp_gt_u32 vcc, 32, v1\n\t"
      "s_nop 1\n\t"
      "v_cndmask_b32 v25, v23, v25, vcc\n\t"
      "v_mfma_f32_32x32x16_bf16 a[48:63], v[116:119], v[180:183], a[48:63]\n\t"
      "ds_read_b64_tr_b16 v[72:73], v31 offset:24576\n\t"
      "ds_read_b64_tr_b16 v[74:75], v31 offset:25600\n\t"
      "v_cmp_gt_u32 vcc, 16, v1\n\t"
      "s_nop 1\n\t"
      "v_cndmask_b32 v25, v25, v26, vcc\n\t"
      "s_waitcnt lgkmcnt(8)\n\t"
      "v_mfma_f32_32x32x16_bf16 v[128:143], v[124:127], v[120:123], v[128:143]\n\t"
      "ds_read_b64_tr_b16 v[76:77], v32 offset:24576\n\t"
      "ds_read_b64_tr_b16 v[78:79], v32 offset:25600\n\t"
      "ds_read_b128 v[80:83], v35 offset:8704\n\t"
      "v_lshl_add_u32 v24, v25, 4, v24\n\t"
      "s_waitcnt lgkmcnt(7)\n\t"
      "v_mfma_f32_32x32x16_bf16 a[0:15], v[60:63], v[184:187], a[0:15]\n\t"
      "ds_read_b64_tr_b16 v[104:105], v28 offset:28672\n\t"
      "ds_read_b64_tr_b16 v[106:107], v28 offset:29696\n\t"
      "s_lshl_b32 s91, s93, 15\n\t"
      "s_add_u32 s91, s91, 262144\n\t"
      "v_mfma_f32_32x32x16_bf16 a[16:31], v[64:67], v[184:187], a[16:31]\n\t"
      "ds_read_b64_tr_b16 v[108:109], v29 offset:28672\n\t"
      "ds_read_b64_tr_b16 v[110:111], v29 offset:29696\n\t"
      "v_lshl_add_u32 v23, v24, 7, s91\n\t"
      "s_lshl_b32 s80, s44, 4\n\t"
      "s_lshl_b32 s91, s44, 9\n\t"
      "s_waitcnt lgkmcnt(7)\n\t"
      "v_mfma_f32_32x32x16_bf16 a[32:47], v[68:71], v[184:187], a[32:47]\n\t"
      "ds_read_b64_tr_b16 v[112:113], v30 offset:28672\n\t"
      "ds_read_b64_tr_b16 v[114:115], v30 offset:29696\n\t"
      "s_add_u32 s91, s91, 153600\n\t"
      "v_add_u32 v4, s91, v59\n\t"
      "v_mfma_f32_32x32x16_bf16 a[48:63], v[72:75], v[184:187], a[48:63]\n\t"
      "ds_read_b64_tr_b16 v[116:117], v31 offset:28672\n\t"
      "ds_read_b64_tr_b16 v[118:119], v31 offset:29696\n\t"
      "v_lshl_add_u32 v5, v27, 11, s66\n\t"
      "v_sub_u32 v27, 0, v27\n\t"
      "s_mov_b32 s66, 0\n\t"
      "s_waitcnt lgkmcnt(8)\n\t"
      "v_mfma_f32_32x32x16_bf16 v[128:143], v[80:83], v[76:79], v[128:143]\n\t"
      "ds_read_b64_tr_b16 v[120:121], v32 offset:28672\n\t"
      "ds_read_b64_tr_b16 v[122:123], v32 offset:29696\n\t"
      "ds_read_b128 v[124:127], v36 offset:8704\n\t"
      "s_mov_b32 s79, 0x4f800000\n\t"
      "s_waitcnt vmcnt(13)\n\t"
      "s_barrier\n\t"
      "s_waitcnt lgkmcnt(7)\n\t"
      "v_mfma_f32_32x32x16_bf16 a[0:15], v[104:107], v[188:191], a[0:15]\n\t"
      "ds_read_b64_tr_b16 v[60:61], v28 offset:32768\n\t"
      "ds_read_b64_tr_b16 v[62:63], v28 offset:33792\n\t"
      "s_bitcmp1_b32 s45, 0\n\t"
      "s_cselect_b32 s64, -1, 0\n\t"
      "s_cselect_b32 s63, 0, -1\n\t"
      "v_mfma_f32_32x32x16_bf16 a[16:31], v[108:111], v[188:191], a[16:31]\n\t"
      "ds_read_b64_tr_b16 v[64:65], v29 offset:32768\n\t"
      "ds_read_b64_tr_b16 v[66:67], v29 offset:33792\n\t"
      "s_bfe_u32 s48, s100, 262168\n\t"
      "s_add_u32 m0, s54, 0\n\t"
      "s_mul_i32 s48, s48, 229376\n\t"
      "s_add_u32 s48, s48, s52\n\t"
      "buffer_load_dwordx4 v47, s[20:23], s48 offen offset:0 lds\n\t"
      "s_waitcnt lgkmcnt(7)\n\t"
      "v_mfma_f32_32x32x16_bf16 a[32:47], v[112:115], v[188:191], a[32:47]\n\t"
      "ds_read_b64_tr_b16 v[68:69], v30 offset:32768\n\t"
      "ds_read_b64_tr_b16 v[70:71], v30 offset:33792\n\t"
      "s_mov_b32 s65, 0\n\t"
      "s_mov_b32 s62, 0\n\t"
      "v_mfma_f32_32x32x16_bf16 a[48:63], v[116:119], v[188:191], a[48:63]\n\t"
      "ds_read_b64_tr_b16 v[72:73], v31 offset:32768\n\t"
      "ds_read_b64_tr_b16 v[74:75], v31 offset:33792\n\t"
      "buffer_load_dwordx4 v40, s[20:23], s48 offen offset:1024 lds\n\t"
      "s_and_b32 s91, s45, 3\n\t"
      "s_lshl_b32 s91, s91, 10\n\t"
      "s_waitcnt lgkmcnt(8)\n\t"
      "v_mfma_f32_32x32x16_bf16 v[128:143], v[124:127], v[120:123], v[128:143]\n\t"
      "ds_read_b64_tr_b16 v[76:77], v32 offset:32768\n\t"
      "ds_read_b64_tr_b16 v[78:79], v32 offset:33792\n\t"
      "ds_read_b128 v[80:83], v33 offset:13056\n\t"
      "v_add_u32 v12, s91, v2\n\t"
      "s_waitcnt lgkmcnt(7)\n\t"
      "v_mfma_f32_32x32x16_bf16 a[0:15], v[60:63], v[192:195], a[0:15]\n\t"
      "ds_read_b64_tr_b16 v[104:105], v28 offset:36864\n\t"
      "ds_read_b64_tr_b16 v[106:107], v28 offset:37888\n\t"
      "s_bfe_u32 s91, s45, 0x10001\n\t"
      "s_add_u32 s91, s91, 4\n\t"
      "v_mfma_f32_32x32x16_bf16 a[16:31], v[64:67], v[192:195], a[16:31]\n\t"
      "ds_read_b64_tr_b16 v[108:109], v29 offset:36864\n\t"
      "ds_read_b64_tr_b16 v[110:111], v29 offset:37888\n\t"
      "buffer_load_dwordx4 v41, s[20:23], s48 offen offset:2048 lds\n\t"
      "s_lshl_b32 s91, s91, 10\n\t"
      "s_and_b32 s92, s45, 1\n\t"
      "s_waitcnt lgkmcnt(7)\n\t"
      "v_mfma_f32_32x32x16_bf16 a[32:47], v[68:71], v[192:195], a[32:47]\n\t"
      "ds_read_b64_tr_b16 v[112:113], v30 offset:36864\n\t"
      "ds_read_b64_tr_b16 v[114:115], v30 offset:37888\n\t"
      "s_lshl_b32 s92, s92, 9\n\t"
      "s_add_u32 s91, s91, s92\n\t"
      "v_mfma_f32_32x32x16_bf16 a[48:63], v[72:75], v[192:195], a[48:63]\n\t"
      "ds_read_b64_tr_b16 v[116:117], v31 offset:36864\n\t"
      "ds_read_b64_tr_b16 v[118:119], v31 offset:37888\n\t"
      "buffer_load_dwordx4 %[tid], s[20:23], s48 offen offset:3072 lds\n\t"
      "v_and_b32 v24, 2147483648, v27\n\t"
      "v_add3_u32 v13, v24, v59, s91\n\t"
      "s_waitcnt lgkmcnt(8)\n\t"
      "v_mfma_f32_32x32x16_bf16 v[128:143], v[80:83], v[76:79], v[128:143]\n\t"
      "ds_read_b64_tr_b16 v[120:121], v32 offset:36864\n\t"
      "ds_read_b64_tr_b16 v[122:123], v32 offset:37888\n\t"
      "ds_read_b128 v[124:127], v34 offset:13056\n\t"
      "s_bfe_u32 s91, s45, 0x10001\n\t"
      "s_waitcnt lgkmcnt(7)\n\t"
      "v_mfma_f32_32x32x16_bf16 a[0:15], v[104:107], v[196:199], a[0:15]\n\t"
      "ds_read_b64_tr_b16 v[60:61], v28 offset:40960\n\t"
      "ds_read_b64_tr_b16 v[62:63], v28 offset:41984\n\t"
      "s_lshl_b32 s91, s91, 9\n\t"
      "s_lshr_b32 s92, s92, 6\n\t"
      "v_mfma_f32_32x32x16_bf16 a[16:31], v[108:111], v[196:199], a[16:31]\n\t"
      "ds_read_b64_tr_b16 v[64:65], v29 offset:40960\n\t"
      "ds_read_b64_tr_b16 v[66:67], v29 offset:41984\n\t"
      "s_bfe_u32 s48, s100, 262168\n\t"
      "s_add_u32 m0, s56, 135424\n\t"
      "s_mul_i32 s48, s48, 128\n\t"
      "s_add_u32 s48, s48, s53\n\t"
      "buffer_load_dwordx4 v48, s[24:27], s48 offen offset:0 lds\n\t"
      "s_waitcnt lgkmcnt(7)\n\t"
      "v_mfma_f32_32x32x16_bf16 a[32:47], v[112:115], v[196:199], a[32:47]\n\t"
      "ds_read_b64_tr_b16 v[68:69], v30 offset:40960\n\t"
      "ds_read_b64_tr_b16 v[70:71], v30 offset:41984\n\t"
      "s_add_u32 s91, s91, s92\n\t"
      "s_add_u32 s91, s91, 6144\n\t"
      "v_mfma_f32_32x32x16_bf16 a[48:63], v[116:119], v[196:199], a[48:63]\n\t"
      "ds_read_b64_tr_b16 v[72:73], v31 offset:40960\n\t"
      "ds_read_b64_tr_b16 v[74:75], v31 offset:41984\n\t"
      "v_xor_b32 v24, 2147483648, v24\n\t"
      "v_add3_u32 v14, v24, v59, s91\n\t"
      "s_lshl_b32 s91, s45, 7\n\t"
      "s_waitcnt lgkmcnt(8)\n\t"
      "v_mfma_f32_32x32x16_bf16 v[128:143], v[124:127], v[120:123], v[128:143]\n\t"
      "ds_read_b64_tr_b16 v[76:77], v32 offset:40960\n\t"
      "ds_read_b64_tr_b16 v[78:79], v32 offset:41984\n\t"
      "ds_read_b128 v[80:83], v35 offset:13056\n\t"
      "s_add_u32 s91, s91, 4096\n\t"
      "s_waitcnt lgkmcnt(7)\n\t"
      "v_mfma_f32_32x32x16_bf16 a[0:15], v[60:63], v[200:203], a[0:15]\n\t"
      "ds_read_b64_tr_b16 v[104:105], v28 offset:45056\n\t"
      "ds_read_b64_tr_b16 v[106:107], v28 offset:46080\n\t"
      "v_and_b32 v24, 15, v1\n\t"
      "v_lshl_add_u32 v6, v24, 2, s91\n\t"
      "v_mfma_f32_32x32x16_bf16 a[16:31], v[64:67], v[200:203], a[16:31]\n\t"
      "ds_read_b64_tr_b16 v[108:109], v29 offset:45056\n\t"
      "ds_read_b64_tr_b16 v[110:111], v29 offset:46080\n\t"
      "s_lshl_b32 s91, s93, 3\n\t"
      "s_add_u32 s91, s91, s57\n\t"
      "s_lshl_b32 s91, s91, 7\n\t"
      "s_waitcnt lgkmcnt(7)\n\t"
      "v_mfma_f32_32x32x16_bf16 a[32:47], v[68:71], v[200:203], a[32:47]\n\t"
      "ds_read_b64_tr_b16 v[112:113], v30 offset:45056\n\t"
      "ds_read_b64_tr_b16 v[114:115], v30 offset:46080\n\t"
      "s_add_u32 s91, s91, 65536\n\t"
      "v_mfma_f32_32x32x16_bf16 a[48:63], v[72:75], v[200:203], a[48:63]\n\t"
      "ds_read_b64_tr_b16 v[116:117], v31 offset:45056\n\t"
      "ds_read_b64_tr_b16 v[118:119], v31 offset:46080\n\t"
      "s_cmp_lt_u32 s46, 14\n\t"
      "s_cselect_b32 exec_lo, -1, 0\n\t"
      "s_mov_b32 exec_hi, 0\n\t"
      "v_lshl_add_u32 v6, v1, 2, s91\n\t"
      "s_mov_b64 exec, -1\n\t"
      "s_waitcnt lgkmcnt(8)\n\t"
      "v_mfma_f32_32x32x16_bf16 v[128:143], v[80:83], v[76:79], v[128:143]\n\t"
      "ds_read_b64_tr_b16 v[120:121], v32 offset:45056\n\t"
      "ds_read_b64_tr_b16 v[122:123], v32 offset:46080\n\t"
      "ds_read_b128 v[124:127], v36 offset:13056\n\t"
      "s_lshl_b32 s91, s57, 4\n\t"
      "s_waitcnt vmcnt(13)\n\t"
      "s_barrier\n\t"
      "s_waitcnt lgkmcnt(7)\n\t"
      "v_mfma_f32_32x32x16_bf16 a[0:15], v[104:107], v[204:207], a[0:15]\n\t"
      "v_add_u32 v28, 65536, v28\n\t"
      "ds_read_b64_tr_b16 v[60:61], v28 offset:0\n\t"
      "ds_read_b64_tr_b16 v[62:63], v28 offset:1024\n\t"
      "s_add_u32 s91, s91, s45\n\t"
      "v_mfma_f32_32x32x16_bf16 a[16:31], v[108:111], v[204:207], a[16:31]\n\t"
      "v_add_u32 v29, 65536, v29\n\t"
      "ds_read_b64_tr_b16 v[64:65], v29 offset:0\n\t"
      "ds_read_b64_tr_b16 v[66:67], v29 offset:1024\n\t"
      "s_lshl_b32 s91, s91, 1\n\t"
      "s_add_u32 s91, s91, s58\n\t"
      "s_waitcnt lgkmcnt(7)\n\t"
      "v_mfma_f32_32x32x16_bf16 a[32:47], v[112:115], v[204:207], a[32:47]\n\t"
      "v_add_u32 v30, 65536, v30\n\t"
      "ds_read_b64_tr_b16 v[68:69], v30 offset:0\n\t"
      "ds_read_b64_tr_b16 v[70:71], v30 offset:1024\n\t"
      "s_lshl_b32 s91, s91, 2\n\t"
      "v_mfma_f32_32x32x16_bf16 a[48:63], v[116:119], v[204:207], a[48:63]\n\t"
      "v_add_u32 v31, 65536, v31\n\t"
      "ds_read_b64_tr_b16 v[72:73], v31 offset:0\n\t"
      "ds_read_b64_tr_b16 v[74:75], v31 offset:1024\n\t"
      "s_add_u32 s91, s91, 65536\n\t"
      "v_mov_b32 v7, 2147483648\n\t"
      "s_waitcnt lgkmcnt(8)\n\t"
      "v_mfma_f32_32x32x16_bf16 v[128:143], v[124:127], v[120:123], v[128:143]\n\t"
      "v_add_u32 v32, 65536, v32\n\t"
      "ds_read_b64_tr_b16 v[76:77], v32 offset:0\n\t"
      "ds_read_b64_tr_b16 v[78:79], v32 offset:1024\n\t"
      "ds_read_b128 v[80:83], v33 offset:17408\n\t"
      "s_waitcnt lgkmcnt(7)\n\t"
      "v_mfma_f32_32x32x16_bf16 a[0:15], v[60:63], v[208:211], a[0:15]\n\t"
      "ds_read_b64_tr_b16 v[104:105], v28 offset:4096\n\t"
      "ds_read_b64_tr_b16 v[106:107], v28 offset:5120\n\t"
      "s_cmp_lt_u32 s46, 14\n\t"
      "s_cselect_b32 s70, 0xff, 0\n\t"
      "v_mfma_f32_32x32x16_bf16 a[16:31], v[64:67], v[208:211], a[16:31]\n\t"
      "ds_read_b64_tr_b16 v[108:109], v29 offset:4096\n\t"
      "ds_read_b64_tr_b16 v[110:111], v29 offset:5120\n\t"
      "s_waitcnt vmcnt(11)\n\t"
      "ds_read_b64_tr_b16 v[84:85], v51 offset:0\n\t"
      "ds_read_b64_tr_b16 v[86:87], v51 offset:1024\n\t"
      "s_waitcnt lgkmcnt(9)\n\t"
      "v_mfma_f32_32x32x16_bf16 a[32:47], v[68:71], v[208:211], a[32:47]\n\t"
      "ds_read_b64_tr_b16 v[112:113], v30 offset:4096\n\t"
      "ds_read_b64_tr_b16 v[114:115], v30 offset:5120\n\t"
      "s_cmp_eq_u32 s44, 0\n\t"
      "s_cselect_b32 s70, s70, 0\n\t"
      "s_cselect_b32 s69, 0x100, 0\n\t"
      "v_mfma_f32_32x32x16_bf16 a[48:63], v[72:75], v[208:211], a[48:63]\n\t"
      "ds_read_b64_tr_b16 v[116:117], v31 offset:4096\n\t"
      "ds_read_b64_tr_b16 v[118:119], v31 offset:5120\n\t"
      "ds_read_b64_tr_b16 v[88:89], v58 offset:0\n\t"
      "ds_read_b64_tr_b16 v[90:91], v58 offset:1024\n\t"
      "s_waitcnt lgkmcnt(12)\n\t"
      "v_mfma_f32_32x32x16_bf16 v[128:143], v[80:83], v[76:79], v[128:143]\n\t"
      "ds_read_b64_tr_b16 v[120:121], v32 offset:4096\n\t"
      "ds_read_b64_tr_b16 v[122:123], v32 offset:5120\n\t"
      "ds_read_b128 v[124:127], v34 offset:17408\n\t"
      "s_mov_b32 exec_lo, s70\n\t"
      "s_mov_b32 exec_hi, 0\n\t"
      "v_lshl_add_u32 v7, v1, 10, s91\n\t"
      "s_mov_b64 exec, -1\n\t"
      "s_lshl_b32 s91, s46, 5\n\t"
      "s_waitcnt lgkmcnt(11)\n\t"
      "v_mfma_f32_32x32x16_bf16 a[0:15], v[104:107], v[212:215], a[0:15]\n\t"
      "ds_read_b64_tr_b16 v[60:61], v28 offset:8192\n\t"
      "ds_read_b64_tr_b16 v[62:63], v28 offset:9216\n\t"
      "s_add_u32 s91, s91, s45\n\t"
      "s_lshl_b32 s91, s91, 2\n\t"
      "v_mfma_f32_32x32x16_bf16 a[16:31], v[108:111], v[212:215], a[16:31]\n\t"
      "ds_read_b64_tr_b16 v[64:65], v29 offset:8192\n\t"
      "ds_read_b64_tr_b16 v[66:67], v29 offset:9216\n\t"
      "buffer_load_dwordx4 v[92:95], v23, s[28:31], 0 offen sc1\n\t"
      "s_add_u32 s91, s91, 4096\n\t"
      "s_waitcnt lgkmcnt(9)\n\t"
      "v_mfma_f32_32x32x16_bf16 a[32:47], v[112:115], v[212:215], a[32:47]\n\t"
      "ds_read_b64_tr_b16 v[68:69], v30 offset:8192\n\t"
      "ds_read_b64_tr_b16 v[70:71], v30 offset:9216\n\t"
      "s_mov_b32 exec_lo, s69\n\t"
      "s_mov_b32 exec_hi, 0\n\t"
      "v_mov_b32 v7, s91\n\t"
      "s_mov_b64 exec, -1\n\t"
      "v_mfma_f32_32x32x16_bf16 a[48:63], v[116:119], v[212:215], a[48:63]\n\t"
      "ds_read_b64_tr_b16 v[72:73], v31 offset:8192\n\t"
      "ds_read_b64_tr_b16 v[74:75], v31 offset:9216\n\t"
      "s_cmp_lt_u32 s46, 14\n\t"
      "s_cselect_b32 s69, s56, 0x30000\n\t"
      "s_cselect_b32 s70, 0x1000, 0\n\t"
      "s_cselect_b32 s18, 27262976, 0\n\t"
      "s_mov_b32 s16, s28\n\t"
      "s_waitcnt lgkmcnt(8)\n\t"
      "v_mfma_f32_32x32x16_bf16 v[128:143], v[124:127], v[120:123], v[128:143]\n\t"
      "ds_read_b64_tr_b16 v[76:77], v32 offset:8192\n\t"
      "ds_read_b64_tr_b16 v[78:79], v32 offset:9216\n\t"
      "ds_read_b128 v[80:83], v35 offset:17408\n\t"
      "s_mov_b32 s17, s29\n\t"
      "s_waitcnt lgkmcnt(7)\n\t"
      "v_mfma_f32_32x32x16_bf16 a[0:15], v[60:63], v[216:219], a[0:15]\n\t"
      "ds_read_b64_tr_b16 v[104:105], v28 offset:12288\n\t"
      "ds_read_b64_tr_b16 v[106:107], v28 offset:13312\n\t"
      "s_lshl_b32 s71, s46, 17\n\t"
      "s_add_u32 s71, s71, s56\n\t"
      "v_mfma_f32_32x32x16_bf16 a[16:31], v[64:67], v[216:219], a[16:31]\n\t"
      "ds_read_b64_tr_b16 v[108:109], v29 offset:12288\n\t"
      "ds_read_b64_tr_b16 v[110:111], v29 offset:13312\n\t"
      "s_add_u32 s71, s71, 9437184\n\t"
      "s_and_b32 s91, s46, 3\n\t"
      "s_lshl_b32 s92, s91, 15\n\t"
      "s_waitcnt lgkmcnt(7)\n\t"
      "v_mfma_f32_32x32x16_bf16 a[32:47], v[68:71], v[216:219], a[32:47]\n\t"
      "ds_read_b64_tr_b16 v[112:113], v30 offset:12288\n\t"
      "ds_read_b64_tr_b16 v[114:115], v30 offset:13312\n\t"
      "s_xor_b32 s71, s71, s92\n\t"
      "s_lshl_b32 s81, s59, 15\n\t"
      "v_mfma_f32_32x32x16_bf16 a[48:63], v[72:75], v[216:219], a[48:63]\n\t"
      "ds_read_b64_tr_b16 v[116:117], v31 offset:12288\n\t"
      "ds_read_b64_tr_b16 v[118:119], v31 offset:13312\n\t"
      "s_add_u32 s81, s81, 7340032\n\t"
      "v_bfe_u32 v24, v1, 2, 2\n\t"
      "v_and_b32 v25, 3, v1\n\t"
      "s_waitcnt lgkmcnt(8)\n\t"
      "v_mfma_f32_32x32x16_bf16 v[128:143], v[80:83], v[76:79], v[128:143]\n\t"
      "ds_read_b64_tr_b16 v[120:121], v32 offset:12288\n\t"
      "ds_read_b64_tr_b16 v[122:123], v32 offset:13312\n\t"
      "ds_read_b128 v[124:127], v36 offset:17408\n\t"
      "v_lshl_add_u32 v24, v24, 3, v25\n\t"
      "s_waitcnt vmcnt(6)\n\t"
      "s_barrier\n\t"
      "s_waitcnt lgkmcnt(7)\n\t"
      "v_mfma_f32_32x32x16_bf16 a[0:15], v[104:107], v[220:223], a[0:15]\n\t"
      "v_add_u32 v28, 4294901760, v28\n\t"
      "ds_read_b64_tr_b16 v[60:61], v28 offset:49152\n\t"
      "ds_read_b64_tr_b16 v[62:63], v28 offset:50176\n\t"
      "s_add_u32 s92, s81, 1008\n\t"
      "v_mfma_f32_32x32x16_bf16 a[16:31], v[108:111], v[220:223], a[16:31]\n\t"
      "v_add_u32 v29, 4294901760, v29\n\t"
      "ds_read_b64_tr_b16 v[64:65], v29 offset:49152\n\t"
      "ds_read_b64_tr_b16 v[66:67], v29 offset:50176\n\t"
      "v_lshl_add_u32 v18, v24, 10, s92\n\t"
      "s_lshl_b32 s91, s91, 13\n\t"
      "s_waitcnt lgkmcnt(7)\n\t"
      "v_mfma_f32_32x32x16_bf16 a[32:47], v[112:115], v[220:223], a[32:47]\n\t"
      "v_add_u32 v30, 4294901760, v30\n\t"
      "ds_read_b64_tr_b16 v[68:69], v30 offset:49152\n\t"
      "ds_read_b64_tr_b16 v[70:71], v30 offset:50176\n\t"
      "s_xor_b32 s81, s81, s91\n\t"
      "v_mfma_f32_32x32x16_bf16 a[48:63], v[116:119], v[220:223], a[48:63]\n\t"
      "v_add_u32 v31, 4294901760, v31\n\t"
      "ds_read_b64_tr_b16 v[72:73], v31 offset:49152\n\t"
      "ds_read_b64_tr_b16 v[74:75], v31 offset:50176\n\t"
      "v_lshlrev_b32 v25, 4, v25\n\t"
      "v_mov_b32 v15, 155140\n\t"
      "s_waitcnt lgkmcnt(8)\n\t"
      "v_mfma_f32_32x32x16_bf16 v[128:143], v[124:127], v[120:123], v[128:143]\n\t"
      "v_add_u32 v32, 4294901760, v32\n\t"
      "ds_read_b64_tr_b16 v[76:77], v32 offset:49152\n\t"
      "ds_read_b64_tr_b16 v[78:79], v32 offset:50176\n\t"
      "ds_read_b128 v[80:83], v33 offset:0\n\t"
      "s_waitcnt vmcnt(0)\n\t"
      "v_xor_b32 v92, s47, v92\n\t"
      "v_xor_b32 v93, s47, v93\n\t"
      "v_xor_b32 v94, s47, v94\n\t"
      "v_xor_b32 v95, s47, v95\n\t"
      "v_or3_b32 v92, v92, v93, v94\n\t"
      "v_or_b32 v92, v92, v95\n\t"
      "v_cmp_ne_u32 vcc, 0, v92\n\t"
      "s_nop 0\n\t"
      "s_cbranch_vccnz zp_%=\n\t"
      "zr_%=:\n\t"
      "buffer_load_dword v58, v53, s[28:31], 0 offen sc1\n\t"
      "s_waitcnt lgkmcnt(7)\n\t"
      "v_mfma_f32_32x32x16_bf16 a[0:15], v[60:63], v[224:227], a[0:15]\n\t"
      "ds_read_b64_tr_b16 v[104:105], v28 offset:53248\n\t"
      "ds_read_b64_tr_b16 v[106:107], v28 offset:54272\n\t"
      "buffer_load_dwordx4 a[96:99], v2, s[12:15], 0 offen offset:0 sc1\n\t"
      "v_add_u32 v16, v25, v15\n\t"
      "v_mfma_f32_32x32x16_bf16 a[16:31], v[64:67], v[224:227], a[16:31]\n\t"
      "ds_read_b64_tr_b16 v[108:109], v29 offset:53248\n\t"
      "ds_read_b64_tr_b16 v[110:111], v29 offset:54272\n\t"
      "buffer_load_dwordx4 a[100:103], v2, s[12:15], 0 offen offset:1024 sc1\n\t"
      "v_add_u32 v17, 0x10000, v2\n\t"
      "s_mul_i32 s72, s57, 229376\n\t"
      "s_waitcnt lgkmcnt(7)\n\t"
      "v_mfma_f32_32x32x16_bf16 a[32:47], v[68:71], v[224:227], a[32:47]\n\t"
      "ds_read_b64_tr_b16 v[112:113], v30 offset:53248\n\t"
      "ds_read_b64_tr_b16 v[114:115], v30 offset:54272\n\t"
      "s_xor_b32 s49, s68, 6144\n\t"
      "buffer_load_dwordx4 a[64:67], v2, s[28:31], s49 offen offset:0 sc1\n\t"
      "v_mfma_f32_32x32x16_bf16 a[48:63], v[72:75], v[224:227], a[48:63]\n\t"
      "ds_read_b64_tr_b16 v[116:117], v31 offset:53248\n\t"
      "ds_read_b64_tr_b16 v[118:119], v31 offset:54272\n\t"
      "buffer_load_dwordx4 a[68:71], v2, s[28:31], s49 offen offset:1024 sc1\n\t"
      "s_mul_i32 s91, s44, 14336\n\t"
      "s_add_u32 s72, s72, s91\n\t"
      "s_waitcnt lgkmcnt(8)\n\t"
      "v_mfma_f32_32x32x16_bf16 v[128:143], v[80:83], v[76:79], v[128:143]\n\t"
      "ds_read_b64_tr_b16 v[120:121], v32 offset:53248\n\t"
      "ds_read_b64_tr_b16 v[122:123], v32 offset:54272\n\t"
      "ds_read_b128 v[124:127], v34 offset:0\n\t"
      "s_xor_b32 s49, s68, 4096\n\t"
      "buffer_load_dwordx4 a[72:75], v2, s[28:31], s49 offen offset:0 sc1\n\t"
      "s_waitcnt lgkmcnt(7)\n\t"
      "v_mfma_f32_32x32x16_bf16 a[0:15], v[104:107], v[228:231], a[0:15]\n\t"
      "ds_read_b64_tr_b16 v[60:61], v28 offset:57344\n\t"
      "ds_read_b64_tr_b16 v[62:63], v28 offset:58368\n\t"
      "buffer_load_dwordx4 a[76:79], v2, s[28:31], s49 offen offset:1024 sc1\n\t"
      "s_add_u32 s72, s72, 11533312\n\t"
      "v_mfma_f32_32x32x16_bf16 a[16:31], v[108:111], v[228:231], a[16:31]\n\t"
      "ds_read_b64_tr_b16 v[64:65], v29 offset:57344\n\t"
      "ds_read_b64_tr_b16 v[66:67], v29 offset:58368\n\t"
      "s_xor_b32 s49, s68, 2048\n\t"
      "buffer_load_dwordx4 a[80:83], v2, s[28:31], s49 offen offset:0 sc1\n\t"
      "s_waitcnt lgkmcnt(7)\n\t"
      "v_mfma_f32_32x32x16_bf16 a[32:47], v[112:115], v[228:231], a[32:47]\n\t"
      "ds_read_b64_tr_b16 v[68:69], v30 offset:57344\n\t"
      "ds_read_b64_tr_b16 v[70:71], v30 offset:58368\n\t"
      "buffer_load_dwordx4 a[84:87], v2, s[28:31], s49 offen offset:1024 sc1\n\t"
      "v_mfma_f32_32x32x16_bf16 a[48:63], v[116:119], v[228:231], a[48:63]\n\t"
      "ds_read_b64_tr_b16 v[72:73], v31 offset:57344\n\t"
      "ds_read_b64_tr_b16 v[74:75], v31 offset:58368\n\t"
      "s_xor_b32 s49, s68, 0\n\t"
      "buffer_load_dwordx4 a[88:91], v2, s[28:31], s49 offen offset:0 sc1\n\t"
      "s_waitcnt lgkmcnt(8)\n\t"
      "v_mfma_f32_32x32x16_bf16 v[128:143], v[124:127], v[120:123], v[128:143]\n\t"
      "ds_read_b64_tr_b16 v[76:77], v32 offset:57344\n\t"
      "ds_read_b64_tr_b16 v[78:79], v32 offset:58368\n\t"
      "ds_read_b128 v[80:83], v35 offset:0\n\t"
      "buffer_load_dwordx4 a[92:95], v2, s[28:31], s49 offen offset:1024 sc1\n\t"
      "s_waitcnt lgkmcnt(7)\n\t"
      "v_mfma_f32_32x32x16_bf16 a[0:15], v[60:63], v[232:235], a[0:15]\n\t"
      "ds_read_b64_tr_b16 v[104:105], v28 offset:61440\n\t"
      "ds_read_b64_tr_b16 v[106:107], v28 offset:62464\n\t"
      "s_bitcmp1_b32 s44, 0\n\t"
      "s_cselect_b32 s92, 0, 0x400\n\t"
      "v_mfma_f32_32x32x16_bf16 a[16:31], v[64:67], v[232:235], a[16:31]\n\t"
      "ds_read_b64_tr_b16 v[108:109], v29 offset:61440\n\t"
      "ds_read_b64_tr_b16 v[110:111], v29 offset:62464\n\t"
      "buffer_load_dwordx4 a[104:107], v2, s[12:15], 0 offen offset:2048 sc1\n\t"
      "s_sub_u32 s91, 0x800, s92\n\t"
      "v_add_u32 v8, s91, v2\n\t"
      "s_waitcnt lgkmcnt(7)\n\t"
      "v_mfma_f32_32x32x16_bf16 a[32:47], v[68:71], v[232:235], a[32:47]\n\t"
      "ds_read_b64_tr_b16 v[112:113], v30 offset:61440\n\t"
      "ds_read_b64_tr_b16 v[114:115], v30 offset:62464\n\t"
      "v_add_u32 v9, s92, v2\n\t"
      "v_lshrrev_b32 v24, 1, v2\n\t"
      "v_mfma_f32_32x32x16_bf16 a[48:63], v[72:75], v[232:235], a[48:63]\n\t"
      "ds_read_b64_tr_b16 v[116:117], v31 offset:61440\n\t"
      "ds_read_b64_tr_b16 v[118:119], v31 offset:62464\n\t"
      "buffer_load_dwordx4 a[108:111], v2, s[12:15], 0 offen offset:3072 sc1\n\t"
      "v_and_b32 v25, 2147483648, v27\n\t"
      "v_or_b32 v10, v24, v25\n\t"
      "s_waitcnt lgkmcnt(8)\n\t"
      "v_mfma_f32_32x32x16_bf16 v[128:143], v[80:83], v[76:79], v[128:143]\n\t"
      "ds_read_b64_tr_b16 v[120:121], v32 offset:61440\n\t"
      "ds_read_b64_tr_b16 v[122:123], v32 offset:62464\n\t"
      "ds_read_b128 v[124:127], v36 offset:0\n\t"
      "s_lshl_b32 s73, s46, 10\n\t"
      "s_waitcnt vmcnt(14)\n\t"
      "s_barrier\n\t"
      "s_waitcnt lgkmcnt(7)\n\t"
      "v_mfma_f32_32x32x16_bf16 a[0:15], v[104:107], v[236:239], a[0:15]\n\t"
      "ds_read_b64_tr_b16 v[60:61], v28 offset:0\n\t"
      "ds_read_b64_tr_b16 v[62:63], v28 offset:1024\n\t"
      "s_add_u32 s73, s73, 131072\n\t"
      "s_xor_b32 s91, s44, 3\n\t"
      "v_mfma_f32_32x32x16_bf16 a[16:31], v[108:111], v[236:239], a[16:31]\n\t"
      "ds_read_b64_tr_b16 v[64:65], v29 offset:0\n\t"
      "ds_read_b64_tr_b16 v[66:67], v29 offset:1024\n\t"
      "s_movk_i32 s49, 4096\n\t"
      "buffer_load_dwordx4 a[112:115], v2, s[12:15], s49 offen offset:0 sc1\n\t"
      "s_mul_i32 s91, s91, 12288\n\t"
      "s_waitcnt lgkmcnt(7)\n\t"
      "v_mfma_f32_32x32x16_bf16 a[32:47], v[112:115], v[236:239], a[32:47]\n\t"
      "ds_read_b64_tr_b16 v[68:69], v30 offset:0\n\t"
      "ds_read_b64_tr_b16 v[70:71], v30 offset:1024\n\t"
      "s_add_u32 s74, s91, 114688\n\t"
      "s_xor_b32 s91, s44, 2\n\t"
      "v_mfma_f32_32x32x16_bf16 a[48:63], v[116:119], v[236:239], a[48:63]\n\t"
      "ds_read_b64_tr_b16 v[72:73], v31 offset:0\n\t"
      "ds_read_b64_tr_b16 v[74:75], v31 offset:1024\n\t"
      "buffer_load_dwordx4 a[116:119], v2, s[12:15], s49 offen offset:1024 sc1\n\t"
      "s_mul_i32 s91, s91, 12288\n\t"
      "s_add_u32 s75, s91, 118784\n\t"
      "s_waitcnt lgkmcnt(8)\n\t"
      "v_mfma_f32_32x32x16_bf16 v[128:143], v[124:127], v[120:123], v[128:143]\n\t"
      "ds_read_b64_tr_b16 v[76:77], v32 offset:0\n\t"
      "ds_read_b64_tr_b16 v[78:79], v32 offset:1024\n\t"
      "ds_read_b128 v[80:83], v33 offset:4352\n\t"
      "s_xor_b32 s91, s44, 1\n\t"
      "s_waitcnt lgkmcnt(7)\n\t"
      "v_mfma_f32_32x32x16_bf16 a[0:15], v[60:63], v[240:243], a[0:15]\n\t"
      "ds_read_b64_tr_b16 v[104:105], v28 offset:4096\n\t"
      "ds_read_b64_tr_b16 v[106:107], v28 offset:5120\n\t"
      "s_mul_i32 s91, s91, 12288\n\t"
      "s_add_u32 s76, s91, 122880\n\t"
      "v_mfma_f32_32x32x16_bf16 a[16:31], v[64:67], v[240:243], a[16:31]\n\t"
      "ds_read_b64_tr_b16 v[108:109], v29 offset:4096\n\t"
      "ds_read_b64_tr_b16 v[110:111], v29 offset:5120\n\t"
      "buffer_load_dwordx4 a[120:123], v2, s[12:15], s49 offen offset:2048 sc1\n\t"
      "s_mul_i32 s91, s44, 12288\n\t"
      "s_add_u32 s77, s91, 114688\n\t"
      "s_waitcnt lgkmcnt(7)\n\t"
      "v_mfma_f32_32x32x16_bf16 a[32:47], v[68:71], v[240:243], a[32:47]\n\t"
      "ds_read_b64_tr_b16 v[112:113], v30 offset:4096\n\t"
      "ds_read_b64_tr_b16 v[114:115], v30 offset:5120\n\t"
      "s_mul_i32 s82, s59, 147456\n\t"
      "s_lshl_b32 s91, s46, 11\n\t"
      "v_mfma_f32_32x32x16_bf16 a[48:63], v[72:75], v[240:243], a[48:63]\n\t"
      "ds_read_b64_tr_b16 v[116:117], v31 offset:4096\n\t"
      "ds_read_b64_tr_b16 v[118:119], v31 offset:5120\n\t"
      "buffer_load_dwordx4 a[124:127], v2, s[12:15], s49 offen offset:3072 sc1\n\t"
      "s_add_u32 s82, s82, s91\n\t"
      "s_add_u32 s82, s82, 13631488\n\t"
      "s_waitcnt lgkmcnt(8)\n\t"
      "v_mfma_f32_32x32x16_bf16 v[128:143], v[80:83], v[76:79], v[128:143]\n\t"
      "ds_read_b64_tr_b16 v[120:121], v32 offset:4096\n\t"
      "ds_read_b64_tr_b16 v[122:123], v32 offset:5120\n\t"
      "ds_read_b128 v[124:127], v34 offset:4352\n\t"
      "s_lshl_b32 s92, s45, 1\n\t"
      "s_waitcnt lgkmcnt(7)\n\t"
      "v_mfma_f32_32x32x16_bf16 a[0:15], v[104:107], v[244:247], a[0:15]\n\t"
      "ds_read_b64_tr_b16 v[60:61], v28 offset:8192\n\t"
      "ds_read_b64_tr_b16 v[62:63], v28 offset:9216\n\t"
      "s_add_u32 s92, s92, s58\n\t"
      "s_lshl_b32 s92, s92, 1\n\t"
      "v_mfma_f32_32x32x16_bf16 a[16:31], v[108:111], v[244:247], a[16:31]\n\t"
      "ds_read_b64_tr_b16 v[64:65], v29 offset:8192\n\t"
      "ds_read_b64_tr_b16 v[66:67], v29 offset:9216\n\t"
      "s_movk_i32 s49, 8192\n\t"
      "buffer_load_dwordx4 a[128:131], v2, s[12:15], s49 offen offset:0 sc1\n\t"
      "s_lshr_b32 s91, s44, 1\n\t"
      "s_waitcnt lgkmcnt(7)\n\t"
      "v_mfma_f32_32x32x16_bf16 a[32:47], v[112:115], v[244:247], a[32:47]\n\t"
      "ds_read_b64_tr_b16 v[68:69], v30 offset:8192\n\t"
      "ds_read_b64_tr_b16 v[70:71], v30 offset:9216\n\t"
      "s_add_u32 s92, s92, s91\n\t"
      "s_mul_i32 s91, s92, 147456\n\t"
      "v_mfma_f32_32x32x16_bf16 a[48:63], v[116:119], v[244:247], a[48:63]\n\t"
      "ds_read_b64_tr_b16 v[72:73], v31 offset:8192\n\t"
      "ds_read_b64_tr_b16 v[74:75], v31 offset:9216\n\t"
      "buffer_load_dwordx4 a[132:135], v2, s[12:15], s49 offen offset:1024 sc1\n\t"
      "v_add_u32 v19, s91, v2\n\t"
      "s_add_u32 s91, s91, 13774848\n\t"
      "s_waitcnt lgkmcnt(8)\n\t"
      "v_mfma_f32_32x32x16_bf16 v[128:143], v[124:127], v[120:123], v[128:143]\n\t"
      "ds_read_b64_tr_b16 v[76:77], v32 offset:8192\n\t"
      "ds_read_b64_tr_b16 v[78:79], v32 offset:9216\n\t"
      "ds_read_b128 v[80:83], v35 offset:4352\n\t"
      "v_add_u32 v21, s91, v59\n\t"
      "s_waitcnt lgkmcnt(7)\n\t"
      "v_mfma_f32_32x32x16_bf16 a[0:15], v[60:63], v[248:251], a[0:15]\n\t"
      "ds_read_b64_tr_b16 v[104:105], v28 offset:12288\n\t"
      "ds_read_b64_tr_b16 v[106:107], v28 offset:13312\n\t"
      "s_xor_b32 s91, s92, 1\n\t"
      "s_mul_i32 s91, s91, 147456\n\t"
      "v_mfma_f32_32x32x16_bf16 a[16:31], v[64:67], v[248:251], a[16:31]\n\t"
      "ds_read_b64_tr_b16 v[108:109], v29 offset:12288\n\t"
      "ds_read_b64_tr_b16 v[110:111], v29 offset:13312\n\t"
      "buffer_load_dwordx4 a[136:139], v2, s[12:15], s49 offen offset:2048 sc1\n\t"
      "v_add_u32 v20, s91, v2\n\t"
      "s_and_b32 s91, s44, 1\n\t"
      "s_waitcnt lgkmcnt(7)\n\t"
      "v_mfma_f32_32x32x16_bf16 a[32:47], v[68:71], v[248:251], a[32:47]\n\t"
      "ds_read_b64_tr_b16 v[112:113], v30 offset:12288\n\t"
      "ds_read_b64_tr_b16 v[114:115], v30 offset:13312\n\t"
      "s_lshl_b32 s83, s57, 1\n\t"
      "s_add_u32 s91, s91, s83\n\t"
      "v_mfma_f32_32x32x16_bf16 a[48:63], v[72:75], v[248:251], a[48:63]\n\t"
      "ds_read_b64_tr_b16 v[116:117], v31 offset:12288\n\t"
      "ds_read_b64_tr_b16 v[118:119], v31 offset:13312\n\t"
      "buffer_load_dwordx4 a[140:143], v2, s[12:15], s49 offen offset:3072 sc1\n\t"
      "s_lshl_b32 s83, s91, 11\n\t"
      "s_add_u32 s83, s83, 13746176\n\t"
      "s_waitcnt lgkmcnt(8)\n\t"
      "v_mfma_f32_32x32x16_bf16 v[128:143], v[80:83], v[76:79], v[128:143]\n\t"
      "ds_read_b64_tr_b16 v[120:121], v32 offset:12288\n\t"
      "ds_read_b64_tr_b16 v[122:123], v32 offset:13312\n\t"
      "ds_read_b128 v[124:127], v36 offset:4352\n\t"
      "s_mul_i32 s92, s92, 28672\n\t"
      "s_waitcnt lgkmcnt(7)\n\t"
      "v_mfma_f32_32x32x16_bf16 a[0:15], v[104:107], v[252:255], a[0:15]\n\t"
      "s_movk_i32 s49, 12288\n\t"
      "buffer_load_dwordx4 a[144:147], v2, s[12:15], s49 offen offset:0 sc1\n\t"
      "s_lshl_b32 s91, s91, 6\n\t"
      "s_add_u32 s91, s91, s92\n\t"
      "v_mfma_f32_32x32x16_bf16 a[16:31], v[108:111], v[252:255], a[16:31]\n\t"
      "buffer_load_dwordx4 a[148:151], v2, s[12:15], s49 offen offset:1024 sc1\n\t"
      "v_mul_u32_u24 v24, 56, v59\n\t"
      "v_and_b32 v25, 16, v27\n\t"
      "v_add3_u32 v22, v24, v25, s91\n\t"
      "s_mov_b32 s42, 1835008\n\t"
      "s_waitcnt lgkmcnt(3)\n\t"
      "v_mfma_f32_32x32x16_bf16 a[32:47], v[112:115], v[252:255], a[32:47]\n\t"
      "s_mov_b32 s43, 0x20000\n\t"
      "s_mul_i32 s91, s44, 7168\n\t"
      "s_add_u32 s91, s91, 13631488\n\t"
      "s_lshl_b32 s92, s57, 10\n\t"
      "v_mfma_f32_32x32x16_bf16 a[48:63], v[116:119], v[252:255], a[48:63]\n\t"
      "buffer_load_dwordx4 a[152:155], v2, s[12:15], s49 offen offset:2048 sc1\n\t"
      "s_add_u32 s84, s91, s92\n\t"
      "s_cmp_ge_u32 s57, 6\n\t"
      "s_cselect_b32 s92, -6, 1\n\t"
      "s_lshl_b32 s92, s92, 10\n\t"
      "s_waitcnt lgkmcnt(0)\n\t"
      "v_mfma_f32_32x32x16_bf16 v[128:143], v[124:127], v[120:123], v[128:143]\n\t"
      "s_add_u32 s85, s84, s92\n\t"
      "s_cmp_ge_u32 s57, 5\n\t"
      "s_cselect_b32 s92, -5, 2\n\t"
      "s_lshl_b32 s92, s92, 10\n\t"
      "s_add_u32 s86, s84, s92\n\t"
      "s_cmp_ge_u32 s57, 4\n\t"
      "s_cselect_b32 s92, -4, 3\n\t"
      "s_lshl_b32 s92, s92, 10\n\t"
      "s_add_u32 s87, s84, s92\n\t"
      "s_cmp_ge_u32 s57, 3\n\t"
      "s_cselect_b32 s92, -3, 4\n\t"
      "s_lshl_b32 s92, s92, 10\n\t"
      "s_add_u32 s88, s84, s92\n\t"
      "s_cmp_ge_u32 s57, 2\n\t"
      "s_cselect_b32 s92, -2, 5\n\t"
      "s_lshl_b32 s92, s92, 10\n\t"
      "s_add_u32 s89, s84, s92\n\t"
      "s_cmp_ge_u32 s57, 1\n\t"
      "s_cselect_b32 s92, -1, 6\n\t"
      "s_lshl_b32 s92, s92, 10\n\t"
      "s_add_u32 s90, s84, s92\n\t"
      "s_lshr_b32 s91, s55, 3\n\t"
      "s_lshl_b32 s91, s91, 1\n\t"
      "s_and_b32 s92, s55, 1\n\t"
      "s_add_u32 s91, s91, s92\n\t"
      "s_mul_i32 s92, s91, 37\n\t"
      "s_lshr_b32 s92, s92, 8\n\t"
      "s_mul_i32 s92, s92, 7\n\t"
      "s_sub_u32 s92, s91, s92\n\t"
      "s_cmp_ge_u32 s92, s57\n\t"
      "s_cselect_b32 s92, 0, 7\n\t"
      "s_add_u32 s91, s91, s92\n\t"
      "s_sub_u32 s91, s91, s57\n\t"
      "s_bfe_u32 s92, s55, 0x20001\n\t"
      "s_mul_i32 s92, s92, 28\n\t"
      "s_add_u32 s91, s91, s92\n\t"
      "s_mul_i32 s92, s57, 112\n\t"
      "s_add_u32 s91, s91, s92\n\t"
      "s_lshl_b32 s91, s91, 11\n\t"
      "s_add_u32 s91, s91, 11534336\n\t"
      "s_cmp_lt_u32 s45, 14\n\t"
      "s_cselect_b32 s91, s91, 2147483648\n\t"
      "v_add_u32 v52, s91, v2\n\t"
      "s_lshl_b32 s91, s44, 8\n\t"
      "s_add_u32 s91, s91, s73\n\t"
      "s_cmp_eq_u32 s45, 0\n\t"
      "s_cselect_b32 s91, s91, 0x40000000\n\t"
      "v_add_u32 v54, s91, v10\n\t"
      "s_and_b32 s91, s45, 3\n\t"
      "s_lshl_b32 s91, s91, 12\n\t"
      "v_add_u32 v55, s91, v2\n\t"
      "s_bfe_u32 s91, s45, 0x10001\n\t"
      "s_add_u32 s91, s91, 4\n\t"
      "s_lshl_b32 s91, s91, 12\n\t"
      "s_and_b32 s92, s45, 1\n\t"
      "s_lshl_b32 s92, s92, 9\n\t"
      "s_add_u32 s91, s91, s92\n\t"
      "v_and_b32 v24, 2147483648, v27\n\t"
      "v_add3_u32 v56, v24, v59, s91\n\t"
      "s_bfe_u32 s91, s45, 0x10001\n\t"
      "s_lshl_b32 s91, s91, 9\n\t"
      "s_lshr_b32 s92, s92, 6\n\t"
      "s_add_u32 s91, s91, s92\n\t"
      "s_add_u32 s91, s91, 24576\n\t"
      "v_xor_b32 v24, 2147483648, v24\n\t"
      "v_add3_u32 v57, v24, v59, s91\n\t"
      "s_lshl_b32 s55, s46, 17\n\t"
      "s_lshr_b32 s91, s45, 2\n\t"
      "s_lshl_b32 s91, s91, 15\n\t"
      "s_add_u32 s55, s55, s91\n\t"
      "s_add_u32 s55, s55, s56\n\t"
      "s_add_u32 s55, s55, 9437184\n\t"
      "s_lshr_b32 s50, %[bid], 3\n\t"
      "s_lshl_b32 s50, s50, 2\n\t"
      "s_add_u32 s50, s50, s44\n\t"
      "s_lshl_b32 s50, s50, 9\n\t"
      "v_mov_b32 v11, 2147483648\n\t"
      "s_mov_b64 exec, 15\n\t"
      "v_lshl_add_u32 v11, v1, 7, s50\n\t"
      "s_mov_b64 exec, -1\n\t"
      "buffer_store_dwordx4 v[84:87], v52, s[28:31], 0 offen sc1\n\t"
      "buffer_store_dwordx4 v[88:91], v52, s[28:31], 0 offen offset:1024 sc1\n\t"
      "s_waitcnt vmcnt(23)\n\t"
      "v_cvt_pk_bf16_f32 v60, v58, v58\n\t"
      "v_mov_b32 v62, 0\n\t"
      "v_and_b32 v61, 0xffff0000, v60\n\t"
      "v_mov_b32 v63, 0\n\t"
      "v_sub_f32 v61, v58, v61\n\t"
      "v_mov_b32 v66, 0\n\t"
      "v_cvt_pk_bf16_f32 v61, v61, 0\n\t"
      "v_mov_b32 v67, 0\n\t"
      "s_nop 0\n\t"
      "v_mov_b32 v24, v140\n\t"
      "v_mov_b32 v25, v140\n\t"
      "s_nop 1\n\t"
      "v_permlane32_swap_b32 v24, v25\n\t"
      "s_nop 0\n\t"
      "v_cvt_pk_bf16_f32 v65, v25, 0\n\t"
      "v_lshlrev_b32 v64, 16, v65\n\t"
      "v_sub_f32 v64, v25, v64\n\t"
      "v_cvt_pk_bf16_f32 v64, v25, v64\n\t"
      "s_nop 1\n\t"
      "v_mfma_f32_32x32x16_bf16 v[128:143], v[60:63], v[64:67], v[128:143]\n\t"
      "buffer_store_dwordx2 v[64:65], v54, s[28:31], 0 offen sc1\n\t"
      "v_mov_b32 v100, -1\n\t"
      "v_mov_b32 v101, -1\n\t"
      "v_mov_b32 v102, -1\n\t"
      "v_mov_b32 v103, -1\n\t"
      "buffer_store_dwordx4 v[100:103], v12, s[28:31], s78 offen sc1\n\t"
      "buffer_store_dwordx4 v[100:103], v13, s[28:31], s78 offen sc1\n\t"
      "buffer_store_dwordx2 v[100:101], v14, s[28:31], s78 offen sc1\n\t"
      "s_nop 3\n\t"
      "v_cvt_pk_bf16_f32 v104, v128, v129\n\t"
      "v_cvt_pk_bf16_f32 v105, v130, v131\n\t"
      "v_cvt_pk_bf16_f32 v106, v132, v133\n\t"
      "v_cvt_pk_bf16_f32 v107, v134, v135\n\t"
      "v_cvt_pk_bf16_f32 v108, v136, v137\n\t"
      "v_cvt_pk_bf16_f32 v109, v138, v139\n\t"
      "v_cvt_pk_bf16_f32 v110, v140, v141\n\t"
      "v_cvt_pk_bf16_f32 v111, v142, v143\n\t"
      "buffer_store_dwordx4 v[104:107], v55, s[28:31], s55 offen sc1\n\t"
      "buffer_store_dwordx4 v[108:111], v56, s[28:31], s55 offen sc1\n\t"
      "buffer_store_dwordx2 v[108:109], v57, s[28:31], s55 offen sc1\n\t"
      "att_%=:\n\t"
      "s_mov_b32 s50, 1\n\t"
      "top_%=:\n\t"
      "s_mov_b32 s48, 0\n\t"
      "v_mov_b32 v132, 0xff800000\n\t"
      "v_accvgpr_write_b32 a164, -1\n\t"
      "v_accvgpr_write_b32 a178, 0\n\t"
      "v_accvgpr_write_b32 a179, 0\n\t"
      "v_mov_b32 v133, 0xff800000\n\t"
      "v_accvgpr_write_b32 a165, -1\n\t"
      "v_accvgpr_write_b32 a182, 0\n\t"
      "v_accvgpr_write_b32 a183, 0\n\t"
      "v_mov_b32 v134, 0xff800000\n\t"
      "v_accvgpr_write_b32 a166, -1\n\t"
      "v_accvgpr_write_b32 a186, 0\n\t"
      "v_accvgpr_write_b32 a187, 0\n\t"
      "v_mov_b32 v135, 0xff800000\n\t"
      "v_accvgpr_write_b32 a167, -1\n\t"
      "v_accvgpr_write_b32 a190, 0\n\t"
      "v_accvgpr_write_b32 a191, 0\n\t"
      "v_xor_b32 v136, 1536, v4\n\t"
      "v_xor_b32 v137, 1024, v4\n\t"
      "v_xor_b32 v138, 512, v4\n\t"
      "v_xor_b32 v139, 0, v4\n\t"
      "v_add_u32 v140, v5, v136\n\t"
      "v_add_u32 v141, v5, v137\n\t"
      "v_add_u32 v142, v5, v138\n\t"
      "v_add_u32 v143, v5, v139\n\t"
      "s_waitcnt vmcnt(13)\n\t"
      "mx_%=:\n\t"
      "v_mfma_f32_32x32x16_bf16 v[192:207], a[96:99], a[64:67], 0\n\t"
      "v_mfma_f32_32x32x16_bf16 v[192:207], a[100:103], a[68:71], v[192:207]\n\t"
      "v_mfma_f32_32x32x16_bf16 v[208:223], a[96:99], a[72:75], 0\n\t"
      "v_mfma_f32_32x32x16_bf16 v[208:223], a[100:103], a[76:79], v[208:223]\n\t"
      "v_mfma_f32_32x32x16_bf16 v[224:239], a[96:99], a[80:83], 0\n\t"
      "v_mfma_f32_32x32x16_bf16 v[224:239], a[100:103], a[84:87], v[224:239]\n\t"
      "v_mfma_f32_32x32x16_bf16 v[240:255], a[96:99], a[88:91], 0\n\t"
      "v_mfma_f32_32x32x16_bf16 v[240:255], a[100:103], a[92:95], v[240:255]\n\t"
      "s_nop 5\n\t"
      "v_max3_f32 v132, v192, v193, v132\n\t"
      "v_max3_f32 v132, v194, v195, v132\n\t"
      "v_max3_f32 v132, v196, v197, v132\n\t"
      "v_max3_f32 v132, v198, v199, v132\n\t"
      "v_max3_f32 v132, v200, v201, v132\n\t"
      "v_max3_f32 v132, v202, v203, v132\n\t"
      "v_max3_f32 v132, v204, v205, v132\n\t"
      "v_max3_f32 v132, v206, v207, v132\n\t"
      "v_max3_f32 v133, v208, v209, v133\n\t"
      "v_max3_f32 v133, v210, v211, v133\n\t"
      "v_max3_f32 v133, v212, v213, v133\n\t"
      "v_max3_f32 v133, v214, v215, v133\n\t"
      "v_max3_f32 v133, v216, v217, v133\n\t"
      "v_max3_f32 v133, v218, v219, v133\n\t"
      "v_max3_f32 v133, v220, v221, v133\n\t"
      "v_max3_f32 v133, v222, v223, v133\n\t"
      "v_max3_f32 v134, v224, v225, v134\n\t"
      "v_max3_f32 v134, v226, v227, v134\n\t"
      "v_max3_f32 v134, v228, v229, v134\n\t"
      "v_max3_f32 v134, v230, v231, v134\n\t"
      "v_max3_f32 v134, v232, v233, v134\n\t"
      "v_max3_f32 v134, v234, v235, v134\n\t"
      "v_max3_f32 v134, v236, v237, v134\n\t"
      "v_max3_f32 v134, v238, v239, v134\n\t"
      "v_max3_f32 v135, v240, v241, v135\n\t"
      "v_max3_f32 v135, v242, v243, v135\n\t"
      "v_max3_f32 v135, v244, v245, v135\n\t"
      "v_max3_f32 v135, v246, v247, v135\n\t"
      "v_max3_f32 v135, v248, v249, v135\n\t"
      "v_max3_f32 v135, v250, v251, v135\n\t"
      "v_max3_f32 v135, v252, v253, v135\n\t"
      "v_max3_f32 v135, v254, v255, v135\n\t"
      "s_sub_u32 s50, s50, 1\n\t"
      "s_cmp_lg_u32 s50, 0\n\t"
      "s_cbranch_scc1 mxm_%=\n\t"
      "ds_write_b32 v140, v132\n\t"
      "ds_write_b32 v141, v133\n\t"
      "ds_write_b32 v142, v134\n\t"
      "ds_write_b32 v143, v135\n\t"
      "s_waitcnt lgkmcnt(0)\n\t"
      "s_barrier\n\t"
      "ds_read_b128 v[192:195], v136\n\t"
      "ds_read_b128 v[196:199], v136 offset:2048\n\t"
      "ds_read_b128 v[200:203], v137\n\t"
      "ds_read_b128 v[204:207], v137 offset:2048\n\t"
      "ds_read_b128 v[208:211], v138\n\t"
      "ds_read_b128 v[212:215], v138 offset:2048\n\t"
      "ds_read_b128 v[216:219], v139\n\t"
      "ds_read_b128 v[220:223], v139 offset:2048\n\t"
      "s_waitcnt lgkmcnt(6)\n\t"
      "v_max3_f32 v132, v192, v193, v194\n\t"
      "v_max3_f32 v196, v196, v197, v198\n\t"
      "v_max3_f32 v132, v132, v195, v199\n\t"
      "v_max_f32 v132, v132, v196\n\t"
      "v_cvt_pk_bf16_f32 v192, -v132, -v132\n\t"
      "v_and_b32 v192, 0xffff0000, v192\n\t"
      "v_sub_f32 v193, -v132, v192\n\t"
      "v_cvt_pk_bf16_f32 v192, -v132, v193\n\t"
      "v_and_b32 v194, 0xffff0000, v192\n\t"
      "v_sub_f32 v193, v193, v194\n\t"
      "v_cvt_pk_bf16_f32 v193, v193, 0\n\t"
      "s_mov_b64 exec, s[64:65]\n\t"
      "v_accvgpr_write_b32 a64, v192\n\t"
      "v_accvgpr_write_b32 a65, v193\n\t"
      "s_mov_b64 exec, s[62:63]\n\t"
      "v_accvgpr_write_b32 a70, v192\n\t"
      "v_accvgpr_write_b32 a71, v193\n\t"
      "s_mov_b64 exec, -1\n\t"
      "s_waitcnt lgkmcnt(4)\n\t"
      "v_max3_f32 v133, v200, v201, v202\n\t"
      "v_max3_f32 v204, v204, v205, v206\n\t"
      "v_max3_f32 v133, v133, v203, v207\n\t"
      "v_max_f32 v133, v133, v204\n\t"
      "v_cvt_pk_bf16_f32 v200, -v133, -v133\n\t"
      "v_and_b32 v200, 0xffff0000, v200\n\t"
      "v_sub_f32 v201, -v133, v200\n\t"
      "v_cvt_pk_bf16_f32 v200, -v133, v201\n\t"
      "v_and_b32 v202, 0xffff0000, v200\n\t"
      "v_sub_f32 v201, v201, v202\n\t"
      "v_cvt_pk_bf16_f32 v201, v201, 0\n\t"
      "s_mov_b64 exec, s[64:65]\n\t"
      "v_accvgpr_write_b32 a72, v200\n\t"
      "v_accvgpr_write_b32 a73, v201\n\t"
      "s_mov_b64 exec, s[62:63]\n\t"
      "v_accvgpr_write_b32 a78, v200\n\t"
      "v_accvgpr_write_b32 a79, v201\n\t"
      "s_mov_b64 exec, -1\n\t"
      "s_nop 0\n\t"
      "v_mfma_f32_32x32x16_bf16 v[144:159], a[96:99], a[64:67], 0\n\t"
      "v_mfma_f32_32x32x16_bf16 v[144:159], a[100:103], a[68:71], v[144:159]\n\t"
      "s_waitcnt lgkmcnt(2)\n\t"
      "v_max3_f32 v134, v208, v209, v210\n\t"
      "v_max3_f32 v212, v212, v213, v214\n\t"
      "v_max3_f32 v134, v134, v211, v215\n\t"
      "v_max_f32 v134, v134, v212\n\t"
      "v_cvt_pk_bf16_f32 v208, -v134, -v134\n\t"
      "v_and_b32 v208, 0xffff0000, v208\n\t"
      "v_sub_f32 v209, -v134, v208\n\t"
      "v_cvt_pk_bf16_f32 v208, -v134, v209\n\t"
      "v_and_b32 v210, 0xffff0000, v208\n\t"
      "v_sub_f32 v209, v209, v210\n\t"
      "v_cvt_pk_bf16_f32 v209, v209, 0\n\t"
      "s_mov_b64 exec, s[64:65]\n\t"
      "v_accvgpr_write_b32 a80, v208\n\t"
      "v_accvgpr_write_b32 a81, v209\n\t"
      "s_mov_b64 exec, s[62:63]\n\t"
      "v_accvgpr_write_b32 a86, v208\n\t"
      "v_accvgpr_write_b32 a87, v209\n\t"
      "s_mov_b64 exec, -1\n\t"
      "s_nop 0\n\t"
      "v_mfma_f32_32x32x16_bf16 v[160:175], a[96:99], a[72:75], 0\n\t"
      "v_mfma_f32_32x32x16_bf16 v[160:175], a[100:103], a[76:79], v[160:175]\n\t"
      "s_waitcnt lgkmcnt(0)\n\t"
      "v_max3_f32 v135, v216, v217, v218\n\t"
      "v_max3_f32 v220, v220, v221, v222\n\t"
      "v_max3_f32 v135, v135, v219, v223\n\t"
      "v_max_f32 v135, v135, v220\n\t"
      "v_cvt_pk_bf16_f32 v216, -v135, -v135\n\t"
      "v_and_b32 v216, 0xffff0000, v216\n\t"
      "v_sub_f32 v217, -v135, v216\n\t"
      "v_cvt_pk_bf16_f32 v216, -v135, v217\n\t"
      "v_and_b32 v218, 0xffff0000, v216\n\t"
      "v_sub_f32 v217, v217, v218\n\t"
      "v_cvt_pk_bf16_f32 v217, v217, 0\n\t"
      "s_mov_b64 exec, s[64:65]\n\t"
      "v_accvgpr_write_b32 a88, v216\n\t"
      "v_accvgpr_write_b32 a89, v217\n\t"
      "s_mov_b64 exec, s[62:63]\n\t"
      "v_accvgpr_write_b32 a94, v216\n\t"
      "v_accvgpr_write_b32 a95, v217\n\t"
      "s_mov_b64 exec, -1\n\t"
      "v_mov_b32 v134, s47\n\t"
      "s_waitcnt vmcnt(11)\n\t"
      "v_exp_f32 v144, v144\n\t"
      "v_exp_f32 v145, v145\n\t"
      "v_exp_f32 v146, v146\n\t"
      "v_exp_f32 v147, v147\n\t"
      "v_cvt_pk_bf16_f32 v136, v144, v145\n\t"
      "v_cvt_pk_bf16_f32 v137, v146, v147\n\t"
      "v_exp_f32 v148, v148\n\t"
      "v_exp_f32 v149, v149\n\t"
      "v_mfma_f32_32x32x16_bf16 v[176:191], a[96:99], a[80:83], 0\n\t"
      "buffer_load_dwordx4 a[156:159], v2, s[12:15], s49 offen offset:3072 sc1\n\t"
      "s_add_u32 s49, s49, 0x1000\n\t"
      "v_exp_f32 v150, v150\n\t"
      "v_exp_f32 v151, v151\n\t"
      "v_cvt_pk_bf16_f32 v138, v148, v149\n\t"
      "v_cvt_pk_bf16_f32 v139, v150, v151\n\t"
      "v_exp_f32 v152, v152\n\t"
      "v_exp_f32 v153, v153\n\t"
      "v_mfma_f32_32x32x16_bf16 v[192:207], a[104:107], v[136:139], 0\n\t"
      "v_exp_f32 v154, v154\n\t"
      "v_exp_f32 v155, v155\n\t"
      "v_cvt_pk_bf16_f32 v140, v152, v153\n\t"
      "v_cvt_pk_bf16_f32 v141, v154, v155\n\t"
      "v_exp_f32 v156, v156\n\t"
      "v_exp_f32 v157, v157\n\t"
      "v_mfma_f32_32x32x16_bf16 v[176:191], a[100:103], a[84:87], v[176:191]\n\t"
      "v_exp_f32 v158, v158\n\t"
      "v_exp_f32 v159, v159\n\t"
      "v_cvt_pk_bf16_f32 v142, v156, v157\n\t"
      "v_cvt_pk_bf16_f32 v143, v158, v159\n\t"
      "v_exp_f32 v160, v160\n\t"
      "v_exp_f32 v161, v161\n\t"
      "v_mfma_f32_32x32x16_bf16 v[192:207], a[108:111], v[140:143], v[192:207]\n\t"
      "v_exp_f32 v162, v162\n\t"
      "v_exp_f32 v163, v163\n\t"
      "v_cvt_pk_bf16_f32 v136, v160, v161\n\t"
      "v_cvt_pk_bf16_f32 v137, v162, v163\n\t"
      "v_exp_f32 v164, v164\n\t"
      "v_exp_f32 v165, v165\n\t"
      "v_mfma_f32_32x32x16_bf16 v[144:159], a[96:99], a[88:91], 0\n\t"
      "v_exp_f32 v166, v166\n\t"
      "v_exp_f32 v167, v167\n\t"
      "v_cvt_pk_bf16_f32 v138, v164, v165\n\t"
      "v_cvt_pk_bf16_f32 v139, v166, v167\n\t"
      "v_exp_f32 v168, v168\n\t"
      "v_exp_f32 v169, v169\n\t"
      "v_mfma_f32_32x32x16_bf16 v[208:223], a[104:107], v[136:139], 0\n\t"
      "buffer_load_dwordx4 a[96:99], v2, s[12:15], s49 offen offset:0 sc1\n\t"
      "v_exp_f32 v170, v170\n\t"
      "v_exp_f32 v171, v171\n\t"
      "v_cvt_pk_bf16_f32 v140, v168, v169\n\t"
      "v_cvt_pk_bf16_f32 v141, v170, v171\n\t"
      "v_exp_f32 v172, v172\n\t"
      "v_exp_f32 v173, v173\n\t"
      "v_mfma_f32_32x32x16_bf16 v[144:159], a[100:103], a[92:95], v[144:159]\n\t"
      "v_exp_f32 v174, v174\n\t"
      "v_exp_f32 v175, v175\n\t"
      "v_cvt_pk_bf16_f32 v142, v172, v173\n\t"
      "v_cvt_pk_bf16_f32 v143, v174, v175\n\t"
      "v_exp_f32 v176, v176\n\t"
      "v_exp_f32 v177, v177\n\t"
      "v_mfma_f32_32x32x16_bf16 v[208:223], a[108:111], v[140:143], v[208:223]\n\t"
      "buffer_load_dwordx4 a[100:103], v2, s[12:15], s49 offen offset:1024 sc1\n\t"
      "s_waitcnt vmcnt(10)\n\t"
      "v_exp_f32 v178, v178\n\t"
      "v_exp_f32 v179, v179\n\t"
      "v_cvt_pk_bf16_f32 v136, v176, v177\n\t"
      "v_cvt_pk_bf16_f32 v137, v178, v179\n\t"
      "v_exp_f32 v180, v180\n\t"
      "v_exp_f32 v181, v181\n\t"
      "v_mfma_f32_32x32x16_bf16 v[160:175], a[112:115], a[64:67], 0\n\t"
      "v_exp_f32 v182, v182\n\t"
      "v_exp_f32 v183, v183\n\t"
      "v_cvt_pk_bf16_f32 v138, v180, v181\n\t"
      "v_cvt_pk_bf16_f32 v139, v182, v183\n\t"
      "v_exp_f32 v184, v184\n\t"
      "v_exp_f32 v185, v185\n\t"
      "v_mfma_f32_32x32x16_bf16 v[224:239], a[104:107], v[136:139], 0\n\t"
      "v_exp_f32 v186, v186\n\t"
      "v_exp_f32 v187, v187\n\t"
      "v_cvt_pk_bf16_f32 v140, v184, v185\n\t"
      "v_cvt_pk_bf16_f32 v141, v186, v187\n\t"
      "v_exp_f32 v188, v188\n\t"
      "v_exp_f32 v189, v189\n\t"
      "v_mfma_f32_32x32x16_bf16 v[160:175], a[116:119], a[68:71], v[160:175]\n\t"
      "v_exp_f32 v190, v190\n\t"
      "v_exp_f32 v191, v191\n\t"
      "v_cvt_pk_bf16_f32 v142, v188, v189\n\t"
      "v_cvt_pk_bf16_f32 v143, v190, v191\n\t"
      "v_exp_f32 v144, v144\n\t"
      "v_exp_f32 v145, v145\n\t"
      "v_mfma_f32_32x32x16_bf16 v[224:239], a[108:111], v[140:143], v[224:239]\n\t"
      "v_exp_f32 v146, v146\n\t"
      "v_exp_f32 v147, v147\n\t"
      "v_cvt_pk_bf16_f32 v136, v144, v145\n\t"
      "v_cvt_pk_bf16_f32 v137, v146, v147\n\t"
      "v_exp_f32 v148, v148\n\t"
      "v_exp_f32 v149, v149\n\t"
      "v_mfma_f32_32x32x16_bf16 v[176:191], a[112:115], a[72:75], 0\n\t"
      "v_exp_f32 v150, v150\n\t"
      "v_exp_f32 v151, v151\n\t"
      "v_cvt_pk_bf16_f32 v138, v148, v149\n\t"
      "v_cvt_pk_bf16_f32 v139, v150, v151\n\t"
      "v_exp_f32 v152, v152\n\t"
      "v_exp_f32 v153, v153\n\t"
      "v_mfma_f32_32x32x16_bf16 v[240:255], a[104:107], v[136:139], 0\n\t"
      "v_exp_f32 v154, v154\n\t"
      "v_exp_f32 v155, v155\n\t"
      "v_cvt_pk_bf16_f32 v140, v152, v153\n\t"
      "v_cvt_pk_bf16_f32 v141, v154, v155\n\t"
      "v_exp_f32 v156, v156\n\t"
      "v_exp_f32 v157, v157\n\t"
      "v_mfma_f32_32x32x16_bf16 v[176:191], a[116:119], a[76:79], v[176:191]\n\t"
      "buffer_load_dwordx4 a[104:107], v2, s[12:15], s49 offen offset:2048 sc1\n\t"
      "v_exp_f32 v158, v158\n\t"
      "v_exp_f32 v159, v159\n\t"
      "v_cvt_pk_bf16_f32 v142, v156, v157\n\t"
      "v_cvt_pk_bf16_f32 v143, v158, v159\n\t"
      "v_exp_f32 v160, v160\n\t"
      "v_exp_f32 v161, v161\n\t"
      "v_mfma_f32_32x32x16_bf16 v[240:255], a[108:111], v[140:143], v[240:255]\n\t"
      "v_exp_f32 v162, v162\n\t"
      "v_exp_f32 v163, v163\n\t"
      "v_cvt_pk_bf16_f32 v136, v160, v161\n\t"
      "v_cvt_pk_bf16_f32 v137, v162, v163\n\t"
      "v_exp_f32 v164, v164\n\t"
      "v_exp_f32 v165, v165\n\t"
      "v_mfma_f32_32x32x16_bf16 v[144:159], a[112:115], a[80:83], 0\n\t"
      "buffer_load_dwordx4 a[108:111], v2, s[12:15], s49 offen offset:3072 sc1\n\t"
      "s_add_u32 s49, s49, 0x1000\n\t"
      "v_exp_f32 v166, v166\n\t"
      "v_exp_f32 v167, v167\n\t"
      "v_cvt_pk_bf16_f32 v138, v164, v165\n\t"
      "v_cvt_pk_bf16_f32 v139, v166, v167\n\t"
      "v_exp_f32 v168, v168\n\t"
      "v_exp_f32 v169, v169\n\t"
      "v_mfma_f32_32x32x16_bf16 v[192:207], a[120:123], v[136:139], v[192:207]\n\t"
      "v_exp_f32 v170, v170\n\t"
      "v_exp_f32 v171, v171\n\t"
      "v_cvt_pk_bf16_f32 v140, v168, v169\n\t"
      "v_cvt_pk_bf16_f32 v141, v170, v171\n\t"
      "v_exp_f32 v172, v172\n\t"
      "v_exp_f32 v173, v173\n\t"
      "v_mfma_f32_32x32x16_bf16 v[144:159], a[116:119], a[84:87], v[144:159]\n\t"
      "v_exp_f32 v174, v174\n\t"
      "v_exp_f32 v175, v175\n\t"
      "v_cvt_pk_bf16_f32 v142, v172, v173\n\t"
      "v_cvt_pk_bf16_f32 v143, v174, v175\n\t"
      "v_exp_f32 v176, v176\n\t"
      "v_exp_f32 v177, v177\n\t"
      "v_mfma_f32_32x32x16_bf16 v[192:207], a[124:127], v[140:143], v[192:207]\n\t"
      "v_exp_f32 v178, v178\n\t"
      "v_exp_f32 v179, v179\n\t"
      "v_cvt_pk_bf16_f32 v136, v176, v177\n\t"
      "v_cvt_pk_bf16_f32 v137, v178, v179\n\t"
      "v_exp_f32 v180, v180\n\t"
      "v_exp_f32 v181, v181\n\t"
      "v_mfma_f32_32x32x16_bf16 v[160:175], a[112:115], a[88:91], 0\n\t"
      "v_exp_f32 v182, v182\n\t"
      "v_exp_f32 v183, v183\n\t"
      "v_cvt_pk_bf16_f32 v138, v180, v181\n\t"
      "v_cvt_pk_bf16_f32 v139, v182, v183\n\t"
      "v_exp_f32 v184, v184\n\t"
      "v_exp_f32 v185, v185\n\t"
      "v_mfma_f32_32x32x16_bf16 v[208:223], a[120:123], v[136:139], v[208:223]\n\t"
      "buffer_load_dwordx4 a[112:115], v2, s[12:15], s49 offen offset:0 sc1\n\t"
      "v_exp_f32 v186, v186\n\t"
      "v_exp_f32 v187, v187\n\t"
      "v_cvt_pk_bf16_f32 v140, v184, v185\n\t"
      "v_cvt_pk_bf16_f32 v141, v186, v187\n\t"
      "v_exp_f32 v188, v188\n\t"
      "v_exp_f32 v189, v189\n\t"
      "v_mfma_f32_32x32x16_bf16 v[160:175], a[116:119], a[92:95], v[160:175]\n\t"
      "v_exp_f32 v190, v190\n\t"
      "v_exp_f32 v191, v191\n\t"
      "v_cvt_pk_bf16_f32 v142, v188, v189\n\t"
      "v_cvt_pk_bf16_f32 v143, v190, v191\n\t"
      "v_exp_f32 v144, v144\n\t"
      "v_exp_f32 v145, v145\n\t"
      "v_mfma_f32_32x32x16_bf16 v[208:223], a[124:127], v[140:143], v[208:223]\n\t"
      "buffer_load_dwordx4 a[116:119], v2, s[12:15], s49 offen offset:1024 sc1\n\t"
      "s_waitcnt vmcnt(10)\n\t"
      "v_exp_f32 v146, v146\n\t"
      "v_exp_f32 v147, v147\n\t"
      "v_cvt_pk_bf16_f32 v136, v144, v145\n\t"
      "v_cvt_pk_bf16_f32 v137, v146, v147\n\t"
      "v_exp_f32 v148, v148\n\t"
      "v_exp_f32 v149, v149\n\t"
      "v_mfma_f32_32x32x16_bf16 v[176:191], a[128:131], a[64:67], 0\n\t"
      "v_exp_f32 v150, v150\n\t"
      "v_exp_f32 v151, v151\n\t"
      "v_cvt_pk_bf16_f32 v138, v148, v149\n\t"
      "v_cvt_pk_bf16_f32 v139, v150, v151\n\t"
      "v_exp_f32 v152, v152\n\t"
      "v_exp_f32 v153, v153\n\t"
      "v_mfma_f32_32x32x16_bf16 v[224:239], a[120:123], v[136:139], v[224:239]\n\t"
      "v_exp_f32 v154, v154\n\t"
      "v_exp_f32 v155, v155\n\t"
      "v_cvt_pk_bf16_f32 v140, v152, v153\n\t"
      "v_cvt_pk_bf16_f32 v141, v154, v155\n\t"
      "v_exp_f32 v156, v156\n\t"
      "v_exp_f32 v157, v157\n\t"
      "v_mfma_f32_32x32x16_bf16 v[176:191], a[132:135], a[68:71], v[176:191]\n\t"
      "v_exp_f32 v158, v158\n\t"
      "v_exp_f32 v159, v159\n\t"
      "v_cvt_pk_bf16_f32 v142, v156, v157\n\t"
      "v_cvt_pk_bf16_f32 v143, v158, v159\n\t"
      "v_exp_f32 v160, v160\n\t"
      "v_exp_f32 v161, v161\n\t"
      "v_mfma_f32_32x32x16_bf16 v[224:239], a[124:127], v[140:143], v[224:239]\n\t"
      "v_exp_f32 v162, v162\n\t"
      "v_exp_f32 v163, v163\n\t"
      "v_cvt_pk_bf16_f32 v136, v160, v161\n\t"
      "v_cvt_pk_bf16_f32 v137, v162, v163\n\t"
      "v_exp_f32 v164, v164\n\t"
      "v_exp_f32 v165, v165\n\t"
      "v_mfma_f32_32x32x16_bf16 v[144:159], a[128:131], a[72:75], 0\n\t"
      "s_waitcnt vmcnt(7)\n\t"
      "s_barrier\n\t"
      "buffer_store_dword v134, v7, s[28:31], 0 offen sc1\n\t"
      "v_exp_f32 v166, v166\n\t"
      "v_exp_f32 v167, v167\n\t"
      "v_cvt_pk_bf16_f32 v138, v164, v165\n\t"
      "v_cvt_pk_bf16_f32 v139, v166, v167\n\t"
      "v_exp_f32 v168, v168\n\t"
      "v_exp_f32 v169, v169\n\t"
      "v_mfma_f32_32x32x16_bf16 v[240:255], a[120:123], v[136:139], v[240:255]\n\t"
      "v_exp_f32 v170, v170\n\t"
      "v_exp_f32 v171, v171\n\t"
      "v_cvt_pk_bf16_f32 v140, v168, v169\n\t"
      "v_cvt_pk_bf16_f32 v141, v170, v171\n\t"
      "v_exp_f32 v172, v172\n\t"
      "v_exp_f32 v173, v173\n\t"
      "v_mfma_f32_32x32x16_bf16 v[144:159], a[132:135], a[76:79], v[144:159]\n\t"
      "buffer_load_dwordx4 a[120:123], v2, s[12:15], s49 offen offset:2048 sc1\n\t"
      "v_exp_f32 v174, v174\n\t"
      "v_exp_f32 v175, v175\n\t"
      "v_cvt_pk_bf16_f32 v142, v172, v173\n\t"
      "v_cvt_pk_bf16_f32 v143, v174, v175\n\t"
      "v_exp_f32 v176, v176\n\t"
      "v_exp_f32 v177, v177\n\t"
      "v_mfma_f32_32x32x16_bf16 v[240:255], a[124:127], v[140:143], v[240:255]\n\t"
      "s_add_u32 s48, s67, 0\n\t"
      "buffer_store_dwordx4 a[164:167], v2, s[28:31], s48 offen sc1\n\t"
      "v_exp_f32 v178, v178\n\t"
      "v_exp_f32 v179, v179\n\t"
      "v_cvt_pk_bf16_f32 v136, v176, v177\n\t"
      "v_cvt_pk_bf16_f32 v137, v178, v179\n\t"
      "v_exp_f32 v180, v180\n\t"
      "v_exp_f32 v181, v181\n\t"
      "v_mfma_f32_32x32x16_bf16 v[160:175], a[128:131], a[80:83], 0\n\t"
      "buffer_load_dwordx4 a[124:127], v2, s[12:15], s49 offen offset:3072 sc1\n\t"
      "s_add_u32 s49, s49, 0x1000\n\t"
      "v_exp_f32 v182, v182\n\t"
      "v_exp_f32 v183, v183\n\t"
      "v_cvt_pk_bf16_f32 v138, v180, v181\n\t"
      "v_cvt_pk_bf16_f32 v139, v182, v183\n\t"
      "v_exp_f32 v184, v184\n\t"
      "v_exp_f32 v185, v185\n\t"
      "v_mfma_f32_32x32x16_bf16 v[192:207], a[136:139], v[136:139], v[192:207]\n\t"
      "v_exp_f32 v186, v186\n\t"
      "v_exp_f32 v187, v187\n\t"
      "v_cvt_pk_bf16_f32 v140, v184, v185\n\t"
      "v_cvt_pk_bf16_f32 v141, v186, v187\n\t"
      "v_exp_f32 v188, v188\n\t"
      "v_exp_f32 v189, v189\n\t"
      "v_mfma_f32_32x32x16_bf16 v[160:175], a[132:135], a[84:87], v[160:175]\n\t"
      "v_exp_f32 v190, v190\n\t"
      "v_exp_f32 v191, v191\n\t"
      "v_cvt_pk_bf16_f32 v142, v188, v189\n\t"
      "v_cvt_pk_bf16_f32 v143, v190, v191\n\t"
      "v_exp_f32 v144, v144\n\t"
      "v_exp_f32 v145, v145\n\t"
      "v_mfma_f32_32x32x16_bf16 v[192:207], a[140:143], v[140:143], v[192:207]\n\t"
      "v_exp_f32 v146, v146\n\t"
      "v_exp_f32 v147, v147\n\t"
      "v_cvt_pk_bf16_f32 v136, v144, v145\n\t"
      "v_cvt_pk_bf16_f32 v137, v146, v147\n\t"
      "v_exp_f32 v148, v148\n\t"
      "v_exp_f32 v149, v149\n\t"
      "v_mfma_f32_32x32x16_bf16 v[176:191], a[128:131], a[88:91], 0\n\t"
      "v_exp_f32 v150, v150\n\t"
      "v_exp_f32 v151, v151\n\t"
      "v_cvt_pk_bf16_f32 v138, v148, v149\n\t"
      "v_cvt_pk_bf16_f32 v139, v150, v151\n\t"
      "v_exp_f32 v152, v152\n\t"
      "v_exp_f32 v153, v153\n\t"
      "v_mfma_f32_32x32x16_bf16 v[208:223], a[136:139], v[136:139], v[208:223]\n\t"
      "buffer_load_dwordx4 a[128:131], v2, s[12:15], s49 offen offset:0 sc1\n\t"
      "v_exp_f32 v154, v154\n\t"
      "v_exp_f32 v155, v155\n\t"
      "v_cvt_pk_bf16_f32 v140, v152, v153\n\t"
      "v_cvt_pk_bf16_f32 v141, v154, v155\n\t"
      "v_exp_f32 v156, v156\n\t"
      "v_exp_f32 v157, v157\n\t"
      "v_mfma_f32_32x32x16_bf16 v[176:191], a[132:135], a[92:95], v[176:191]\n\t"
      "v_exp_f32 v158, v158\n\t"
      "v_exp_f32 v159, v159\n\t"
      "v_cvt_pk_bf16_f32 v142, v156, v157\n\t"
      "v_cvt_pk_bf16_f32 v143, v158, v159\n\t"
      "v_exp_f32 v160, v160\n\t"
      "v_exp_f32 v161, v161\n\t"
      "v_mfma_f32_32x32x16_bf16 v[208:223], a[140:143], v[140:143], v[208:223]\n\t"
      "buffer_load_dwordx4 a[132:135], v2, s[12:15], s49 offen offset:1024 sc1\n\t"
      "s_waitcnt vmcnt(12)\n\t"
      "v_exp_f32 v162, v162\n\t"
      "v_exp_f32 v163, v163\n\t"
      "v_cvt_pk_bf16_f32 v136, v160, v161\n\t"
      "v_cvt_pk_bf16_f32 v137, v162, v163\n\t"
      "v_exp_f32 v164, v164\n\t"
      "v_exp_f32 v165, v165\n\t"
      "v_mfma_f32_32x32x16_bf16 v[144:159], a[144:147], a[64:67], 0\n\t"
      "v_exp_f32 v166, v166\n\t"
      "v_exp_f32 v167, v167\n\t"
      "v_cvt_pk_bf16_f32 v138, v164, v165\n\t"
      "v_cvt_pk_bf16_f32 v139, v166, v167\n\t"
      "v_exp_f32 v168, v168\n\t"
      "v_exp_f32 v169, v169\n\t"
      "v_mfma_f32_32x32x16_bf16 v[224:239], a[136:139], v[136:139], v[224:239]\n\t"
      "v_exp_f32 v170, v170\n\t"
      "v_exp_f32 v171, v171\n\t"
      "v_cvt_pk_bf16_f32 v140, v168, v169\n\t"
      "v_cvt_pk_bf16_f32 v141, v170, v171\n\t"
      "v_exp_f32 v172, v172\n\t"
      "v_exp_f32 v173, v173\n\t"
      "v_mfma_f32_32x32x16_bf16 v[144:159], a[148:151], a[68:71], v[144:159]\n\t"
      "v_exp_f32 v174, v174\n\t"
      "v_exp_f32 v175, v175\n\t"
      "v_cvt_pk_bf16_f32 v142, v172, v173\n\t"
      "v_cvt_pk_bf16_f32 v143, v174, v175\n\t"
      "v_exp_f32 v176, v176\n\t"
      "v_exp_f32 v177, v177\n\t"
      "v_mfma_f32_32x32x16_bf16 v[224:239], a[140:143], v[140:143], v[224:239]\n\t"
      "v_exp_f32 v178, v178\n\t"
      "v_exp_f32 v179, v179\n\t"
      "v_cvt_pk_bf16_f32 v136, v176, v177\n\t"
      "v_cvt_pk_bf16_f32 v137, v178, v179\n\t"
      "v_exp_f32 v180, v180\n\t"
      "v_exp_f32 v181, v181\n\t"
      "v_mfma_f32_32x32x16_bf16 v[160:175], a[144:147], a[72:75], 0\n\t"
      "v_exp_f32 v182, v182\n\t"
      "v_exp_f32 v183, v183\n\t"
      "v_cvt_pk_bf16_f32 v138, v180, v181\n\t"
      "v_cvt_pk_bf16_f32 v139, v182, v183\n\t"
      "v_exp_f32 v184, v184\n\t"
      "v_exp_f32 v185, v185\n\t"
      "v_mfma_f32_32x32x16_bf16 v[240:255], a[136:139], v[136:139], v[240:255]\n\t"
      "v_exp_f32 v186, v186\n\t"
      "v_exp_f32 v187, v187\n\t"
      "v_cvt_pk_bf16_f32 v140, v184, v185\n\t"
      "v_cvt_pk_bf16_f32 v141, v186, v187\n\t"
      "v_exp_f32 v188, v188\n\t"
      "v_exp_f32 v189, v189\n\t"
      "v_mfma_f32_32x32x16_bf16 v[160:175], a[148:151], a[76:79], v[160:175]\n\t"
      "buffer_load_dwordx4 a[136:139], v2, s[12:15], s49 offen offset:2048 sc1\n\t"
      "v_exp_f32 v190, v190\n\t"
      "v_exp_f32 v191, v191\n\t"
      "v_cvt_pk_bf16_f32 v142, v188, v189\n\t"
      "v_cvt_pk_bf16_f32 v143, v190, v191\n\t"
      "v_exp_f32 v144, v144\n\t"
      "v_exp_f32 v145, v145\n\t"
      "v_mfma_f32_32x32x16_bf16 v[240:255], a[140:143], v[140:143], v[240:255]\n\t"
      "s_add_u32 s48, s67, 1024\n\t"
      "buffer_store_dwordx4 a[164:167], v2, s[28:31], s48 offen sc1\n\t"
      "v_exp_f32 v146, v146\n\t"
      "v_exp_f32 v147, v147\n\t"
      "v_cvt_pk_bf16_f32 v136, v144, v145\n\t"
      "v_cvt_pk_bf16_f32 v137, v146, v147\n\t"
      "v_exp_f32 v148, v148\n\t"
      "v_exp_f32 v149, v149\n\t"
      "v_mfma_f32_32x32x16_bf16 v[176:191], a[144:147], a[80:83], 0\n\t"
      "buffer_load_dwordx4 a[140:143], v2, s[12:15], s49 offen offset:3072 sc1\n\t"
      "s_add_u32 s49, s49, 0x1000\n\t"
      "v_exp_f32 v150, v150\n\t"
      "v_exp_f32 v151, v151\n\t"
      "v_cvt_pk_bf16_f32 v138, v148, v149\n\t"
      "v_cvt_pk_bf16_f32 v139, v150, v151\n\t"
      "v_exp_f32 v152, v152\n\t"
      "v_exp_f32 v153, v153\n\t"
      "v_mfma_f32_32x32x16_bf16 v[192:207], a[152:155], v[136:139], v[192:207]\n\t"
      "v_exp_f32 v154, v154\n\t"
      "v_exp_f32 v155, v155\n\t"
      "v_cvt_pk_bf16_f32 v140, v152, v153\n\t"
      "v_cvt_pk_bf16_f32 v141, v154, v155\n\t"
      "v_exp_f32 v156, v156\n\t"
      "v_exp_f32 v157, v157\n\t"
      "v_mfma_f32_32x32x16_bf16 v[176:191], a[148:151], a[84:87], v[176:191]\n\t"
      "v_exp_f32 v158, v158\n\t"
      "v_exp_f32 v159, v159\n\t"
      "v_cvt_pk_bf16_f32 v142, v156, v157\n\t"
      "v_cvt_pk_bf16_f32 v143, v158, v159\n\t"
      "v_exp_f32 v160, v160\n\t"
      "v_exp_f32 v161, v161\n\t"
      "v_mfma_f32_32x32x16_bf16 v[192:207], a[156:159], v[140:143], v[192:207]\n\t"
      "v_exp_f32 v162, v162\n\t"
      "v_exp_f32 v163, v163\n\t"
      "v_cvt_pk_bf16_f32 v136, v160, v161\n\t"
      "v_cvt_pk_bf16_f32 v137, v162, v163\n\t"
      "v_exp_f32 v164, v164\n\t"
      "v_exp_f32 v165, v165\n\t"
      "v_mfma_f32_32x32x16_bf16 v[144:159], a[144:147], a[88:91], 0\n\t"
      "v_exp_f32 v166, v166\n\t"
      "v_exp_f32 v167, v167\n\t"
      "v_cvt_pk_bf16_f32 v138, v164, v165\n\t"
      "v_cvt_pk_bf16_f32 v139, v166, v167\n\t"
      "v_exp_f32 v168, v168\n\t"
      "v_exp_f32 v169, v169\n\t"
      "v_mfma_f32_32x32x16_bf16 v[208:223], a[152:155], v[136:139], v[208:223]\n\t"
      "buffer_load_dwordx4 a[144:147], v2, s[12:15], s49 offen offset:0 sc1\n\t"
      "v_exp_f32 v170, v170\n\t"
      "v_exp_f32 v171, v171\n\t"
      "v_cvt_pk_bf16_f32 v140, v168, v169\n\t"
      "v_cvt_pk_bf16_f32 v141, v170, v171\n\t"
      "v_exp_f32 v172, v172\n\t"
      "v_exp_f32 v173, v173\n\t"
      "v_mfma_f32_32x32x16_bf16 v[144:159], a[148:151], a[92:95], v[144:159]\n\t"
      "v_exp_f32 v174, v174\n\t"
      "v_exp_f32 v175, v175\n\t"
      "v_cvt_pk_bf16_f32 v142, v172, v173\n\t"
      "v_cvt_pk_bf16_f32 v143, v174, v175\n\t"
      "v_exp_f32 v176, v176\n\t"
      "v_exp_f32 v177, v177\n\t"
      "v_mfma_f32_32x32x16_bf16 v[208:223], a[156:159], v[140:143], v[208:223]\n\t"
      "buffer_load_dwordx4 a[148:151], v2, s[12:15], s49 offen offset:1024 sc1\n\t"
      "s_waitcnt vmcnt(13)\n\t"
      "v_exp_f32 v178, v178\n\t"
      "v_exp_f32 v179, v179\n\t"
      "v_cvt_pk_bf16_f32 v136, v176, v177\n\t"
      "v_cvt_pk_bf16_f32 v137, v178, v179\n\t"
      "v_exp_f32 v180, v180\n\t"
      "v_exp_f32 v181, v181\n\t"
      "v_mfma_f32_32x32x16_bf16 v[160:175], a[96:99], a[64:67], 0\n\t"
      "v_exp_f32 v182, v182\n\t"
      "v_exp_f32 v183, v183\n\t"
      "v_cvt_pk_bf16_f32 v138, v180, v181\n\t"
      "v_cvt_pk_bf16_f32 v139, v182, v183\n\t"
      "v_exp_f32 v184, v184\n\t"
      "v_exp_f32 v185, v185\n\t"
      "v_mfma_f32_32x32x16_bf16 v[224:239], a[152:155], v[136:139], v[224:239]\n\t"
      "v_exp_f32 v186, v186\n\t"
      "v_exp_f32 v187, v187\n\t"
      "v_cvt_pk_bf16_f32 v140, v184, v185\n\t"
      "v_cvt_pk_bf16_f32 v141, v186, v187\n\t"
      "v_exp_f32 v188, v188\n\t"
      "v_exp_f32 v189, v189\n\t"
      "v_mfma_f32_32x32x16_bf16 v[160:175], a[100:103], a[68:71], v[160:175]\n\t"
      "v_exp_f32 v190, v190\n\t"
      "v_exp_f32 v191, v191\n\t"
      "v_cvt_pk_bf16_f32 v142, v188, v189\n\t"
      "v_cvt_pk_bf16_f32 v143, v190, v191\n\t"
      "v_exp_f32 v144, v144\n\t"
      "v_exp_f32 v145, v145\n\t"
      "v_mfma_f32_32x32x16_bf16 v[224:239], a[156:159], v[140:143], v[224:239]\n\t"
      "v_exp_f32 v146, v146\n\t"
      "v_exp_f32 v147, v147\n\t"
      "v_cvt_pk_bf16_f32 v136, v144, v145\n\t"
      "v_cvt_pk_bf16_f32 v137, v146, v147\n\t"
      "v_exp_f32 v148, v148\n\t"
      "v_exp_f32 v149, v149\n\t"
      "v_mfma_f32_32x32x16_bf16 v[176:191], a[96:99], a[72:75], 0\n\t"
      "v_exp_f32 v150, v150\n\t"
      "v_exp_f32 v151, v151\n\t"
      "v_cvt_pk_bf16_f32 v138, v148, v149\n\t"
      "v_cvt_pk_bf16_f32 v139, v150, v151\n\t"
      "v_exp_f32 v152, v152\n\t"
      "v_exp_f32 v153, v153\n\t"
      "v_mfma_f32_32x32x16_bf16 v[240:255], a[152:155], v[136:139], v[240:255]\n\t"
      "v_exp_f32 v154, v154\n\t"
      "v_exp_f32 v155, v155\n\t"
      "v_cvt_pk_bf16_f32 v140, v152, v153\n\t"
      "v_cvt_pk_bf16_f32 v141, v154, v155\n\t"
      "v_exp_f32 v156, v156\n\t"
      "v_exp_f32 v157, v157\n\t"
      "v_mfma_f32_32x32x16_bf16 v[176:191], a[100:103], a[76:79], v[176:191]\n\t"
      "buffer_load_dwordx4 a[152:155], v2, s[12:15], s49 offen offset:2048 sc1\n\t"
      "v_exp_f32 v158, v158\n\t"
      "v_exp_f32 v159, v159\n\t"
      "v_cvt_pk_bf16_f32 v142, v156, v157\n\t"
      "v_cvt_pk_bf16_f32 v143, v158, v159\n\t"
      "v_exp_f32 v160, v160\n\t"
      "v_exp_f32 v161, v161\n\t"
      "v_mfma_f32_32x32x16_bf16 v[240:255], a[156:159], v[140:143], v[240:255]\n\t"
      "s_add_u32 s48, s67, 2048\n\t"
      "buffer_store_dwordx4 a[164:167], v2, s[28:31], s48 offen sc1\n\t"
      "v_exp_f32 v162, v162\n\t"
      "v_exp_f32 v163, v163\n\t"
      "v_cvt_pk_bf16_f32 v136, v160, v161\n\t"
      "v_cvt_pk_bf16_f32 v137, v162, v163\n\t"
      "v_exp_f32 v164, v164\n\t"
      "v_exp_f32 v165, v165\n\t"
      "v_mfma_f32_32x32x16_bf16 v[144:159], a[96:99], a[80:83], 0\n\t"
      "buffer_load_dwordx4 a[156:159], v2, s[12:15], s49 offen offset:3072 sc1\n\t"
      "s_add_u32 s49, s49, 0x1000\n\t"
      "v_exp_f32 v166, v166\n\t"
      "v_exp_f32 v167, v167\n\t"
      "v_cvt_pk_bf16_f32 v138, v164, v165\n\t"
      "v_cvt_pk_bf16_f32 v139, v166, v167\n\t"
      "v_exp_f32 v168, v168\n\t"
      "v_exp_f32 v169, v169\n\t"
      "v_mfma_f32_32x32x16_bf16 v[192:207], a[104:107], v[136:139], v[192:207]\n\t"
      "v_exp_f32 v170, v170\n\t"
      "v_exp_f32 v171, v171\n\t"
      "v_cvt_pk_bf16_f32 v140, v168, v169\n\t"
      "v_cvt_pk_bf16_f32 v141, v170, v171\n\t"
      "v_exp_f32 v172, v172\n\t"
      "v_exp_f32 v173, v173\n\t"
      "v_mfma_f32_32x32x16_bf16 v[144:159], a[100:103], a[84:87], v[144:159]\n\t"
      "v_exp_f32 v174, v174\n\t"
      "v_exp_f32 v175, v175\n\t"
      "v_cvt_pk_bf16_f32 v142, v172, v173\n\t"
      "v_cvt_pk_bf16_f32 v143, v174, v175\n\t"
      "v_exp_f32 v176, v176\n\t"
      "v_exp_f32 v177, v177\n\t"
      "v_mfma_f32_32x32x16_bf16 v[192:207], a[108:111], v[140:143], v[192:207]\n\t"
      "buffer_load_dword v132, v6, s[28:31], 0 offen sc1\n\t"
      "v_exp_f32 v178, v178\n\t"
      "v_exp_f32 v179, v179\n\t"
      "v_cvt_pk_bf16_f32 v136, v176, v177\n\t"
      "v_cvt_pk_bf16_f32 v137, v178, v179\n\t"
      "v_exp_f32 v180, v180\n\t"
      "v_exp_f32 v181, v181\n\t"
      "v_mfma_f32_32x32x16_bf16 v[160:175], a[96:99], a[88:91], 0\n\t"
      "v_exp_f32 v182, v182\n\t"
      "v_exp_f32 v183, v183\n\t"
      "v_cvt_pk_bf16_f32 v138, v180, v181\n\t"
      "v_cvt_pk_bf16_f32 v139, v182, v183\n\t"
      "v_exp_f32 v184, v184\n\t"
      "v_exp_f32 v185, v185\n\t"
      "v_mfma_f32_32x32x16_bf16 v[208:223], a[104:107], v[136:139], v[208:223]\n\t"
      "buffer_load_dwordx4 a[96:99], v2, s[12:15], s49 offen offset:0 sc1\n\t"
      "v_exp_f32 v186, v186\n\t"
      "v_exp_f32 v187, v187\n\t"
      "v_cvt_pk_bf16_f32 v140, v184, v185\n\t"
      "v_cvt_pk_bf16_f32 v141, v186, v187\n\t"
      "v_exp_f32 v188, v188\n\t"
      "v_exp_f32 v189, v189\n\t"
      "v_mfma_f32_32x32x16_bf16 v[160:175], a[100:103], a[92:95], v[160:175]\n\t"
      "v_exp_f32 v190, v190\n\t"
      "v_exp_f32 v191, v191\n\t"
      "v_cvt_pk_bf16_f32 v142, v188, v189\n\t"
      "v_cvt_pk_bf16_f32 v143, v190, v191\n\t"
      "v_exp_f32 v144, v144\n\t"
      "v_exp_f32 v145, v145\n\t"
      "v_mfma_f32_32x32x16_bf16 v[208:223], a[108:111], v[140:143], v[208:223]\n\t"
      "buffer_load_dwordx4 a[100:103], v2, s[12:15], s49 offen offset:1024 sc1\n\t"
      "s_waitcnt vmcnt(13)\n\t"
      "v_exp_f32 v146, v146\n\t"
      "v_exp_f32 v147, v147\n\t"
      "v_cvt_pk_bf16_f32 v136, v144, v145\n\t"
      "v_cvt_pk_bf16_f32 v137, v146, v147\n\t"
      "v_exp_f32 v148, v148\n\t"
      "v_exp_f32 v149, v149\n\t"
      "v_mfma_f32_32x32x16_bf16 v[176:191], a[112:115], a[64:67], 0\n\t"
      "v_exp_f32 v150, v150\n\t"
      "v_exp_f32 v151, v151\n\t"
      "v_cvt_pk_bf16_f32 v138, v148, v149\n\t"
      "v_cvt_pk_bf16_f32 v139, v150, v151\n\t"
      "v_exp_f32 v152, v152\n\t"
      "v_exp_f32 v153, v153\n\t"
      "v_mfma_f32_32x32x16_bf16 v[224:239], a[104:107], v[136:139], v[224:239]\n\t"
      "v_exp_f32 v154, v154\n\t"
      "v_exp_f32 v155, v155\n\t"
      "v_cvt_pk_bf16_f32 v140, v152, v153\n\t"
      "v_cvt_pk_bf16_f32 v141, v154, v155\n\t"
      "v_exp_f32 v156, v156\n\t"
      "v_exp_f32 v157, v157\n\t"
      "v_mfma_f32_32x32x16_bf16 v[176:191], a[116:119], a[68:71], v[176:191]\n\t"
      "v_exp_f32 v158, v158\n\t"
      "v_exp_f32 v159, v159\n\t"
      "v_cvt_pk_bf16_f32 v142, v156, v157\n\t"
      "v_cvt_pk_bf16_f32 v143, v158, v159\n\t"
      "v_exp_f32 v160, v160\n\t"
      "v_exp_f32 v161, v161\n\t"
      "v_mfma_f32_32x32x16_bf16 v[224:239], a[108:111], v[140:143], v[224:239]\n\t"
      "v_exp_f32 v162, v162\n\t"
      "v_exp_f32 v163, v163\n\t"
      "v_cvt_pk_bf16_f32 v136, v160, v161\n\t"
      "v_cvt_pk_bf16_f32 v137, v162, v163\n\t"
      "v_exp_f32 v164, v164\n\t"
      "v_exp_f32 v165, v165\n\t"
      "v_mfma_f32_32x32x16_bf16 v[144:159], a[112:115], a[72:75], 0\n\t"
      "v_exp_f32 v166, v166\n\t"
      "v_exp_f32 v167, v167\n\t"
      "v_cvt_pk_bf16_f32 v138, v164, v165\n\t"
      "v_cvt_pk_bf16_f32 v139, v166, v167\n\t"
      "v_exp_f32 v168, v168\n\t"
      "v_exp_f32 v169, v169\n\t"
      "v_mfma_f32_32x32x16_bf16 v[240:255], a[104:107], v[136:139], v[240:255]\n\t"
      "v_exp_f32 v170, v170\n\t"
      "v_exp_f32 v171, v171\n\t"
      "v_cvt_pk_bf16_f32 v140, v168, v169\n\t"
      "v_cvt_pk_bf16_f32 v141, v170, v171\n\t"
      "v_exp_f32 v172, v172\n\t"
      "v_exp_f32 v173, v173\n\t"
      "v_mfma_f32_32x32x16_bf16 v[144:159], a[116:119], a[76:79], v[144:159]\n\t"
      "buffer_load_dwordx4 a[104:107], v2, s[12:15], s49 offen offset:2048 sc1\n\t"
      "v_exp_f32 v174, v174\n\t"
      "v_exp_f32 v175, v175\n\t"
      "v_cvt_pk_bf16_f32 v142, v172, v173\n\t"
      "v_cvt_pk_bf16_f32 v143, v174, v175\n\t"
      "s_waitcnt vmcnt(3)\n\t"
      "v_cmp_ne_u32 vcc, s47, v132\n\t"
      "v_add_u32 v134, s76, v2\n\t"
      "v_add_u32 v135, s77, v2\n\t"
      "s_nop 1\n\t"
      "s_cbranch_vccnz fw_%=\n\t"
      "fok_%=:\n\t"
      "v_add_u32 v132, s74, v2\n\t"
      "v_add_u32 v133, s75, v2\n\t"
      "v_exp_f32 v176, v176\n\t"
      "v_exp_f32 v177, v177\n\t"
      "v_mfma_f32_32x32x16_bf16 v[240:255], a[108:111], v[140:143], v[240:255]\n\t"
      "s_add_u32 s48, s67, 3072\n\t"
      "buffer_store_dwordx4 a[164:167], v2, s[28:31], s48 offen sc1\n\t"
      "v_exp_f32 v178, v178\n\t"
      "v_exp_f32 v179, v179\n\t"
      "v_cvt_pk_bf16_f32 v136, v176, v177\n\t"
      "v_cvt_pk_bf16_f32 v137, v178, v179\n\t"
      "v_exp_f32 v180, v180\n\t"
      "v_exp_f32 v181, v181\n\t"
      "v_mfma_f32_32x32x16_bf16 v[160:175], a[112:115], a[80:83], 0\n\t"
      "buffer_load_dwordx4 a[108:111], v2, s[12:15], s49 offen offset:3072 sc1\n\t"
      "s_add_u32 s49, s49, 0x1000\n\t"
      "v_exp_f32 v182, v182\n\t"
      "v_exp_f32 v183, v183\n\t"
      "v_cvt_pk_bf16_f32 v138, v180, v181\n\t"
      "v_cvt_pk_bf16_f32 v139, v182, v183\n\t"
      "v_exp_f32 v184, v184\n\t"
      "v_exp_f32 v185, v185\n\t"
      "v_mfma_f32_32x32x16_bf16 v[192:207], a[120:123], v[136:139], v[192:207]\n\t"
      "v_exp_f32 v186, v186\n\t"
      "v_exp_f32 v187, v187\n\t"
      "v_cvt_pk_bf16_f32 v140, v184, v185\n\t"
      "v_cvt_pk_bf16_f32 v141, v186, v187\n\t"
      "v_exp_f32 v188, v188\n\t"
      "v_exp_f32 v189, v189\n\t"
      "v_mfma_f32_32x32x16_bf16 v[160:175], a[116:119], a[84:87], v[160:175]\n\t"
      "s_mov_b32 m0, s69\n\t"
      "s_xor_b32 s48, s71, 0\n\t"
      "buffer_load_dwordx4 v2, s[16:19], s48 offen sc1 lds\n\t"
      "v_exp_f32 v190, v190\n\t"
      "v_exp_f32 v191, v191\n\t"
      "v_cvt_pk_bf16_f32 v142, v188, v189\n\t"
      "v_cvt_pk_bf16_f32 v143, v190, v191\n\t"
      "v_exp_f32 v144, v144\n\t"
      "v_exp_f32 v145, v145\n\t"
      "v_mfma_f32_32x32x16_bf16 v[192:207], a[124:127], v[140:143], v[192:207]\n\t"
      "s_add_u32 m0, m0, s70\n\t"
      "s_xor_b32 s48, s71, 4096\n\t"
      "buffer_load_dwordx4 v2, s[16:19], s48 offen sc1 lds\n\t"
      "v_exp_f32 v146, v146\n\t"
      "v_exp_f32 v147, v147\n\t"
      "v_cvt_pk_bf16_f32 v136, v144, v145\n\t"
      "v_cvt_pk_bf16_f32 v137, v146, v147\n\t"
      "v_exp_f32 v148, v148\n\t"
      "v_exp_f32 v149, v149\n\t"
      "v_mfma_f32_32x32x16_bf16 v[176:191], a[112:115], a[88:91], 0\n\t"
      "v_exp_f32 v150, v150\n\t"
      "v_exp_f32 v151, v151\n\t"
      "v_cvt_pk_bf16_f32 v138, v148, v149\n\t"
      "v_cvt_pk_bf16_f32 v139, v150, v151\n\t"
      "v_exp_f32 v152, v152\n\t"
      "v_exp_f32 v153, v153\n\t"
      "v_mfma_f32_32x32x16_bf16 v[208:223], a[120:123], v[136:139], v[208:223]\n\t"
      "buffer_load_dwordx4 a[112:115], v2, s[12:15], s49 offen offset:0 sc1\n\t"
      "v_exp_f32 v154, v154\n\t"
      "v_exp_f32 v155, v155\n\t"
      "v_cvt_pk_bf16_f32 v140, v152, v153\n\t"
      "v_cvt_pk_bf16_f32 v141, v154, v155\n\t"
      "v_exp_f32 v156, v156\n\t"
      "v_exp_f32 v157, v157\n\t"
      "v_mfma_f32_32x32x16_bf16 v[176:191], a[116:119], a[92:95], v[176:191]\n\t"
      "buffer_load_dwordx2 a[176:177], v10, s[16:19], s73 offen offset:0 sc1\n\t"
      "v_exp_f32 v158, v158\n\t"
      "v_exp_f32 v159, v159\n\t"
      "v_cvt_pk_bf16_f32 v142, v156, v157\n\t"
      "v_cvt_pk_bf16_f32 v143, v158, v159\n\t"
      "v_exp_f32 v160, v160\n\t"
      "v_exp_f32 v161, v161\n\t"
      "v_mfma_f32_32x32x16_bf16 v[208:223], a[124:127], v[140:143], v[208:223]\n\t"
      "buffer_load_dwordx4 a[116:119], v2, s[12:15], s49 offen offset:1024 sc1\n\t"
      "s_waitcnt vmcnt(16)\n\t"
      "v_exp_f32 v162, v162\n\t"
      "v_exp_f32 v163, v163\n\t"
      "v_cvt_pk_bf16_f32 v136, v160, v161\n\t"
      "v_cvt_pk_bf16_f32 v137, v162, v163\n\t"
      "v_exp_f32 v164, v164\n\t"
      "v_exp_f32 v165, v165\n\t"
      "v_mfma_f32_32x32x16_bf16 v[144:159], a[128:131], a[64:67], 0\n\t"
      "buffer_load_dwordx2 a[180:181], v10, s[16:19], s73 offen offset:256 sc1\n\t"
      "v_exp_f32 v166, v166\n\t"
      "v_exp_f32 v167, v167\n\t"
      "v_cvt_pk_bf16_f32 v138, v164, v165\n\t"
      "v_cvt_pk_bf16_f32 v139, v166, v167\n\t"
      "v_exp_f32 v168, v168\n\t"
      "v_exp_f32 v169, v169\n\t"
      "v_mfma_f32_32x32x16_bf16 v[224:239], a[120:123], v[136:139], v[224:239]\n\t"
      "s_add_u32 m0, m0, s70\n\t"
      "s_xor_b32 s48, s71, 8192\n\t"
      "buffer_load_dwordx4 v2, s[16:19], s48 offen sc1 lds\n\t"
      "v_exp_f32 v170, v170\n\t"
      "v_exp_f32 v171, v171\n\t"
      "v_cvt_pk_bf16_f32 v140, v168, v169\n\t"
      "v_cvt_pk_bf16_f32 v141, v170, v171\n\t"
      "v_exp_f32 v172, v172\n\t"
      "v_exp_f32 v173, v173\n\t"
      "v_mfma_f32_32x32x16_bf16 v[144:159], a[132:135], a[68:71], v[144:159]\n\t"
      "v_exp_f32 v174, v174\n\t"
      "v_exp_f32 v175, v175\n\t"
      "v_cvt_pk_bf16_f32 v142, v172, v173\n\t"
      "v_cvt_pk_bf16_f32 v143, v174, v175\n\t"
      "v_exp_f32 v176, v176\n\t"
      "v_exp_f32 v177, v177\n\t"
      "v_mfma_f32_32x32x16_bf16 v[224:239], a[124:127], v[140:143], v[224:239]\n\t"
      "buffer_load_dwordx2 a[184:185], v10, s[16:19], s73 offen offset:512 sc1\n\t"
      "v_exp_f32 v178, v178\n\t"
      "v_exp_f32 v179, v179\n\t"
      "v_cvt_pk_bf16_f32 v136, v176, v177\n\t"
      "v_cvt_pk_bf16_f32 v137, v178, v179\n\t"
      "v_exp_f32 v180, v180\n\t"
      "v_exp_f32 v181, v181\n\t"
      "v_mfma_f32_32x32x16_bf16 v[160:175], a[128:131], a[72:75], 0\n\t"
      "s_add_u32 m0, m0, s70\n\t"
      "s_xor_b32 s48, s71, 12288\n\t"
      "buffer_load_dwordx4 v2, s[16:19], s48 offen sc1 lds\n\t"
      "v_exp_f32 v182, v182\n\t"
      "v_exp_f32 v183, v183\n\t"
      "v_cvt_pk_bf16_f32 v138, v180, v181\n\t"
      "v_cvt_pk_bf16_f32 v139, v182, v183\n\t"
      "v_exp_f32 v184, v184\n\t"
      "v_exp_f32 v185, v185\n\t"
      "v_mfma_f32_32x32x16_bf16 v[240:255], a[120:123], v[136:139], v[240:255]\n\t"
      "v_exp_f32 v186, v186\n\t"
      "v_exp_f32 v187, v187\n\t"
      "v_cvt_pk_bf16_f32 v140, v184, v185\n\t"
      "v_cvt_pk_bf16_f32 v141, v186, v187\n\t"
      "v_exp_f32 v188, v188\n\t"
      "v_exp_f32 v189, v189\n\t"
      "v_mfma_f32_32x32x16_bf16 v[160:175], a[132:135], a[76:79], v[160:175]\n\t"
      "buffer_load_dwordx4 a[120:123], v2, s[12:15], s49 offen offset:2048 sc1\n\t"
      "v_exp_f32 v190, v190\n\t"
      "v_exp_f32 v191, v191\n\t"
      "v_cvt_pk_bf16_f32 v142, v188, v189\n\t"
      "v_cvt_pk_bf16_f32 v143, v190, v191\n\t"
      "v_exp_f32 v144, v144\n\t"
      "v_exp_f32 v145, v145\n\t"
      "v_mfma_f32_32x32x16_bf16 v[240:255], a[124:127], v[140:143], v[240:255]\n\t"
      "s_add_u32 s48, s67, 4096\n\t"
      "buffer_store_dwordx4 a[164:167], v2, s[28:31], s48 offen sc1\n\t"
      "v_exp_f32 v146, v146\n\t"
      "v_exp_f32 v147, v147\n\t"
      "v_cvt_pk_bf16_f32 v136, v144, v145\n\t"
      "v_cvt_pk_bf16_f32 v137, v146, v147\n\t"
      "v_exp_f32 v148, v148\n\t"
      "v_exp_f32 v149, v149\n\t"
      "v_mfma_f32_32x32x16_bf16 v[176:191], a[128:131], a[80:83], 0\n\t"
      "buffer_load_dwordx4 a[124:127], v2, s[12:15], s49 offen offset:3072 sc1\n\t"
      "s_add_u32 s49, s49, 0x1000\n\t"
      "v_exp_f32 v150, v150\n\t"
      "v_exp_f32 v151, v151\n\t"
      "v_cvt_pk_bf16_f32 v138, v148, v149\n\t"
      "v_cvt_pk_bf16_f32 v139, v150, v151\n\t"
      "v_exp_f32 v152, v152\n\t"
      "v_exp_f32 v153, v153\n\t"
      "v_mfma_f32_32x32x16_bf16 v[192:207], a[136:139], v[136:139], v[192:207]\n\t"
      "s_add_u32 s48, s72, 0\n\t"
      "buffer_load_dwordx4 v[36:39], v8, s[16:19], s48 offen offset:0 sc1\n\t"
      "v_exp_f32 v154, v154\n\t"
      "v_exp_f32 v155, v155\n\t"
      "v_cvt_pk_bf16_f32 v140, v152, v153\n\t"
      "v_cvt_pk_bf16_f32 v141, v154, v155\n\t"
      "v_exp_f32 v156, v156\n\t"
      "v_exp_f32 v157, v157\n\t"
      "v_mfma_f32_32x32x16_bf16 v[176:191], a[132:135], a[84:87], v[176:191]\n\t"
      "s_add_u32 m0, m0, s70\n\t"
      "s_xor_b32 s48, s71, 16384\n\t"
      "buffer_load_dwordx4 v2, s[16:19], s48 offen sc1 lds\n\t"
      "v_exp_f32 v158, v158\n\t"
      "v_exp_f32 v159, v159\n\t"
      "v_cvt_pk_bf16_f32 v142, v156, v157\n\t"
      "v_cvt_pk_bf16_f32 v143, v158, v159\n\t"
      "v_exp_f32 v160, v160\n\t"
      "v_exp_f32 v161, v161\n\t"
      "v_mfma_f32_32x32x16_bf16 v[192:207], a[140:143], v[140:143], v[192:207]\n\t"
      "s_add_u32 m0, m0, s70\n\t"
      "s_xor_b32 s48, s71, 20480\n\t"
      "buffer_load_dwordx4 v2, s[16:19], s48 offen sc1 lds\n\t"
      "v_exp_f32 v162, v162\n\t"
      "v_exp_f32 v163, v163\n\t"
      "v_cvt_pk_bf16_f32 v136, v160, v161\n\t"
      "v_cvt_pk_bf16_f32 v137, v162, v163\n\t"
      "v_exp_f32 v164, v164\n\t"
      "v_exp_f32 v165, v165\n\t"
      "v_mfma_f32_32x32x16_bf16 v[144:159], a[128:131], a[88:91], 0\n\t"
      "v_exp_f32 v166, v166\n\t"
      "v_exp_f32 v167, v167\n\t"
      "v_cvt_pk_bf16_f32 v138, v164, v165\n\t"
      "v_cvt_pk_bf16_f32 v139, v166, v167\n\t"
      "v_exp_f32 v168, v168\n\t"
      "v_exp_f32 v169, v169\n\t"
      "v_mfma_f32_32x32x16_bf16 v[208:223], a[136:139], v[136:139], v[208:223]\n\t"
      "buffer_load_dwordx4 a[128:131], v2, s[12:15], s49 offen offset:0 sc1\n\t"
      "v_exp_f32 v170, v170\n\t"
      "v_exp_f32 v171, v171\n\t"
      "v_cvt_pk_bf16_f32 v140, v168, v169\n\t"
      "v_cvt_pk_bf16_f32 v141, v170, v171\n\t"
      "v_exp_f32 v172, v172\n\t"
      "v_exp_f32 v173, v173\n\t"
      "v_mfma_f32_32x32x16_bf16 v[144:159], a[132:135], a[92:95], v[144:159]\n\t"
      "buffer_load_dwordx2 a[188:189], v10, s[16:19], s73 offen offset:768 sc1\n\t"
      "v_exp_f32 v174, v174\n\t"
      "v_exp_f32 v175, v175\n\t"
      "v_cvt_pk_bf16_f32 v142, v172, v173\n\t"
      "v_cvt_pk_bf16_f32 v143, v174, v175\n\t"
      "v_exp_f32 v176, v176\n\t"
      "v_exp_f32 v177, v177\n\t"
      "v_mfma_f32_32x32x16_bf16 v[208:223], a[140:143], v[140:143], v[208:223]\n\t"
      "buffer_load_dwordx4 a[132:135], v2, s[12:15], s49 offen offset:1024 sc1\n\t"
      "s_waitcnt vmcnt(24)\n\t"
      "v_exp_f32 v178, v178\n\t"
      "v_exp_f32 v179, v179\n\t"
      "v_cvt_pk_bf16_f32 v136, v176, v177\n\t"
      "v_cvt_pk_bf16_f32 v137, v178, v179\n\t"
      "v_exp_f32 v180, v180\n\t"
      "v_exp_f32 v181, v181\n\t"
      "v_mfma_f32_32x32x16_bf16 v[160:175], a[144:147], a[64:67], 0\n\t"
      "s_add_u32 s48, s72, 0\n\t"
      "buffer_load_dwordx4 v[40:43], v9, s[16:19], s48 offen offset:1024 sc1\n\t"
      "v_exp_f32 v182, v182\n\t"
      "v_exp_f32 v183, v183\n\t"
      "v_cvt_pk_bf16_f32 v138, v180, v181\n\t"
      "v_cvt_pk_bf16_f32 v139, v182, v183\n\t"
      "v_exp_f32 v184, v184\n\t"
      "v_exp_f32 v185, v185\n\t"
      "v_mfma_f32_32x32x16_bf16 v[224:239], a[136:139], v[136:139], v[224:239]\n\t"
      "s_add_u32 m0, m0, s70\n\t"
      "s_xor_b32 s48, s71, 24576\n\t"
      "buffer_load_dwordx4 v2, s[16:19], s48 offen sc1 lds\n\t"
      "v_exp_f32 v186, v186\n\t"
      "v_exp_f32 v187, v187\n\t"
      "v_cvt_pk_bf16_f32 v140, v184, v185\n\t"
      "v_cvt_pk_bf16_f32 v141, v186, v187\n\t"
      "v_exp_f32 v188, v188\n\t"
      "v_exp_f32 v189, v189\n\t"
      "v_mfma_f32_32x32x16_bf16 v[160:175], a[148:151], a[68:71], v[160:175]\n\t"
      "v_exp_f32 v190, v190\n\t"
      "v_exp_f32 v191, v191\n\t"
      "v_cvt_pk_bf16_f32 v142, v188, v189\n\t"
      "v_cvt_pk_bf16_f32 v143, v190, v191\n\t"
      "v_exp_f32 v144, v144\n\t"
      "v_exp_f32 v145, v145\n\t"
      "v_mfma_f32_32x32x16_bf16 v[224:239], a[140:143], v[140:143], v[224:239]\n\t"
      "s_add_u32 s48, s72, 0\n\t"
      "buffer_load_dwordx4 v[44:47], v8, s[16:19], s48 offen offset:2048 sc1\n\t"
      "v_exp_f32 v146, v146\n\t"
      "v_exp_f32 v147, v147\n\t"
      "v_cvt_pk_bf16_f32 v136, v144, v145\n\t"
      "v_cvt_pk_bf16_f32 v137, v146, v147\n\t"
      "v_exp_f32 v148, v148\n\t"
      "v_exp_f32 v149, v149\n\t"
      "v_mfma_f32_32x32x16_bf16 v[176:191], a[144:147], a[72:75], 0\n\t"
      "s_add_u32 m0, m0, s70\n\t"
      "s_xor_b32 s48, s71, 32768\n\t"
      "buffer_load_dwordx4 v2, s[16:19], s48 offen sc1 lds\n\t"
      "v_exp_f32 v150, v150\n\t"
      "v_exp_f32 v151, v151\n\t"
      "v_cvt_pk_bf16_f32 v138, v148, v149\n\t"
      "v_cvt_pk_bf16_f32 v139, v150, v151\n\t"
      "v_exp_f32 v152, v152\n\t"
      "v_exp_f32 v153, v153\n\t"
      "v_mfma_f32_32x32x16_bf16 v[240:255], a[136:139], v[136:139], v[240:255]\n\t"
      "v_exp_f32 v154, v154\n\t"
      "v_exp_f32 v155, v155\n\t"
      "v_cvt_pk_bf16_f32 v140, v152, v153\n\t"
      "v_cvt_pk_bf16_f32 v141, v154, v155\n\t"
      "v_exp_f32 v156, v156\n\t"
      "v_exp_f32 v157, v157\n\t"
      "v_mfma_f32_32x32x16_bf16 v[176:191], a[148:151], a[76:79], v[176:191]\n\t"
      "buffer_load_dwordx4 a[136:139], v2, s[12:15], s49 offen offset:2048 sc1\n\t"
      "v_exp_f32 v158, v158\n\t"
      "v_exp_f32 v159, v159\n\t"
      "v_cvt_pk_bf16_f32 v142, v156, v157\n\t"
      "v_cvt_pk_bf16_f32 v143, v158, v159\n\t"
      "v_exp_f32 v160, v160\n\t"
      "v_exp_f32 v161, v161\n\t"
      "v_mfma_f32_32x32x16_bf16 v[240:255], a[140:143], v[140:143], v[240:255]\n\t"
      "s_add_u32 s48, s67, 5120\n\t"
      "buffer_store_dwordx4 a[164:167], v2, s[28:31], s48 offen sc1\n\t"
      "v_exp_f32 v162, v162\n\t"
      "v_exp_f32 v163, v163\n\t"
      "v_cvt_pk_bf16_f32 v136, v160, v161\n\t"
      "v_cvt_pk_bf16_f32 v137, v162, v163\n\t"
      "v_exp_f32 v164, v164\n\t"
      "v_exp_f32 v165, v165\n\t"
      "v_mfma_f32_32x32x16_bf16 v[144:159], a[144:147], a[80:83], 0\n\t"
      "buffer_load_dwordx4 a[140:143], v2, s[12:15], s49 offen offset:3072 sc1\n\t"
      "s_add_u32 s49, s49, 0x1000\n\t"
      "v_exp_f32 v166, v166\n\t"
      "v_exp_f32 v167, v167\n\t"
      "v_cvt_pk_bf16_f32 v138, v164, v165\n\t"
      "v_cvt_pk_bf16_f32 v139, v166, v167\n\t"
      "v_exp_f32 v168, v168\n\t"
      "v_exp_f32 v169, v169\n\t"
      "v_mfma_f32_32x32x16_bf16 v[192:207], a[152:155], v[136:139], v[192:207]\n\t"
      "s_add_u32 s48, s72, 0\n\t"
      "buffer_load_dwordx4 v[48:51], v9, s[16:19], s48 offen offset:3072 sc1\n\t"
      "v_exp_f32 v170, v170\n\t"
      "v_exp_f32 v171, v171\n\t"
      "v_cvt_pk_bf16_f32 v140, v168, v169\n\t"
      "v_cvt_pk_bf16_f32 v141, v170, v171\n\t"
      "v_exp_f32 v172, v172\n\t"
      "v_exp_f32 v173, v173\n\t"
      "v_mfma_f32_32x32x16_bf16 v[144:159], a[148:151], a[84:87], v[144:159]\n\t"
      "s_add_u32 m0, m0, s70\n\t"
      "s_xor_b32 s48, s71, 36864\n\t"
      "buffer_load_dwordx4 v2, s[16:19], s48 offen sc1 lds\n\t"
      "v_exp_f32 v174, v174\n\t"
      "v_exp_f32 v175, v175\n\t"
      "v_cvt_pk_bf16_f32 v142, v172, v173\n\t"
      "v_cvt_pk_bf16_f32 v143, v174, v175\n\t"
      "v_exp_f32 v176, v176\n\t"
      "v_exp_f32 v177, v177\n\t"
      "v_mfma_f32_32x32x16_bf16 v[192:207], a[156:159], v[140:143], v[192:207]\n\t"
      "s_add_u32 m0, m0, s70\n\t"
      "s_xor_b32 s48, s71, 40960\n\t"
      "buffer_load_dwordx4 v2, s[16:19], s48 offen sc1 lds\n\t"
      "v_exp_f32 v178, v178\n\t"
      "v_exp_f32 v179, v179\n\t"
      "v_cvt_pk_bf16_f32 v136, v176, v177\n\t"
      "v_cvt_pk_bf16_f32 v137, v178, v179\n\t"
      "v_exp_f32 v180, v180\n\t"
      "v_exp_f32 v181, v181\n\t"
      "v_mfma_f32_32x32x16_bf16 v[160:175], a[144:147], a[88:91], 0\n\t"
      "v_exp_f32 v182, v182\n\t"
      "v_exp_f32 v183, v183\n\t"
      "v_cvt_pk_bf16_f32 v138, v180, v181\n\t"
      "v_cvt_pk_bf16_f32 v139, v182, v183\n\t"
      "v_exp_f32 v184, v184\n\t"
      "v_exp_f32 v185, v185\n\t"
      "v_mfma_f32_32x32x16_bf16 v[208:223], a[152:155], v[136:139], v[208:223]\n\t"
      "buffer_load_dwordx4 a[144:147], v2, s[12:15], s49 offen offset:0 sc1\n\t"
      "v_exp_f32 v186, v186\n\t"
      "v_exp_f32 v187, v187\n\t"
      "v_cvt_pk_bf16_f32 v140, v184, v185\n\t"
      "v_cvt_pk_bf16_f32 v141, v186, v187\n\t"
      "v_exp_f32 v188, v188\n\t"
      "v_exp_f32 v189, v189\n\t"
      "v_mfma_f32_32x32x16_bf16 v[160:175], a[148:151], a[92:95], v[160:175]\n\t"
      "s_add_u32 s48, s72, 4096\n\t"
      "buffer_load_dwordx4 v[52:55], v8, s[16:19], s48 offen offset:0 sc1\n\t"
      "v_exp_f32 v190, v190\n\t"
      "v_exp_f32 v191, v191\n\t"
      "v_cvt_pk_bf16_f32 v142, v188, v189\n\t"
      "v_cvt_pk_bf16_f32 v143, v190, v191\n\t"
      "v_exp_f32 v144, v144\n\t"
      "v_exp_f32 v145, v145\n\t"
      "v_mfma_f32_32x32x16_bf16 v[208:223], a[156:159], v[140:143], v[208:223]\n\t"
      "buffer_load_dwordx4 a[148:151], v2, s[12:15], s49 offen offset:1024 sc1\n\t"
      "s_waitcnt vmcnt(31)\n\t"
      "v_exp_f32 v146, v146\n\t"
      "v_exp_f32 v147, v147\n\t"
      "v_cvt_pk_bf16_f32 v136, v144, v145\n\t"
      "v_cvt_pk_bf16_f32 v137, v146, v147\n\t"
      "v_exp_f32 v148, v148\n\t"
      "v_exp_f32 v149, v149\n\t"
      "v_mfma_f32_32x32x16_bf16 v[176:191], a[96:99], a[64:67], 0\n\t"
      "s_add_u32 s48, s72, 4096\n\t"
      "buffer_load_dwordx4 v[56:59], v9, s[16:19], s48 offen offset:1024 sc1\n\t"
      "v_exp_f32 v150, v150\n\t"
      "v_exp_f32 v151, v151\n\t"
      "v_cvt_pk_bf16_f32 v138, v148, v149\n\t"
      "v_cvt_pk_bf16_f32 v139, v150, v151\n\t"
      "v_exp_f32 v152, v152\n\t"
      "v_exp_f32 v153, v153\n\t"
      "v_mfma_f32_32x32x16_bf16 v[224:239], a[152:155], v[136:139], v[224:239]\n\t"
      "s_add_u32 m0, m0, s70\n\t"
      "s_xor_b32 s48, s71, 45056\n\t"
      "buffer_load_dwordx4 v2, s[16:19], s48 offen sc1 lds\n\t"
      "v_exp_f32 v154, v154\n\t"
      "v_exp_f32 v155, v155\n\t"
      "v_cvt_pk_bf16_f32 v140, v152, v153\n\t"
      "v_cvt_pk_bf16_f32 v141, v154, v155\n\t"
      "v_exp_f32 v156, v156\n\t"
      "v_exp_f32 v157, v157\n\t"
      "v_mfma_f32_32x32x16_bf16 v[176:191], a[100:103], a[68:71], v[176:191]\n\t"
      "v_exp_f32 v158, v158\n\t"
      "v_exp_f32 v159, v159\n\t"
      "v_cvt_pk_bf16_f32 v142, v156, v157\n\t"
      "v_cvt_pk_bf16_f32 v143, v158, v159\n\t"
      "v_exp_f32 v160, v160\n\t"
      "v_exp_f32 v161, v161\n\t"
      "v_mfma_f32_32x32x16_bf16 v[224:239], a[156:159], v[140:143], v[224:239]\n\t"
      "s_add_u32 s48, s72, 4096\n\t"
      "buffer_load_dwordx4 v[60:63], v8, s[16:19], s48 offen offset:2048 sc1\n\t"
      "v_exp_f32 v162, v162\n\t"
      "v_exp_f32 v163, v163\n\t"
      "v_cvt_pk_bf16_f32 v136, v160, v161\n\t"
      "v_cvt_pk_bf16_f32 v137, v162, v163\n\t"
      "v_exp_f32 v164, v164\n\t"
      "v_exp_f32 v165, v165\n\t"
      "v_mfma_f32_32x32x16_bf16 v[144:159], a[96:99], a[72:75], 0\n\t"
      "s_add_u32 m0, m0, s70\n\t"
      "s_xor_b32 s48, s71, 49152\n\t"
      "buffer_load_dwordx4 v2, s[16:19], s48 offen sc1 lds\n\t"
      "v_exp_f32 v166, v166\n\t"
      "v_exp_f32 v167, v167\n\t"
      "v_cvt_pk_bf16_f32 v138, v164, v165\n\t"
      "v_cvt_pk_bf16_f32 v139, v166, v167\n\t"
      "v_exp_f32 v168, v168\n\t"
      "v_exp_f32 v169, v169\n\t"
      "v_mfma_f32_32x32x16_bf16 v[240:255], a[152:155], v[136:139], v[240:255]\n\t"
      "v_exp_f32 v170, v170\n\t"
      "v_exp_f32 v171, v171\n\t"
      "v_cvt_pk_bf16_f32 v140, v168, v169\n\t"
      "v_cvt_pk_bf16_f32 v141, v170, v171\n\t"
      "v_exp_f32 v172, v172\n\t"
      "v_exp_f32 v173, v173\n\t"
      "v_mfma_f32_32x32x16_bf16 v[144:159], a[100:103], a[76:79], v[144:159]\n\t"
      "buffer_load_dwordx4 a[152:155], v2, s[12:15], s49 offen offset:2048 sc1\n\t"
      "v_exp_f32 v174, v174\n\t"
      "v_exp_f32 v175, v175\n\t"
      "v_cvt_pk_bf16_f32 v142, v172, v173\n\t"
      "v_cvt_pk_bf16_f32 v143, v174, v175\n\t"
      "v_exp_f32 v176, v176\n\t"
      "v_exp_f32 v177, v177\n\t"
      "v_mfma_f32_32x32x16_bf16 v[240:255], a[156:159], v[140:143], v[240:255]\n\t"
      "s_add_u32 s48, s67, 6144\n\t"
      "buffer_store_dwordx4 a[164:167], v2, s[28:31], s48 offen sc1\n\t"
      "v_exp_f32 v178, v178\n\t"
      "v_exp_f32 v179, v179\n\t"
      "v_cvt_pk_bf16_f32 v136, v176, v177\n\t"
      "v_cvt_pk_bf16_f32 v137, v178, v179\n\t"
      "v_exp_f32 v180, v180\n\t"
      "v_exp_f32 v181, v181\n\t"
      "v_mfma_f32_32x32x16_bf16 v[160:175], a[96:99], a[80:83], 0\n\t"
      "buffer_load_dwordx4 a[156:159], v2, s[12:15], s49 offen offset:3072 sc1\n\t"
      "s_add_u32 s49, s49, 0x1000\n\t"
      "v_exp_f32 v182, v182\n\t"
      "v_exp_f32 v183, v183\n\t"
      "v_cvt_pk_bf16_f32 v138, v180, v181\n\t"
      "v_cvt_pk_bf16_f32 v139, v182, v183\n\t"
      "v_exp_f32 v184, v184\n\t"
      "v_exp_f32 v185, v185\n\t"
      "v_mfma_f32_32x32x16_bf16 v[192:207], a[104:107], v[136:139], v[192:207]\n\t"
      "s_waitcnt vmcnt(21)\n\t"
      "v_mfma_f32_32x32x16_bf16 a[0:15], a[176:179], a[168:171], a[0:15]\n\t"
      "v_exp_f32 v186, v186\n\t"
      "v_exp_f32 v187, v187\n\t"
      "v_cvt_pk_bf16_f32 v140, v184, v185\n\t"
      "v_cvt_pk_bf16_f32 v141, v186, v187\n\t"
      "v_exp_f32 v188, v188\n\t"
      "v_exp_f32 v189, v189\n\t"
      "v_mfma_f32_32x32x16_bf16 v[160:175], a[100:103], a[84:87], v[160:175]\n\t"
      "s_add_u32 m0, m0, s70\n\t"
      "s_xor_b32 s48, s71, 53248\n\t"
      "buffer_load_dwordx4 v2, s[16:19], s48 offen sc1 lds\n\t"
      "v_exp_f32 v190, v190\n\t"
      "v_exp_f32 v191, v191\n\t"
      "v_cvt_pk_bf16_f32 v142, v188, v189\n\t"
      "v_cvt_pk_bf16_f32 v143, v190, v191\n\t"
      "v_exp_f32 v144, v144\n\t"
      "v_exp_f32 v145, v145\n\t"
      "v_mfma_f32_32x32x16_bf16 v[192:207], a[108:111], v[140:143], v[192:207]\n\t"
      "s_add_u32 m0, m0, s70\n\t"
      "s_xor_b32 s48, s71, 57344\n\t"
      "buffer_load_dwordx4 v2, s[16:19], s48 offen sc1 lds\n\t"
      "v_exp_f32 v146, v146\n\t"
      "v_exp_f32 v147, v147\n\t"
      "v_cvt_pk_bf16_f32 v136, v144, v145\n\t"
      "v_cvt_pk_bf16_f32 v137, v146, v147\n\t"
      "v_exp_f32 v148, v148\n\t"
      "v_exp_f32 v149, v149\n\t"
      "v_mfma_f32_32x32x16_bf16 v[176:191], a[96:99], a[88:91], 0\n\t"
      "v_exp_f32 v150, v150\n\t"
      "v_exp_f32 v151, v151\n\t"
      "v_cvt_pk_bf16_f32 v138, v148, v149\n\t"
      "v_cvt_pk_bf16_f32 v139, v150, v151\n\t"
      "v_exp_f32 v152, v152\n\t"
      "v_exp_f32 v153, v153\n\t"
      "v_mfma_f32_32x32x16_bf16 v[208:223], a[104:107], v[136:139], v[208:223]\n\t"
      "buffer_load_dwordx4 a[96:99], v2, s[12:15], s49 offen offset:0 sc1\n\t"
      "v_exp_f32 v154, v154\n\t"
      "v_exp_f32 v155, v155\n\t"
      "v_cvt_pk_bf16_f32 v140, v152, v153\n\t"
      "v_cvt_pk_bf16_f32 v141, v154, v155\n\t"
      "v_exp_f32 v156, v156\n\t"
      "v_exp_f32 v157, v157\n\t"
      "v_mfma_f32_32x32x16_bf16 v[176:191], a[100:103], a[92:95], v[176:191]\n\t"
      "v_mfma_f32_32x32x16_bf16 a[16:31], a[180:183], a[168:171], a[16:31]\n\t"
      "v_exp_f32 v158, v158\n\t"
      "v_exp_f32 v159, v159\n\t"
      "v_cvt_pk_bf16_f32 v142, v156, v157\n\t"
      "v_cvt_pk_bf16_f32 v143, v158, v159\n\t"
      "v_exp_f32 v160, v160\n\t"
      "v_exp_f32 v161, v161\n\t"
      "v_mfma_f32_32x32x16_bf16 v[208:223], a[108:111], v[140:143], v[208:223]\n\t"
      "buffer_load_dwordx4 a[100:103], v2, s[12:15], s49 offen offset:1024 sc1\n\t"
      "s_waitcnt vmcnt(30)\n\t"
      "v_exp_f32 v162, v162\n\t"
      "v_exp_f32 v163, v163\n\t"
      "v_cvt_pk_bf16_f32 v136, v160, v161\n\t"
      "v_cvt_pk_bf16_f32 v137, v162, v163\n\t"
      "v_exp_f32 v164, v164\n\t"
      "v_exp_f32 v165, v165\n\t"
      "v_mfma_f32_32x32x16_bf16 v[144:159], a[112:115], a[64:67], 0\n\t"
      "v_mfma_f32_32x32x16_bf16 a[32:47], a[184:187], a[168:171], a[32:47]\n\t"
      "v_exp_f32 v166, v166\n\t"
      "v_exp_f32 v167, v167\n\t"
      "v_cvt_pk_bf16_f32 v138, v164, v165\n\t"
      "v_cvt_pk_bf16_f32 v139, v166, v167\n\t"
      "v_exp_f32 v168, v168\n\t"
      "v_exp_f32 v169, v169\n\t"
      "v_mfma_f32_32x32x16_bf16 v[224:239], a[104:107], v[136:139], v[224:239]\n\t"
      "s_add_u32 m0, m0, s70\n\t"
      "s_xor_b32 s48, s71, 65536\n\t"
      "buffer_load_dwordx4 v2, s[16:19], s48 offen sc1 lds\n\t"
      "v_exp_f32 v170, v170\n\t"
      "v_exp_f32 v171, v171\n\t"
      "v_cvt_pk_bf16_f32 v140, v168, v169\n\t"
      "v_cvt_pk_bf16_f32 v141, v170, v171\n\t"
      "v_exp_f32 v172, v172\n\t"
      "v_exp_f32 v173, v173\n\t"
      "v_mfma_f32_32x32x16_bf16 v[144:159], a[116:119], a[68:71], v[144:159]\n\t"
      "v_exp_f32 v174, v174\n\t"
      "v_exp_f32 v175, v175\n\t"
      "v_cvt_pk_bf16_f32 v142, v172, v173\n\t"
      "v_cvt_pk_bf16_f32 v143, v174, v175\n\t"
      "v_exp_f32 v176, v176\n\t"
      "v_exp_f32 v177, v177\n\t"
      "v_mfma_f32_32x32x16_bf16 v[224:239], a[108:111], v[140:143], v[224:239]\n\t"
      "v_mfma_f32_32x32x16_bf16 a[48:63], a[188:191], a[168:171], a[48:63]\n\t"
      "v_exp_f32 v178, v178\n\t"
      "v_exp_f32 v179, v179\n\t"
      "v_cvt_pk_bf16_f32 v136, v176, v177\n\t"
      "v_cvt_pk_bf16_f32 v137, v178, v179\n\t"
      "v_exp_f32 v180, v180\n\t"
      "v_exp_f32 v181, v181\n\t"
      "v_mfma_f32_32x32x16_bf16 v[160:175], a[112:115], a[72:75], 0\n\t"
      "s_add_u32 m0, m0, s70\n\t"
      "s_xor_b32 s48, s71, 69632\n\t"
      "buffer_load_dwordx4 v2, s[16:19], s48 offen sc1 lds\n\t"
      "v_exp_f32 v182, v182\n\t"
      "v_exp_f32 v183, v183\n\t"
      "v_cvt_pk_bf16_f32 v138, v180, v181\n\t"
      "v_cvt_pk_bf16_f32 v139, v182, v183\n\t"
      "v_exp_f32 v184, v184\n\t"
      "v_exp_f32 v185, v185\n\t"
      "v_mfma_f32_32x32x16_bf16 v[240:255], a[104:107], v[136:139], v[240:255]\n\t"
      "v_exp_f32 v186, v186\n\t"
      "v_exp_f32 v187, v187\n\t"
      "v_cvt_pk_bf16_f32 v140, v184, v185\n\t"
      "v_cvt_pk_bf16_f32 v141, v186, v187\n\t"
      "v_exp_f32 v188, v188\n\t"
      "v_exp_f32 v189, v189\n\t"
      "v_mfma_f32_32x32x16_bf16 v[160:175], a[116:119], a[76:79], v[160:175]\n\t"
      "buffer_load_dwordx4 a[104:107], v2, s[12:15], s49 offen offset:2048 sc1\n\t"
      "v_exp_f32 v190, v190\n\t"
      "v_exp_f32 v191, v191\n\t"
      "v_cvt_pk_bf16_f32 v142, v188, v189\n\t"
      "v_cvt_pk_bf16_f32 v143, v190, v191\n\t"
      "v_exp_f32 v144, v144\n\t"
      "v_exp_f32 v145, v145\n\t"
      "v_mfma_f32_32x32x16_bf16 v[240:255], a[108:111], v[140:143], v[240:255]\n\t"
      "s_add_u32 s48, s67, 7168\n\t"
      "buffer_store_dwordx4 a[164:167], v2, s[28:31], s48 offen sc1\n\t"
      "v_exp_f32 v146, v146\n\t"
      "v_exp_f32 v147, v147\n\t"
      "v_cvt_pk_bf16_f32 v136, v144, v145\n\t"
      "v_cvt_pk_bf16_f32 v137, v146, v147\n\t"
      "v_exp_f32 v148, v148\n\t"
      "v_exp_f32 v149, v149\n\t"
      "v_mfma_f32_32x32x16_bf16 v[176:191], a[112:115], a[80:83], 0\n\t"
      "buffer_load_dwordx4 a[108:111], v2, s[12:15], s49 offen offset:3072 sc1\n\t"
      "s_add_u32 s49, s49, 0x1000\n\t"
      "v_exp_f32 v150, v150\n\t"
      "v_exp_f32 v151, v151\n\t"
      "v_cvt_pk_bf16_f32 v138, v148, v149\n\t"
      "v_cvt_pk_bf16_f32 v139, v150, v151\n\t"
      "v_exp_f32 v152, v152\n\t"
      "v_exp_f32 v153, v153\n\t"
      "v_mfma_f32_32x32x16_bf16 v[192:207], a[120:123], v[136:139], v[192:207]\n\t"
      "s_add_u32 s48, s72, 4096\n\t"
      "buffer_load_dwordx4 v[64:67], v9, s[16:19], s48 offen offset:3072 sc1\n\t"
      "v_exp_f32 v154, v154\n\t"
      "v_exp_f32 v155, v155\n\t"
      "v_cvt_pk_bf16_f32 v140, v152, v153\n\t"
      "v_cvt_pk_bf16_f32 v141, v154, v155\n\t"
      "v_exp_f32 v156, v156\n\t"
      "v_exp_f32 v157, v157\n\t"
      "v_mfma_f32_32x32x16_bf16 v[176:191], a[116:119], a[84:87], v[176:191]\n\t"
      "s_add_u32 m0, m0, s70\n\t"
      "s_xor_b32 s48, s71, 73728\n\t"
      "buffer_load_dwordx4 v2, s[16:19], s48 offen sc1 lds\n\t"
      "v_exp_f32 v158, v158\n\t"
      "v_exp_f32 v159, v159\n\t"
      "v_cvt_pk_bf16_f32 v142, v156, v157\n\t"
      "v_cvt_pk_bf16_f32 v143, v158, v159\n\t"
      "v_exp_f32 v160, v160\n\t"
      "v_exp_f32 v161, v161\n\t"
      "v_mfma_f32_32x32x16_bf16 v[192:207], a[124:127], v[140:143], v[192:207]\n\t"
      "s_add_u32 m0, m0, s70\n\t"
      "s_xor_b32 s48, s71, 77824\n\t"
      "buffer_load_dwordx4 v2, s[16:19], s48 offen sc1 lds\n\t"
      "v_exp_f32 v162, v162\n\t"
      "v_exp_f32 v163, v163\n\t"
      "v_cvt_pk_bf16_f32 v136, v160, v161\n\t"
      "v_cvt_pk_bf16_f32 v137, v162, v163\n\t"
      "v_exp_f32 v164, v164\n\t"
      "v_exp_f32 v165, v165\n\t"
      "v_mfma_f32_32x32x16_bf16 v[144:159], a[112:115], a[88:91], 0\n\t"
      "v_exp_f32 v166, v166\n\t"
      "v_exp_f32 v167, v167\n\t"
      "v_cvt_pk_bf16_f32 v138, v164, v165\n\t"
      "v_cvt_pk_bf16_f32 v139, v166, v167\n\t"
      "v_exp_f32 v168, v168\n\t"
      "v_exp_f32 v169, v169\n\t"
      "v_mfma_f32_32x32x16_bf16 v[208:223], a[120:123], v[136:139], v[208:223]\n\t"
      "buffer_load_dwordx4 a[112:115], v2, s[12:15], s49 offen offset:0 sc1\n\t"
      "v_exp_f32 v170, v170\n\t"
      "v_exp_f32 v171, v171\n\t"
      "v_cvt_pk_bf16_f32 v140, v168, v169\n\t"
      "v_cvt_pk_bf16_f32 v141, v170, v171\n\t"
      "v_exp_f32 v172, v172\n\t"
      "v_exp_f32 v173, v173\n\t"
      "v_mfma_f32_32x32x16_bf16 v[144:159], a[116:119], a[92:95], v[144:159]\n\t"
      "s_add_u32 s48, s72, 8192\n\t"
      "buffer_load_dwordx4 v[68:71], v8, s[16:19], s48 offen offset:0 sc1\n\t"
      "v_exp_f32 v174, v174\n\t"
      "v_exp_f32 v175, v175\n\t"
      "v_cvt_pk_bf16_f32 v142, v172, v173\n\t"
      "v_cvt_pk_bf16_f32 v143, v174, v175\n\t"
      "v_exp_f32 v176, v176\n\t"
      "v_exp_f32 v177, v177\n\t"
      "v_mfma_f32_32x32x16_bf16 v[208:223], a[124:127], v[140:143], v[208:223]\n\t"
      "buffer_load_dwordx4 a[116:119], v2, s[12:15], s49 offen offset:1024 sc1\n\t"
      "s_waitcnt vmcnt(28)\n\t"
      "v_exp_f32 v178, v178\n\t"
      "v_exp_f32 v179, v179\n\t"
      "v_cvt_pk_bf16_f32 v136, v176, v177\n\t"
      "v_cvt_pk_bf16_f32 v137, v178, v179\n\t"
      "v_exp_f32 v180, v180\n\t"
      "v_exp_f32 v181, v181\n\t"
      "v_mfma_f32_32x32x16_bf16 v[160:175], a[128:131], a[64:67], 0\n\t"
      "s_add_u32 s48, s72, 8192\n\t"
      "buffer_load_dwordx4 v[72:75], v9, s[16:19], s48 offen offset:1024 sc1\n\t"
      "v_exp_f32 v182, v182\n\t"
      "v_exp_f32 v183, v183\n\t"
      "v_cvt_pk_bf16_f32 v138, v180, v181\n\t"
      "v_cvt_pk_bf16_f32 v139, v182, v183\n\t"
      "v_exp_f32 v184, v184\n\t"
      "v_exp_f32 v185, v185\n\t"
      "v_mfma_f32_32x32x16_bf16 v[224:239], a[120:123], v[136:139], v[224:239]\n\t"
      "s_add_u32 m0, m0, s70\n\t"
      "s_xor_b32 s48, s71, 81920\n\t"
      "buffer_load_dwordx4 v2, s[16:19], s48 offen sc1 lds\n\t"
      "v_exp_f32 v186, v186\n\t"
      "v_exp_f32 v187, v187\n\t"
      "v_cvt_pk_bf16_f32 v140, v184, v185\n\t"
      "v_cvt_pk_bf16_f32 v141, v186, v187\n\t"
      "v_exp_f32 v188, v188\n\t"
      "v_exp_f32 v189, v189\n\t"
      "v_mfma_f32_32x32x16_bf16 v[160:175], a[132:135], a[68:71], v[160:175]\n\t"
      "v_exp_f32 v190, v190\n\t"
      "v_exp_f32 v191, v191\n\t"
      "v_cvt_pk_bf16_f32 v142, v188, v189\n\t"
      "v_cvt_pk_bf16_f32 v143, v190, v191\n\t"
      "v_exp_f32 v144, v144\n\t"
      "v_exp_f32 v145, v145\n\t"
      "v_mfma_f32_32x32x16_bf16 v[224:239], a[124:127], v[140:143], v[224:239]\n\t"
      "s_add_u32 s48, s72, 8192\n\t"
      "buffer_load_dwordx4 v[76:79], v8, s[16:19], s48 offen offset:2048 sc1\n\t"
      "v_exp_f32 v146, v146\n\t"
      "v_exp_f32 v147, v147\n\t"
      "v_cvt_pk_bf16_f32 v136, v144, v145\n\t"
      "v_cvt_pk_bf16_f32 v137, v146, v147\n\t"
      "v_exp_f32 v148, v148\n\t"
      "v_exp_f32 v149, v149\n\t"
      "v_mfma_f32_32x32x16_bf16 v[176:191], a[128:131], a[72:75], 0\n\t"
      "s_add_u32 m0, m0, s70\n\t"
      "s_xor_b32 s48, s71, 86016\n\t"
      "buffer_load_dwordx4 v2, s[16:19], s48 offen sc1 lds\n\t"
      "v_exp_f32 v150, v150\n\t"
      "v_exp_f32 v151, v151\n\t"
      "v_cvt_pk_bf16_f32 v138, v148, v149\n\t"
      "v_cvt_pk_bf16_f32 v139, v150, v151\n\t"
      "v_exp_f32 v152, v152\n\t"
      "v_exp_f32 v153, v153\n\t"
      "v_mfma_f32_32x32x16_bf16 v[240:255], a[120:123], v[136:139], v[240:255]\n\t"
      "v_exp_f32 v154, v154\n\t"
      "v_exp_f32 v155, v155\n\t"
      "v_cvt_pk_bf16_f32 v140, v152, v153\n\t"
      "v_cvt_pk_bf16_f32 v141, v154, v155\n\t"
      "v_exp_f32 v156, v156\n\t"
      "v_exp_f32 v157, v157\n\t"
      "v_mfma_f32_32x32x16_bf16 v[176:191], a[132:135], a[76:79], v[176:191]\n\t"
      "buffer_load_dwordx4 a[120:123], v2, s[12:15], s49 offen offset:2048 sc1\n\t"
      "v_exp_f32 v158, v158\n\t"
      "v_exp_f32 v159, v159\n\t"
      "v_cvt_pk_bf16_f32 v142, v156, v157\n\t"
      "v_cvt_pk_bf16_f32 v143, v158, v159\n\t"
      "v_exp_f32 v160, v160\n\t"
      "v_exp_f32 v161, v161\n\t"
      "v_mfma_f32_32x32x16_bf16 v[240:255], a[124:127], v[140:143], v[240:255]\n\t"
      "s_add_u32 s48, s67, 8192\n\t"
      "buffer_store_dwordx4 a[164:167], v2, s[28:31], s48 offen sc1\n\t"
      "v_exp_f32 v162, v162\n\t"
      "v_exp_f32 v163, v163\n\t"
      "v_cvt_pk_bf16_f32 v136, v160, v161\n\t"
      "v_cvt_pk_bf16_f32 v137, v162, v163\n\t"
      "v_exp_f32 v164, v164\n\t"
      "v_exp_f32 v165, v165\n\t"
      "v_mfma_f32_32x32x16_bf16 v[144:159], a[128:131], a[80:83], 0\n\t"
      "buffer_load_dwordx4 a[124:127], v2, s[12:15], s49 offen offset:3072 sc1\n\t"
      "s_add_u32 s49, s49, 0x1000\n\t"
      "v_exp_f32 v166, v166\n\t"
      "v_exp_f32 v167, v167\n\t"
      "v_cvt_pk_bf16_f32 v138, v164, v165\n\t"
      "v_cvt_pk_bf16_f32 v139, v166, v167\n\t"
      "v_exp_f32 v168, v168\n\t"
      "v_exp_f32 v169, v169\n\t"
      "v_mfma_f32_32x32x16_bf16 v[192:207], a[136:139], v[136:139], v[192:207]\n\t"
      "s_add_u32 s48, s72, 8192\n\t"
      "buffer_load_dwordx4 v[80:83], v9, s[16:19], s48 offen offset:3072 sc1\n\t"
      "v_exp_f32 v170, v170\n\t"
      "v_exp_f32 v171, v171\n\t"
      "v_cvt_pk_bf16_f32 v140, v168, v169\n\t"
      "v_cvt_pk_bf16_f32 v141, v170, v171\n\t"
      "v_exp_f32 v172, v172\n\t"
      "v_exp_f32 v173, v173\n\t"
      "v_mfma_f32_32x32x16_bf16 v[144:159], a[132:135], a[84:87], v[144:159]\n\t"
      "s_add_u32 m0, m0, s70\n\t"
      "s_xor_b32 s48, s71, 90112\n\t"
      "buffer_load_dwordx4 v2, s[16:19], s48 offen sc1 lds\n\t"
      "v_exp_f32 v174, v174\n\t"
      "v_exp_f32 v175, v175\n\t"
      "v_cvt_pk_bf16_f32 v142, v172, v173\n\t"
      "v_cvt_pk_bf16_f32 v143, v174, v175\n\t"
      "v_exp_f32 v176, v176\n\t"
      "v_exp_f32 v177, v177\n\t"
      "v_mfma_f32_32x32x16_bf16 v[192:207], a[140:143], v[140:143], v[192:207]\n\t"
      "s_add_u32 m0, m0, s70\n\t"
      "s_xor_b32 s48, s71, 98304\n\t"
      "buffer_load_dwordx4 v2, s[16:19], s48 offen sc1 lds\n\t"
      "v_exp_f32 v178, v178\n\t"
      "v_exp_f32 v179, v179\n\t"
      "v_cvt_pk_bf16_f32 v136, v176, v177\n\t"
      "v_cvt_pk_bf16_f32 v137, v178, v179\n\t"
      "v_exp_f32 v180, v180\n\t"
      "v_exp_f32 v181, v181\n\t"
      "v_mfma_f32_32x32x16_bf16 v[160:175], a[128:131], a[88:91], 0\n\t"
      "v_exp_f32 v182, v182\n\t"
      "v_exp_f32 v183, v183\n\t"
      "v_cvt_pk_bf16_f32 v138, v180, v181\n\t"
      "v_cvt_pk_bf16_f32 v139, v182, v183\n\t"
      "v_exp_f32 v184, v184\n\t"
      "v_exp_f32 v185, v185\n\t"
      "v_mfma_f32_32x32x16_bf16 v[208:223], a[136:139], v[136:139], v[208:223]\n\t"
      "buffer_load_dwordx4 a[128:131], v2, s[12:15], s49 offen offset:0 sc1\n\t"
      "v_exp_f32 v186, v186\n\t"
      "v_exp_f32 v187, v187\n\t"
      "v_cvt_pk_bf16_f32 v140, v184, v185\n\t"
      "v_cvt_pk_bf16_f32 v141, v186, v187\n\t"
      "v_exp_f32 v188, v188\n\t"
      "v_exp_f32 v189, v189\n\t"
      "v_mfma_f32_32x32x16_bf16 v[160:175], a[132:135], a[92:95], v[160:175]\n\t"
      "s_add_u32 s48, s72, 12288\n\t"
      "buffer_load_dwordx4 v[84:87], v8, s[16:19], s48 offen offset:0 sc1\n\t"
      "v_exp_f32 v190, v190\n\t"
      "v_exp_f32 v191, v191\n\t"
      "v_cvt_pk_bf16_f32 v142, v188, v189\n\t"
      "v_cvt_pk_bf16_f32 v143, v190, v191\n\t"
      "v_exp_f32 v144, v144\n\t"
      "v_exp_f32 v145, v145\n\t"
      "v_mfma_f32_32x32x16_bf16 v[208:223], a[140:143], v[140:143], v[208:223]\n\t"
      "buffer_load_dwordx4 a[132:135], v2, s[12:15], s49 offen offset:1024 sc1\n\t"
      "s_waitcnt vmcnt(28)\n\t"
      "v_exp_f32 v146, v146\n\t"
      "v_exp_f32 v147, v147\n\t"
      "v_cvt_pk_bf16_f32 v136, v144, v145\n\t"
      "v_cvt_pk_bf16_f32 v137, v146, v147\n\t"
      "v_exp_f32 v148, v148\n\t"
      "v_exp_f32 v149, v149\n\t"
      "v_mfma_f32_32x32x16_bf16 v[176:191], a[144:147], a[64:67], 0\n\t"
      "s_add_u32 s48, s72, 12288\n\t"
      "buffer_load_dwordx4 v[88:91], v9, s[16:19], s48 offen offset:1024 sc1\n\t"
      "v_exp_f32 v150, v150\n\t"
      "v_exp_f32 v151, v151\n\t"
      "v_cvt_pk_bf16_f32 v138, v148, v149\n\t"
      "v_cvt_pk_bf16_f32 v139, v150, v151\n\t"
      "v_exp_f32 v152, v152\n\t"
      "v_exp_f32 v153, v153\n\t"
      "v_mfma_f32_32x32x16_bf16 v[224:239], a[136:139], v[136:139], v[224:239]\n\t"
      "s_add_u32 m0, m0, s70\n\t"
      "s_xor_b32 s48, s71, 102400\n\t"
      "buffer_load_dwordx4 v2, s[16:19], s48 offen sc1 lds\n\t"
      "v_exp_f32 v154, v154\n\t"
      "v_exp_f32 v155, v155\n\t"
      "v_cvt_pk_bf16_f32 v140, v152, v153\n\t"
      "v_cvt_pk_bf16_f32 v141, v154, v155\n\t"
      "v_exp_f32 v156, v156\n\t"
      "v_exp_f32 v157, v157\n\t"
      "v_mfma_f32_32x32x16_bf16 v[176:191], a[148:151], a[68:71], v[176:191]\n\t"
      "v_exp_f32 v158, v158\n\t"
      "v_exp_f32 v159, v159\n\t"
      "v_cvt_pk_bf16_f32 v142, v156, v157\n\t"
      "v_cvt_pk_bf16_f32 v143, v158, v159\n\t"
      "v_exp_f32 v160, v160\n\t"
      "v_exp_f32 v161, v161\n\t"
      "v_mfma_f32_32x32x16_bf16 v[224:239], a[140:143], v[140:143], v[224:239]\n\t"
      "s_add_u32 s48, s72, 57344\n\t"
      "buffer_load_dwordx4 v[92:95], v8, s[16:19], s48 offen offset:0 sc1\n\t"
      "v_exp_f32 v162, v162\n\t"
      "v_exp_f32 v163, v163\n\t"
      "v_cvt_pk_bf16_f32 v136, v160, v161\n\t"
      "v_cvt_pk_bf16_f32 v137, v162, v163\n\t"
      "v_exp_f32 v164, v164\n\t"
      "v_exp_f32 v165, v165\n\t"
      "v_mfma_f32_32x32x16_bf16 v[144:159], a[144:147], a[72:75], 0\n\t"
      "s_add_u32 m0, m0, s70\n\t"
      "s_xor_b32 s48, s71, 106496\n\t"
      "buffer_load_dwordx4 v2, s[16:19], s48 offen sc1 lds\n\t"
      "v_exp_f32 v166, v166\n\t"
      "v_exp_f32 v167, v167\n\t"
      "v_cvt_pk_bf16_f32 v138, v164, v165\n\t"
      "v_cvt_pk_bf16_f32 v139, v166, v167\n\t"
      "v_exp_f32 v168, v168\n\t"
      "v_exp_f32 v169, v169\n\t"
      "v_mfma_f32_32x32x16_bf16 v[240:255], a[136:139], v[136:139], v[240:255]\n\t"
      "v_exp_f32 v170, v170\n\t"
      "v_exp_f32 v171, v171\n\t"
      "v_cvt_pk_bf16_f32 v140, v168, v169\n\t"
      "v_cvt_pk_bf16_f32 v141, v170, v171\n\t"
      "v_exp_f32 v172, v172\n\t"
      "v_exp_f32 v173, v173\n\t"
      "v_mfma_f32_32x32x16_bf16 v[144:159], a[148:151], a[76:79], v[144:159]\n\t"
      "buffer_load_dwordx4 a[136:139], v2, s[12:15], s49 offen offset:2048 sc1\n\t"
      "v_exp_f32 v174, v174\n\t"
      "v_exp_f32 v175, v175\n\t"
      "v_cvt_pk_bf16_f32 v142, v172, v173\n\t"
      "v_cvt_pk_bf16_f32 v143, v174, v175\n\t"
      "v_exp_f32 v176, v176\n\t"
      "v_exp_f32 v177, v177\n\t"
      "v_mfma_f32_32x32x16_bf16 v[240:255], a[140:143], v[140:143], v[240:255]\n\t"
      "v_exp_f32 v178, v178\n\t"
      "v_exp_f32 v179, v179\n\t"
      "v_cvt_pk_bf16_f32 v136, v176, v177\n\t"
      "v_cvt_pk_bf16_f32 v137, v178, v179\n\t"
      "v_exp_f32 v180, v180\n\t"
      "v_exp_f32 v181, v181\n\t"
      "v_mfma_f32_32x32x16_bf16 v[160:175], a[144:147], a[80:83], 0\n\t"
      "buffer_load_dwordx4 a[140:143], v2, s[12:15], s49 offen offset:3072 sc1\n\t"
      "s_add_u32 s49, s49, 0x1000\n\t"
      "v_exp_f32 v182, v182\n\t"
      "v_exp_f32 v183, v183\n\t"
      "v_cvt_pk_bf16_f32 v138, v180, v181\n\t"
      "v_cvt_pk_bf16_f32 v139, v182, v183\n\t"
      "v_exp_f32 v184, v184\n\t"
      "v_exp_f32 v185, v185\n\t"
      "v_mfma_f32_32x32x16_bf16 v[192:207], a[152:155], v[136:139], v[192:207]\n\t"
      "s_add_u32 s48, s72, 57344\n\t"
      "buffer_load_dwordx4 v[96:99], v9, s[16:19], s48 offen offset:1024 sc1\n\t"
      "v_exp_f32 v186, v186\n\t"
      "v_exp_f32 v187, v187\n\t"
      "v_cvt_pk_bf16_f32 v140, v184, v185\n\t"
      "v_cvt_pk_bf16_f32 v141, v186, v187\n\t"
      "v_exp_f32 v188, v188\n\t"
      "v_exp_f32 v189, v189\n\t"
      "v_mfma_f32_32x32x16_bf16 v[160:175], a[148:151], a[84:87], v[160:175]\n\t"
      "s_add_u32 m0, m0, s70\n\t"
      "s_xor_b32 s48, s71, 110592\n\t"
      "buffer_load_dwordx4 v2, s[16:19], s48 offen sc1 lds\n\t"
      "v_exp_f32 v190, v190\n\t"
      "v_exp_f32 v191, v191\n\t"
      "v_cvt_pk_bf16_f32 v142, v188, v189\n\t"
      "v_cvt_pk_bf16_f32 v143, v190, v191\n\t"
      "v_exp_f32 v144, v144\n\t"
      "v_exp_f32 v145, v145\n\t"
      "v_mfma_f32_32x32x16_bf16 v[192:207], a[156:159], v[140:143], v[192:207]\n\t"
      "s_add_u32 m0, m0, s70\n\t"
      "s_xor_b32 s48, s71, 114688\n\t"
      "buffer_load_dwordx4 v2, s[16:19], s48 offen sc1 lds\n\t"
      "v_exp_f32 v146, v146\n\t"
      "v_exp_f32 v147, v147\n\t"
      "v_cvt_pk_bf16_f32 v136, v144, v145\n\t"
      "v_cvt_pk_bf16_f32 v137, v146, v147\n\t"
      "v_exp_f32 v148, v148\n\t"
      "v_exp_f32 v149, v149\n\t"
      "v_mfma_f32_32x32x16_bf16 v[176:191], a[144:147], a[88:91], 0\n\t"
      "v_exp_f32 v150, v150\n\t"
      "v_exp_f32 v151, v151\n\t"
      "v_cvt_pk_bf16_f32 v138, v148, v149\n\t"
      "v_cvt_pk_bf16_f32 v139, v150, v151\n\t"
      "v_exp_f32 v152, v152\n\t"
      "v_exp_f32 v153, v153\n\t"
      "v_mfma_f32_32x32x16_bf16 v[208:223], a[152:155], v[136:139], v[208:223]\n\t"
      "buffer_load_dwordx4 a[144:147], v2, s[12:15], s49 offen offset:0 sc1\n\t"
      "v_exp_f32 v154, v154\n\t"
      "v_exp_f32 v155, v155\n\t"
      "v_cvt_pk_bf16_f32 v140, v152, v153\n\t"
      "v_cvt_pk_bf16_f32 v141, v154, v155\n\t"
      "v_exp_f32 v156, v156\n\t"
      "v_exp_f32 v157, v157\n\t"
      "v_mfma_f32_32x32x16_bf16 v[176:191], a[148:151], a[92:95], v[176:191]\n\t"
      "buffer_load_dword a160, v11, s[32:35], 0 offen\n\t"
      "v_exp_f32 v158, v158\n\t"
      "v_exp_f32 v159, v159\n\t"
      "v_cvt_pk_bf16_f32 v142, v156, v157\n\t"
      "v_cvt_pk_bf16_f32 v143, v158, v159\n\t"
      "v_exp_f32 v160, v160\n\t"
      "v_exp_f32 v161, v161\n\t"
      "v_mfma_f32_32x32x16_bf16 v[208:223], a[156:159], v[140:143], v[208:223]\n\t"
      "buffer_load_dwordx4 a[148:151], v2, s[12:15], s49 offen offset:1024 sc1\n\t"
      "s_waitcnt vmcnt(31)\n\t"
      "v_exp_f32 v162, v162\n\t"
      "v_exp_f32 v163, v163\n\t"
      "v_cvt_pk_bf16_f32 v136, v160, v161\n\t"
      "v_cvt_pk_bf16_f32 v137, v162, v163\n\t"
      "v_exp_f32 v164, v164\n\t"
      "v_exp_f32 v165, v165\n\t"
      "v_mfma_f32_32x32x16_bf16 v[144:159], a[96:99], a[64:67], 0\n\t"
      "s_add_u32 s48, s72, 57344\n\t"
      "buffer_load_dwordx4 v[100:103], v8, s[16:19], s48 offen offset:2048 sc1\n\t"
      "v_exp_f32 v166, v166\n\t"
      "v_exp_f32 v167, v167\n\t"
      "v_cvt_pk_bf16_f32 v138, v164, v165\n\t"
      "v_cvt_pk_bf16_f32 v139, v166, v167\n\t"
      "v_exp_f32 v168, v168\n\t"
      "v_exp_f32 v169, v169\n\t"
      "v_mfma_f32_32x32x16_bf16 v[224:239], a[152:155], v[136:139], v[224:239]\n\t"
      "s_add_u32 m0, m0, s70\n\t"
      "s_xor_b32 s48, s71, 118784\n\t"
      "buffer_load_dwordx4 v2, s[16:19], s48 offen sc1 lds\n\t"
      "v_exp_f32 v170, v170\n\t"
      "v_exp_f32 v171, v171\n\t"
      "v_cvt_pk_bf16_f32 v140, v168, v169\n\t"
      "v_cvt_pk_bf16_f32 v141, v170, v171\n\t"
      "v_exp_f32 v172, v172\n\t"
      "v_exp_f32 v173, v173\n\t"
      "v_mfma_f32_32x32x16_bf16 v[144:159], a[100:103], a[68:71], v[144:159]\n\t"
      "v_exp_f32 v174, v174\n\t"
      "v_exp_f32 v175, v175\n\t"
      "v_cvt_pk_bf16_f32 v142, v172, v173\n\t"
      "v_cvt_pk_bf16_f32 v143, v174, v175\n\t"
      "v_exp_f32 v176, v176\n\t"
      "v_exp_f32 v177, v177\n\t"
      "v_mfma_f32_32x32x16_bf16 v[224:239], a[156:159], v[140:143], v[224:239]\n\t"
      "s_add_u32 s48, s72, 57344\n\t"
      "buffer_load_dwordx4 v[104:107], v9, s[16:19], s48 offen offset:3072 sc1\n\t"
      "v_exp_f32 v178, v178\n\t"
      "v_exp_f32 v179, v179\n\t"
      "v_cvt_pk_bf16_f32 v136, v176, v177\n\t"
      "v_cvt_pk_bf16_f32 v137, v178, v179\n\t"
      "v_exp_f32 v180, v180\n\t"
      "v_exp_f32 v181, v181\n\t"
      "v_mfma_f32_32x32x16_bf16 v[160:175], a[96:99], a[72:75], 0\n\t"
      "s_add_u32 m0, m0, s70\n\t"
      "s_xor_b32 s48, s71, 122880\n\t"
      "buffer_load_dwordx4 v2, s[16:19], s48 offen sc1 lds\n\t"
      "v_exp_f32 v182, v182\n\t"
      "v_exp_f32 v183, v183\n\t"
      "v_cvt_pk_bf16_f32 v138, v180, v181\n\t"
      "v_cvt_pk_bf16_f32 v139, v182, v183\n\t"
      "v_exp_f32 v184, v184\n\t"
      "v_exp_f32 v185, v185\n\t"
      "v_mfma_f32_32x32x16_bf16 v[240:255], a[152:155], v[136:139], v[240:255]\n\t"
      "v_exp_f32 v186, v186\n\t"
      "v_exp_f32 v187, v187\n\t"
      "v_cvt_pk_bf16_f32 v140, v184, v185\n\t"
      "v_cvt_pk_bf16_f32 v141, v186, v187\n\t"
      "v_exp_f32 v188, v188\n\t"
      "v_exp_f32 v189, v189\n\t"
      "v_mfma_f32_32x32x16_bf16 v[160:175], a[100:103], a[76:79], v[160:175]\n\t"
      "buffer_load_dwordx4 a[152:155], v2, s[12:15], s49 offen offset:2048 sc1\n\t"
      "v_exp_f32 v190, v190\n\t"
      "v_exp_f32 v191, v191\n\t"
      "v_cvt_pk_bf16_f32 v142, v188, v189\n\t"
      "v_cvt_pk_bf16_f32 v143, v190, v191\n\t"
      "v_exp_f32 v144, v144\n\t"
      "v_exp_f32 v145, v145\n\t"
      "v_mfma_f32_32x32x16_bf16 v[240:255], a[156:159], v[140:143], v[240:255]\n\t"
      "v_exp_f32 v146, v146\n\t"
      "v_exp_f32 v147, v147\n\t"
      "v_cvt_pk_bf16_f32 v136, v144, v145\n\t"
      "v_cvt_pk_bf16_f32 v137, v146, v147\n\t"
      "v_exp_f32 v148, v148\n\t"
      "v_exp_f32 v149, v149\n\t"
      "v_mfma_f32_32x32x16_bf16 v[176:191], a[96:99], a[80:83], 0\n\t"
      "buffer_load_dwordx4 a[156:159], v2, s[12:15], s49 offen offset:3072 sc1\n\t"
      "s_add_u32 s49, s49, 0x1000\n\t"
      "v_exp_f32 v150, v150\n\t"
      "v_exp_f32 v151, v151\n\t"
      "v_cvt_pk_bf16_f32 v138, v148, v149\n\t"
      "v_cvt_pk_bf16_f32 v139, v150, v151\n\t"
      "v_exp_f32 v152, v152\n\t"
      "v_exp_f32 v153, v153\n\t"
      "v_mfma_f32_32x32x16_bf16 v[192:207], a[104:107], v[136:139], v[192:207]\n\t"
      "s_add_u32 s48, s72, 61440\n\t"
      "buffer_load_dwordx4 v[112:115], v9, s[16:19], s48 offen offset:1024 sc1\n\t"
      "v_exp_f32 v154, v154\n\t"
      "v_exp_f32 v155, v155\n\t"
      "v_cvt_pk_bf16_f32 v140, v152, v153\n\t"
      "v_cvt_pk_bf16_f32 v141, v154, v155\n\t"
      "v_exp_f32 v156, v156\n\t"
      "v_exp_f32 v157, v157\n\t"
      "v_mfma_f32_32x32x16_bf16 v[176:191], a[100:103], a[84:87], v[176:191]\n\t"
      "s_add_u32 s48, s72, 61440\n\t"
      "buffer_load_dwordx4 v[108:111], v8, s[16:19], s48 offen offset:0 sc1\n\t"
      "v_exp_f32 v158, v158\n\t"
      "v_exp_f32 v159, v159\n\t"
      "v_cvt_pk_bf16_f32 v142, v156, v157\n\t"
      "v_cvt_pk_bf16_f32 v143, v158, v159\n\t"
      "v_exp_f32 v160, v160\n\t"
      "v_exp_f32 v161, v161\n\t"
      "v_mfma_f32_32x32x16_bf16 v[192:207], a[108:111], v[140:143], v[192:207]\n\t"
      "s_add_u32 s48, s72, 61440\n\t"
      "buffer_load_dwordx4 v[116:119], v8, s[16:19], s48 offen offset:2048 sc1\n\t"
      "v_exp_f32 v162, v162\n\t"
      "v_exp_f32 v163, v163\n\t"
      "v_cvt_pk_bf16_f32 v136, v160, v161\n\t"
      "v_cvt_pk_bf16_f32 v137, v162, v163\n\t"
      "v_exp_f32 v164, v164\n\t"
      "v_exp_f32 v165, v165\n\t"
      "v_mfma_f32_32x32x16_bf16 v[144:159], a[96:99], a[88:91], 0\n\t"
      "v_exp_f32 v166, v166\n\t"
      "v_exp_f32 v167, v167\n\t"
      "v_cvt_pk_bf16_f32 v138, v164, v165\n\t"
      "v_cvt_pk_bf16_f32 v139, v166, v167\n\t"
      "v_exp_f32 v168, v168\n\t"
      "v_exp_f32 v169, v169\n\t"
      "v_mfma_f32_32x32x16_bf16 v[208:223], a[104:107], v[136:139], v[208:223]\n\t"
      "s_add_u32 s48, s72, 61440\n\t"
      "buffer_load_dwordx4 v[120:123], v9, s[16:19], s48 offen offset:3072 sc1\n\t"
      "v_exp_f32 v170, v170\n\t"
      "v_exp_f32 v171, v171\n\t"
      "v_cvt_pk_bf16_f32 v140, v168, v169\n\t"
      "v_cvt_pk_bf16_f32 v141, v170, v171\n\t"
      "v_exp_f32 v172, v172\n\t"
      "v_exp_f32 v173, v173\n\t"
      "v_mfma_f32_32x32x16_bf16 v[144:159], a[100:103], a[92:95], v[144:159]\n\t"
      "s_add_u32 s48, s72, 65536\n\t"
      "buffer_load_dwordx4 v[124:127], v8, s[16:19], s48 offen offset:0 sc1\n\t"
      "v_exp_f32 v174, v174\n\t"
      "v_exp_f32 v175, v175\n\t"
      "v_cvt_pk_bf16_f32 v142, v172, v173\n\t"
      "v_cvt_pk_bf16_f32 v143, v174, v175\n\t"
      "v_exp_f32 v176, v176\n\t"
      "v_exp_f32 v177, v177\n\t"
      "v_mfma_f32_32x32x16_bf16 v[208:223], a[108:111], v[140:143], v[208:223]\n\t"
      "s_add_u32 s48, s72, 114688\n\t"
      "buffer_load_dwordx4 a[176:179], v8, s[16:19], s48 offen offset:0 sc1\n\t"
      "s_waitcnt vmcnt(30)\n\t"
      "v_exp_f32 v178, v178\n\t"
      "v_exp_f32 v179, v179\n\t"
      "v_cvt_pk_bf16_f32 v136, v176, v177\n\t"
      "v_cvt_pk_bf16_f32 v137, v178, v179\n\t"
      "v_exp_f32 v180, v180\n\t"
      "v_exp_f32 v181, v181\n\t"
      "v_mfma_f32_32x32x16_bf16 v[160:175], a[112:115], a[64:67], 0\n\t"
      "s_add_u32 s48, s72, 114688\n\t"
      "buffer_load_dwordx4 a[180:183], v9, s[16:19], s48 offen offset:1024 sc1\n\t"
      "v_exp_f32 v182, v182\n\t"
      "v_exp_f32 v183, v183\n\t"
      "v_cvt_pk_bf16_f32 v138, v180, v181\n\t"
      "v_cvt_pk_bf16_f32 v139, v182, v183\n\t"
      "v_exp_f32 v184, v184\n\t"
      "v_exp_f32 v185, v185\n\t"
      "v_mfma_f32_32x32x16_bf16 v[224:239], a[104:107], v[136:139], v[224:239]\n\t"
      "s_add_u32 s48, s72, 65536\n\t"
      "buffer_load_dwordx4 v[128:131], v9, s[16:19], s48 offen offset:1024 sc1\n\t"
      "v_exp_f32 v186, v186\n\t"
      "v_exp_f32 v187, v187\n\t"
      "v_cvt_pk_bf16_f32 v140, v184, v185\n\t"
      "v_cvt_pk_bf16_f32 v141, v186, v187\n\t"
      "v_exp_f32 v188, v188\n\t"
      "v_exp_f32 v189, v189\n\t"
      "v_mfma_f32_32x32x16_bf16 v[160:175], a[116:119], a[68:71], v[160:175]\n\t"
      "v_exp_f32 v190, v190\n\t"
      "v_exp_f32 v191, v191\n\t"
      "v_cvt_pk_bf16_f32 v142, v188, v189\n\t"
      "v_cvt_pk_bf16_f32 v143, v190, v191\n\t"
      "v_exp_f32 v144, v144\n\t"
      "v_exp_f32 v145, v145\n\t"
      "v_mfma_f32_32x32x16_bf16 v[224:239], a[108:111], v[140:143], v[224:239]\n\t"
      "v_exp_f32 v146, v146\n\t"
      "v_exp_f32 v147, v147\n\t"
      "v_cvt_pk_bf16_f32 v136, v144, v145\n\t"
      "v_cvt_pk_bf16_f32 v137, v146, v147\n\t"
      "v_exp_f32 v148, v148\n\t"
      "v_exp_f32 v149, v149\n\t"
      "v_mfma_f32_32x32x16_bf16 v[176:191], a[112:115], a[72:75], 0\n\t"
      "s_add_u32 s48, s72, 114688\n\t"
      "buffer_load_dwordx4 a[184:187], v8, s[16:19], s48 offen offset:2048 sc1\n\t"
      "v_exp_f32 v150, v150\n\t"
      "v_exp_f32 v151, v151\n\t"
      "v_cvt_pk_bf16_f32 v138, v148, v149\n\t"
      "v_cvt_pk_bf16_f32 v139, v150, v151\n\t"
      "v_exp_f32 v152, v152\n\t"
      "v_exp_f32 v153, v153\n\t"
      "v_mfma_f32_32x32x16_bf16 v[240:255], a[104:107], v[136:139], v[240:255]\n\t"
      "v_exp_f32 v154, v154\n\t"
      "v_exp_f32 v155, v155\n\t"
      "v_cvt_pk_bf16_f32 v140, v152, v153\n\t"
      "v_cvt_pk_bf16_f32 v141, v154, v155\n\t"
      "v_exp_f32 v156, v156\n\t"
      "v_exp_f32 v157, v157\n\t"
      "v_mfma_f32_32x32x16_bf16 v[176:191], a[116:119], a[76:79], v[176:191]\n\t"
      "s_add_u32 s48, s72, 114688\n\t"
      "buffer_load_dwordx4 a[188:191], v9, s[16:19], s48 offen offset:3072 sc1\n\t"
      "v_exp_f32 v158, v158\n\t"
      "v_exp_f32 v159, v159\n\t"
      "v_cvt_pk_bf16_f32 v142, v156, v157\n\t"
      "v_cvt_pk_bf16_f32 v143, v158, v159\n\t"
      "v_exp_f32 v160, v160\n\t"
      "v_exp_f32 v161, v161\n\t"
      "v_mfma_f32_32x32x16_bf16 v[240:255], a[108:111], v[140:143], v[240:255]\n\t"
      "v_exp_f32 v162, v162\n\t"
      "v_exp_f32 v163, v163\n\t"
      "v_cvt_pk_bf16_f32 v136, v160, v161\n\t"
      "v_cvt_pk_bf16_f32 v137, v162, v163\n\t"
      "v_exp_f32 v164, v164\n\t"
      "v_exp_f32 v165, v165\n\t"
      "v_mfma_f32_32x32x16_bf16 v[144:159], a[112:115], a[80:83], 0\n\t"
      "s_add_u32 s48, s72, 118784\n\t"
      "buffer_load_dwordx4 a[196:199], v9, s[16:19], s48 offen offset:1024 sc1\n\t"
      "v_exp_f32 v166, v166\n\t"
      "v_exp_f32 v167, v167\n\t"
      "v_cvt_pk_bf16_f32 v138, v164, v165\n\t"
      "v_cvt_pk_bf16_f32 v139, v166, v167\n\t"
      "v_exp_f32 v168, v168\n\t"
      "v_exp_f32 v169, v169\n\t"
      "v_mfma_f32_32x32x16_bf16 v[192:207], a[120:123], v[136:139], v[192:207]\n\t"
      "v_exp_f32 v170, v170\n\t"
      "v_exp_f32 v171, v171\n\t"
      "v_cvt_pk_bf16_f32 v140, v168, v169\n\t"
      "v_cvt_pk_bf16_f32 v141, v170, v171\n\t"
      "v_exp_f32 v172, v172\n\t"
      "v_exp_f32 v173, v173\n\t"
      "v_mfma_f32_32x32x16_bf16 v[144:159], a[116:119], a[84:87], v[144:159]\n\t"
      "s_add_u32 s48, s72, 118784\n\t"
      "buffer_load_dwordx4 a[192:195], v8, s[16:19], s48 offen offset:0 sc1\n\t"
      "v_exp_f32 v174, v174\n\t"
      "v_exp_f32 v175, v175\n\t"
      "v_cvt_pk_bf16_f32 v142, v172, v173\n\t"
      "v_cvt_pk_bf16_f32 v143, v174, v175\n\t"
      "v_exp_f32 v176, v176\n\t"
      "v_exp_f32 v177, v177\n\t"
      "v_mfma_f32_32x32x16_bf16 v[192:207], a[124:127], v[140:143], v[192:207]\n\t"
      "s_add_u32 s48, s72, 118784\n\t"
      "buffer_load_dwordx4 a[200:203], v8, s[16:19], s48 offen offset:2048 sc1\n\t"
      "v_exp_f32 v178, v178\n\t"
      "v_exp_f32 v179, v179\n\t"
      "v_cvt_pk_bf16_f32 v136, v176, v177\n\t"
      "v_cvt_pk_bf16_f32 v137, v178, v179\n\t"
      "v_exp_f32 v180, v180\n\t"
      "v_exp_f32 v181, v181\n\t"
      "v_mfma_f32_32x32x16_bf16 v[160:175], a[112:115], a[88:91], 0\n\t"
      "v_exp_f32 v182, v182\n\t"
      "v_exp_f32 v183, v183\n\t"
      "v_cvt_pk_bf16_f32 v138, v180, v181\n\t"
      "v_cvt_pk_bf16_f32 v139, v182, v183\n\t"
      "v_exp_f32 v184, v184\n\t"
      "v_exp_f32 v185, v185\n\t"
      "v_mfma_f32_32x32x16_bf16 v[208:223], a[120:123], v[136:139], v[208:223]\n\t"
      "s_add_u32 s48, s72, 118784\n\t"
      "buffer_load_dwordx4 a[204:207], v9, s[16:19], s48 offen offset:3072 sc1\n\t"
      "v_exp_f32 v186, v186\n\t"
      "v_exp_f32 v187, v187\n\t"
      "v_cvt_pk_bf16_f32 v140, v184, v185\n\t"
      "v_cvt_pk_bf16_f32 v141, v186, v187\n\t"
      "v_exp_f32 v188, v188\n\t"
      "v_exp_f32 v189, v189\n\t"
      "v_mfma_f32_32x32x16_bf16 v[160:175], a[116:119], a[92:95], v[160:175]\n\t"
      "v_exp_f32 v190, v190\n\t"
      "v_exp_f32 v191, v191\n\t"
      "v_cvt_pk_bf16_f32 v142, v188, v189\n\t"
      "v_cvt_pk_bf16_f32 v143, v190, v191\n\t"
      "v_exp_f32 v144, v144\n\t"
      "v_exp_f32 v145, v145\n\t"
      "v_mfma_f32_32x32x16_bf16 v[208:223], a[124:127], v[140:143], v[208:223]\n\t"
      "s_add_u32 s48, s72, 122880\n\t"
      "buffer_load_dwordx4 a[212:215], v9, s[16:19], s48 offen offset:1024 sc1\n\t"
      "s_waitcnt vmcnt(27)\n\t"
      "v_exp_f32 v146, v146\n\t"
      "v_exp_f32 v147, v147\n\t"
      "v_cvt_pk_bf16_f32 v136, v144, v145\n\t"
      "v_cvt_pk_bf16_f32 v137, v146, v147\n\t"
      "v_exp_f32 v148, v148\n\t"
      "v_exp_f32 v149, v149\n\t"
      "v_mfma_f32_32x32x16_bf16 v[176:191], a[128:131], a[64:67], 0\n\t"
      "v_exp_f32 v150, v150\n\t"
      "v_exp_f32 v151, v151\n\t"
      "v_cvt_pk_bf16_f32 v138, v148, v149\n\t"
      "v_cvt_pk_bf16_f32 v139, v150, v151\n\t"
      "v_exp_f32 v152, v152\n\t"
      "v_exp_f32 v153, v153\n\t"
      "v_mfma_f32_32x32x16_bf16 v[224:239], a[120:123], v[136:139], v[224:239]\n\t"
      "s_add_u32 s48, s72, 122880\n\t"
      "buffer_load_dwordx4 a[208:211], v8, s[16:19], s48 offen offset:0 sc1\n\t"
      "v_exp_f32 v154, v154\n\t"
      "v_exp_f32 v155, v155\n\t"
      "v_cvt_pk_bf16_f32 v140, v152, v153\n\t"
      "v_cvt_pk_bf16_f32 v141, v154, v155\n\t"
      "v_exp_f32 v156, v156\n\t"
      "v_exp_f32 v157, v157\n\t"
      "v_mfma_f32_32x32x16_bf16 v[176:191], a[132:135], a[68:71], v[176:191]\n\t"
      "v_exp_f32 v158, v158\n\t"
      "v_exp_f32 v159, v159\n\t"
      "v_cvt_pk_bf16_f32 v142, v156, v157\n\t"
      "v_cvt_pk_bf16_f32 v143, v158, v159\n\t"
      "v_exp_f32 v160, v160\n\t"
      "v_exp_f32 v161, v161\n\t"
      "v_mfma_f32_32x32x16_bf16 v[224:239], a[124:127], v[140:143], v[224:239]\n\t"
      "v_exp_f32 v162, v162\n\t"
      "v_exp_f32 v163, v163\n\t"
      "v_cvt_pk_bf16_f32 v136, v160, v161\n\t"
      "v_cvt_pk_bf16_f32 v137, v162, v163\n\t"
      "v_exp_f32 v164, v164\n\t"
      "v_exp_f32 v165, v165\n\t"
      "v_mfma_f32_32x32x16_bf16 v[144:159], a[128:131], a[72:75], 0\n\t"
      "s_add_u32 s48, s72, 122880\n\t"
      "buffer_load_dwordx4 a[216:219], v8, s[16:19], s48 offen offset:2048 sc1\n\t"
      "v_exp_f32 v166, v166\n\t"
      "v_exp_f32 v167, v167\n\t"
      "v_cvt_pk_bf16_f32 v138, v164, v165\n\t"
      "v_cvt_pk_bf16_f32 v139, v166, v167\n\t"
      "v_exp_f32 v168, v168\n\t"
      "v_exp_f32 v169, v169\n\t"
      "v_mfma_f32_32x32x16_bf16 v[240:255], a[120:123], v[136:139], v[240:255]\n\t"
      "v_exp_f32 v170, v170\n\t"
      "v_exp_f32 v171, v171\n\t"
      "v_cvt_pk_bf16_f32 v140, v168, v169\n\t"
      "v_cvt_pk_bf16_f32 v141, v170, v171\n\t"
      "v_exp_f32 v172, v172\n\t"
      "v_exp_f32 v173, v173\n\t"
      "v_mfma_f32_32x32x16_bf16 v[144:159], a[132:135], a[76:79], v[144:159]\n\t"
      "s_add_u32 s48, s72, 122880\n\t"
      "buffer_load_dwordx4 a[220:223], v9, s[16:19], s48 offen offset:3072 sc1\n\t"
      "v_exp_f32 v174, v174\n\t"
      "v_exp_f32 v175, v175\n\t"
      "v_cvt_pk_bf16_f32 v142, v172, v173\n\t"
      "v_cvt_pk_bf16_f32 v143, v174, v175\n\t"
      "v_exp_f32 v176, v176\n\t"
      "v_exp_f32 v177, v177\n\t"
      "v_mfma_f32_32x32x16_bf16 v[240:255], a[124:127], v[140:143], v[240:255]\n\t"
      "v_exp_f32 v178, v178\n\t"
      "v_exp_f32 v179, v179\n\t"
      "v_cvt_pk_bf16_f32 v136, v176, v177\n\t"
      "v_cvt_pk_bf16_f32 v137, v178, v179\n\t"
      "v_exp_f32 v180, v180\n\t"
      "v_exp_f32 v181, v181\n\t"
      "v_mfma_f32_32x32x16_bf16 v[160:175], a[128:131], a[80:83], 0\n\t"
      "s_add_u32 s48, s72, 126976\n\t"
      "buffer_load_dwordx4 a[228:231], v9, s[16:19], s48 offen offset:1024 sc1\n\t"
      "v_exp_f32 v182, v182\n\t"
      "v_exp_f32 v183, v183\n\t"
      "v_cvt_pk_bf16_f32 v138, v180, v181\n\t"
      "v_cvt_pk_bf16_f32 v139, v182, v183\n\t"
      "v_exp_f32 v184, v184\n\t"
      "v_exp_f32 v185, v185\n\t"
      "v_mfma_f32_32x32x16_bf16 v[192:207], a[136:139], v[136:139], v[192:207]\n\t"
      "v_exp_f32 v186, v186\n\t"
      "v_exp_f32 v187, v187\n\t"
      "v_cvt_pk_bf16_f32 v140, v184, v185\n\t"
      "v_cvt_pk_bf16_f32 v141, v186, v187\n\t"
      "v_exp_f32 v188, v188\n\t"
      "v_exp_f32 v189, v189\n\t"
      "v_mfma_f32_32x32x16_bf16 v[160:175], a[132:135], a[84:87], v[160:175]\n\t"
      "s_add_u32 s48, s72, 126976\n\t"
      "buffer_load_dwordx4 a[224:227], v8, s[16:19], s48 offen offset:0 sc1\n\t"
      "v_exp_f32 v190, v190\n\t"
      "v_exp_f32 v191, v191\n\t"
      "v_cvt_pk_bf16_f32 v142, v188, v189\n\t"
      "v_cvt_pk_bf16_f32 v143, v190, v191\n\t"
      "v_exp_f32 v144, v144\n\t"
      "v_exp_f32 v145, v145\n\t"
      "v_mfma_f32_32x32x16_bf16 v[192:207], a[140:143], v[140:143], v[192:207]\n\t"
      "s_add_u32 s48, s72, 172032\n\t"
      "buffer_load_dwordx4 a[232:235], v8, s[16:19], s48 offen offset:0 sc1\n\t"
      "v_exp_f32 v146, v146\n\t"
      "v_exp_f32 v147, v147\n\t"
      "v_cvt_pk_bf16_f32 v136, v144, v145\n\t"
      "v_cvt_pk_bf16_f32 v137, v146, v147\n\t"
      "v_exp_f32 v148, v148\n\t"
      "v_exp_f32 v149, v149\n\t"
      "v_mfma_f32_32x32x16_bf16 v[176:191], a[128:131], a[88:91], 0\n\t"
      "v_exp_f32 v150, v150\n\t"
      "v_exp_f32 v151, v151\n\t"
      "v_cvt_pk_bf16_f32 v138, v148, v149\n\t"
      "v_cvt_pk_bf16_f32 v139, v150, v151\n\t"
      "v_exp_f32 v152, v152\n\t"
      "v_exp_f32 v153, v153\n\t"
      "v_mfma_f32_32x32x16_bf16 v[208:223], a[136:139], v[136:139], v[208:223]\n\t"
      "s_add_u32 s48, s72, 172032\n\t"
      "buffer_load_dwordx4 a[236:239], v9, s[16:19], s48 offen offset:1024 sc1\n\t"
      "v_exp_f32 v154, v154\n\t"
      "v_exp_f32 v155, v155\n\t"
      "v_cvt_pk_bf16_f32 v140, v152, v153\n\t"
      "v_cvt_pk_bf16_f32 v141, v154, v155\n\t"
      "v_exp_f32 v156, v156\n\t"
      "v_exp_f32 v157, v157\n\t"
      "v_mfma_f32_32x32x16_bf16 v[176:191], a[132:135], a[92:95], v[176:191]\n\t"
      "v_exp_f32 v158, v158\n\t"
      "v_exp_f32 v159, v159\n\t"
      "v_cvt_pk_bf16_f32 v142, v156, v157\n\t"
      "v_cvt_pk_bf16_f32 v143, v158, v159\n\t"
      "v_exp_f32 v160, v160\n\t"
      "v_exp_f32 v161, v161\n\t"
      "v_mfma_f32_32x32x16_bf16 v[208:223], a[140:143], v[140:143], v[208:223]\n\t"
      "s_add_u32 s48, s72, 172032\n\t"
      "buffer_load_dwordx4 a[244:247], v9, s[16:19], s48 offen offset:3072 sc1\n\t"
      "s_waitcnt vmcnt(23)\n\t"
      "v_exp_f32 v162, v162\n\t"
      "v_exp_f32 v163, v163\n\t"
      "v_cvt_pk_bf16_f32 v136, v160, v161\n\t"
      "v_cvt_pk_bf16_f32 v137, v162, v163\n\t"
      "v_exp_f32 v164, v164\n\t"
      "v_exp_f32 v165, v165\n\t"
      "v_mfma_f32_32x32x16_bf16 v[144:159], a[144:147], a[64:67], 0\n\t"
      "v_exp_f32 v166, v166\n\t"
      "v_exp_f32 v167, v167\n\t"
      "v_cvt_pk_bf16_f32 v138, v164, v165\n\t"
      "v_cvt_pk_bf16_f32 v139, v166, v167\n\t"
      "v_exp_f32 v168, v168\n\t"
      "v_exp_f32 v169, v169\n\t"
      "v_mfma_f32_32x32x16_bf16 v[224:239], a[136:139], v[136:139], v[224:239]\n\t"
      "s_add_u32 s48, s72, 172032\n\t"
      "buffer_load_dwordx4 a[240:243], v8, s[16:19], s48 offen offset:2048 sc1\n\t"
      "v_exp_f32 v170, v170\n\t"
      "v_exp_f32 v171, v171\n\t"
      "v_cvt_pk_bf16_f32 v140, v168, v169\n\t"
      "v_cvt_pk_bf16_f32 v141, v170, v171\n\t"
      "v_exp_f32 v172, v172\n\t"
      "v_exp_f32 v173, v173\n\t"
      "v_mfma_f32_32x32x16_bf16 v[144:159], a[148:151], a[68:71], v[144:159]\n\t"
      "v_exp_f32 v174, v174\n\t"
      "v_exp_f32 v175, v175\n\t"
      "v_cvt_pk_bf16_f32 v142, v172, v173\n\t"
      "v_cvt_pk_bf16_f32 v143, v174, v175\n\t"
      "v_exp_f32 v176, v176\n\t"
      "v_exp_f32 v177, v177\n\t"
      "v_mfma_f32_32x32x16_bf16 v[224:239], a[140:143], v[140:143], v[224:239]\n\t"
      "v_exp_f32 v178, v178\n\t"
      "v_exp_f32 v179, v179\n\t"
      "v_cvt_pk_bf16_f32 v136, v176, v177\n\t"
      "v_cvt_pk_bf16_f32 v137, v178, v179\n\t"
      "v_exp_f32 v180, v180\n\t"
      "v_exp_f32 v181, v181\n\t"
      "v_mfma_f32_32x32x16_bf16 v[160:175], a[144:147], a[72:75], 0\n\t"
      "s_add_u32 s48, s72, 176128\n\t"
      "buffer_load_dwordx4 a[248:251], v8, s[16:19], s48 offen offset:0 sc1\n\t"
      "v_exp_f32 v182, v182\n\t"
      "v_exp_f32 v183, v183\n\t"
      "v_cvt_pk_bf16_f32 v138, v180, v181\n\t"
      "v_cvt_pk_bf16_f32 v139, v182, v183\n\t"
      "v_exp_f32 v184, v184\n\t"
      "v_exp_f32 v185, v185\n\t"
      "v_mfma_f32_32x32x16_bf16 v[240:255], a[136:139], v[136:139], v[240:255]\n\t"
      "v_exp_f32 v186, v186\n\t"
      "v_exp_f32 v187, v187\n\t"
      "v_cvt_pk_bf16_f32 v140, v184, v185\n\t"
      "v_cvt_pk_bf16_f32 v141, v186, v187\n\t"
      "v_exp_f32 v188, v188\n\t"
      "v_exp_f32 v189, v189\n\t"
      "v_mfma_f32_32x32x16_bf16 v[160:175], a[148:151], a[76:79], v[160:175]\n\t"
      "s_add_u32 s48, s72, 176128\n\t"
      "buffer_load_dwordx4 a[252:255], v9, s[16:19], s48 offen offset:1024 sc1\n\t"
      "v_exp_f32 v190, v190\n\t"
      "v_exp_f32 v191, v191\n\t"
      "v_cvt_pk_bf16_f32 v142, v188, v189\n\t"
      "v_cvt_pk_bf16_f32 v143, v190, v191\n\t"
      "v_exp_f32 v144, v144\n\t"
      "v_exp_f32 v145, v145\n\t"
      "v_mfma_f32_32x32x16_bf16 v[240:255], a[140:143], v[140:143], v[240:255]\n\t"
      "v_exp_f32 v146, v146\n\t"
      "v_exp_f32 v147, v147\n\t"
      "v_cvt_pk_bf16_f32 v136, v144, v145\n\t"
      "v_cvt_pk_bf16_f32 v137, v146, v147\n\t"
      "v_exp_f32 v148, v148\n\t"
      "v_exp_f32 v149, v149\n\t"
      "v_mfma_f32_32x32x16_bf16 v[176:191], a[144:147], a[80:83], 0\n\t"
      "v_exp_f32 v150, v150\n\t"
      "v_exp_f32 v151, v151\n\t"
      "v_cvt_pk_bf16_f32 v138, v148, v149\n\t"
      "v_cvt_pk_bf16_f32 v139, v150, v151\n\t"
      "v_exp_f32 v152, v152\n\t"
      "v_exp_f32 v153, v153\n\t"
      "v_mfma_f32_32x32x16_bf16 v[192:207], a[152:155], v[136:139], v[192:207]\n\t"
      "v_exp_f32 v154, v154\n\t"
      "v_exp_f32 v155, v155\n\t"
      "v_cvt_pk_bf16_f32 v140, v152, v153\n\t"
      "v_cvt_pk_bf16_f32 v141, v154, v155\n\t"
      "v_exp_f32 v156, v156\n\t"
      "v_exp_f32 v157, v157\n\t"
      "v_mfma_f32_32x32x16_bf16 v[176:191], a[148:151], a[84:87], v[176:191]\n\t"
      "v_exp_f32 v158, v158\n\t"
      "v_exp_f32 v159, v159\n\t"
      "v_cvt_pk_bf16_f32 v142, v156, v157\n\t"
      "v_cvt_pk_bf16_f32 v143, v158, v159\n\t"
      "v_exp_f32 v160, v160\n\t"
      "v_exp_f32 v161, v161\n\t"
      "v_mfma_f32_32x32x16_bf16 v[192:207], a[156:159], v[140:143], v[192:207]\n\t"
      "v_exp_f32 v162, v162\n\t"
      "v_exp_f32 v163, v163\n\t"
      "v_cvt_pk_bf16_f32 v136, v160, v161\n\t"
      "v_cvt_pk_bf16_f32 v137, v162, v163\n\t"
      "v_exp_f32 v164, v164\n\t"
      "v_exp_f32 v165, v165\n\t"
      "v_mfma_f32_32x32x16_bf16 v[144:159], a[144:147], a[88:91], 0\n\t"
      "v_exp_f32 v166, v166\n\t"
      "v_exp_f32 v167, v167\n\t"
      "v_cvt_pk_bf16_f32 v138, v164, v165\n\t"
      "v_cvt_pk_bf16_f32 v139, v166, v167\n\t"
      "v_exp_f32 v168, v168\n\t"
      "v_exp_f32 v169, v169\n\t"
      "v_mfma_f32_32x32x16_bf16 v[208:223], a[152:155], v[136:139], v[208:223]\n\t"
      "ds_write_b128 v132, v[192:195] offset:0\n\t"
      "v_exp_f32 v170, v170\n\t"
      "v_exp_f32 v171, v171\n\t"
      "v_cvt_pk_bf16_f32 v140, v168, v169\n\t"
      "v_cvt_pk_bf16_f32 v141, v170, v171\n\t"
      "v_exp_f32 v172, v172\n\t"
      "v_exp_f32 v173, v173\n\t"
      "v_mfma_f32_32x32x16_bf16 v[144:159], a[148:151], a[92:95], v[144:159]\n\t"
      "ds_write_b128 v132, v[196:199] offset:1024\n\t"
      "v_exp_f32 v174, v174\n\t"
      "v_exp_f32 v175, v175\n\t"
      "v_cvt_pk_bf16_f32 v142, v172, v173\n\t"
      "v_cvt_pk_bf16_f32 v143, v174, v175\n\t"
      "v_exp_f32 v176, v176\n\t"
      "v_exp_f32 v177, v177\n\t"
      "v_mfma_f32_32x32x16_bf16 v[208:223], a[156:159], v[140:143], v[208:223]\n\t"
      "ds_write_b128 v132, v[200:203] offset:2048\n\t"
      "v_exp_f32 v178, v178\n\t"
      "v_exp_f32 v179, v179\n\t"
      "v_cvt_pk_bf16_f32 v136, v176, v177\n\t"
      "v_cvt_pk_bf16_f32 v137, v178, v179\n\t"
      "v_exp_f32 v180, v180\n\t"
      "v_exp_f32 v181, v181\n\t"
      "ds_write_b128 v132, v[204:207] offset:3072\n\t"
      "v_exp_f32 v182, v182\n\t"
      "v_exp_f32 v183, v183\n\t"
      "v_cvt_pk_bf16_f32 v138, v180, v181\n\t"
      "v_cvt_pk_bf16_f32 v139, v182, v183\n\t"
      "v_exp_f32 v184, v184\n\t"
      "v_exp_f32 v185, v185\n\t"
      "v_mfma_f32_32x32x16_bf16 v[224:239], a[152:155], v[136:139], v[224:239]\n\t"
      "ds_write_b128 v133, v[208:211] offset:0\n\t"
      "v_exp_f32 v186, v186\n\t"
      "v_exp_f32 v187, v187\n\t"
      "v_cvt_pk_bf16_f32 v140, v184, v185\n\t"
      "v_cvt_pk_bf16_f32 v141, v186, v187\n\t"
      "v_exp_f32 v188, v188\n\t"
      "v_exp_f32 v189, v189\n\t"
      "ds_write_b128 v133, v[212:215] offset:1024\n\t"
      "v_exp_f32 v190, v190\n\t"
      "v_exp_f32 v191, v191\n\t"
      "v_cvt_pk_bf16_f32 v142, v188, v189\n\t"
      "v_cvt_pk_bf16_f32 v143, v190, v191\n\t"
      "v_exp_f32 v144, v144\n\t"
      "v_exp_f32 v145, v145\n\t"
      "v_mfma_f32_32x32x16_bf16 v[224:239], a[156:159], v[140:143], v[224:239]\n\t"
      "ds_write_b128 v133, v[216:219] offset:2048\n\t"
      "v_exp_f32 v146, v146\n\t"
      "v_exp_f32 v147, v147\n\t"
      "v_cvt_pk_bf16_f32 v136, v144, v145\n\t"
      "v_cvt_pk_bf16_f32 v137, v146, v147\n\t"
      "v_exp_f32 v148, v148\n\t"
      "v_exp_f32 v149, v149\n\t"
      "ds_write_b128 v133, v[220:223] offset:3072\n\t"
      "v_exp_f32 v150, v150\n\t"
      "v_exp_f32 v151, v151\n\t"
      "v_cvt_pk_bf16_f32 v138, v148, v149\n\t"
      "v_cvt_pk_bf16_f32 v139, v150, v151\n\t"
      "v_exp_f32 v152, v152\n\t"
      "v_exp_f32 v153, v153\n\t"
      "v_mfma_f32_32x32x16_bf16 v[240:255], a[152:155], v[136:139], v[240:255]\n\t"
      "ds_write_b128 v134, v[224:227] offset:0\n\t"
      "v_exp_f32 v154, v154\n\t"
      "v_exp_f32 v155, v155\n\t"
      "v_cvt_pk_bf16_f32 v140, v152, v153\n\t"
      "v_cvt_pk_bf16_f32 v141, v154, v155\n\t"
      "v_exp_f32 v156, v156\n\t"
      "v_exp_f32 v157, v157\n\t"
      "ds_write_b128 v134, v[228:231] offset:1024\n\t"
      "v_exp_f32 v158, v158\n\t"
      "v_exp_f32 v159, v159\n\t"
      "v_cvt_pk_bf16_f32 v142, v156, v157\n\t"
      "v_cvt_pk_bf16_f32 v143, v158, v159\n\t"
      "ds_write_b128 v134, v[232:235] offset:2048\n\t"
      "ds_write_b128 v134, v[236:239] offset:3072\n\t"
      "v_mfma_f32_32x32x16_bf16 v[240:255], a[156:159], v[140:143], v[240:255]\n\t"
      "s_waitcnt lgkmcnt(0)\n\t"
      "s_barrier\n\t"
      "ds_read_b128 v[144:147], v135 offset:3072\n\t"
      "ds_read_b128 v[148:151], v135 offset:7168\n\t"
      "ds_read_b128 v[152:155], v135 offset:11264\n\t"
      "ds_read_b128 v[156:159], v135 offset:2048\n\t"
      "ds_read_b128 v[160:163], v135 offset:6144\n\t"
      "ds_read_b128 v[164:167], v135 offset:10240\n\t"
      "ds_read_b128 v[168:171], v135 offset:0\n\t"
      "ds_read_b128 v[172:175], v135 offset:4096\n\t"
      "ds_read_b128 v[176:179], v135 offset:8192\n\t"
      "ds_read_b128 v[180:183], v135 offset:1024\n\t"
      "ds_read_b128 v[184:187], v135 offset:5120\n\t"
      "ds_read_b128 v[188:191], v135 offset:9216\n\t"
      "s_waitcnt lgkmcnt(11)\n\t"
      "v_pk_add_f32 v[252:253], v[252:253], v[144:145]\n\t"
      "v_pk_add_f32 v[254:255], v[254:255], v[146:147]\n\t"
      "s_waitcnt lgkmcnt(10)\n\t"
      "v_pk_add_f32 v[252:253], v[252:253], v[148:149]\n\t"
      "v_pk_add_f32 v[254:255], v[254:255], v[150:151]\n\t"
      "s_waitcnt lgkmcnt(9)\n\t"
      "v_pk_add_f32 v[252:253], v[252:253], v[152:153]\n\t"
      "v_pk_add_f32 v[254:255], v[254:255], v[154:155]\n\t"
      "v_mov_b32 v140, v252\n\t"
      "v_mov_b32 v141, v252\n\t"
      "s_waitcnt lgkmcnt(8)\n\t"
      "v_pk_add_f32 v[248:249], v[248:249], v[156:157]\n\t"
      "v_pk_add_f32 v[250:251], v[250:251], v[158:159]\n\t"
      "s_waitcnt lgkmcnt(7)\n\t"
      "v_pk_add_f32 v[248:249], v[248:249], v[160:161]\n\t"
      "v_pk_add_f32 v[250:251], v[250:251], v[162:163]\n\t"
      "s_waitcnt lgkmcnt(6)\n\t"
      "v_pk_add_f32 v[248:249], v[248:249], v[164:165]\n\t"
      "v_pk_add_f32 v[250:251], v[250:251], v[166:167]\n\t"
      "v_permlane32_swap_b32 v140, v141\n\t"
      "s_waitcnt lgkmcnt(5)\n\t"
      "v_pk_add_f32 v[240:241], v[240:241], v[168:169]\n\t"
      "v_pk_add_f32 v[242:243], v[242:243], v[170:171]\n\t"
      "v_rcp_f32 v136, v141\n\t"
      "v_cmp_nlt_f32 s[60:61], v141, s79\n\t"
      "s_waitcnt lgkmcnt(4)\n\t"
      "v_pk_add_f32 v[240:241], v[240:241], v[172:173]\n\t"
      "v_pk_add_f32 v[242:243], v[242:243], v[174:175]\n\t"
      "s_waitcnt lgkmcnt(3)\n\t"
      "v_pk_add_f32 v[240:241], v[240:241], v[176:177]\n\t"
      "v_pk_add_f32 v[242:243], v[242:243], v[178:179]\n\t"
      "v_pk_mul_f32 v[248:249], v[248:249], v[136:137] op_sel_hi:[1,0]\n\t"
      "v_pk_mul_f32 v[250:251], v[250:251], v[136:137] op_sel_hi:[1,0]\n\t"
      "v_cvt_pk_bf16_f32 v196, v248, v249\n\t"
      "v_cvt_pk_bf16_f32 v197, v250, v251\n\t"
      "v_pk_mul_f32 v[252:253], v[252:253], v[136:137] op_sel_hi:[1,0]\n\t"
      "v_pk_mul_f32 v[254:255], v[254:255], v[136:137] op_sel_hi:[1,0]\n\t"
      "v_cvt_pk_bf16_f32 v198, v252, v253\n\t"
      "v_cvt_pk_bf16_f32 v199, v254, v255\n\t"
      "s_cmp_lg_u64 s[60:61], 0\n\t"
      "s_cselect_b32 s50, 1, 0\n\t"
      "s_andn2_b32 s50, s50, s66\n\t"
      "s_cselect_b32 s48, 0x7fff0000, s78\n\t"
      "v_min_u32 v196, -2, v196\n\t"
      "v_min_u32 v197, -2, v197\n\t"
      "v_min_u32 v198, -2, v198\n\t"
      "v_min_u32 v199, -2, v199\n\t"
      "buffer_store_dwordx2 v[196:197], v14, s[28:31], s48 offen sc1\n\t"
      "buffer_store_dwordx4 v[196:199], v13, s[28:31], s48 offen sc1\n\t"
      "s_waitcnt lgkmcnt(2)\n\t"
      "v_pk_add_f32 v[244:245], v[244:245], v[180:181]\n\t"
      "v_pk_add_f32 v[246:247], v[246:247], v[182:183]\n\t"
      "s_waitcnt lgkmcnt(1)\n\t"
      "v_pk_add_f32 v[244:245], v[244:245], v[184:185]\n\t"
      "v_pk_add_f32 v[246:247], v[246:247], v[186:187]\n\t"
      "s_waitcnt lgkmcnt(0)\n\t"
      "v_pk_add_f32 v[244:245], v[244:245], v[188:189]\n\t"
      "v_pk_add_f32 v[246:247], v[246:247], v[190:191]\n\t"
      "v_pk_mul_f32 v[240:241], v[240:241], v[136:137] op_sel_hi:[1,0]\n\t"
      "v_pk_mul_f32 v[242:243], v[242:243], v[136:137] op_sel_hi:[1,0]\n\t"
      "v_cvt_pk_bf16_f32 v192, v240, v241\n\t"
      "v_cvt_pk_bf16_f32 v193, v242, v243\n\t"
      "v_pk_mul_f32 v[244:245], v[244:245], v[136:137] op_sel_hi:[1,0]\n\t"
      "v_pk_mul_f32 v[246:247], v[246:247], v[136:137] op_sel_hi:[1,0]\n\t"
      "v_cvt_pk_bf16_f32 v194, v244, v245\n\t"
      "v_cvt_pk_bf16_f32 v195, v246, v247\n\t"
      "v_min_u32 v192, -2, v192\n\t"
      "v_min_u32 v193, -2, v193\n\t"
      "v_min_u32 v194, -2, v194\n\t"
      "v_min_u32 v195, -2, v195\n\t"
      "buffer_store_dwordx4 v[192:195], v12, s[28:31], s48 offen sc1\n\t"
      "v_mov_b32 v144, s50\n\t"
      "v_add_u32 v145, s80, v15\n\t"
      "ds_write_b32 v145, v144\n\t"
      "s_waitcnt lgkmcnt(0)\n\t"
      "s_barrier\n\t"
      "ds_read_b32 v144, v16\n\t"
      "s_waitcnt lgkmcnt(0)\n\t"
      "v_cmp_ne_u32 vcc, 0, v144\n\t"
      "s_nop 1\n\t"
      "s_cbranch_vccnz redo_%=\n\t"
      "s_cmp_eq_u32 s70, 0\n\t"
      "s_cbranch_scc1 opj_%=\n\t"
      "s_waitcnt vmcnt(3)\n\t"
      "s_add_u32 s48, s72, 65536\n\t"
      "buffer_load_dwordx4 a[160:163], v8, s[28:31], s48 offen offset:2048 sc1\n\t"
      "v_accvgpr_write_b32 a64, v36\n\t"
      "v_accvgpr_write_b32 a65, v37\n\t"
      "v_accvgpr_write_b32 a66, v38\n\t"
      "v_accvgpr_write_b32 a67, v39\n\t"
      "v_accvgpr_write_b32 a68, v40\n\t"
      "v_accvgpr_write_b32 a69, v41\n\t"
      "v_accvgpr_write_b32 a70, v42\n\t"
      "v_accvgpr_write_b32 a71, v43\n\t"
      "v_accvgpr_write_b32 a72, v44\n\t"
      "v_accvgpr_write_b32 a73, v45\n\t"
      "v_accvgpr_write_b32 a74, v46\n\t"
      "v_accvgpr_write_b32 a75, v47\n\t"
      "v_accvgpr_write_b32 a76, v48\n\t"
      "v_accvgpr_write_b32 a77, v49\n\t"
      "v_accvgpr_write_b32 a78, v50\n\t"
      "v_accvgpr_write_b32 a79, v51\n\t"
      "v_accvgpr_write_b32 a80, v52\n\t"
      "v_accvgpr_write_b32 a81, v53\n\t"
      "v_accvgpr_write_b32 a82, v54\n\t"
      "v_accvgpr_write_b32 a83, v55\n\t"
      "v_accvgpr_write_b32 a84, v56\n\t"
      "v_accvgpr_write_b32 a85, v57\n\t"
      "v_accvgpr_write_b32 a86, v58\n\t"
      "v_accvgpr_write_b32 a87, v59\n\t"
      "v_accvgpr_write_b32 a88, v60\n\t"
      "v_accvgpr_write_b32 a89, v61\n\t"
      "v_accvgpr_write_b32 a90, v62\n\t"
      "v_accvgpr_write_b32 a91, v63\n\t"
      "v_accvgpr_write_b32 a92, v64\n\t"
      "v_accvgpr_write_b32 a93, v65\n\t"
      "v_accvgpr_write_b32 a94, v66\n\t"
      "v_accvgpr_write_b32 a95, v67\n\t"
      "v_accvgpr_write_b32 a96, v68\n\t"
      "v_accvgpr_write_b32 a97, v69\n\t"
      "v_accvgpr_write_b32 a98, v70\n\t"
      "v_accvgpr_write_b32 a99, v71\n\t"
      "v_accvgpr_write_b32 a100, v72\n\t"
      "v_accvgpr_write_b32 a101, v73\n\t"
      "v_accvgpr_write_b32 a102, v74\n\t"
      "v_accvgpr_write_b32 a103, v75\n\t"
      "s_add_u32 s48, s72, 65536\n\t"
      "buffer_load_dwordx4 a[164:167], v9, s[28:31], s48 offen offset:3072 sc1\n\t"
      "v_accvgpr_write_b32 a104, v76\n\t"
      "v_accvgpr_write_b32 a105, v77\n\t"
      "v_accvgpr_write_b32 a106, v78\n\t"
      "v_accvgpr_write_b32 a107, v79\n\t"
      "v_accvgpr_write_b32 a108, v80\n\t"
      "v_accvgpr_write_b32 a109, v81\n\t"
      "v_accvgpr_write_b32 a110, v82\n\t"
      "v_accvgpr_write_b32 a111, v83\n\t"
      "v_accvgpr_write_b32 a112, v84\n\t"
      "v_accvgpr_write_b32 a113, v85\n\t"
      "v_accvgpr_write_b32 a114, v86\n\t"
      "v_accvgpr_write_b32 a115, v87\n\t"
      "v_accvgpr_write_b32 a116, v88\n\t"
      "v_accvgpr_write_b32 a117, v89\n\t"
      "v_accvgpr_write_b32 a118, v90\n\t"
      "v_accvgpr_write_b32 a119, v91\n\t"
      "v_accvgpr_write_b32 a120, v92\n\t"
      "v_accvgpr_write_b32 a121, v93\n\t"
      "v_accvgpr_write_b32 a122, v94\n\t"
      "v_accvgpr_write_b32 a123, v95\n\t"
      "v_accvgpr_write_b32 a124, v96\n\t"
      "v_accvgpr_write_b32 a125, v97\n\t"
      "v_accvgpr_write_b32 a126, v98\n\t"
      "v_accvgpr_write_b32 a127, v99\n\t"
      "v_accvgpr_write_b32 a128, v100\n\t"
      "v_accvgpr_write_b32 a129, v101\n\t"
      "v_accvgpr_write_b32 a130, v102\n\t"
      "v_accvgpr_write_b32 a131, v103\n\t"
      "v_accvgpr_write_b32 a132, v104\n\t"
      "v_accvgpr_write_b32 a133, v105\n\t"
      "v_accvgpr_write_b32 a134, v106\n\t"
      "v_accvgpr_write_b32 a135, v107\n\t"
      "v_accvgpr_write_b32 a136, v108\n\t"
      "v_accvgpr_write_b32 a137, v109\n\t"
      "v_accvgpr_write_b32 a138, v110\n\t"
      "v_accvgpr_write_b32 a139, v111\n\t"
      "v_accvgpr_write_b32 a140, v112\n\t"
      "v_accvgpr_write_b32 a141, v113\n\t"
      "v_accvgpr_write_b32 a142, v114\n\t"
      "v_accvgpr_write_b32 a143, v115\n\t"
      "s_add_u32 s48, s72, 69632\n\t"
      "buffer_load_dwordx4 a[168:171], v8, s[28:31], s48 offen offset:0 sc1\n\t"
      "v_accvgpr_write_b32 a144, v116\n\t"
      "v_accvgpr_write_b32 a145, v117\n\t"
      "v_accvgpr_write_b32 a146, v118\n\t"
      "v_accvgpr_write_b32 a147, v119\n\t"
      "v_accvgpr_write_b32 a148, v120\n\t"
      "v_accvgpr_write_b32 a149, v121\n\t"
      "v_accvgpr_write_b32 a150, v122\n\t"
      "v_accvgpr_write_b32 a151, v123\n\t"
      "v_accvgpr_write_b32 a152, v124\n\t"
      "v_accvgpr_write_b32 a153, v125\n\t"
      "v_accvgpr_write_b32 a154, v126\n\t"
      "v_accvgpr_write_b32 a155, v127\n\t"
      "v_accvgpr_write_b32 a156, v128\n\t"
      "v_accvgpr_write_b32 a157, v129\n\t"
      "v_accvgpr_write_b32 a158, v130\n\t"
      "v_accvgpr_write_b32 a159, v131\n\t"
      "v_accvgpr_read_b32 v64, a0\n\t"
      "v_accvgpr_read_b32 v65, a1\n\t"
      "v_accvgpr_read_b32 v66, a2\n\t"
      "v_accvgpr_read_b32 v67, a3\n\t"
      "v_accvgpr_read_b32 v68, a4\n\t"
      "v_accvgpr_read_b32 v69, a5\n\t"
      "v_accvgpr_read_b32 v70, a6\n\t"
      "v_accvgpr_read_b32 v71, a7\n\t"
      "v_accvgpr_read_b32 v72, a8\n\t"
      "v_accvgpr_read_b32 v73, a9\n\t"
      "v_accvgpr_read_b32 v74, a10\n\t"
      "v_accvgpr_read_b32 v75, a11\n\t"
      "v_accvgpr_read_b32 v76, a12\n\t"
      "v_accvgpr_read_b32 v77, a13\n\t"
      "v_accvgpr_read_b32 v78, a14\n\t"
      "v_accvgpr_read_b32 v79, a15\n\t"
      "v_accvgpr_read_b32 v80, a16\n\t"
      "v_accvgpr_read_b32 v81, a17\n\t"
      "v_accvgpr_read_b32 v82, a18\n\t"
      "v_accvgpr_read_b32 v83, a19\n\t"
      "v_accvgpr_read_b32 v84, a20\n\t"
      "v_accvgpr_read_b32 v85, a21\n\t"
      "v_accvgpr_read_b32 v86, a22\n\t"
      "v_accvgpr_read_b32 v87, a23\n\t"
      "s_add_u32 s48, s72, 69632\n\t"
      "buffer_load_dwordx4 a[172:175], v9, s[28:31], s48 offen offset:1024 sc1\n\t"
      "v_accvgpr_read_b32 v88, a24\n\t"
      "v_accvgpr_read_b32 v89, a25\n\t"
      "v_accvgpr_read_b32 v90, a26\n\t"
      "v_accvgpr_read_b32 v91, a27\n\t"
      "v_accvgpr_read_b32 v92, a28\n\t"
      "v_accvgpr_read_b32 v93, a29\n\t"
      "v_accvgpr_read_b32 v94, a30\n\t"
      "v_accvgpr_read_b32 v95, a31\n\t"
      "v_accvgpr_read_b32 v96, a32\n\t"
      "v_accvgpr_read_b32 v97, a33\n\t"
      "v_accvgpr_read_b32 v98, a34\n\t"
      "v_accvgpr_read_b32 v99, a35\n\t"
      "v_accvgpr_read_b32 v100, a36\n\t"
      "v_accvgpr_read_b32 v101, a37\n\t"
      "v_accvgpr_read_b32 v102, a38\n\t"
      "v_accvgpr_read_b32 v103, a39\n\t"
      "v_accvgpr_read_b32 v104, a40\n\t"
      "v_accvgpr_read_b32 v105, a41\n\t"
      "v_accvgpr_read_b32 v106, a42\n\t"
      "v_accvgpr_read_b32 v107, a43\n\t"
      "v_accvgpr_read_b32 v108, a44\n\t"
      "v_accvgpr_read_b32 v109, a45\n\t"
      "v_accvgpr_read_b32 v110, a46\n\t"
      "v_accvgpr_read_b32 v111, a47\n\t"
      "v_accvgpr_read_b32 v112, a48\n\t"
      "v_accvgpr_read_b32 v113, a49\n\t"
      "v_accvgpr_read_b32 v114, a50\n\t"
      "v_accvgpr_read_b32 v115, a51\n\t"
      "v_accvgpr_read_b32 v116, a52\n\t"
      "v_accvgpr_read_b32 v117, a53\n\t"
      "v_accvgpr_read_b32 v118, a54\n\t"
      "v_accvgpr_read_b32 v119, a55\n\t"
      "v_accvgpr_read_b32 v120, a56\n\t"
      "v_accvgpr_read_b32 v121, a57\n\t"
      "v_accvgpr_read_b32 v122, a58\n\t"
      "v_accvgpr_read_b32 v123, a59\n\t"
      "v_accvgpr_read_b32 v124, a60\n\t"
      "v_accvgpr_read_b32 v125, a61\n\t"
      "v_accvgpr_read_b32 v126, a62\n\t"
      "v_accvgpr_read_b32 v127, a63\n\t"
      "ds_read_b128 v[48:51], v2 offset:0\n\t"
      "ds_read_b128 v[52:55], v2 offset:1024\n\t"
      "ds_read_b128 v[56:59], v2 offset:4096\n\t"
      "ds_read_b128 v[60:63], v2 offset:5120\n\t"
      "ds_read_b128 v[240:243], v2 offset:8192\n\t"
      "ds_read_b128 v[244:247], v2 offset:9216\n\t"
      "s_mov_b32 s50, 0x10000\n\t"
      "pr_%=:\n\t"
      "buffer_load_dword v28, v18, s[28:31], 0 offen sc1\n\t"
      "s_sub_u32 s50, s50, 1\n\t"
      "s_waitcnt vmcnt(0)\n\t"
      "v_cmp_eq_u32 vcc, -1, v28\n\t"
      "s_cmp_eq_u32 s50, 0\n\t"
      "s_cbranch_scc1 prd_%=\n\t"
      "s_cbranch_vccnz pr_%=\n\t"
      "prd_%=:\n\t"
      "s_xor_b32 s49, s81, 0\n\t"
      "buffer_load_dwordx4 v[128:131], v2, s[28:31], s49 offen offset:0 sc1\n\t"
      "buffer_load_dwordx4 v[132:135], v2, s[28:31], s49 offen offset:1024 sc1\n\t"
      "buffer_load_dwordx4 v[136:139], v2, s[28:31], s49 offen offset:2048 sc1\n\t"
      "buffer_load_dwordx4 v[140:143], v2, s[28:31], s49 offen offset:3072 sc1\n\t"
      "s_xor_b32 s49, s81, 4096\n\t"
      "buffer_load_dwordx4 v[144:147], v2, s[28:31], s49 offen offset:0 sc1\n\t"
      "buffer_load_dwordx4 v[148:151], v2, s[28:31], s49 offen offset:1024 sc1\n\t"
      "buffer_load_dwordx4 v[152:155], v2, s[28:31], s49 offen offset:2048 sc1\n\t"
      "s_xor_b32 s49, s81, 8192\n\t"
      "buffer_load_dwordx4 v[156:159], v2, s[28:31], s49 offen offset:0 sc1\n\t"
      "buffer_load_dwordx4 v[160:163], v2, s[28:31], s49 offen offset:1024 sc1\n\t"
      "buffer_load_dwordx4 v[164:167], v2, s[28:31], s49 offen offset:2048 sc1\n\t"
      "buffer_load_dwordx4 v[168:171], v2, s[28:31], s49 offen offset:3072 sc1\n\t"
      "s_barrier\n\t"
      "s_waitcnt vmcnt(7)\n\t"
      "v_max3_u32 v28, v128, v129, v130\n\t"
      "v_max3_u32 v28, v28, v131, v132\n\t"
      "v_max3_u32 v28, v28, v133, v134\n\t"
      "v_max3_u32 v28, v28, v135, v136\n\t"
      "v_max3_u32 v28, v28, v137, v138\n\t"
      "v_max3_u32 v28, v28, v139, v140\n\t"
      "v_max3_u32 v28, v28, v141, v142\n\t"
      "v_max_u32 v28, v28, v143\n\t"
      "v_cmp_eq_u32 vcc, -1, v28\n\t"
      "s_nop 0\n\t"
      "s_cbranch_vccnz so0_%=\n\t"
      "ro0_%=:\n\t"
      "s_waitcnt lgkmcnt(4)\n\t"
      "v_mfma_f32_32x32x16_bf16 v[64:79], v[48:51], v[128:131], v[64:79]\n\t"
      "ds_read_b128 v[248:251], v2 offset:12288\n\t"
      "s_xor_b32 s49, s81, 12288\n\t"
      "buffer_load_dwordx4 v[172:175], v2, s[28:31], s49 offen offset:0 sc1\n\t"
      "v_mfma_f32_32x32x16_bf16 v[80:95], v[52:55], v[128:131], v[80:95]\n\t"
      "ds_read_b128 v[252:255], v2 offset:13312\n\t"
      "s_waitcnt lgkmcnt(4)\n\t"
      "v_mfma_f32_32x32x16_bf16 v[64:79], v[56:59], v[132:135], v[64:79]\n\t"
      "ds_read_b128 v[48:51], v2 offset:16384\n\t"
      "buffer_load_dwordx4 v[176:179], v2, s[28:31], s49 offen offset:1024 sc1\n\t"
      "v_mfma_f32_32x32x16_bf16 v[80:95], v[60:63], v[132:135], v[80:95]\n\t"
      "ds_read_b128 v[52:55], v2 offset:17408\n\t"
      "s_waitcnt lgkmcnt(4)\n\t"
      "v_mfma_f32_32x32x16_bf16 v[64:79], v[240:243], v[136:139], v[64:79]\n\t"
      "ds_read_b128 v[56:59], v2 offset:20480\n\t"
      "buffer_load_dwordx4 v[180:183], v2, s[28:31], s49 offen offset:2048 sc1\n\t"
      "v_mfma_f32_32x32x16_bf16 v[80:95], v[244:247], v[136:139], v[80:95]\n\t"
      "ds_read_b128 v[60:63], v2 offset:21504\n\t"
      "s_waitcnt vmcnt(7)\n\t"
      "v_max3_u32 v28, v144, v145, v146\n\t"
      "v_max3_u32 v28, v28, v147, v148\n\t"
      "v_max3_u32 v28, v28, v149, v150\n\t"
      "s_waitcnt lgkmcnt(4)\n\t"
      "v_mfma_f32_32x32x16_bf16 v[64:79], v[248:251], v[140:143], v[64:79]\n\t"
      "ds_read_b128 v[240:243], v2 offset:24576\n\t"
      "s_xor_b32 s49, s81, 16384\n\t"
      "buffer_load_dwordx4 v[184:187], v2, s[28:31], s49 offen offset:0 sc1\n\t"
      "v_max3_u32 v28, v28, v151, v152\n\t"
      "v_max3_u32 v28, v28, v153, v154\n\t"
      "v_mfma_f32_32x32x16_bf16 v[80:95], v[252:255], v[140:143], v[80:95]\n\t"
      "ds_read_b128 v[244:247], v2 offset:25600\n\t"
      "v_max_u32 v28, v28, v155\n\t"
      "v_cmp_eq_u32 vcc, -1, v28\n\t"
      "s_nop 0\n\t"
      "s_cbranch_vccnz so1_%=\n\t"
      "ro1_%=:\n\t"
      "s_waitcnt lgkmcnt(4)\n\t"
      "v_mfma_f32_32x32x16_bf16 v[64:79], v[48:51], v[144:147], v[64:79]\n\t"
      "ds_read_b128 v[248:251], v2 offset:28672\n\t"
      "buffer_load_dwordx4 v[188:191], v2, s[28:31], s49 offen offset:1024 sc1\n\t"
      "v_mfma_f32_32x32x16_bf16 v[80:95], v[52:55], v[144:147], v[80:95]\n\t"
      "ds_read_b128 v[252:255], v2 offset:29696\n\t"
      "s_waitcnt lgkmcnt(4)\n\t"
      "v_mfma_f32_32x32x16_bf16 v[64:79], v[56:59], v[148:151], v[64:79]\n\t"
      "ds_read_b128 v[48:51], v2 offset:32768\n\t"
      "buffer_load_dwordx4 v[192:195], v2, s[28:31], s49 offen offset:2048 sc1\n\t"
      "v_mfma_f32_32x32x16_bf16 v[80:95], v[60:63], v[148:151], v[80:95]\n\t"
      "ds_read_b128 v[52:55], v2 offset:33792\n\t"
      "s_waitcnt vmcnt(6)\n\t"
      "v_max3_u32 v28, v156, v157, v158\n\t"
      "v_max3_u32 v28, v28, v159, v160\n\t"
      "v_max3_u32 v28, v28, v161, v162\n\t"
      "s_waitcnt lgkmcnt(4)\n\t"
      "v_mfma_f32_32x32x16_bf16 v[64:79], v[240:243], v[152:155], v[64:79]\n\t"
      "ds_read_b128 v[56:59], v2 offset:36864\n\t"
      "buffer_load_dwordx4 v[196:199], v2, s[28:31], s49 offen offset:3072 sc1\n\t"
      "v_max3_u32 v28, v28, v163, v164\n\t"
      "v_max3_u32 v28, v28, v165, v166\n\t"
      "v_mfma_f32_32x32x16_bf16 v[80:95], v[244:247], v[152:155], v[80:95]\n\t"
      "ds_read_b128 v[60:63], v2 offset:37888\n\t"
      "v_max3_u32 v28, v28, v167, v168\n\t"
      "v_max3_u32 v28, v28, v169, v170\n\t"
      "v_max_u32 v28, v28, v171\n\t"
      "v_cmp_eq_u32 vcc, -1, v28\n\t"
      "s_nop 0\n\t"
      "s_cbranch_vccnz so2_%=\n\t"
      "ro2_%=:\n\t"
      "s_waitcnt lgkmcnt(4)\n\t"
      "v_mfma_f32_32x32x16_bf16 v[64:79], v[248:251], v[156:159], v[64:79]\n\t"
      "ds_read_b128 v[240:243], v2 offset:40960\n\t"
      "s_xor_b32 s49, s81, 20480\n\t"
      "buffer_load_dwordx4 v[200:203], v2, s[28:31], s49 offen offset:0 sc1\n\t"
      "v_mfma_f32_32x32x16_bf16 v[80:95], v[252:255], v[156:159], v[80:95]\n\t"
      "ds_read_b128 v[244:247], v2 offset:41984\n\t"
      "s_waitcnt lgkmcnt(4)\n\t"
      "v_mfma_f32_32x32x16_bf16 v[64:79], v[48:51], v[160:163], v[64:79]\n\t"
      "ds_read_b128 v[248:251], v2 offset:45056\n\t"
      "buffer_load_dwordx4 v[204:207], v2, s[28:31], s49 offen offset:1024 sc1\n\t"
      "v_mfma_f32_32x32x16_bf16 v[80:95], v[52:55], v[160:163], v[80:95]\n\t"
      "ds_read_b128 v[252:255], v2 offset:46080\n\t"
      "s_waitcnt lgkmcnt(4)\n\t"
      "v_mfma_f32_32x32x16_bf16 v[64:79], v[56:59], v[164:167], v[64:79]\n\t"
      "ds_read_b128 v[48:51], v2 offset:49152\n\t"
      "buffer_load_dwordx4 v[208:211], v2, s[28:31], s49 offen offset:2048 sc1\n\t"
      "v_mfma_f32_32x32x16_bf16 v[80:95], v[60:63], v[164:167], v[80:95]\n\t"
      "ds_read_b128 v[52:55], v2 offset:50176\n\t"
      "s_waitcnt vmcnt(7)\n\t"
      "v_max3_u32 v28, v172, v173, v174\n\t"
      "v_max3_u32 v28, v28, v175, v176\n\t"
      "v_max3_u32 v28, v28, v177, v178\n\t"
      "s_waitcnt lgkmcnt(4)\n\t"
      "v_mfma_f32_32x32x16_bf16 v[64:79], v[240:243], v[168:171], v[64:79]\n\t"
      "ds_read_b128 v[56:59], v2 offset:53248\n\t"
      "s_xor_b32 s49, s81, 24576\n\t"
      "buffer_load_dwordx4 v[212:215], v2, s[28:31], s49 offen offset:0 sc1\n\t"
      "v_max3_u32 v28, v28, v179, v180\n\t"
      "v_max3_u32 v28, v28, v181, v182\n\t"
      "v_mfma_f32_32x32x16_bf16 v[80:95], v[244:247], v[168:171], v[80:95]\n\t"
      "ds_read_b128 v[60:63], v2 offset:54272\n\t"
      "v_max_u32 v28, v28, v183\n\t"
      "v_cmp_eq_u32 vcc, -1, v28\n\t"
      "s_nop 0\n\t"
      "s_cbranch_vccnz so3_%=\n\t"
      "ro3_%=:\n\t"
      "s_waitcnt lgkmcnt(4)\n\t"
      "v_mfma_f32_32x32x16_bf16 v[64:79], v[248:251], v[172:175], v[64:79]\n\t"
      "ds_read_b128 v[240:243], v2 offset:57344\n\t"
      "buffer_load_dwordx4 v[216:219], v2, s[28:31], s49 offen offset:1024 sc1\n\t"
      "v_mfma_f32_32x32x16_bf16 v[80:95], v[252:255], v[172:175], v[80:95]\n\t"
      "ds_read_b128 v[244:247], v2 offset:58368\n\t"
      "s_waitcnt lgkmcnt(4)\n\t"
      "v_mfma_f32_32x32x16_bf16 v[64:79], v[48:51], v[176:179], v[64:79]\n\t"
      "ds_read_b128 v[248:251], v2 offset:61440\n\t"
      "buffer_load_dwordx4 v[220:223], v2, s[28:31], s49 offen offset:2048 sc1\n\t"
      "v_mfma_f32_32x32x16_bf16 v[80:95], v[52:55], v[176:179], v[80:95]\n\t"
      "ds_read_b128 v[252:255], v2 offset:62464\n\t"
      "s_waitcnt vmcnt(6)\n\t"
      "v_max3_u32 v28, v184, v185, v186\n\t"
      "v_max3_u32 v28, v28, v187, v188\n\t"
      "v_max3_u32 v28, v28, v189, v190\n\t"
      "s_waitcnt lgkmcnt(4)\n\t"
      "v_mfma_f32_32x32x16_bf16 v[64:79], v[56:59], v[180:183], v[64:79]\n\t"
      "ds_read_b128 v[48:51], v17 offset:0\n\t"
      "buffer_load_dwordx4 v[224:227], v2, s[28:31], s49 offen offset:3072 sc1\n\t"
      "v_max3_u32 v28, v28, v191, v192\n\t"
      "v_max3_u32 v28, v28, v193, v194\n\t"
      "v_mfma_f32_32x32x16_bf16 v[80:95], v[60:63], v[180:183], v[80:95]\n\t"
      "ds_read_b128 v[52:55], v17 offset:1024\n\t"
      "v_max3_u32 v28, v28, v195, v196\n\t"
      "v_max3_u32 v28, v28, v197, v198\n\t"
      "v_max_u32 v28, v28, v199\n\t"
      "v_cmp_eq_u32 vcc, -1, v28\n\t"
      "s_nop 0\n\t"
      "s_cbranch_vccnz so4_%=\n\t"
      "ro4_%=:\n\t"
      "s_waitcnt lgkmcnt(4)\n\t"
      "v_mfma_f32_32x32x16_bf16 v[64:79], v[240:243], v[184:187], v[64:79]\n\t"
      "ds_read_b128 v[56:59], v17 offset:4096\n\t"
      "s_xor_b32 s49, s81, 28672\n\t"
      "buffer_load_dwordx4 v[228:231], v2, s[28:31], s49 offen offset:0 sc1\n\t"
      "v_mfma_f32_32x32x16_bf16 v[80:95], v[244:247], v[184:187], v[80:95]\n\t"
      "ds_read_b128 v[60:63], v17 offset:5120\n\t"
      "s_waitcnt lgkmcnt(4)\n\t"
      "v_mfma_f32_32x32x16_bf16 v[64:79], v[248:251], v[188:191], v[64:79]\n\t"
      "ds_read_b128 v[240:243], v17 offset:8192\n\t"
      "buffer_load_dwordx4 v[232:235], v2, s[28:31], s49 offen offset:1024 sc1\n\t"
      "v_mfma_f32_32x32x16_bf16 v[80:95], v[252:255], v[188:191], v[80:95]\n\t"
      "ds_read_b128 v[244:247], v17 offset:9216\n\t"
      "s_waitcnt lgkmcnt(4)\n\t"
      "v_mfma_f32_32x32x16_bf16 v[64:79], v[48:51], v[192:195], v[64:79]\n\t"
      "ds_read_b128 v[248:251], v17 offset:12288\n\t"
      "buffer_load_dwordx4 v[236:239], v2, s[28:31], s49 offen offset:2048 sc1\n\t"
      "v_mfma_f32_32x32x16_bf16 v[80:95], v[52:55], v[192:195], v[80:95]\n\t"
      "ds_read_b128 v[252:255], v17 offset:13312\n\t"
      "s_waitcnt vmcnt(7)\n\t"
      "v_max3_u32 v28, v200, v201, v202\n\t"
      "v_max3_u32 v28, v28, v203, v204\n\t"
      "v_max3_u32 v28, v28, v205, v206\n\t"
      "s_waitcnt lgkmcnt(4)\n\t"
      "v_mfma_f32_32x32x16_bf16 v[64:79], v[56:59], v[196:199], v[64:79]\n\t"
      "ds_read_b128 v[48:51], v17 offset:16384\n\t"
      "s_add_u32 s48, s72, 176128\n\t"
      "buffer_load_dwordx4 a[0:3], v8, s[28:31], s48 offen offset:2048 sc1\n\t"
      "v_max3_u32 v28, v28, v207, v208\n\t"
      "v_max3_u32 v28, v28, v209, v210\n\t"
      "v_mfma_f32_32x32x16_bf16 v[80:95], v[60:63], v[196:199], v[80:95]\n\t"
      "ds_read_b128 v[52:55], v17 offset:17408\n\t"
      "v_max_u32 v28, v28, v211\n\t"
      "v_cmp_eq_u32 vcc, -1, v28\n\t"
      "s_nop 0\n\t"
      "s_cbranch_vccnz so5_%=\n\t"
      "ro5_%=:\n\t"
      "s_waitcnt lgkmcnt(4)\n\t"
      "v_mfma_f32_32x32x16_bf16 v[64:79], v[240:243], v[200:203], v[64:79]\n\t"
      "ds_read_b128 v[56:59], v17 offset:20480\n\t"
      "s_add_u32 s48, s72, 176128\n\t"
      "buffer_load_dwordx4 a[4:7], v9, s[28:31], s48 offen offset:3072 sc1\n\t"
      "v_mfma_f32_32x32x16_bf16 v[80:95], v[244:247], v[200:203], v[80:95]\n\t"
      "ds_read_b128 v[60:63], v17 offset:21504\n\t"
      "s_waitcnt lgkmcnt(4)\n\t"
      "v_mfma_f32_32x32x16_bf16 v[64:79], v[248:251], v[204:207], v[64:79]\n\t"
      "ds_read_b128 v[240:243], v17 offset:24576\n\t"
      "s_add_u32 s48, s72, 180224\n\t"
      "buffer_load_dwordx4 a[8:11], v8, s[28:31], s48 offen offset:0 sc1\n\t"
      "v_mfma_f32_32x32x16_bf16 v[80:95], v[252:255], v[204:207], v[80:95]\n\t"
      "ds_read_b128 v[244:247], v17 offset:25600\n\t"
      "s_waitcnt vmcnt(6)\n\t"
      "v_max3_u32 v28, v212, v213, v214\n\t"
      "v_max3_u32 v28, v28, v215, v216\n\t"
      "v_max3_u32 v28, v28, v217, v218\n\t"
      "s_waitcnt lgkmcnt(4)\n\t"
      "v_mfma_f32_32x32x16_bf16 v[64:79], v[48:51], v[208:211], v[64:79]\n\t"
      "ds_read_b128 v[248:251], v17 offset:28672\n\t"
      "s_add_u32 s48, s72, 180224\n\t"
      "buffer_load_dwordx4 a[12:15], v9, s[28:31], s48 offen offset:1024 sc1\n\t"
      "v_max3_u32 v28, v28, v219, v220\n\t"
      "v_max3_u32 v28, v28, v221, v222\n\t"
      "v_mfma_f32_32x32x16_bf16 v[80:95], v[52:55], v[208:211], v[80:95]\n\t"
      "ds_read_b128 v[252:255], v17 offset:29696\n\t"
      "v_max3_u32 v28, v28, v223, v224\n\t"
      "v_max3_u32 v28, v28, v225, v226\n\t"
      "v_max_u32 v28, v28, v227\n\t"
      "v_cmp_eq_u32 vcc, -1, v28\n\t"
      "s_nop 0\n\t"
      "s_cbranch_vccnz so6_%=\n\t"
      "ro6_%=:\n\t"
      "s_waitcnt lgkmcnt(4)\n\t"
      "v_mfma_f32_32x32x16_bf16 v[64:79], v[56:59], v[212:215], v[64:79]\n\t"
      "ds_read_b128 v[48:51], v17 offset:32768\n\t"
      "s_add_u32 s48, s72, 180224\n\t"
      "buffer_load_dwordx4 a[16:19], v8, s[28:31], s48 offen offset:2048 sc1\n\t"
      "v_mfma_f32_32x32x16_bf16 v[80:95], v[60:63], v[212:215], v[80:95]\n\t"
      "ds_read_b128 v[52:55], v17 offset:33792\n\t"
      "s_waitcnt lgkmcnt(4)\n\t"
      "v_mfma_f32_32x32x16_bf16 v[64:79], v[240:243], v[216:219], v[64:79]\n\t"
      "ds_read_b128 v[56:59], v17 offset:36864\n\t"
      "s_add_u32 s48, s72, 180224\n\t"
      "buffer_load_dwordx4 a[20:23], v9, s[28:31], s48 offen offset:3072 sc1\n\t"
      "v_mfma_f32_32x32x16_bf16 v[80:95], v[244:247], v[216:219], v[80:95]\n\t"
      "ds_read_b128 v[60:63], v17 offset:37888\n\t"
      "s_waitcnt lgkmcnt(4)\n\t"
      "v_mfma_f32_32x32x16_bf16 v[64:79], v[248:251], v[220:223], v[64:79]\n\t"
      "ds_read_b128 v[240:243], v17 offset:40960\n\t"
      "s_add_u32 s48, s72, 184320\n\t"
      "buffer_load_dwordx4 a[24:27], v8, s[28:31], s48 offen offset:0 sc1\n\t"
      "v_mfma_f32_32x32x16_bf16 v[80:95], v[252:255], v[220:223], v[80:95]\n\t"
      "ds_read_b128 v[244:247], v17 offset:41984\n\t"
      "s_waitcnt vmcnt(7)\n\t"
      "v_max3_u32 v28, v228, v229, v230\n\t"
      "v_max3_u32 v28, v28, v231, v232\n\t"
      "v_max3_u32 v28, v28, v233, v234\n\t"
      "s_waitcnt lgkmcnt(4)\n\t"
      "v_mfma_f32_32x32x16_bf16 v[64:79], v[48:51], v[224:227], v[64:79]\n\t"
      "ds_read_b128 v[248:251], v17 offset:45056\n\t"
      "s_add_u32 s48, s72, 184320\n\t"
      "buffer_load_dwordx4 a[28:31], v9, s[28:31], s48 offen offset:1024 sc1\n\t"
      "v_max3_u32 v28, v28, v235, v236\n\t"
      "v_max3_u32 v28, v28, v237, v238\n\t"
      "v_mfma_f32_32x32x16_bf16 v[80:95], v[52:55], v[224:227], v[80:95]\n\t"
      "ds_read_b128 v[252:255], v17 offset:46080\n\t"
      "v_max_u32 v28, v28, v239\n\t"
      "v_cmp_eq_u32 vcc, -1, v28\n\t"
      "s_nop 0\n\t"
      "s_cbranch_vccnz so7_%=\n\t"
      "ro7_%=:\n\t"
      "s_waitcnt lgkmcnt(4)\n\t"
      "v_mfma_f32_32x32x16_bf16 v[64:79], v[56:59], v[228:231], v[64:79]\n\t"
      "ds_read_b128 v[48:51], v2 offset:2048\n\t"
      "v_mfma_f32_32x32x16_bf16 v[80:95], v[60:63], v[228:231], v[80:95]\n\t"
      "ds_read_b128 v[52:55], v2 offset:6144\n\t"
      "s_waitcnt lgkmcnt(4)\n\t"
      "v_mfma_f32_32x32x16_bf16 v[64:79], v[240:243], v[232:235], v[64:79]\n\t"
      "ds_read_b128 v[56:59], v2 offset:10240\n\t"
      "v_mfma_f32_32x32x16_bf16 v[80:95], v[244:247], v[232:235], v[80:95]\n\t"
      "ds_read_b128 v[60:63], v2 offset:14336\n\t"
      "s_waitcnt lgkmcnt(4)\n\t"
      "v_mfma_f32_32x32x16_bf16 v[64:79], v[248:251], v[236:239], v[64:79]\n\t"
      "ds_read_b128 v[240:243], v2 offset:18432\n\t"
      "v_mfma_f32_32x32x16_bf16 v[80:95], v[252:255], v[236:239], v[80:95]\n\t"
      "ds_read_b128 v[244:247], v2 offset:22528\n\t"
      "s_waitcnt lgkmcnt(4)\n\t"
      "v_mfma_f32_32x32x16_bf16 v[96:111], v[48:51], v[128:131], v[96:111]\n\t"
      "ds_read_b128 v[248:251], v2 offset:26624\n\t"
      "v_mfma_f32_32x32x16_bf16 v[96:111], v[52:55], v[132:135], v[96:111]\n\t"
      "ds_read_b128 v[252:255], v2 offset:30720\n\t"
      "v_lshlrev_b32 v30, 3, v2\n\t"
      "v_cmp_gt_u32 vcc, 0x800, v30\n\t"
      "v_mov_b32 v31, 0x80000000\n\t"
      "s_waitcnt lgkmcnt(4)\n\t"
      "v_mfma_f32_32x32x16_bf16 v[96:111], v[56:59], v[136:139], v[96:111]\n\t"
      "ds_read_b128 v[48:51], v2 offset:34816\n\t"
      "v_cndmask_b32 v30, v31, v30, vcc\n\t"
      "v_mfma_f32_32x32x16_bf16 v[96:111], v[60:63], v[140:143], v[96:111]\n\t"
      "ds_read_b128 v[52:55], v2 offset:38912\n\t"
      "s_waitcnt lgkmcnt(4)\n\t"
      "v_mfma_f32_32x32x16_bf16 v[96:111], v[240:243], v[144:147], v[96:111]\n\t"
      "ds_read_b128 v[56:59], v2 offset:43008\n\t"
      "v_cvt_pk_bf16_f32 v32, v64, v65\n\t"
      "v_cvt_pk_bf16_f32 v33, v66, v67\n\t"
      "v_pk_max_i16 v32, v32, 0\n\t"
      "v_pk_max_i16 v33, v33, 0\n\t"
      "v_mfma_f32_32x32x16_bf16 v[96:111], v[244:247], v[148:151], v[96:111]\n\t"
      "ds_read_b128 v[60:63], v2 offset:47104\n\t"
      "v_cvt_pk_bf16_f32 v34, v68, v69\n\t"
      "v_cvt_pk_bf16_f32 v35, v70, v71\n\t"
      "v_pk_max_i16 v34, v34, 0\n\t"
      "v_pk_max_i16 v35, v35, 0\n\t"
      "s_waitcnt lgkmcnt(4)\n\t"
      "v_mfma_f32_32x32x16_bf16 v[96:111], v[248:251], v[152:155], v[96:111]\n\t"
      "ds_read_b128 v[240:243], v2 offset:51200\n\t"
      "s_add_u32 s49, s82, 0\n\t"
      "buffer_store_dwordx4 v[32:35], v2, s[28:31], s49 offen offset:0 sc1\n\t"
      "v_mfma_f32_32x32x16_bf16 v[96:111], v[252:255], v[156:159], v[96:111]\n\t"
      "ds_read_b128 v[244:247], v2 offset:55296\n\t"
      "v_cvt_pk_bf16_f32 v36, v72, v73\n\t"
      "v_cvt_pk_bf16_f32 v37, v74, v75\n\t"
      "v_pk_max_i16 v36, v36, 0\n\t"
      "v_pk_max_i16 v37, v37, 0\n\t"
      "s_waitcnt lgkmcnt(4)\n\t"
      "v_mfma_f32_32x32x16_bf16 v[96:111], v[48:51], v[160:163], v[96:111]\n\t"
      "ds_read_b128 v[248:251], v2 offset:59392\n\t"
      "v_cvt_pk_bf16_f32 v38, v76, v77\n\t"
      "v_cvt_pk_bf16_f32 v39, v78, v79\n\t"
      "v_pk_max_i16 v38, v38, 0\n\t"
      "v_pk_max_i16 v39, v39, 0\n\t"
      "v_mfma_f32_32x32x16_bf16 v[96:111], v[52:55], v[164:167], v[96:111]\n\t"
      "ds_read_b128 v[252:255], v2 offset:63488\n\t"
      "buffer_store_dwordx4 v[36:39], v2, s[28:31], s49 offen offset:1024 sc1\n\t"
      "s_waitcnt lgkmcnt(4)\n\t"
      "v_mfma_f32_32x32x16_bf16 v[96:111], v[56:59], v[168:171], v[96:111]\n\t"
      "ds_read_b128 v[48:51], v17 offset:2048\n\t"
      "v_cvt_pk_bf16_f32 v32, v80, v81\n\t"
      "v_cvt_pk_bf16_f32 v33, v82, v83\n\t"
      "v_pk_max_i16 v32, v32, 0\n\t"
      "v_pk_max_i16 v33, v33, 0\n\t"
      "v_mfma_f32_32x32x16_bf16 v[96:111], v[60:63], v[172:175], v[96:111]\n\t"
      "ds_read_b128 v[52:55], v17 offset:6144\n\t"
      "v_cvt_pk_bf16_f32 v34, v84, v85\n\t"
      "v_cvt_pk_bf16_f32 v35, v86, v87\n\t"
      "v_pk_max_i16 v34, v34, 0\n\t"
      "v_pk_max_i16 v35, v35, 0\n\t"
      "s_waitcnt lgkmcnt(4)\n\t"
      "v_mfma_f32_32x32x16_bf16 v[96:111], v[240:243], v[176:179], v[96:111]\n\t"
      "ds_read_b128 v[56:59], v17 offset:10240\n\t"
      "s_add_u32 s49, s82, 28672\n\t"
      "buffer_store_dwordx4 v[32:35], v2, s[28:31], s49 offen offset:0 sc1\n\t"
      "v_mfma_f32_32x32x16_bf16 v[96:111], v[244:247], v[180:183], v[96:111]\n\t"
      "ds_read_b128 v[60:63], v17 offset:14336\n\t"
      "v_cvt_pk_bf16_f32 v36, v88, v89\n\t"
      "v_cvt_pk_bf16_f32 v37, v90, v91\n\t"
      "v_pk_max_i16 v36, v36, 0\n\t"
      "v_pk_max_i16 v37, v37, 0\n\t"
      "s_waitcnt lgkmcnt(4)\n\t"
      "v_mfma_f32_32x32x16_bf16 v[96:111], v[248:251], v[184:187], v[96:111]\n\t"
      "ds_read_b128 v[240:243], v17 offset:18432\n\t"
      "v_cvt_pk_bf16_f32 v38, v92, v93\n\t"
      "v_cvt_pk_bf16_f32 v39, v94, v95\n\t"
      "v_pk_max_i16 v38, v38, 0\n\t"
      "v_pk_max_i16 v39, v39, 0\n\t"
      "v_mfma_f32_32x32x16_bf16 v[96:111], v[252:255], v[188:191], v[96:111]\n\t"
      "ds_read_b128 v[244:247], v17 offset:22528\n\t"
      "buffer_store_dwordx4 v[36:39], v2, s[28:31], s49 offen offset:1024 sc1\n\t"
      "s_waitcnt lgkmcnt(4)\n\t"
      "v_mfma_f32_32x32x16_bf16 v[96:111], v[48:51], v[192:195], v[96:111]\n\t"
      "ds_read_b128 v[248:251], v17 offset:26624\n\t"
      "v_mfma_f32_32x32x16_bf16 v[96:111], v[52:55], v[196:199], v[96:111]\n\t"
      "ds_read_b128 v[252:255], v17 offset:30720\n\t"
      "s_waitcnt lgkmcnt(4)\n\t"
      "v_mfma_f32_32x32x16_bf16 v[96:111], v[56:59], v[200:203], v[96:111]\n\t"
      "ds_read_b128 v[48:51], v17 offset:34816\n\t"
      "v_mfma_f32_32x32x16_bf16 v[96:111], v[60:63], v[204:207], v[96:111]\n\t"
      "ds_read_b128 v[52:55], v17 offset:38912\n\t"
      "s_waitcnt lgkmcnt(4)\n\t"
      "v_mfma_f32_32x32x16_bf16 v[96:111], v[240:243], v[208:211], v[96:111]\n\t"
      "ds_read_b128 v[56:59], v17 offset:43008\n\t"
      "v_mfma_f32_32x32x16_bf16 v[96:111], v[244:247], v[212:215], v[96:111]\n\t"
      "ds_read_b128 v[60:63], v17 offset:47104\n\t"
      "s_waitcnt lgkmcnt(4)\n\t"
      "v_mfma_f32_32x32x16_bf16 v[96:111], v[248:251], v[216:219], v[96:111]\n\t"
      "ds_read_b128 v[240:243], v2 offset:3072\n\t"
      "v_mfma_f32_32x32x16_bf16 v[96:111], v[252:255], v[220:223], v[96:111]\n\t"
      "ds_read_b128 v[244:247], v2 offset:7168\n\t"
      "s_waitcnt lgkmcnt(4)\n\t"
      "v_mfma_f32_32x32x16_bf16 v[96:111], v[48:51], v[224:227], v[96:111]\n\t"
      "ds_read_b128 v[248:251], v2 offset:11264\n\t"
      "v_mfma_f32_32x32x16_bf16 v[96:111], v[52:55], v[228:231], v[96:111]\n\t"
      "ds_read_b128 v[252:255], v2 offset:15360\n\t"
      "s_waitcnt lgkmcnt(4)\n\t"
      "v_mfma_f32_32x32x16_bf16 v[96:111], v[56:59], v[232:235], v[96:111]\n\t"
      "ds_read_b128 v[48:51], v2 offset:19456\n\t"
      "v_mfma_f32_32x32x16_bf16 v[96:111], v[60:63], v[236:239], v[96:111]\n\t"
      "ds_read_b128 v[52:55], v2 offset:23552\n\t"
      "s_waitcnt lgkmcnt(4)\n\t"
      "v_mfma_f32_32x32x16_bf16 v[112:127], v[240:243], v[128:131], v[112:127]\n\t"
      "ds_read_b128 v[56:59], v2 offset:27648\n\t"
      "v_mfma_f32_32x32x16_bf16 v[112:127], v[244:247], v[132:135], v[112:127]\n\t"
      "ds_read_b128 v[60:63], v2 offset:31744\n\t"
      "s_waitcnt lgkmcnt(4)\n\t"
      "v_mfma_f32_32x32x16_bf16 v[112:127], v[248:251], v[136:139], v[112:127]\n\t"
      "ds_read_b128 v[240:243], v2 offset:35840\n\t"
      "s_add_u32 s48, s84, 0\n\t"
      "buffer_load_dwordx4 v[128:131], v19, s[28:31], s48 offen sc1\n\t"
      "v_mfma_f32_32x32x16_bf16 v[112:127], v[252:255], v[140:143], v[112:127]\n\t"
      "ds_read_b128 v[244:247], v2 offset:39936\n\t"
      "s_waitcnt lgkmcnt(4)\n\t"
      "v_mfma_f32_32x32x16_bf16 v[112:127], v[48:51], v[144:147], v[112:127]\n\t"
      "ds_read_b128 v[248:251], v2 offset:44032\n\t"
      "buffer_load_dwordx4 v[132:135], v20, s[28:31], s48 offen sc1\n\t"
      "v_mfma_f32_32x32x16_bf16 v[112:127], v[52:55], v[148:151], v[112:127]\n\t"
      "ds_read_b128 v[252:255], v2 offset:48128\n\t"
      "v_cvt_pk_bf16_f32 v32, v96, v97\n\t"
      "v_cvt_pk_bf16_f32 v33, v98, v99\n\t"
      "v_pk_max_i16 v32, v32, 0\n\t"
      "v_pk_max_i16 v33, v33, 0\n\t"
      "s_waitcnt lgkmcnt(4)\n\t"
      "v_mfma_f32_32x32x16_bf16 v[112:127], v[56:59], v[152:155], v[112:127]\n\t"
      "ds_read_b128 v[48:51], v2 offset:52224\n\t"
      "v_cvt_pk_bf16_f32 v34, v100, v101\n\t"
      "v_cvt_pk_bf16_f32 v35, v102, v103\n\t"
      "v_pk_max_i16 v34, v34, 0\n\t"
      "v_pk_max_i16 v35, v35, 0\n\t"
      "v_mfma_f32_32x32x16_bf16 v[112:127], v[60:63], v[156:159], v[112:127]\n\t"
      "ds_read_b128 v[52:55], v2 offset:56320\n\t"
      "s_add_u32 s49, s82, 57344\n\t"
      "buffer_store_dwordx4 v[32:35], v2, s[28:31], s49 offen offset:0 sc1\n\t"
      "s_waitcnt lgkmcnt(4)\n\t"
      "v_mfma_f32_32x32x16_bf16 v[112:127], v[240:243], v[160:163], v[112:127]\n\t"
      "ds_read_b128 v[56:59], v2 offset:60416\n\t"
      "v_cvt_pk_bf16_f32 v36, v104, v105\n\t"
      "v_cvt_pk_bf16_f32 v37, v106, v107\n\t"
      "v_pk_max_i16 v36, v36, 0\n\t"
      "v_pk_max_i16 v37, v37, 0\n\t"
      "v_mfma_f32_32x32x16_bf16 v[112:127], v[244:247], v[164:167], v[112:127]\n\t"
      "ds_read_b128 v[60:63], v2 offset:64512\n\t"
      "v_cvt_pk_bf16_f32 v38, v108, v109\n\t"
      "v_cvt_pk_bf16_f32 v39, v110, v111\n\t"
      "v_pk_max_i16 v38, v38, 0\n\t"
      "v_pk_max_i16 v39, v39, 0\n\t"
      "s_waitcnt lgkmcnt(4)\n\t"
      "v_mfma_f32_32x32x16_bf16 v[112:127], v[248:251], v[168:171], v[112:127]\n\t"
      "ds_read_b128 v[240:243], v17 offset:3072\n\t"
      "buffer_store_dwordx4 v[36:39], v2, s[28:31], s49 offen offset:1024 sc1\n\t"
      "v_mfma_f32_32x32x16_bf16 v[112:127], v[252:255], v[172:175], v[112:127]\n\t"
      "ds_read_b128 v[244:247], v17 offset:7168\n\t"
      "s_add_u32 s48, s85, 0\n\t"
      "buffer_load_dwordx4 v[136:139], v19, s[28:31], s48 offen sc1\n\t"
      "s_waitcnt lgkmcnt(4)\n\t"
      "v_mfma_f32_32x32x16_bf16 v[112:127], v[48:51], v[176:179], v[112:127]\n\t"
      "ds_read_b128 v[248:251], v17 offset:11264\n\t"
      "buffer_load_dwordx4 v[140:143], v20, s[28:31], s48 offen sc1\n\t"
      "v_mfma_f32_32x32x16_bf16 v[112:127], v[52:55], v[180:183], v[112:127]\n\t"
      "ds_read_b128 v[252:255], v17 offset:15360\n\t"
      "s_waitcnt lgkmcnt(4)\n\t"
      "v_mfma_f32_32x32x16_bf16 v[112:127], v[56:59], v[184:187], v[112:127]\n\t"
      "ds_read_b128 v[48:51], v17 offset:19456\n\t"
      "s_add_u32 s48, s86, 0\n\t"
      "buffer_load_dwordx4 v[144:147], v19, s[28:31], s48 offen sc1\n\t"
      "v_mfma_f32_32x32x16_bf16 v[112:127], v[60:63], v[188:191], v[112:127]\n\t"
      "ds_read_b128 v[52:55], v17 offset:23552\n\t"
      "s_waitcnt lgkmcnt(4)\n\t"
      "v_mfma_f32_32x32x16_bf16 v[112:127], v[240:243], v[192:195], v[112:127]\n\t"
      "ds_read_b128 v[56:59], v17 offset:27648\n\t"
      "buffer_load_dwordx4 v[148:151], v20, s[28:31], s48 offen sc1\n\t"
      "v_mfma_f32_32x32x16_bf16 v[112:127], v[244:247], v[196:199], v[112:127]\n\t"
      "ds_read_b128 v[60:63], v17 offset:31744\n\t"
      "s_waitcnt lgkmcnt(4)\n\t"
      "v_mfma_f32_32x32x16_bf16 v[112:127], v[248:251], v[200:203], v[112:127]\n\t"
      "ds_read_b128 v[240:243], v17 offset:35840\n\t"
      "s_add_u32 s48, s87, 0\n\t"
      "buffer_load_dwordx4 v[152:155], v19, s[28:31], s48 offen sc1\n\t"
      "v_mfma_f32_32x32x16_bf16 v[112:127], v[252:255], v[204:207], v[112:127]\n\t"
      "ds_read_b128 v[244:247], v17 offset:39936\n\t"
      "s_waitcnt lgkmcnt(4)\n\t"
      "v_mfma_f32_32x32x16_bf16 v[112:127], v[48:51], v[208:211], v[112:127]\n\t"
      "ds_read_b128 v[248:251], v17 offset:44032\n\t"
      "buffer_load_dwordx4 v[156:159], v20, s[28:31], s48 offen sc1\n\t"
      "v_mfma_f32_32x32x16_bf16 v[112:127], v[52:55], v[212:215], v[112:127]\n\t"
      "ds_read_b128 v[252:255], v17 offset:48128\n\t"
      "s_waitcnt lgkmcnt(4)\n\t"
      "v_mfma_f32_32x32x16_bf16 v[112:127], v[56:59], v[216:219], v[112:127]\n\t"
      "s_add_u32 s48, s88, 0\n\t"
      "buffer_load_dwordx4 v[160:163], v19, s[28:31], s48 offen sc1\n\t"
      "v_mfma_f32_32x32x16_bf16 v[112:127], v[60:63], v[220:223], v[112:127]\n\t"
      "s_waitcnt lgkmcnt(2)\n\t"
      "v_mfma_f32_32x32x16_bf16 v[112:127], v[240:243], v[224:227], v[112:127]\n\t"
      "buffer_load_dwordx4 v[164:167], v20, s[28:31], s48 offen sc1\n\t"
      "v_mfma_f32_32x32x16_bf16 v[112:127], v[244:247], v[228:231], v[112:127]\n\t"
      "s_waitcnt lgkmcnt(0)\n\t"
      "v_mfma_f32_32x32x16_bf16 v[112:127], v[248:251], v[232:235], v[112:127]\n\t"
      "s_add_u32 s48, s89, 0\n\t"
      "buffer_load_dwordx4 v[168:171], v19, s[28:31], s48 offen sc1\n\t"
      "v_mfma_f32_32x32x16_bf16 v[112:127], v[252:255], v[236:239], v[112:127]\n\t"
      "buffer_load_dwordx4 v[172:175], v20, s[28:31], s48 offen sc1\n\t"
      "s_add_u32 s48, s90, 0\n\t"
      "buffer_load_dwordx4 v[176:179], v19, s[28:31], s48 offen sc1\n\t"
      "buffer_load_dwordx4 v[180:183], v20, s[28:31], s48 offen sc1\n\t"
      "s_nop 7\n\t"
      "v_cvt_pk_bf16_f32 v32, v112, v113\n\t"
      "v_cvt_pk_bf16_f32 v33, v114, v115\n\t"
      "v_pk_max_i16 v32, v32, 0\n\t"
      "v_pk_max_i16 v33, v33, 0\n\t"
      "v_cvt_pk_bf16_f32 v34, v116, v117\n\t"
      "v_cvt_pk_bf16_f32 v35, v118, v119\n\t"
      "v_pk_max_i16 v34, v34, 0\n\t"
      "v_pk_max_i16 v35, v35, 0\n\t"
      "s_add_u32 s49, s82, 86016\n\t"
      "buffer_store_dwordx4 v[32:35], v2, s[28:31], s49 offen offset:0 sc1\n\t"
      "v_cvt_pk_bf16_f32 v36, v120, v121\n\t"
      "v_cvt_pk_bf16_f32 v37, v122, v123\n\t"
      "v_pk_max_i16 v36, v36, 0\n\t"
      "v_pk_max_i16 v37, v37, 0\n\t"
      "v_cvt_pk_bf16_f32 v38, v124, v125\n\t"
      "v_cvt_pk_bf16_f32 v39, v126, v127\n\t"
      "v_pk_max_i16 v38, v38, 0\n\t"
      "v_pk_max_i16 v39, v39, 0\n\t"
      "buffer_store_dwordx4 v[36:39], v2, s[28:31], s49 offen offset:1024 sc1\n\t"
      "s_waitcnt vmcnt(8)\n\t"
      "v_max3_u32 v28, v128, v129, v130\n\t"
      "v_max3_u32 v28, v28, v131, v132\n\t"
      "v_max3_u32 v28, v28, v133, v134\n\t"
      "v_max3_u32 v28, v28, v135, v136\n\t"
      "v_max3_u32 v28, v28, v137, v138\n\t"
      "v_max3_u32 v28, v28, v139, v140\n\t"
      "v_max3_u32 v28, v28, v141, v142\n\t"
      "v_max3_u32 v28, v28, v143, v144\n\t"
      "v_max3_u32 v28, v28, v145, v146\n\t"
      "v_max3_u32 v28, v28, v147, v148\n\t"
      "v_max3_u32 v28, v28, v149, v150\n\t"
      "v_max3_u32 v28, v28, v151, v152\n\t"
      "v_max3_u32 v28, v28, v153, v154\n\t"
      "v_max3_u32 v28, v28, v155, v156\n\t"
      "v_max3_u32 v28, v28, v157, v158\n\t"
      "v_max_u32 v28, v28, v159\n\t"
      "v_cmp_eq_u32 vcc, -1, v28\n\t"
      "s_nop 0\n\t"
      "s_cbranch_vccnz sh0_%=\n\t"
      "rh0_%=:\n\t"
      "v_mfma_f32_32x32x16_bf16 v[64:79], a[64:67], v[128:131], 0\n\t"
      "s_add_u32 s48, s84, 28672\n\t"
      "buffer_load_dwordx4 v[192:195], v19, s[28:31], s48 offen sc1\n\t"
      "v_mfma_f32_32x32x16_bf16 v[80:95], a[68:71], v[128:131], 0\n\t"
      "v_mfma_f32_32x32x16_bf16 v[96:111], a[64:67], v[132:135], 0\n\t"
      "buffer_load_dwordx4 v[196:199], v20, s[28:31], s48 offen sc1\n\t"
      "v_mfma_f32_32x32x16_bf16 v[112:127], a[68:71], v[132:135], 0\n\t"
      "v_mfma_f32_32x32x16_bf16 v[64:79], a[72:75], v[136:139], v[64:79]\n\t"
      "s_add_u32 s48, s85, 28672\n\t"
      "buffer_load_dwordx4 v[200:203], v19, s[28:31], s48 offen sc1\n\t"
      "v_mfma_f32_32x32x16_bf16 v[80:95], a[76:79], v[136:139], v[80:95]\n\t"
      "v_mfma_f32_32x32x16_bf16 v[96:111], a[72:75], v[140:143], v[96:111]\n\t"
      "buffer_load_dwordx4 v[204:207], v20, s[28:31], s48 offen sc1\n\t"
      "v_mfma_f32_32x32x16_bf16 v[112:127], a[76:79], v[140:143], v[112:127]\n\t"
      "v_mfma_f32_32x32x16_bf16 v[64:79], a[80:83], v[144:147], v[64:79]\n\t"
      "s_add_u32 s48, s86, 28672\n\t"
      "buffer_load_dwordx4 v[208:211], v19, s[28:31], s48 offen sc1\n\t"
      "v_mfma_f32_32x32x16_bf16 v[80:95], a[84:87], v[144:147], v[80:95]\n\t"
      "v_mfma_f32_32x32x16_bf16 v[96:111], a[80:83], v[148:151], v[96:111]\n\t"
      "buffer_load_dwordx4 v[212:215], v20, s[28:31], s48 offen sc1\n\t"
      "v_mfma_f32_32x32x16_bf16 v[112:127], a[84:87], v[148:151], v[112:127]\n\t"
      "s_waitcnt vmcnt(8)\n\t"
      "v_max3_u32 v28, v160, v161, v162\n\t"
      "v_max3_u32 v28, v28, v163, v164\n\t"
      "v_max3_u32 v28, v28, v165, v166\n\t"
      "v_mfma_f32_32x32x16_bf16 v[64:79], a[88:91], v[152:155], v[64:79]\n\t"
      "s_add_u32 s48, s87, 28672\n\t"
      "buffer_load_dwordx4 v[216:219], v19, s[28:31], s48 offen sc1\n\t"
      "v_max3_u32 v28, v28, v167, v168\n\t"
      "v_max3_u32 v28, v28, v169, v170\n\t"
      "v_max3_u32 v28, v28, v171, v172\n\t"
      "v_mfma_f32_32x32x16_bf16 v[80:95], a[92:95], v[152:155], v[80:95]\n\t"
      "v_max3_u32 v28, v28, v173, v174\n\t"
      "v_max3_u32 v28, v28, v175, v176\n\t"
      "v_max3_u32 v28, v28, v177, v178\n\t"
      "v_mfma_f32_32x32x16_bf16 v[96:111], a[88:91], v[156:159], v[96:111]\n\t"
      "buffer_load_dwordx4 v[220:223], v20, s[28:31], s48 offen sc1\n\t"
      "v_max3_u32 v28, v28, v179, v180\n\t"
      "v_max3_u32 v28, v28, v181, v182\n\t"
      "v_max_u32 v28, v28, v183\n\t"
      "v_mfma_f32_32x32x16_bf16 v[112:127], a[92:95], v[156:159], v[112:127]\n\t"
      "v_cmp_eq_u32 vcc, -1, v28\n\t"
      "s_nop 0\n\t"
      "s_cbranch_vccnz sh1_%=\n\t"
      "rh1_%=:\n\t"
      "v_mfma_f32_32x32x16_bf16 v[64:79], a[96:99], v[160:163], v[64:79]\n\t"
      "s_add_u32 s48, s88, 28672\n\t"
      "buffer_load_dwordx4 v[224:227], v19, s[28:31], s48 offen sc1\n\t"
      "v_mfma_f32_32x32x16_bf16 v[80:95], a[100:103], v[160:163], v[80:95]\n\t"
      "v_mfma_f32_32x32x16_bf16 v[96:111], a[96:99], v[164:167], v[96:111]\n\t"
      "buffer_load_dwordx4 v[228:231], v20, s[28:31], s48 offen sc1\n\t"
      "v_mfma_f32_32x32x16_bf16 v[112:127], a[100:103], v[164:167], v[112:127]\n\t"
      "v_mfma_f32_32x32x16_bf16 v[64:79], a[104:107], v[168:171], v[64:79]\n\t"
      "s_add_u32 s48, s89, 28672\n\t"
      "buffer_load_dwordx4 v[232:235], v19, s[28:31], s48 offen sc1\n\t"
      "v_mfma_f32_32x32x16_bf16 v[80:95], a[108:111], v[168:171], v[80:95]\n\t"
      "v_mfma_f32_32x32x16_bf16 v[96:111], a[104:107], v[172:175], v[96:111]\n\t"
      "buffer_load_dwordx4 v[236:239], v20, s[28:31], s48 offen sc1\n\t"
      "v_mfma_f32_32x32x16_bf16 v[112:127], a[108:111], v[172:175], v[112:127]\n\t"
      "s_waitcnt vmcnt(4)\n\t"
      "v_max3_u32 v28, v192, v193, v194\n\t"
      "v_max3_u32 v28, v28, v195, v196\n\t"
      "v_max3_u32 v28, v28, v197, v198\n\t"
      "v_mfma_f32_32x32x16_bf16 v[64:79], a[112:115], v[176:179], v[64:79]\n\t"
      "s_add_u32 s48, s90, 28672\n\t"
      "buffer_load_dwordx4 v[240:243], v19, s[28:31], s48 offen sc1\n\t"
      "v_max3_u32 v28, v28, v199, v200\n\t"
      "v_max3_u32 v28, v28, v201, v202\n\t"
      "v_max3_u32 v28, v28, v203, v204\n\t"
      "v_mfma_f32_32x32x16_bf16 v[80:95], a[116:119], v[176:179], v[80:95]\n\t"
      "v_max3_u32 v28, v28, v205, v206\n\t"
      "v_max3_u32 v28, v28, v207, v208\n\t"
      "v_max3_u32 v28, v28, v209, v210\n\t"
      "v_mfma_f32_32x32x16_bf16 v[96:111], a[112:115], v[180:183], v[96:111]\n\t"
      "buffer_load_dwordx4 v[244:247], v20, s[28:31], s48 offen sc1\n\t"
      "v_max3_u32 v28, v28, v211, v212\n\t"
      "v_max3_u32 v28, v28, v213, v214\n\t"
      "v_max3_u32 v28, v28, v215, v216\n\t"
      "v_mfma_f32_32x32x16_bf16 v[112:127], a[116:119], v[180:183], v[112:127]\n\t"
      "v_max3_u32 v28, v28, v217, v218\n\t"
      "v_max3_u32 v28, v28, v219, v220\n\t"
      "v_max3_u32 v28, v28, v221, v222\n\t"
      "v_max_u32 v28, v28, v223\n\t"
      "v_cmp_eq_u32 vcc, -1, v28\n\t"
      "s_nop 0\n\t"
      "s_cbranch_vccnz sh2_%=\n\t"
      "rh2_%=:\n\t"
      "v_mfma_f32_32x32x16_bf16 v[64:79], a[120:123], v[192:195], v[64:79]\n\t"
      "s_add_u32 s48, s84, 57344\n\t"
      "buffer_load_dwordx4 v[128:131], v19, s[28:31], s48 offen sc1\n\t"
      "v_mfma_f32_32x32x16_bf16 v[80:95], a[124:127], v[192:195], v[80:95]\n\t"
      "v_mfma_f32_32x32x16_bf16 v[96:111], a[120:123], v[196:199], v[96:111]\n\t"
      "buffer_load_dwordx4 v[132:135], v20, s[28:31], s48 offen sc1\n\t"
      "v_mfma_f32_32x32x16_bf16 v[112:127], a[124:127], v[196:199], v[112:127]\n\t"
      "v_mfma_f32_32x32x16_bf16 v[64:79], a[128:131], v[200:203], v[64:79]\n\t"
      "s_add_u32 s48, s85, 57344\n\t"
      "buffer_load_dwordx4 v[136:139], v19, s[28:31], s48 offen sc1\n\t"
      "v_mfma_f32_32x32x16_bf16 v[80:95], a[132:135], v[200:203], v[80:95]\n\t"
      "v_mfma_f32_32x32x16_bf16 v[96:111], a[128:131], v[204:207], v[96:111]\n\t"
      "buffer_load_dwordx4 v[140:143], v20, s[28:31], s48 offen sc1\n\t"
      "v_mfma_f32_32x32x16_bf16 v[112:127], a[132:135], v[204:207], v[112:127]\n\t"
      "v_mfma_f32_32x32x16_bf16 v[64:79], a[136:139], v[208:211], v[64:79]\n\t"
      "s_add_u32 s48, s86, 57344\n\t"
      "buffer_load_dwordx4 v[144:147], v19, s[28:31], s48 offen sc1\n\t"
      "v_mfma_f32_32x32x16_bf16 v[80:95], a[140:143], v[208:211], v[80:95]\n\t"
      "v_mfma_f32_32x32x16_bf16 v[96:111], a[136:139], v[212:215], v[96:111]\n\t"
      "buffer_load_dwordx4 v[148:151], v20, s[28:31], s48 offen sc1\n\t"
      "v_mfma_f32_32x32x16_bf16 v[112:127], a[140:143], v[212:215], v[112:127]\n\t"
      "s_waitcnt vmcnt(6)\n\t"
      "v_max3_u32 v28, v224, v225, v226\n\t"
      "v_max3_u32 v28, v28, v227, v228\n\t"
      "v_max3_u32 v28, v28, v229, v230\n\t"
      "v_mfma_f32_32x32x16_bf16 v[64:79], a[144:147], v[216:219], v[64:79]\n\t"
      "s_add_u32 s48, s87, 57344\n\t"
      "buffer_load_dwordx4 v[152:155], v19, s[28:31], s48 offen sc1\n\t"
      "v_max3_u32 v28, v28, v231, v232\n\t"
      "v_max3_u32 v28, v28, v233, v234\n\t"
      "v_max3_u32 v28, v28, v235, v236\n\t"
      "v_mfma_f32_32x32x16_bf16 v[80:95], a[148:151], v[216:219], v[80:95]\n\t"
      "v_max3_u32 v28, v28, v237, v238\n\t"
      "v_max3_u32 v28, v28, v239, v240\n\t"
      "v_max3_u32 v28, v28, v241, v242\n\t"
      "v_mfma_f32_32x32x16_bf16 v[96:111], a[144:147], v[220:223], v[96:111]\n\t"
      "buffer_load_dwordx4 v[156:159], v20, s[28:31], s48 offen sc1\n\t"
      "v_max3_u32 v28, v28, v243, v244\n\t"
      "v_max3_u32 v28, v28, v245, v246\n\t"
      "v_max_u32 v28, v28, v247\n\t"
      "v_mfma_f32_32x32x16_bf16 v[112:127], a[148:151], v[220:223], v[112:127]\n\t"
      "v_cmp_eq_u32 vcc, -1, v28\n\t"
      "s_nop 0\n\t"
      "s_cbranch_vccnz sh3_%=\n\t"
      "rh3_%=:\n\t"
      "v_mfma_f32_32x32x16_bf16 v[64:79], a[152:155], v[224:227], v[64:79]\n\t"
      "s_add_u32 s48, s88, 57344\n\t"
      "buffer_load_dwordx4 v[160:163], v19, s[28:31], s48 offen sc1\n\t"
      "v_mfma_f32_32x32x16_bf16 v[80:95], a[156:159], v[224:227], v[80:95]\n\t"
      "v_mfma_f32_32x32x16_bf16 v[96:111], a[152:155], v[228:231], v[96:111]\n\t"
      "buffer_load_dwordx4 v[164:167], v20, s[28:31], s48 offen sc1\n\t"
      "v_mfma_f32_32x32x16_bf16 v[112:127], a[156:159], v[228:231], v[112:127]\n\t"
      "v_mfma_f32_32x32x16_bf16 v[64:79], a[160:163], v[232:235], v[64:79]\n\t"
      "s_add_u32 s48, s89, 57344\n\t"
      "buffer_load_dwordx4 v[168:171], v19, s[28:31], s48 offen sc1\n\t"
      "v_mfma_f32_32x32x16_bf16 v[80:95], a[164:167], v[232:235], v[80:95]\n\t"
      "v_mfma_f32_32x32x16_bf16 v[96:111], a[160:163], v[236:239], v[96:111]\n\t"
      "buffer_load_dwordx4 v[172:175], v20, s[28:31], s48 offen sc1\n\t"
      "v_mfma_f32_32x32x16_bf16 v[112:127], a[164:167], v[236:239], v[112:127]\n\t"
      "s_waitcnt vmcnt(4)\n\t"
      "v_max3_u32 v28, v128, v129, v130\n\t"
      "v_max3_u32 v28, v28, v131, v132\n\t"
      "v_max3_u32 v28, v28, v133, v134\n\t"
      "v_mfma_f32_32x32x16_bf16 v[64:79], a[168:171], v[240:243], v[64:79]\n\t"
      "s_add_u32 s48, s90, 57344\n\t"
      "buffer_load_dwordx4 v[176:179], v19, s[28:31], s48 offen sc1\n\t"
      "v_max3_u32 v28, v28, v135, v136\n\t"
      "v_max3_u32 v28, v28, v137, v138\n\t"
      "v_max3_u32 v28, v28, v139, v140\n\t"
      "v_mfma_f32_32x32x16_bf16 v[80:95], a[172:175], v[240:243], v[80:95]\n\t"
      "v_max3_u32 v28, v28, v141, v142\n\t"
      "v_max3_u32 v28, v28, v143, v144\n\t"
      "v_max3_u32 v28, v28, v145, v146\n\t"
      "v_mfma_f32_32x32x16_bf16 v[96:111], a[168:171], v[244:247], v[96:111]\n\t"
      "buffer_load_dwordx4 v[180:183], v20, s[28:31], s48 offen sc1\n\t"
      "v_max3_u32 v28, v28, v147, v148\n\t"
      "v_max3_u32 v28, v28, v149, v150\n\t"
      "v_max3_u32 v28, v28, v151, v152\n\t"
      "v_mfma_f32_32x32x16_bf16 v[112:127], a[172:175], v[244:247], v[112:127]\n\t"
      "v_max3_u32 v28, v28, v153, v154\n\t"
      "v_max3_u32 v28, v28, v155, v156\n\t"
      "v_max3_u32 v28, v28, v157, v158\n\t"
      "v_max_u32 v28, v28, v159\n\t"
      "v_cmp_eq_u32 vcc, -1, v28\n\t"
      "s_nop 0\n\t"
      "s_cbranch_vccnz sh4_%=\n\t"
      "rh4_%=:\n\t"
      "v_mfma_f32_32x32x16_bf16 v[64:79], a[176:179], v[128:131], v[64:79]\n\t"
      "s_add_u32 s48, s84, 86016\n\t"
      "buffer_load_dwordx4 v[192:195], v19, s[28:31], s48 offen sc1\n\t"
      "v_mfma_f32_32x32x16_bf16 v[80:95], a[180:183], v[128:131], v[80:95]\n\t"
      "v_mfma_f32_32x32x16_bf16 v[96:111], a[176:179], v[132:135], v[96:111]\n\t"
      "buffer_load_dwordx4 v[196:199], v20, s[28:31], s48 offen sc1\n\t"
      "v_mfma_f32_32x32x16_bf16 v[112:127], a[180:183], v[132:135], v[112:127]\n\t"
      "v_mfma_f32_32x32x16_bf16 v[64:79], a[184:187], v[136:139], v[64:79]\n\t"
      "s_add_u32 s48, s85, 86016\n\t"
      "buffer_load_dwordx4 v[200:203], v19, s[28:31], s48 offen sc1\n\t"
      "v_mfma_f32_32x32x16_bf16 v[80:95], a[188:191], v[136:139], v[80:95]\n\t"
      "v_mfma_f32_32x32x16_bf16 v[96:111], a[184:187], v[140:143], v[96:111]\n\t"
      "buffer_load_dwordx4 v[204:207], v20, s[28:31], s48 offen sc1\n\t"
      "v_mfma_f32_32x32x16_bf16 v[112:127], a[188:191], v[140:143], v[112:127]\n\t"
      "v_mfma_f32_32x32x16_bf16 v[64:79], a[192:195], v[144:147], v[64:79]\n\t"
      "s_add_u32 s48, s86, 86016\n\t"
      "buffer_load_dwordx4 v[208:211], v19, s[28:31], s48 offen sc1\n\t"
      "v_mfma_f32_32x32x16_bf16 v[80:95], a[196:199], v[144:147], v[80:95]\n\t"
      "v_mfma_f32_32x32x16_bf16 v[96:111], a[192:195], v[148:151], v[96:111]\n\t"
      "buffer_load_dwordx4 v[212:215], v20, s[28:31], s48 offen sc1\n\t"
      "v_mfma_f32_32x32x16_bf16 v[112:127], a[196:199], v[148:151], v[112:127]\n\t"
      "s_waitcnt vmcnt(6)\n\t"
      "v_max3_u32 v28, v160, v161, v162\n\t"
      "v_max3_u32 v28, v28, v163, v164\n\t"
      "v_max3_u32 v28, v28, v165, v166\n\t"
      "v_mfma_f32_32x32x16_bf16 v[64:79], a[200:203], v[152:155], v[64:79]\n\t"
      "s_add_u32 s48, s87, 86016\n\t"
      "buffer_load_dwordx4 v[216:219], v19, s[28:31], s48 offen sc1\n\t"
      "v_max3_u32 v28, v28, v167, v168\n\t"
      "v_max3_u32 v28, v28, v169, v170\n\t"
      "v_max3_u32 v28, v28, v171, v172\n\t"
      "v_mfma_f32_32x32x16_bf16 v[80:95], a[204:207], v[152:155], v[80:95]\n\t"
      "v_max3_u32 v28, v28, v173, v174\n\t"
      "v_max3_u32 v28, v28, v175, v176\n\t"
      "v_max3_u32 v28, v28, v177, v178\n\t"
      "v_mfma_f32_32x32x16_bf16 v[96:111], a[200:203], v[156:159], v[96:111]\n\t"
      "buffer_load_dwordx4 v[220:223], v20, s[28:31], s48 offen sc1\n\t"
      "v_max3_u32 v28, v28, v179, v180\n\t"
      "v_max3_u32 v28, v28, v181, v182\n\t"
      "v_max_u32 v28, v28, v183\n\t"
      "v_mfma_f32_32x32x16_bf16 v[112:127], a[204:207], v[156:159], v[112:127]\n\t"
      "v_cmp_eq_u32 vcc, -1, v28\n\t"
      "s_nop 0\n\t"
      "s_cbranch_vccnz sh5_%=\n\t"
      "rh5_%=:\n\t"
      "v_mfma_f32_32x32x16_bf16 v[64:79], a[208:211], v[160:163], v[64:79]\n\t"
      "s_add_u32 s48, s88, 86016\n\t"
      "buffer_load_dwordx4 v[224:227], v19, s[28:31], s48 offen sc1\n\t"
      "v_mfma_f32_32x32x16_bf16 v[80:95], a[212:215], v[160:163], v[80:95]\n\t"
      "s_barrier\n\t"
      "v_mfma_f32_32x32x16_bf16 v[96:111], a[208:211], v[164:167], v[96:111]\n\t"
      "buffer_load_dwordx4 v[228:231], v20, s[28:31], s48 offen sc1\n\t"
      "v_mfma_f32_32x32x16_bf16 v[112:127], a[212:215], v[164:167], v[112:127]\n\t"
      "v_mfma_f32_32x32x16_bf16 v[64:79], a[216:219], v[168:171], v[64:79]\n\t"
      "s_add_u32 s48, s89, 86016\n\t"
      "buffer_load_dwordx4 v[232:235], v19, s[28:31], s48 offen sc1\n\t"
      "v_mfma_f32_32x32x16_bf16 v[80:95], a[220:223], v[168:171], v[80:95]\n\t"
      "v_mfma_f32_32x32x16_bf16 v[96:111], a[216:219], v[172:175], v[96:111]\n\t"
      "buffer_load_dwordx4 v[236:239], v20, s[28:31], s48 offen sc1\n\t"
      "v_mfma_f32_32x32x16_bf16 v[112:127], a[220:223], v[172:175], v[112:127]\n\t"
      "s_waitcnt vmcnt(4)\n\t"
      "v_max3_u32 v28, v192, v193, v194\n\t"
      "v_max3_u32 v28, v28, v195, v196\n\t"
      "v_max3_u32 v28, v28, v197, v198\n\t"
      "v_mfma_f32_32x32x16_bf16 v[64:79], a[224:227], v[176:179], v[64:79]\n\t"
      "s_add_u32 s48, s90, 86016\n\t"
      "buffer_load_dwordx4 v[240:243], v19, s[28:31], s48 offen sc1\n\t"
      "v_max3_u32 v28, v28, v199, v200\n\t"
      "v_max3_u32 v28, v28, v201, v202\n\t"
      "v_max3_u32 v28, v28, v203, v204\n\t"
      "v_mfma_f32_32x32x16_bf16 v[80:95], a[228:231], v[176:179], v[80:95]\n\t"
      "v_max3_u32 v28, v28, v205, v206\n\t"
      "v_max3_u32 v28, v28, v207, v208\n\t"
      "v_max3_u32 v28, v28, v209, v210\n\t"
      "v_mfma_f32_32x32x16_bf16 v[96:111], a[224:227], v[180:183], v[96:111]\n\t"
      "buffer_load_dwordx4 v[244:247], v20, s[28:31], s48 offen sc1\n\t"
      "v_max3_u32 v28, v28, v211, v212\n\t"
      "v_max3_u32 v28, v28, v213, v214\n\t"
      "v_max3_u32 v28, v28, v215, v216\n\t"
      "v_mfma_f32_32x32x16_bf16 v[112:127], a[228:231], v[180:183], v[112:127]\n\t"
      "v_max3_u32 v28, v28, v217, v218\n\t"
      "v_max3_u32 v28, v28, v219, v220\n\t"
      "v_max3_u32 v28, v28, v221, v222\n\t"
      "v_max_u32 v28, v28, v223\n\t"
      "v_cmp_eq_u32 vcc, -1, v28\n\t"
      "s_nop 0\n\t"
      "s_cbranch_vccnz sh6_%=\n\t"
      "rh6_%=:\n\t"
      "v_mfma_f32_32x32x16_bf16 v[64:79], a[232:235], v[192:195], v[64:79]\n\t"
      "v_mfma_f32_32x32x16_bf16 v[80:95], a[236:239], v[192:195], v[80:95]\n\t"
      "v_mfma_f32_32x32x16_bf16 v[96:111], a[232:235], v[196:199], v[96:111]\n\t"
      "v_mfma_f32_32x32x16_bf16 v[112:127], a[236:239], v[196:199], v[112:127]\n\t"
      "v_mfma_f32_32x32x16_bf16 v[64:79], a[240:243], v[200:203], v[64:79]\n\t"
      "v_add_u32 v44, s74, v2\n\t"
      "v_add_u32 v45, s75, v2\n\t"
      "v_add_u32 v46, s76, v2\n\t"
      "v_add_u32 v47, s77, v2\n\t"
      "v_mfma_f32_32x32x16_bf16 v[80:95], a[244:247], v[200:203], v[80:95]\n\t"
      "v_mfma_f32_32x32x16_bf16 v[96:111], a[240:243], v[204:207], v[96:111]\n\t"
      "v_mfma_f32_32x32x16_bf16 v[112:127], a[244:247], v[204:207], v[112:127]\n\t"
      "v_mfma_f32_32x32x16_bf16 v[64:79], a[248:251], v[208:211], v[64:79]\n\t"
      "v_mfma_f32_32x32x16_bf16 v[80:95], a[252:255], v[208:211], v[80:95]\n\t"
      "v_mfma_f32_32x32x16_bf16 v[96:111], a[248:251], v[212:215], v[96:111]\n\t"
      "v_mfma_f32_32x32x16_bf16 v[112:127], a[252:255], v[212:215], v[112:127]\n\t"
      "s_waitcnt vmcnt(0)\n\t"
      "v_max3_u32 v28, v224, v225, v226\n\t"
      "v_max3_u32 v28, v28, v227, v228\n\t"
      "v_max3_u32 v28, v28, v229, v230\n\t"
      "v_mfma_f32_32x32x16_bf16 v[64:79], a[0:3], v[216:219], v[64:79]\n\t"
      "v_max3_u32 v28, v28, v231, v232\n\t"
      "v_max3_u32 v28, v28, v233, v234\n\t"
      "v_max3_u32 v28, v28, v235, v236\n\t"
      "v_mfma_f32_32x32x16_bf16 v[80:95], a[4:7], v[216:219], v[80:95]\n\t"
      "v_max3_u32 v28, v28, v237, v238\n\t"
      "v_max3_u32 v28, v28, v239, v240\n\t"
      "v_max3_u32 v28, v28, v241, v242\n\t"
      "v_mfma_f32_32x32x16_bf16 v[96:111], a[0:3], v[220:223], v[96:111]\n\t"
      "v_max3_u32 v28, v28, v243, v244\n\t"
      "v_max3_u32 v28, v28, v245, v246\n\t"
      "v_max_u32 v28, v28, v247\n\t"
      "v_mfma_f32_32x32x16_bf16 v[112:127], a[4:7], v[220:223], v[112:127]\n\t"
      "v_cmp_eq_u32 vcc, -1, v28\n\t"
      "s_nop 0\n\t"
      "s_cbranch_vccnz sh7_%=\n\t"
      "rh7_%=:\n\t"
      "v_mfma_f32_32x32x16_bf16 v[112:127], a[12:15], v[228:231], v[112:127]\n\t"
      "buffer_load_dwordx4 v[36:39], v19, s[28:31], s83 offen sc1\n\t"
      "v_mfma_f32_32x32x16_bf16 v[112:127], a[20:23], v[236:239], v[112:127]\n\t"
      "buffer_load_dwordx4 v[40:43], v19, s[28:31], s83 offen offset:1024 sc1\n\t"
      "v_mfma_f32_32x32x16_bf16 v[112:127], a[28:31], v[244:247], v[112:127]\n\t"
      "buffer_load_dwordx4 v[32:35], v21, s[28:31], 0 offen sc1\n\t"
      "v_mfma_f32_32x32x16_bf16 v[96:111], a[8:11], v[228:231], v[96:111]\n\t"
      "v_mfma_f32_32x32x16_bf16 v[96:111], a[16:19], v[236:239], v[96:111]\n\t"
      "v_mfma_f32_32x32x16_bf16 v[96:111], a[24:27], v[244:247], v[96:111]\n\t"
      "s_nop 7\n\t"
      "v_mfma_f32_32x32x16_bf16 v[80:95], a[12:15], v[224:227], v[80:95]\n\t"
      "ds_write_b128 v44, v[112:115] offset:0\n\t"
      "ds_write_b128 v44, v[116:119] offset:1024\n\t"
      "v_mfma_f32_32x32x16_bf16 v[80:95], a[20:23], v[232:235], v[80:95]\n\t"
      "ds_write_b128 v44, v[120:123] offset:2048\n\t"
      "ds_write_b128 v44, v[124:127] offset:3072\n\t"
      "v_mfma_f32_32x32x16_bf16 v[80:95], a[28:31], v[240:243], v[80:95]\n\t"
      "s_nop 5\n\t"
      "v_mfma_f32_32x32x16_bf16 v[64:79], a[8:11], v[224:227], v[64:79]\n\t"
      "ds_write_b128 v45, v[96:99] offset:0\n\t"
      "ds_write_b128 v45, v[100:103] offset:1024\n\t"
      "v_mfma_f32_32x32x16_bf16 v[64:79], a[16:19], v[232:235], v[64:79]\n\t"
      "ds_write_b128 v45, v[104:107] offset:2048\n\t"
      "ds_write_b128 v45, v[108:111] offset:3072\n\t"
      "v_mfma_f32_32x32x16_bf16 v[64:79], a[24:27], v[240:243], v[64:79]\n\t"
      "ds_write_b128 v46, v[80:83] offset:0\n\t"
      "ds_write_b128 v46, v[84:87] offset:1024\n\t"
      "ds_write_b128 v46, v[88:91] offset:2048\n\t"
      "ds_write_b128 v46, v[92:95] offset:3072\n\t"
      "s_waitcnt vmcnt(0)\n\t"
      "v_max3_u32 v28, v32, v33, v34\n\t"
      "v_max3_u32 v28, v28, v35, v36\n\t"
      "v_max3_u32 v28, v28, v37, v38\n\t"
      "v_max3_u32 v28, v28, v39, v40\n\t"
      "v_max3_u32 v28, v28, v41, v42\n\t"
      "v_max_u32 v28, v28, v43\n\t"
      "v_cmp_eq_u32 vcc, -1, v28\n\t"
      "s_nop 0\n\t"
      "s_cbranch_vccnz shv_%=\n\t"
      "rhv_%=:\n\t"
      "v_pk_add_f32 v[32:33], v[32:33], v[34:35]\n\t"
      "v_mul_f32 v32, 0x3b124925, v32\n\t"
      "v_mul_f32 v33, 0x3b124925, v33\n\t"
      "v_fma_f32 v33, -v32, v32, v33\n\t"
      "v_max_f32 v33, 0, v33\n\t"
      "v_add_f32 v33, 0x3727c5ac, v33\n\t"
      "v_rsq_f32 v32, v33\n\t"
      "s_waitcnt lgkmcnt(0)\n\t"
      "s_barrier\n\t"
      "ds_read_b128 v[128:131], v47 offset:0\n\t"
      "ds_read_b128 v[132:135], v47 offset:4096\n\t"
      "ds_read_b128 v[136:139], v47 offset:8192\n\t"
      "ds_read_b128 v[140:143], v47 offset:1024\n\t"
      "ds_read_b128 v[144:147], v47 offset:5120\n\t"
      "ds_read_b128 v[148:151], v47 offset:9216\n\t"
      "ds_read_b128 v[152:155], v47 offset:2048\n\t"
      "ds_read_b128 v[156:159], v47 offset:6144\n\t"
      "ds_read_b128 v[160:163], v47 offset:10240\n\t"
      "ds_read_b128 v[164:167], v47 offset:3072\n\t"
      "ds_read_b128 v[168:171], v47 offset:7168\n\t"
      "ds_read_b128 v[172:175], v47 offset:11264\n\t"
      "v_lshlrev_b32 v176, 16, v36\n\t"
      "v_and_b32 v177, 0xffff0000, v36\n\t"
      "v_lshlrev_b32 v178, 16, v37\n\t"
      "v_and_b32 v179, 0xffff0000, v37\n\t"
      "v_lshlrev_b32 v180, 16, v38\n\t"
      "v_and_b32 v181, 0xffff0000, v38\n\t"
      "v_lshlrev_b32 v182, 16, v39\n\t"
      "v_and_b32 v183, 0xffff0000, v39\n\t"
      "v_lshlrev_b32 v184, 16, v40\n\t"
      "v_and_b32 v185, 0xffff0000, v40\n\t"
      "v_lshlrev_b32 v186, 16, v41\n\t"
      "v_and_b32 v187, 0xffff0000, v41\n\t"
      "v_lshlrev_b32 v188, 16, v42\n\t"
      "v_and_b32 v189, 0xffff0000, v42\n\t"
      "v_lshlrev_b32 v190, 16, v43\n\t"
      "v_and_b32 v191, 0xffff0000, v43\n\t"
      "s_waitcnt lgkmcnt(11)\n\t"
      "v_pk_add_f32 v[64:65], v[64:65], v[128:129]\n\t"
      "v_pk_add_f32 v[66:67], v[66:67], v[130:131]\n\t"
      "s_waitcnt lgkmcnt(10)\n\t"
      "v_pk_add_f32 v[64:65], v[64:65], v[132:133]\n\t"
      "v_pk_add_f32 v[66:67], v[66:67], v[134:135]\n\t"
      "s_waitcnt lgkmcnt(9)\n\t"
      "v_pk_add_f32 v[64:65], v[64:65], v[136:137]\n\t"
      "v_pk_add_f32 v[66:67], v[66:67], v[138:139]\n\t"
      "v_pk_fma_f32 v[64:65], v[64:65], v[32:33], v[176:177] op_sel_hi:[1,0,1]\n\t"
      "v_pk_fma_f32 v[66:67], v[66:67], v[32:33], v[178:179] op_sel_hi:[1,0,1]\n\t"
      "v_cvt_pk_bf16_f32 v36, v64, v65\n\t"
      "v_cvt_pk_bf16_f32 v37, v66, v67\n\t"
      "s_waitcnt lgkmcnt(8)\n\t"
      "v_pk_add_f32 v[68:69], v[68:69], v[140:141]\n\t"
      "v_pk_add_f32 v[70:71], v[70:71], v[142:143]\n\t"
      "s_waitcnt lgkmcnt(7)\n\t"
      "v_pk_add_f32 v[68:69], v[68:69], v[144:145]\n\t"
      "v_pk_add_f32 v[70:71], v[70:71], v[146:147]\n\t"
      "s_waitcnt lgkmcnt(6)\n\t"
      "v_pk_add_f32 v[68:69], v[68:69], v[148:149]\n\t"
      "v_pk_add_f32 v[70:71], v[70:71], v[150:151]\n\t"
      "v_pk_fma_f32 v[68:69], v[68:69], v[32:33], v[180:181] op_sel_hi:[1,0,1]\n\t"
      "v_pk_fma_f32 v[70:71], v[70:71], v[32:33], v[182:183] op_sel_hi:[1,0,1]\n\t"
      "v_cvt_pk_bf16_f32 v38, v68, v69\n\t"
      "v_cvt_pk_bf16_f32 v39, v70, v71\n\t"
      "s_nop 1\n\t"
      "v_permlane32_swap_b32 v36, v38\n\t"
      "v_permlane32_swap_b32 v37, v39\n\t"
      "buffer_store_dwordx4 v[36:39], v22, s[40:43], 0 offen offset:0\n\t"
      "s_waitcnt lgkmcnt(5)\n\t"
      "v_pk_add_f32 v[72:73], v[72:73], v[152:153]\n\t"
      "v_pk_add_f32 v[74:75], v[74:75], v[154:155]\n\t"
      "s_waitcnt lgkmcnt(4)\n\t"
      "v_pk_add_f32 v[72:73], v[72:73], v[156:157]\n\t"
      "v_pk_add_f32 v[74:75], v[74:75], v[158:159]\n\t"
      "s_waitcnt lgkmcnt(3)\n\t"
      "v_pk_add_f32 v[72:73], v[72:73], v[160:161]\n\t"
      "v_pk_add_f32 v[74:75], v[74:75], v[162:163]\n\t"
      "v_pk_fma_f32 v[72:73], v[72:73], v[32:33], v[184:185] op_sel_hi:[1,0,1]\n\t"
      "v_pk_fma_f32 v[74:75], v[74:75], v[32:33], v[186:187] op_sel_hi:[1,0,1]\n\t"
      "v_cvt_pk_bf16_f32 v40, v72, v73\n\t"
      "v_cvt_pk_bf16_f32 v41, v74, v75\n\t"
      "s_waitcnt lgkmcnt(2)\n\t"
      "v_pk_add_f32 v[76:77], v[76:77], v[164:165]\n\t"
      "v_pk_add_f32 v[78:79], v[78:79], v[166:167]\n\t"
      "s_waitcnt lgkmcnt(1)\n\t"
      "v_pk_add_f32 v[76:77], v[76:77], v[168:169]\n\t"
      "v_pk_add_f32 v[78:79], v[78:79], v[170:171]\n\t"
      "s_waitcnt lgkmcnt(0)\n\t"
      "v_pk_add_f32 v[76:77], v[76:77], v[172:173]\n\t"
      "v_pk_add_f32 v[78:79], v[78:79], v[174:175]\n\t"
      "v_pk_fma_f32 v[76:77], v[76:77], v[32:33], v[188:189] op_sel_hi:[1,0,1]\n\t"
      "v_pk_fma_f32 v[78:79], v[78:79], v[32:33], v[190:191] op_sel_hi:[1,0,1]\n\t"
      "v_cvt_pk_bf16_f32 v42, v76, v77\n\t"
      "v_cvt_pk_bf16_f32 v43, v78, v79\n\t"
      "s_nop 1\n\t"
      "v_permlane32_swap_b32 v40, v42\n\t"
      "v_permlane32_swap_b32 v41, v43\n\t"
      "buffer_store_dwordx4 v[40:43], v22, s[40:43], 0 offen offset:32\n\t"
      "s_endpgm\n\t"
      "vq_%=:\n\t"
      "s_cmp_eq_u32 s46, 9\n\t"
      "s_cbranch_scc1 mq_%=\n\t"
      "s_bfe_u32 s48, s100, 262144\n\t"
      "s_add_u32 m0, s54, 0\n\t"
      "s_mul_i32 s48, s48, 128\n\t"
      "s_add_u32 s48, s48, s51\n\t"
      "buffer_load_dwordx4 v28, s[12:15], s48 offen offset:0 lds\n\t"
      "buffer_load_dwordx4 v29, s[12:15], s48 offen offset:1024 lds\n\t"
      "buffer_load_dwordx4 v40, s[12:15], s48 offen offset:2048 lds\n\t"
      "buffer_load_dwordx4 v41, s[12:15], s48 offen offset:3072 lds\n\t"
      "s_bfe_u32 s48, s100, 262144\n\t"
      "s_add_u32 m0, s55, 65536\n\t"
      "s_mul_i32 s48, s48, 172032\n\t"
      "s_add_u32 s48, s48, s93\n\t"
      "buffer_load_dwordx4 v30, s[16:19], s48 offen offset:0 lds\n\t"
      "buffer_load_dwordx4 v31, s[16:19], s48 offen offset:1024 lds\n\t"
      "buffer_load_dwordx4 v32, s[16:19], s48 offen offset:2048 lds\n\t"
      "s_bfe_u32 s48, s100, 262148\n\t"
      "s_add_u32 m0, s54, 16384\n\t"
      "s_mul_i32 s48, s48, 128\n\t"
      "s_add_u32 s48, s48, s51\n\t"
      "buffer_load_dwordx4 v28, s[12:15], s48 offen offset:0 lds\n\t"
      "v_bfe_u32 v24, v1, 1, 2\n\t"
      "v_bfe_u32 v25, v1, 4, 1\n\t"
      "v_lshl_or_b32 v24, v25, 2, v24\n\t"
      "v_lshrrev_b32 v26, 5, v1\n\t"
      "v_xor_b32 v24, v24, v26\n\t"
      "v_and_b32 v59, 31, v1\n\t"
      "v_lshlrev_b32 v24, 4, v24\n\t"
      "v_lshl_add_u32 v24, v59, 7, v24\n\t"
      "buffer_load_dwordx4 v29, s[12:15], s48 offen offset:1024 lds\n\t"
      "v_add_u32 v33, s54, v24\n\t"
      "v_xor_b32 v34, 32, v33\n\t"
      "v_xor_b32 v35, 64, v33\n\t"
      "v_xor_b32 v36, 0x60, v33\n\t"
      "v_lshlrev_b32 v59, 4, v59\n\t"
      "v_lshlrev_b32 v2, 4, v1\n\t"
      "v_bfe_u32 v24, v1, 2, 2\n\t"
      "v_lshrrev_b32 v25, 5, v1\n\t"
      "buffer_load_dwordx4 v40, s[12:15], s48 offen offset:2048 lds\n\t"
      "v_lshl_add_u32 v24, v25, 3, v24\n\t"
      "v_mul_u32_u24 v37, 192, v24\n\t"
      "v_bfe_u32 v25, v1, 4, 1\n\t"
      "v_lshl_add_u32 v37, v25, 5, v37\n\t"
      "v_bfrev_b32 v25, v1\n\t"
      "v_lshrrev_b32 v25, 30, v25\n\t"
      "v_lshl_add_u32 v37, v25, 3, v37\n\t"
      "v_add_u32 v37, 65536, v37\n\t"
      "buffer_load_dwordx4 v41, s[12:15], s48 offen offset:3072 lds\n\t"
      "v_add_u32 v38, s55, v37\n\t"
      "v_mov_b32 v39, 1065369472\n\t"
      "v_mov_b32 v42, 0\n\t"
      "v_mov_b32 v43, 0\n\t"
      "v_mov_b32 v44, 0\n\t"
      "s_mov_b32 s30, 27262976\n\t"
      "s_mov_b32 s31, 0x20000\n\t"
      "s_mov_b32 s34, 64512\n\t"
      "s_bfe_u32 s48, s100, 262148\n\t"
      "s_add_u32 m0, s55, 77824\n\t"
      "s_mul_i32 s48, s48, 172032\n\t"
      "s_add_u32 s48, s48, s93\n\t"
      "buffer_load_dwordx4 v30, s[16:19], s48 offen offset:0 lds\n\t"
      "s_mov_b32 s35, 0x20000\n\t"
      "s_lshr_b32 s50, %[bid], 3\n\t"
      "s_lshl_b32 s50, s50, 2\n\t"
      "s_add_u32 s50, s50, s44\n\t"
      "s_lshl_b32 s50, s50, 9\n\t"
      "v_mov_b32 v26, 2147483648\n\t"
      "buffer_load_dwordx4 v31, s[16:19], s48 offen offset:1024 lds\n\t"
      "s_mov_b64 exec, 15\n\t"
      "v_lshl_add_u32 v26, v1, 7, s50\n\t"
      "s_mov_b64 exec, -1\n\t"
      "buffer_load_dwordx4 v32, s[16:19], s48 offen offset:2048 lds\n\t"
      "buffer_load_dword a253, v26, s[32:35], 0 offen\n\t"
      "s_mov_b32 s22, 1605632\n\t"
      "s_mov_b32 s23, 0x20000\n\t"
      "s_mov_b32 s26, 401408\n\t"
      "s_mov_b32 s27, 0x20000\n\t"
      "s_mov_b32 s38, 1605632\n\t"
      "s_mov_b32 s39, 0x20000\n\t"
      "s_mul_i32 s52, s44, 57344\n\t"
      "s_lshl_b32 s50, s46, 8\n\t"
      "s_add_u32 s52, s52, s50\n\t"
      "s_mul_i32 s53, s45, 25088\n\t"
      "s_lshl_b32 s56, s44, 10\n\t"
      "v_lshrrev_b32 v24, 4, v1\n\t"
      "v_and_b32 v25, 15, v1\n\t"
      "v_lshlrev_b32 v26, 2, v24\n\t"
      "v_xor_b32 v25, v25, v26\n\t"
      "v_mul_u32_u24 v47, 3584, v24\n\t"
      "s_waitcnt vmcnt(8) lgkmcnt(0)\n\t"
      "s_barrier\n\t"
      "ds_read_b128 v[144:147], v33 offset:0\n\t"
      "ds_read_b64_tr_b16 v[60:61], v37 offset:0\n\t"
      "ds_read_b64_tr_b16 v[62:63], v37 offset:768\n\t"
      "ds_read_b64_tr_b16 v[64:65], v37 offset:64\n\t"
      "ds_read_b64_tr_b16 v[66:67], v37 offset:832\n\t"
      "ds_read_b64_tr_b16 v[68:69], v37 offset:128\n\t"
      "ds_read_b64_tr_b16 v[70:71], v37 offset:896\n\t"
      "ds_read_b128 v[148:151], v34 offset:0\n\t"
      "ds_read_b64_tr_b16 v[72:73], v37 offset:3072\n\t"
      "ds_read_b64_tr_b16 v[74:75], v37 offset:3840\n\t"
      "ds_read_b64_tr_b16 v[76:77], v37 offset:3136\n\t"
      "ds_read_b64_tr_b16 v[78:79], v37 offset:3904\n\t"
      "ds_read_b64_tr_b16 v[80:81], v37 offset:3200\n\t"
      "ds_read_b64_tr_b16 v[82:83], v37 offset:3968\n\t"
      "s_add_u32 s47, s47, 1\n\t"
      "s_bfe_u32 s48, s100, 262152\n\t"
      "s_add_u32 m0, s54, 32768\n\t"
      "s_mul_i32 s48, s48, 128\n\t"
      "s_add_u32 s48, s48, s51\n\t"
      "buffer_load_dwordx4 v28, s[12:15], s48 offen offset:0 lds\n\t"
      "buffer_load_dwordx4 v29, s[12:15], s48 offen offset:1024 lds\n\t"
      "v_lshl_add_u32 v47, v25, 4, v47\n\t"
      "s_lshl_b32 s50, s44, 3\n\t"
      "v_lshrrev_b32 v25, 3, v1\n\t"
      "v_add_u32 v25, s50, v25\n\t"
      "v_min_u32 v25, 27, v25\n\t"
      "v_mul_u32_u24 v25, 896, v25\n\t"
      "s_lshr_b32 s50, s44, 1\n\t"
      "s_lshl_b32 s50, s50, 6\n\t"
      "s_waitcnt lgkmcnt(7)\n\t"
      "v_mfma_f32_32x32x16_bf16 v[96:111], v[144:147], v[60:63], 0\n\t"
      "ds_read_b128 v[152:155], v35 offset:0\n\t"
      "ds_read_b64_tr_b16 v[84:85], v37 offset:6144\n\t"
      "ds_read_b64_tr_b16 v[86:87], v37 offset:6912\n\t"
      "v_and_b32 v26, 0x70, v28\n\t"
      "v_xor_b32 v26, s50, v26\n\t"
      "v_mfma_f32_32x32x16_bf16 v[112:127], v[144:147], v[64:67], 0\n\t"
      "ds_read_b64_tr_b16 v[88:89], v37 offset:6208\n\t"
      "ds_read_b64_tr_b16 v[90:91], v37 offset:6976\n\t"
      "ds_read_b64_tr_b16 v[4:5], v38 offset:0\n\t"
      "ds_read_b64_tr_b16 v[6:7], v38 offset:768\n\t"
      "buffer_load_dwordx4 v40, s[12:15], s48 offen offset:2048 lds\n\t"
      "v_add_u32 v48, v25, v26\n\t"
      "v_mfma_f32_32x32x16_bf16 v[128:143], v[144:147], v[68:71], 0\n\t"
      "ds_read_b64_tr_b16 v[92:93], v37 offset:6272\n\t"
      "ds_read_b64_tr_b16 v[94:95], v37 offset:7040\n\t"
      "buffer_load_dwordx4 v41, s[12:15], s48 offen offset:3072 lds\n\t"
      "v_lshrrev_b32 v27, 5, v1\n\t"
      "s_lshl_b32 s50, s44, 2\n\t"
      "s_waitcnt lgkmcnt(9)\n\t"
      "v_mfma_f32_32x32x16_bf16 v[96:111], v[148:151], v[72:75], v[96:111]\n\t"
      "ds_read_b128 v[156:159], v36 offset:0\n\t"
      "ds_read_b64_tr_b16 v[60:61], v37 offset:9216\n\t"
      "ds_read_b64_tr_b16 v[62:63], v37 offset:9984\n\t"
      "s_add_u32 s50, s50, 157696\n\t"
      "v_add_u32 v45, s50, v2\n\t"
      "v_mfma_f32_32x32x16_bf16 v[112:127], v[148:151], v[76:79], v[112:127]\n\t"
      "ds_read_b64_tr_b16 v[64:65], v37 offset:9280\n\t"
      "ds_read_b64_tr_b16 v[66:67], v37 offset:10048\n\t"
      "ds_read_b64_tr_b16 v[8:9], v38 offset:64\n\t"
      "ds_read_b64_tr_b16 v[10:11], v38 offset:832\n\t"
      "s_bfe_u32 s48, s100, 262152\n\t"
      "s_add_u32 m0, s55, 90112\n\t"
      "s_mul_i32 s48, s48, 172032\n\t"
      "s_add_u32 s48, s48, s93\n\t"
      "buffer_load_dwordx4 v30, s[16:19], s48 offen offset:0 lds\n\t"
      "v_mfma_f32_32x32x16_bf16 v[128:143], v[148:151], v[80:83], v[128:143]\n\t"
      "ds_read_b64_tr_b16 v[68:69], v37 offset:9344\n\t"
      "ds_read_b64_tr_b16 v[70:71], v37 offset:10112\n\t"
      "buffer_load_dwordx4 v31, s[16:19], s48 offen offset:1024 lds\n\t"
      "v_add_u32 v46, 157696, v59\n\t"
      "v_sub_u32 v27, 0, v27\n\t"
      "s_waitcnt vmcnt(7)\n\t"
      "s_barrier\n\t"
      "s_waitcnt lgkmcnt(9)\n\t"
      "v_mfma_f32_32x32x16_bf16 v[96:111], v[152:155], v[84:87], v[96:111]\n\t"
      "ds_read_b128 v[160:163], v33 offset:16384\n\t"
      "ds_read_b64_tr_b16 v[72:73], v37 offset:12288\n\t"
      "ds_read_b64_tr_b16 v[74:75], v37 offset:13056\n\t"
      "v_lshrrev_b32 v24, 2, v1\n\t"
      "v_min_u32 v24, 4, v24\n\t"
      "v_mfma_f32_32x32x16_bf16 v[112:127], v[152:155], v[88:91], v[112:127]\n\t"
      "ds_read_b64_tr_b16 v[76:77], v37 offset:12352\n\t"
      "ds_read_b64_tr_b16 v[78:79], v37 offset:13120\n\t"
      "ds_read_b64_tr_b16 v[12:13], v38 offset:128\n\t"
      "ds_read_b64_tr_b16 v[14:15], v38 offset:896\n\t"
      "buffer_load_dwordx4 v32, s[16:19], s48 offen offset:2048 lds\n\t"
      "v_mul_u32_u24 v24, 4352, v24\n\t"
      "v_mfma_f32_32x32x16_bf16 v[128:143], v[152:155], v[92:95], v[128:143]\n\t"
      "ds_read_b64_tr_b16 v[80:81], v37 offset:12416\n\t"
      "ds_read_b64_tr_b16 v[82:83], v37 offset:13184\n\t"
      "s_bfe_u32 s48, s100, 262156\n\t"
      "s_add_u32 m0, s54, 49152\n\t"
      "s_mul_i32 s48, s48, 128\n\t"
      "s_add_u32 s48, s48, s51\n\t"
      "buffer_load_dwordx4 v28, s[12:15], s48 offen offset:0 lds\n\t"
      "s_waitcnt lgkmcnt(9)\n\t"
      "v_mfma_f32_32x32x16_bf16 v[96:111], v[156:159], v[60:63], v[96:111]\n\t"
      "ds_read_b128 v[164:167], v34 offset:16384\n\t"
      "ds_read_b64_tr_b16 v[84:85], v37 offset:15360\n\t"
      "ds_read_b64_tr_b16 v[86:87], v37 offset:16128\n\t"
      "v_and_b32 v25, 3, v1\n\t"
      "v_lshl_add_u32 v24, v25, 2, v24\n\t"
      "v_mfma_f32_32x32x16_bf16 v[112:127], v[156:159], v[64:67], v[112:127]\n\t"
      "ds_read_b64_tr_b16 v[88:89], v37 offset:15424\n\t"
      "ds_read_b64_tr_b16 v[90:91], v37 offset:16192\n\t"
      "buffer_load_dwordx4 v29, s[12:15], s48 offen offset:1024 lds\n\t"
      "v_add_u32 v24, 131072, v24\n\t"
      "ds_write_b32 v24, v39 offset:4096\n\t"
      "s_lshl_b32 s50, s45, 4\n\t"
      "v_mfma_f32_32x32x16_bf16 v[128:143], v[156:159], v[68:71], v[128:143]\n\t"
      "ds_read_b64_tr_b16 v[92:93], v37 offset:15488\n\t"
      "ds_read_b64_tr_b16 v[94:95], v37 offset:16256\n\t"
      "buffer_load_dwordx4 v40, s[12:15], s48 offen offset:2048 lds\n\t"
      "s_add_u32 s50, s50, s46\n\t"
      "s_lshl_b32 s50, s50, 5\n\t"
      "buffer_load_dwordx4 v41, s[12:15], s48 offen offset:3072 lds\n\t"
      "s_waitcnt lgkmcnt(10)\n\t"
      "v_dot2c_f32_bf16 v42, v4, v39\n\t"
      "v_dot2c_f32_bf16 v43, v8, v39\n\t"
      "v_dot2c_f32_bf16 v44, v12, v39\n\t"
      "v_dot2c_f32_bf16 v42, v5, v39\n\t"
      "v_dot2c_f32_bf16 v43, v9, v39\n\t"
      "v_dot2c_f32_bf16 v44, v13, v39\n\t"
      "v_dot2c_f32_bf16 v42, v6, v39\n\t"
      "v_dot2c_f32_bf16 v43, v10, v39\n\t"
      "v_dot2c_f32_bf16 v44, v14, v39\n\t"
      "v_dot2c_f32_bf16 v42, v7, v39\n\t"
      "v_dot2c_f32_bf16 v43, v11, v39\n\t"
      "v_dot2c_f32_bf16 v44, v15, v39\n\t"
      "s_waitcnt lgkmcnt(8)\n\t"
      "v_mfma_f32_32x32x16_bf16 v[96:111], v[160:163], v[72:75], v[96:111]\n\t"
      "ds_read_b128 v[168:171], v35 offset:16384\n\t"
      "ds_read_b64_tr_b16 v[60:61], v37 offset:18432\n\t"
      "ds_read_b64_tr_b16 v[62:63], v37 offset:19200\n\t"
      "s_add_u32 s50, s50, s44\n\t"
      "s_lshl_b32 s50, s50, 2\n\t"
      "v_mfma_f32_32x32x16_bf16 v[112:127], v[160:163], v[76:79], v[112:127]\n\t"
      "ds_read_b64_tr_b16 v[64:65], v37 offset:18496\n\t"
      "ds_read_b64_tr_b16 v[66:67], v37 offset:19264\n\t"
      "ds_read_b64_tr_b16 v[4:5], v38 offset:12288\n\t"
      "ds_read_b64_tr_b16 v[6:7], v38 offset:13056\n\t"
      "s_bfe_u32 s48, s100, 262156\n\t"
      "s_add_u32 m0, s55, 102400\n\t"
      "s_mul_i32 s48, s48, 172032\n\t"
      "s_add_u32 s48, s48, s93\n\t"
      "buffer_load_dwordx4 v30, s[16:19], s48 offen offset:0 lds\n\t"
      "v_mfma_f32_32x32x16_bf16 v[128:143], v[160:163], v[80:83], v[128:143]\n\t"
      "ds_read_b64_tr_b16 v[68:69], v37 offset:18560\n\t"
      "ds_read_b64_tr_b16 v[70:71], v37 offset:19328\n\t"
      "buffer_load_dwordx4 v31, s[16:19], s48 offen offset:1024 lds\n\t"
      "s_add_u32 s50, s50, 262144\n\t"
      "v_lshl_add_u32 v49, v1, 15, s50\n\t"
      "s_waitcnt lgkmcnt(9)\n\t"
      "v_mfma_f32_32x32x16_bf16 v[96:111], v[164:167], v[84:87], v[96:111]\n\t"
      "ds_read_b128 v[172:175], v36 offset:16384\n\t"
      "ds_read_b64_tr_b16 v[72:73], v37 offset:21504\n\t"
      "ds_read_b64_tr_b16 v[74:75], v37 offset:22272\n\t"
      "s_lshl_b32 s57, s59, 10\n\t"
      "s_add_u32 s57, s57, 524288\n\t"
      "v_mfma_f32_32x32x16_bf16 v[112:127], v[164:167], v[88:91], v[112:127]\n\t"
      "ds_read_b64_tr_b16 v[76:77], v37 offset:21568\n\t"
      "ds_read_b64_tr_b16 v[78:79], v37 offset:22336\n\t"
      "ds_read_b64_tr_b16 v[8:9], v38 offset:12352\n\t"
      "ds_read_b64_tr_b16 v[10:11], v38 offset:13120\n\t"
      "buffer_load_dwordx4 v32, s[16:19], s48 offen offset:2048 lds\n\t"
      "s_lshl_b32 s49, s59, 12\n\t"
      "v_mfma_f32_32x32x16_bf16 v[128:143], v[164:167], v[92:95], v[128:143]\n\t"
      "ds_read_b64_tr_b16 v[80:81], v37 offset:21632\n\t"
      "ds_read_b64_tr_b16 v[82:83], v37 offset:22400\n\t"
      "s_bfe_u32 s48, s100, 262160\n\t"
      "s_add_u32 m0, s54, 0\n\t"
      "s_mul_i32 s48, s48, 128\n\t"
      "s_add_u32 s48, s48, s51\n\t"
      "buffer_load_dwordx4 v28, s[12:15], s48 offen offset:0 lds\n\t"
      "s_waitcnt vmcnt(8)\n\t"
      "s_barrier\n\t"
      "s_waitcnt lgkmcnt(9)\n\t"
      "v_mfma_f32_32x32x16_bf16 v[96:111], v[168:171], v[60:63], v[96:111]\n\t"
      "ds_read_b128 v[176:179], v33 offset:32768\n\t"
      "ds_read_b64_tr_b16 v[84:85], v37 offset:24576\n\t"
      "ds_read_b64_tr_b16 v[86:87], v37 offset:25344\n\t"
      "v_lshrrev_b32 v24, 1, v2\n\t"
      "v_xor_b32 v24, v24, v2\n\t"
      "v_mfma_f32_32x32x16_bf16 v[112:127], v[168:171], v[64:67], v[112:127]\n\t"
      "ds_read_b64_tr_b16 v[88:89], v37 offset:24640\n\t"
      "ds_read_b64_tr_b16 v[90:91], v37 offset:25408\n\t"
      "ds_read_b64_tr_b16 v[12:13], v38 offset:12416\n\t"
      "ds_read_b64_tr_b16 v[14:15], v38 offset:13184\n\t"
      "buffer_load_dwordx4 v29, s[12:15], s48 offen offset:1024 lds\n\t"
      "v_and_b32 v24, 64, v24\n\t"
      "v_mfma_f32_32x32x16_bf16 v[128:143], v[168:171], v[68:71], v[128:143]\n\t"
      "ds_read_b64_tr_b16 v[92:93], v37 offset:24704\n\t"
      "ds_read_b64_tr_b16 v[94:95], v37 offset:25472\n\t"
      "buffer_load_dwordx4 v40, s[12:15], s48 offen offset:2048 lds\n\t"
      "v_lshl_or_b32 v24, v24, 1, v24\n\t"
      "v_xor_b32 v26, v24, v2\n\t"
      "s_waitcnt lgkmcnt(9)\n\t"
      "v_mfma_f32_32x32x16_bf16 v[96:111], v[172:175], v[72:75], v[96:111]\n\t"
      "ds_read_b128 v[180:183], v34 offset:32768\n\t"
      "ds_read_b64_tr_b16 v[60:61], v37 offset:27648\n\t"
      "ds_read_b64_tr_b16 v[62:63], v37 offset:28416\n\t"
      "v_add_u32 v50, s68, v26\n\t"
      "v_mfma_f32_32x32x16_bf16 v[112:127], v[172:175], v[76:79], v[112:127]\n\t"
      "ds_read_b64_tr_b16 v[64:65], v37 offset:27712\n\t"
      "ds_read_b64_tr_b16 v[66:67], v37 offset:28480\n\t"
      "buffer_load_dwordx4 v41, s[12:15], s48 offen offset:3072 lds\n\t"
      "s_mov_b64 exec, s[60:61]\n\t"
      "v_add_u32 v50, 261696, v50\n\t"
      "s_mov_b64 exec, -1\n\t"
      "v_mfma_f32_32x32x16_bf16 v[128:143], v[172:175], v[80:83], v[128:143]\n\t"
      "ds_read_b64_tr_b16 v[68:69], v37 offset:27776\n\t"
      "ds_read_b64_tr_b16 v[70:71], v37 offset:28544\n\t"
      "s_bfe_u32 s48, s100, 262160\n\t"
      "s_add_u32 m0, s55, 114688\n\t"
      "s_mul_i32 s48, s48, 172032\n\t"
      "s_add_u32 s48, s48, s93\n\t"
      "buffer_load_dwordx4 v30, s[16:19], s48 offen offset:0 lds\n\t"
      "buffer_load_dwordx4 v31, s[16:19], s48 offen offset:1024 lds\n\t"
      "s_waitcnt lgkmcnt(9)\n\t"
      "v_dot2c_f32_bf16 v42, v4, v39\n\t"
      "v_dot2c_f32_bf16 v43, v8, v39\n\t"
      "v_dot2c_f32_bf16 v44, v12, v39\n\t"
      "v_dot2c_f32_bf16 v42, v5, v39\n\t"
      "v_dot2c_f32_bf16 v43, v9, v39\n\t"
      "v_dot2c_f32_bf16 v44, v13, v39\n\t"
      "v_dot2c_f32_bf16 v42, v6, v39\n\t"
      "v_dot2c_f32_bf16 v43, v10, v39\n\t"
      "v_dot2c_f32_bf16 v44, v14, v39\n\t"
      "v_dot2c_f32_bf16 v42, v7, v39\n\t"
      "v_dot2c_f32_bf16 v43, v11, v39\n\t"
      "v_dot2c_f32_bf16 v44, v15, v39\n\t"
      "s_waitcnt lgkmcnt(7)\n\t"
      "v_mfma_f32_32x32x16_bf16 v[96:111], v[176:179], v[84:87], v[96:111]\n\t"
      "ds_read_b128 v[184:187], v35 offset:32768\n\t"
      "ds_read_b64_tr_b16 v[72:73], v37 offset:30720\n\t"
      "ds_read_b64_tr_b16 v[74:75], v37 offset:31488\n\t"
      "s_mov_b64 exec, s[62:63]\n\t"
      "v_add_u32 v50, 261696, v50\n\t"
      "s_mov_b64 exec, -1\n\t"
      "v_mfma_f32_32x32x16_bf16 v[112:127], v[176:179], v[88:91], v[112:127]\n\t"
      "ds_read_b64_tr_b16 v[76:77], v37 offset:30784\n\t"
      "ds_read_b64_tr_b16 v[78:79], v37 offset:31552\n\t"
      "ds_read_b64_tr_b16 v[4:5], v38 offset:24576\n\t"
      "ds_read_b64_tr_b16 v[6:7], v38 offset:25344\n\t"
      "buffer_load_dwordx4 v32, s[16:19], s48 offen offset:2048 lds\n\t"
      "v_cndmask_b32 v16, 0, v39, s[66:67]\n\t"
      "v_mfma_f32_32x32x16_bf16 v[128:143], v[176:179], v[92:95], v[128:143]\n\t"
      "ds_read_b64_tr_b16 v[80:81], v37 offset:30848\n\t"
      "ds_read_b64_tr_b16 v[82:83], v37 offset:31616\n\t"
      "s_bfe_u32 s48, s100, 262164\n\t"
      "s_add_u32 m0, s54, 16384\n\t"
      "s_mul_i32 s48, s48, 128\n\t"
      "s_add_u32 s48, s48, s51\n\t"
      "buffer_load_dwordx4 v28, s[12:15], s48 offen offset:0 lds\n\t"
      "s_waitcnt lgkmcnt(9)\n\t"
      "v_mfma_f32_32x32x16_bf16 v[96:111], v[180:183], v[60:63], v[96:111]\n\t"
      "ds_read_b128 v[188:191], v36 offset:32768\n\t"
      "ds_read_b64_tr_b16 v[84:85], v37 offset:33792\n\t"
      "ds_read_b64_tr_b16 v[86:87], v37 offset:34560\n\t"
      "v_cndmask_b32 v17, 0, v39, s[66:67]\n\t"
      "v_cndmask_b32 v18, 0, v39, s[66:67]\n\t"
      "v_mfma_f32_32x32x16_bf16 v[112:127], v[180:183], v[64:67], v[112:127]\n\t"
      "ds_read_b64_tr_b16 v[88:89], v37 offset:33856\n\t"
      "ds_read_b64_tr_b16 v[90:91], v37 offset:34624\n\t"
      "ds_read_b64_tr_b16 v[8:9], v38 offset:24640\n\t"
      "ds_read_b64_tr_b16 v[10:11], v38 offset:25408\n\t"
      "buffer_load_dwordx4 v29, s[12:15], s48 offen offset:1024 lds\n\t"
      "v_cndmask_b32 v19, 0, v39, s[66:67]\n\t"
      "v_mfma_f32_32x32x16_bf16 v[128:143], v[180:183], v[68:71], v[128:143]\n\t"
      "ds_read_b64_tr_b16 v[92:93], v37 offset:33920\n\t"
      "ds_read_b64_tr_b16 v[94:95], v37 offset:34688\n\t"
      "buffer_load_dwordx4 v40, s[12:15], s48 offen offset:2048 lds\n\t"
      "s_mov_b64 exec, s[64:65]\n\t"
      "buffer_store_dwordx4 v[16:19], v50, s[28:31], s49 offen offset:64 sc1\n\t"
      "s_mov_b64 exec, -1\n\t"
      "s_waitcnt vmcnt(10)\n\t"
      "s_barrier\n\t"
      "s_waitcnt lgkmcnt(9)\n\t"
      "v_mfma_f32_32x32x16_bf16 v[96:111], v[184:187], v[72:75], v[96:111]\n\t"
      "ds_read_b128 v[192:195], v33 offset:49152\n\t"
      "ds_read_b64_tr_b16 v[60:61], v37 offset:36864\n\t"
      "ds_read_b64_tr_b16 v[62:63], v37 offset:37632\n\t"
      "s_mov_b64 exec, s[64:65]\n\t"
      "buffer_store_dwordx4 v[16:19], v50, s[28:31], s49 offen offset:1088 sc1\n\t"
      "s_mov_b64 exec, -1\n\t"
      "v_mfma_f32_32x32x16_bf16 v[112:127], v[184:187], v[76:79], v[112:127]\n\t"
      "ds_read_b64_tr_b16 v[64:65], v37 offset:36928\n\t"
      "ds_read_b64_tr_b16 v[66:67], v37 offset:37696\n\t"
      "ds_read_b64_tr_b16 v[12:13], v38 offset:24704\n\t"
      "ds_read_b64_tr_b16 v[14:15], v38 offset:25472\n\t"
      "buffer_load_dwordx4 v41, s[12:15], s48 offen offset:3072 lds\n\t"
      "v_add_u32 v53, s71, v26\n\t"
      "v_mfma_f32_32x32x16_bf16 v[128:143], v[184:187], v[80:83], v[128:143]\n\t"
      "ds_read_b64_tr_b16 v[68:69], v37 offset:36992\n\t"
      "ds_read_b64_tr_b16 v[70:71], v37 offset:37760\n\t"
      "s_bfe_u32 s48, s100, 262164\n\t"
      "s_add_u32 m0, s55, 65536\n\t"
      "s_mul_i32 s48, s48, 172032\n\t"
      "s_add_u32 s48, s48, s93\n\t"
      "buffer_load_dwordx4 v30, s[16:19], s48 offen offset:0 lds\n\t"
      "s_waitcnt lgkmcnt(9)\n\t"
      "v_mfma_f32_32x32x16_bf16 v[96:111], v[188:191], v[84:87], v[96:111]\n\t"
      "ds_read_b128 v[196:199], v34 offset:49152\n\t"
      "ds_read_b64_tr_b16 v[72:73], v37 offset:39936\n\t"
      "ds_read_b64_tr_b16 v[74:75], v37 offset:40704\n\t"
      "s_mov_b64 exec, s[72:73]\n\t"
      "v_add_u32 v53, 261696, v53\n\t"
      "s_mov_b64 exec, -1\n\t"
      "v_mfma_f32_32x32x16_bf16 v[112:127], v[188:191], v[88:91], v[112:127]\n\t"
      "ds_read_b64_tr_b16 v[76:77], v37 offset:40000\n\t"
      "ds_read_b64_tr_b16 v[78:79], v37 offset:40768\n\t"
      "buffer_load_dwordx4 v31, s[16:19], s48 offen offset:1024 lds\n\t"
      "s_mov_b64 exec, s[74:75]\n\t"
      "v_add_u32 v53, 261696, v53\n\t"
      "s_mov_b64 exec, -1\n\t"
      "v_mfma_f32_32x32x16_bf16 v[128:143], v[188:191], v[92:95], v[128:143]\n\t"
      "ds_read_b64_tr_b16 v[80:81], v37 offset:40064\n\t"
      "ds_read_b64_tr_b16 v[82:83], v37 offset:40832\n\t"
      "buffer_load_dwordx4 v32, s[16:19], s48 offen offset:2048 lds\n\t"
      "v_cndmask_b32 v16, 0, v39, s[78:79]\n\t"
      "v_cndmask_b32 v17, 0, v39, s[78:79]\n\t"
      "s_waitcnt lgkmcnt(9)\n\t"
      "v_dot2c_f32_bf16 v42, v4, v39\n\t"
      "v_dot2c_f32_bf16 v43, v8, v39\n\t"
      "v_dot2c_f32_bf16 v44, v12, v39\n\t"
      "v_dot2c_f32_bf16 v42, v5, v39\n\t"
      "v_dot2c_f32_bf16 v43, v9, v39\n\t"
      "v_dot2c_f32_bf16 v44, v13, v39\n\t"
      "v_dot2c_f32_bf16 v42, v6, v39\n\t"
      "v_dot2c_f32_bf16 v43, v10, v39\n\t"
      "v_dot2c_f32_bf16 v44, v14, v39\n\t"
      "v_dot2c_f32_bf16 v42, v7, v39\n\t"
      "v_dot2c_f32_bf16 v43, v11, v39\n\t"
      "v_dot2c_f32_bf16 v44, v15, v39\n\t"
      "s_waitcnt lgkmcnt(7)\n\t"
      "v_mfma_f32_32x32x16_bf16 v[96:111], v[192:195], v[60:63], v[96:111]\n\t"
      "ds_read_b128 v[200:203], v35 offset:49152\n\t"
      "ds_read_b64_tr_b16 v[84:85], v37 offset:43008\n\t"
      "ds_read_b64_tr_b16 v[86:87], v37 offset:43776\n\t"
      "v_cndmask_b32 v18, 0, v39, s[78:79]\n\t"
      "v_cndmask_b32 v19, 0, v39, s[78:79]\n\t"
      "v_mfma_f32_32x32x16_bf16 v[112:127], v[192:195], v[64:67], v[112:127]\n\t"
      "ds_read_b64_tr_b16 v[88:89], v37 offset:43072\n\t"
      "ds_read_b64_tr_b16 v[90:91], v37 offset:43840\n\t"
      "ds_read_b64_tr_b16 v[4:5], v38 offset:36864\n\t"
      "ds_read_b64_tr_b16 v[6:7], v38 offset:37632\n\t"
      "s_bfe_u32 s48, s100, 262168\n\t"
      "s_add_u32 m0, s54, 32768\n\t"
      "s_mul_i32 s48, s48, 128\n\t"
      "s_add_u32 s48, s48, s51\n\t"
      "buffer_load_dwordx4 v28, s[12:15], s48 offen offset:0 lds\n\t"
      "v_mfma_f32_32x32x16_bf16 v[128:143], v[192:195], v[68:71], v[128:143]\n\t"
      "ds_read_b64_tr_b16 v[92:93], v37 offset:43136\n\t"
      "ds_read_b64_tr_b16 v[94:95], v37 offset:43904\n\t"
      "buffer_load_dwordx4 v29, s[12:15], s48 offen offset:1024 lds\n\t"
      "s_mov_b64 exec, s[76:77]\n\t"
      "buffer_store_dwordx4 v[16:19], v53, s[28:31], s49 offen offset:64 sc1\n\t"
      "s_mov_b64 exec, -1\n\t"
      "s_waitcnt lgkmcnt(9)\n\t"
      "v_mfma_f32_32x32x16_bf16 v[96:111], v[196:199], v[72:75], v[96:111]\n\t"
      "ds_read_b128 v[204:207], v36 offset:49152\n\t"
      "ds_read_b64_tr_b16 v[60:61], v37 offset:46080\n\t"
      "ds_read_b64_tr_b16 v[62:63], v37 offset:46848\n\t"
      "s_mov_b64 exec, s[76:77]\n\t"
      "buffer_store_dwordx4 v[16:19], v53, s[28:31], s49 offen offset:1088 sc1\n\t"
      "s_mov_b64 exec, -1\n\t"
      "v_mfma_f32_32x32x16_bf16 v[112:127], v[196:199], v[76:79], v[112:127]\n\t"
      "ds_read_b64_tr_b16 v[64:65], v37 offset:46144\n\t"
      "ds_read_b64_tr_b16 v[66:67], v37 offset:46912\n\t"
      "ds_read_b64_tr_b16 v[8:9], v38 offset:36928\n\t"
      "ds_read_b64_tr_b16 v[10:11], v38 offset:37696\n\t"
      "buffer_load_dwordx4 v40, s[12:15], s48 offen offset:2048 lds\n\t"
      "v_add_u32 v56, s90, v26\n\t"
      "v_mfma_f32_32x32x16_bf16 v[128:143], v[196:199], v[80:83], v[128:143]\n\t"
      "ds_read_b64_tr_b16 v[68:69], v37 offset:46208\n\t"
      "ds_read_b64_tr_b16 v[70:71], v37 offset:46976\n\t"
      "buffer_load_dwordx4 v41, s[12:15], s48 offen offset:3072 lds\n\t"
      "s_mov_b64 exec, s[82:83]\n\t"
      "v_add_u32 v56, 261696, v56\n\t"
      "s_mov_b64 exec, -1\n\t"
      "s_waitcnt vmcnt(11)\n\t"
      "s_barrier\n\t"
      "s_waitcnt lgkmcnt(9)\n\t"
      "v_mfma_f32_32x32x16_bf16 v[96:111], v[200:203], v[84:87], v[96:111]\n\t"
      "ds_read_b128 v[208:211], v33 offset:0\n\t"
      "ds_read_b64_tr_b16 v[72:73], v37 offset:49152\n\t"
      "ds_read_b64_tr_b16 v[74:75], v37 offset:49920\n\t"
      "s_mov_b64 exec, s[84:85]\n\t"
      "v_add_u32 v56, 261696, v56\n\t"
      "s_mov_b64 exec, -1\n\t"
      "v_mfma_f32_32x32x16_bf16 v[112:127], v[200:203], v[88:91], v[112:127]\n\t"
      "ds_read_b64_tr_b16 v[76:77], v37 offset:49216\n\t"
      "ds_read_b64_tr_b16 v[78:79], v37 offset:49984\n\t"
      "ds_read_b64_tr_b16 v[12:13], v38 offset:36992\n\t"
      "ds_read_b64_tr_b16 v[14:15], v38 offset:37760\n\t"
      "s_bfe_u32 s48, s100, 262168\n\t"
      "s_add_u32 m0, s55, 77824\n\t"
      "s_mul_i32 s48, s48, 172032\n\t"
      "s_add_u32 s48, s48, s93\n\t"
      "buffer_load_dwordx4 v30, s[16:19], s48 offen offset:0 lds\n\t"
      "v_mfma_f32_32x32x16_bf16 v[128:143], v[200:203], v[92:95], v[128:143]\n\t"
      "ds_read_b64_tr_b16 v[80:81], v37 offset:49280\n\t"
      "ds_read_b64_tr_b16 v[82:83], v37 offset:50048\n\t"
      "buffer_load_dwordx4 v31, s[16:19], s48 offen offset:1024 lds\n\t"
      "v_cndmask_b32 v16, 0, v39, s[88:89]\n\t"
      "v_cndmask_b32 v17, 0, v39, s[88:89]\n\t"
      "s_waitcnt lgkmcnt(9)\n\t"
      "v_mfma_f32_32x32x16_bf16 v[96:111], v[204:207], v[60:63], v[96:111]\n\t"
      "ds_read_b128 v[212:215], v34 offset:0\n\t"
      "ds_read_b64_tr_b16 v[84:85], v37 offset:52224\n\t"
      "ds_read_b64_tr_b16 v[86:87], v37 offset:52992\n\t"
      "v_cndmask_b32 v18, 0, v39, s[88:89]\n\t"
      "v_cndmask_b32 v19, 0, v39, s[88:89]\n\t"
      "v_mfma_f32_32x32x16_bf16 v[112:127], v[204:207], v[64:67], v[112:127]\n\t"
      "ds_read_b64_tr_b16 v[88:89], v37 offset:52288\n\t"
      "ds_read_b64_tr_b16 v[90:91], v37 offset:53056\n\t"
      "buffer_load_dwordx4 v32, s[16:19], s48 offen offset:2048 lds\n\t"
      "s_mov_b64 exec, s[86:87]\n\t"
      "buffer_store_dwordx4 v[16:19], v56, s[28:31], s49 offen offset:64 sc1\n\t"
      "s_mov_b64 exec, -1\n\t"
      "v_mfma_f32_32x32x16_bf16 v[128:143], v[204:207], v[68:71], v[128:143]\n\t"
      "ds_read_b64_tr_b16 v[92:93], v37 offset:52352\n\t"
      "ds_read_b64_tr_b16 v[94:95], v37 offset:53120\n\t"
      "s_mov_b64 exec, s[86:87]\n\t"
      "buffer_store_dwordx4 v[16:19], v56, s[28:31], s49 offen offset:1088 sc1\n\t"
      "s_mov_b64 exec, -1\n\t"
      "s_waitcnt lgkmcnt(9)\n\t"
      "v_dot2c_f32_bf16 v42, v4, v39\n\t"
      "v_dot2c_f32_bf16 v43, v8, v39\n\t"
      "v_dot2c_f32_bf16 v44, v12, v39\n\t"
      "v_dot2c_f32_bf16 v42, v5, v39\n\t"
      "v_dot2c_f32_bf16 v43, v9, v39\n\t"
      "v_dot2c_f32_bf16 v44, v13, v39\n\t"
      "v_dot2c_f32_bf16 v42, v6, v39\n\t"
      "v_dot2c_f32_bf16 v43, v10, v39\n\t"
      "v_dot2c_f32_bf16 v44, v14, v39\n\t"
      "v_dot2c_f32_bf16 v42, v7, v39\n\t"
      "v_dot2c_f32_bf16 v43, v11, v39\n\t"
      "v_dot2c_f32_bf16 v44, v15, v39\n\t"
      "s_waitcnt lgkmcnt(7)\n\t"
      "v_mfma_f32_32x32x16_bf16 v[96:111], v[208:211], v[72:75], v[96:111]\n\t"
      "ds_read_b128 v[216:219], v35 offset:0\n\t"
      "ds_read_b64_tr_b16 v[60:61], v37 offset:55296\n\t"
      "ds_read_b64_tr_b16 v[62:63], v37 offset:56064\n\t"
      "v_bfe_u32 v24, v1, 2, 2\n\t"
      "v_mul_u32_u24 v28, 320, v24\n\t"
      "v_mfma_f32_32x32x16_bf16 v[112:127], v[208:211], v[76:79], v[112:127]\n\t"
      "ds_read_b64_tr_b16 v[64:65], v37 offset:55360\n\t"
      "ds_read_b64_tr_b16 v[66:67], v37 offset:56128\n\t"
      "ds_read_b64_tr_b16 v[4:5], v38 offset:49152\n\t"
      "ds_read_b64_tr_b16 v[6:7], v38 offset:49920\n\t"
      "v_add_u32 v40, 13312, v47\n\t"
      "v_add_u32 v41, 26624, v47\n\t"
      "v_add_u32 %[tid], 39936, v47\n\t"
      "s_bfe_u32 s48, s100, 262144\n\t"
      "s_add_u32 m0, s54, 49152\n\t"
      "s_mul_i32 s48, s48, 229376\n\t"
      "s_add_u32 s48, s48, s52\n\t"
      "buffer_load_dwordx4 v47, s[20:23], s48 offen offset:0 lds\n\t"
      "v_mfma_f32_32x32x16_bf16 v[128:143], v[208:211], v[80:83], v[128:143]\n\t"
      "ds_read_b64_tr_b16 v[68:69], v37 offset:55424\n\t"
      "ds_read_b64_tr_b16 v[70:71], v37 offset:56192\n\t"
      "buffer_load_dwordx4 v40, s[20:23], s48 offen offset:1024 lds\n\t"
      "v_and_b32 v24, 0x800, v27\n\t"
      "v_bfe_u32 v25, v1, 4, 1\n\t"
      "s_waitcnt lgkmcnt(9)\n\t"
      "v_mfma_f32_32x32x16_bf16 v[96:111], v[212:215], v[84:87], v[96:111]\n\t"
      "ds_read_b128 v[220:223], v36 offset:0\n\t"
      "ds_read_b64_tr_b16 v[72:73], v37 offset:58368\n\t"
      "ds_read_b64_tr_b16 v[74:75], v37 offset:59136\n\t"
      "v_lshl_add_u32 v24, v25, 5, v24\n\t"
      "v_and_b32 v25, 3, v1\n\t"
      "v_mfma_f32_32x32x16_bf16 v[112:127], v[212:215], v[88:91], v[112:127]\n\t"
      "ds_read_b64_tr_b16 v[76:77], v37 offset:58432\n\t"
      "ds_read_b64_tr_b16 v[78:79], v37 offset:59200\n\t"
      "ds_read_b64_tr_b16 v[8:9], v38 offset:49216\n\t"
      "ds_read_b64_tr_b16 v[10:11], v38 offset:49984\n\t"
      "buffer_load_dwordx4 v41, s[20:23], s48 offen offset:2048 lds\n\t"
      "v_lshl_add_u32 v24, v25, 3, v24\n\t"
      "v_mfma_f32_32x32x16_bf16 v[128:143], v[212:215], v[92:95], v[128:143]\n\t"
      "ds_read_b64_tr_b16 v[80:81], v37 offset:58496\n\t"
      "ds_read_b64_tr_b16 v[82:83], v37 offset:59264\n\t"
      "buffer_load_dwordx4 %[tid], s[20:23], s48 offen offset:3072 lds\n\t"
      "v_add_u32 v28, v28, v24\n\t"
      "v_xor_b32 v29, 64, v28\n\t"
      "s_waitcnt vmcnt(11)\n\t"
      "s_barrier\n\t"
      "s_waitcnt lgkmcnt(9)\n\t"
      "v_mfma_f32_32x32x16_bf16 v[96:111], v[216:219], v[60:63], v[96:111]\n\t"
      "ds_read_b128 v[224:227], v33 offset:16384\n\t"
      "ds_read_b64_tr_b16 v[84:85], v37 offset:0\n\t"
      "ds_read_b64_tr_b16 v[86:87], v37 offset:768\n\t"
      "v_xor_b32 v30, 0x80, v28\n\t"
      "v_xor_b32 v31, 0xc0, v28\n\t"
      "v_mfma_f32_32x32x16_bf16 v[112:127], v[216:219], v[64:67], v[112:127]\n\t"
      "ds_read_b64_tr_b16 v[88:89], v37 offset:64\n\t"
      "ds_read_b64_tr_b16 v[90:91], v37 offset:832\n\t"
      "ds_read_b64_tr_b16 v[12:13], v38 offset:49280\n\t"
      "ds_read_b64_tr_b16 v[14:15], v38 offset:50048\n\t"
      "s_bfe_u32 s48, s100, 262144\n\t"
      "s_add_u32 m0, s56, 131072\n\t"
      "s_mul_i32 s48, s48, 128\n\t"
      "s_add_u32 s48, s48, s53\n\t"
      "buffer_load_dwordx4 v48, s[24:27], s48 offen offset:0 lds\n\t"
      "v_mfma_f32_32x32x16_bf16 v[128:143], v[216:219], v[68:71], v[128:143]\n\t"
      "ds_read_b64_tr_b16 v[92:93], v37 offset:128\n\t"
      "ds_read_b64_tr_b16 v[94:95], v37 offset:896\n\t"
      "s_lshl_b32 s50, s44, 6\n\t"
      "v_xor_b32 v32, s50, v28\n\t"
      "v_accvgpr_write_b32 a170, 0\n\t"
      "v_accvgpr_write_b32 a171, 0\n\t"
      "s_waitcnt lgkmcnt(9)\n\t"
      "v_mfma_f32_32x32x16_bf16 v[96:111], v[220:223], v[72:75], v[96:111]\n\t"
      "ds_read_b128 v[228:231], v34 offset:16384\n\t"
      "ds_read_b64_tr_b16 v[60:61], v37 offset:3072\n\t"
      "ds_read_b64_tr_b16 v[62:63], v37 offset:3840\n\t"
      "v_mfma_f32_32x32x16_bf16 v[112:127], v[220:223], v[76:79], v[112:127]\n\t"
      "ds_read_b64_tr_b16 v[64:65], v37 offset:3136\n\t"
      "ds_read_b64_tr_b16 v[66:67], v37 offset:3904\n\t"
      "v_mfma_f32_32x32x16_bf16 v[128:143], v[220:223], v[80:83], v[128:143]\n\t"
      "ds_read_b64_tr_b16 v[68:69], v37 offset:3200\n\t"
      "ds_read_b64_tr_b16 v[70:71], v37 offset:3968\n\t"
      "buffer_load_dwordx2 v[20:21], v2, s[28:31], s57 offen sc1\n\t"
      "buffer_load_dwordx2 v[22:23], v2, s[28:31], s57 offen offset:8 sc1\n\t"
      "s_waitcnt lgkmcnt(9)\n\t"
      "v_dot2c_f32_bf16 v42, v4, v39\n\t"
      "v_dot2c_f32_bf16 v43, v8, v39\n\t"
      "v_dot2c_f32_bf16 v44, v12, v39\n\t"
      "v_dot2c_f32_bf16 v42, v5, v39\n\t"
      "v_dot2c_f32_bf16 v43, v9, v39\n\t"
      "v_dot2c_f32_bf16 v44, v13, v39\n\t"
      "v_dot2c_f32_bf16 v42, v6, v39\n\t"
      "v_dot2c_f32_bf16 v43, v10, v39\n\t"
      "v_dot2c_f32_bf16 v44, v14, v39\n\t"
      "v_dot2c_f32_bf16 v42, v7, v39\n\t"
      "v_dot2c_f32_bf16 v43, v11, v39\n\t"
      "v_dot2c_f32_bf16 v44, v15, v39\n\t"
      "s_waitcnt lgkmcnt(7)\n\t"
      "v_mfma_f32_32x32x16_bf16 v[96:111], v[224:227], v[84:87], v[96:111]\n\t"
      "ds_read_b128 v[232:235], v35 offset:16384\n\t"
      "ds_read_b64_tr_b16 v[72:73], v37 offset:6144\n\t"
      "ds_read_b64_tr_b16 v[74:75], v37 offset:6912\n\t"
      "v_mfma_f32_32x32x16_bf16 v[112:127], v[224:227], v[88:91], v[112:127]\n\t"
      "ds_read_b64_tr_b16 v[76:77], v37 offset:6208\n\t"
      "ds_read_b64_tr_b16 v[78:79], v37 offset:6976\n\t"
      "ds_read_b64_tr_b16 v[4:5], v38 offset:0\n\t"
      "ds_read_b64_tr_b16 v[6:7], v38 offset:768\n\t"
      "s_bfe_u32 s48, s100, 262148\n\t"
      "s_add_u32 m0, s54, 0\n\t"
      "s_mul_i32 s48, s48, 229376\n\t"
      "s_add_u32 s48, s48, s52\n\t"
      "buffer_load_dwordx4 v47, s[20:23], s48 offen offset:0 lds\n\t"
      "v_mfma_f32_32x32x16_bf16 v[128:143], v[224:227], v[92:95], v[128:143]\n\t"
      "ds_read_b64_tr_b16 v[80:81], v37 offset:6272\n\t"
      "ds_read_b64_tr_b16 v[82:83], v37 offset:7040\n\t"
      "buffer_load_dwordx4 v40, s[20:23], s48 offen offset:1024 lds\n\t"
      "s_waitcnt lgkmcnt(9)\n\t"
      "v_mfma_f32_32x32x16_bf16 v[96:111], v[228:231], v[60:63], v[96:111]\n\t"
      "ds_read_b128 v[236:239], v36 offset:16384\n\t"
      "ds_read_b64_tr_b16 v[84:85], v37 offset:9216\n\t"
      "ds_read_b64_tr_b16 v[86:87], v37 offset:9984\n\t"
      "v_mfma_f32_32x32x16_bf16 v[112:127], v[228:231], v[64:67], v[112:127]\n\t"
      "ds_read_b64_tr_b16 v[88:89], v37 offset:9280\n\t"
      "ds_read_b64_tr_b16 v[90:91], v37 offset:10048\n\t"
      "ds_read_b64_tr_b16 v[8:9], v38 offset:64\n\t"
      "ds_read_b64_tr_b16 v[10:11], v38 offset:832\n\t"
      "buffer_load_dwordx4 v41, s[20:23], s48 offen offset:2048 lds\n\t"
      "v_mfma_f32_32x32x16_bf16 v[128:143], v[228:231], v[68:71], v[128:143]\n\t"
      "ds_read_b64_tr_b16 v[92:93], v37 offset:9344\n\t"
      "ds_read_b64_tr_b16 v[94:95], v37 offset:10112\n\t"
      "buffer_load_dwordx4 %[tid], s[20:23], s48 offen offset:3072 lds\n\t"
      "s_waitcnt vmcnt(11)\n\t"
      "s_barrier\n\t"
      "s_waitcnt lgkmcnt(9)\n\t"
      "v_mfma_f32_32x32x16_bf16 v[96:111], v[232:235], v[72:75], v[96:111]\n\t"
      "ds_read_b128 v[240:243], v33 offset:32768\n\t"
      "ds_read_b64_tr_b16 v[60:61], v37 offset:12288\n\t"
      "ds_read_b64_tr_b16 v[62:63], v37 offset:13056\n\t"
      "v_mfma_f32_32x32x16_bf16 v[112:127], v[232:235], v[76:79], v[112:127]\n\t"
      "ds_read_b64_tr_b16 v[64:65], v37 offset:12352\n\t"
      "ds_read_b64_tr_b16 v[66:67], v37 offset:13120\n\t"
      "ds_read_b64_tr_b16 v[12:13], v38 offset:128\n\t"
      "ds_read_b64_tr_b16 v[14:15], v38 offset:896\n\t"
      "s_bfe_u32 s48, s100, 262148\n\t"
      "s_add_u32 m0, s56, 135424\n\t"
      "s_mul_i32 s48, s48, 128\n\t"
      "s_add_u32 s48, s48, s53\n\t"
      "buffer_load_dwordx4 v48, s[24:27], s48 offen offset:0 lds\n\t"
      "v_mfma_f32_32x32x16_bf16 v[128:143], v[232:235], v[80:83], v[128:143]\n\t"
      "ds_read_b64_tr_b16 v[68:69], v37 offset:12416\n\t"
      "ds_read_b64_tr_b16 v[70:71], v37 offset:13184\n\t"
      "s_waitcnt lgkmcnt(9)\n\t"
      "v_mfma_f32_32x32x16_bf16 v[96:111], v[236:239], v[84:87], v[96:111]\n\t"
      "ds_read_b128 v[244:247], v34 offset:32768\n\t"
      "ds_read_b64_tr_b16 v[72:73], v37 offset:15360\n\t"
      "ds_read_b64_tr_b16 v[74:75], v37 offset:16128\n\t"
      "v_mfma_f32_32x32x16_bf16 v[112:127], v[236:239], v[88:91], v[112:127]\n\t"
      "ds_read_b64_tr_b16 v[76:77], v37 offset:15424\n\t"
      "ds_read_b64_tr_b16 v[78:79], v37 offset:16192\n\t"
      "v_mfma_f32_32x32x16_bf16 v[128:143], v[236:239], v[92:95], v[128:143]\n\t"
      "ds_read_b64_tr_b16 v[80:81], v37 offset:15488\n\t"
      "ds_read_b64_tr_b16 v[82:83], v37 offset:16256\n\t"
      "s_waitcnt lgkmcnt(9)\n\t"
      "v_dot2c_f32_bf16 v42, v4, v39\n\t"
      "v_dot2c_f32_bf16 v43, v8, v39\n\t"
      "v_dot2c_f32_bf16 v44, v12, v39\n\t"
      "v_dot2c_f32_bf16 v42, v5, v39\n\t"
      "v_dot2c_f32_bf16 v43, v9, v39\n\t"
      "v_dot2c_f32_bf16 v44, v13, v39\n\t"
      "v_dot2c_f32_bf16 v42, v6, v39\n\t"
      "v_dot2c_f32_bf16 v43, v10, v39\n\t"
      "v_dot2c_f32_bf16 v44, v14, v39\n\t"
      "v_dot2c_f32_bf16 v42, v7, v39\n\t"
      "v_dot2c_f32_bf16 v43, v11, v39\n\t"
      "v_dot2c_f32_bf16 v44, v15, v39\n\t"
      "ds_read_b64_tr_b16 v[4:5], v38 offset:12288\n\t"
      "ds_read_b64_tr_b16 v[6:7], v38 offset:13056\n\t"
      "ds_read_b64_tr_b16 v[8:9], v38 offset:12352\n\t"
      "ds_read_b64_tr_b16 v[10:11], v38 offset:13120\n\t"
      "ds_read_b64_tr_b16 v[12:13], v38 offset:12416\n\t"
      "ds_read_b64_tr_b16 v[14:15], v38 offset:13184\n\t"
      "s_waitcnt vmcnt(5)\n\t"
      "v_xor_b32 v21, s47, v21\n\t"
      "v_xor_b32 v23, s47, v23\n\t"
      "v_or_b32 v21, v21, v23\n\t"
      "v_cmp_ne_u32 vcc, 0, v21\n\t"
      "v_permlane32_swap_b32 v20, v22\n\t"
      "s_cbranch_vccnz xpb_%=\n\t"
      "xrb_%=:\n\t"
      "v_mov_b32 v25, 0\n\t"
      "v_add_f32 v20, v20, v22\n\t"
      "v_mov_b32 v18, 0\n\t"
      "v_mov_b32 v22, v20\n\t"
      "s_waitcnt lgkmcnt(0)\n\t"
      "v_dot2c_f32_bf16 v42, v4, v39\n\t"
      "v_dot2c_f32_bf16 v43, v8, v39\n\t"
      "v_dot2c_f32_bf16 v44, v12, v39\n\t"
      "v_dot2c_f32_bf16 v42, v5, v39\n\t"
      "v_dot2c_f32_bf16 v43, v9, v39\n\t"
      "v_dot2c_f32_bf16 v44, v13, v39\n\t"
      "v_dot2c_f32_bf16 v42, v6, v39\n\t"
      "v_dot2c_f32_bf16 v43, v10, v39\n\t"
      "v_dot2c_f32_bf16 v44, v14, v39\n\t"
      "v_dot2c_f32_bf16 v42, v7, v39\n\t"
      "v_dot2c_f32_bf16 v43, v11, v39\n\t"
      "v_dot2c_f32_bf16 v44, v15, v39\n\t"
      "s_waitcnt lgkmcnt(13)\n\t"
      "v_mfma_f32_32x32x16_bf16 v[96:111], v[240:243], v[60:63], v[96:111]\n\t"
      "ds_read_b128 v[248:251], v35 offset:32768\n\t"
      "ds_read_b64_tr_b16 v[84:85], v37 offset:18432\n\t"
      "ds_read_b64_tr_b16 v[86:87], v37 offset:19200\n\t"
      "v_permlane32_swap_b32 v20, v22\n\t"
      "v_mov_b32 v19, 0\n\t"
      "v_permlane32_swap_b32 v42, v43\n\t"
      "v_mfma_f32_32x32x16_bf16 v[112:127], v[240:243], v[64:67], v[112:127]\n\t"
      "ds_read_b64_tr_b16 v[88:89], v37 offset:18496\n\t"
      "ds_read_b64_tr_b16 v[90:91], v37 offset:19264\n\t"
      "s_bfe_u32 s48, s100, 262152\n\t"
      "s_add_u32 m0, s54, 16384\n\t"
      "s_mul_i32 s48, s48, 229376\n\t"
      "s_add_u32 s48, s48, s52\n\t"
      "buffer_load_dwordx4 v47, s[20:23], s48 offen offset:0 lds\n\t"
      "v_mfma_f32_32x32x16_bf16 v[128:143], v[240:243], v[68:71], v[128:143]\n\t"
      "ds_read_b64_tr_b16 v[92:93], v37 offset:18560\n\t"
      "ds_read_b64_tr_b16 v[94:95], v37 offset:19328\n\t"
      "v_permlane32_swap_b32 v44, v25\n\t"
      "v_mul_f32 v20, 0xbb124925, v20\n\t"
      "v_add_f32 v42, v42, v43\n\t"
      "v_add_f32 v44, v44, v25\n\t"
      "s_waitcnt lgkmcnt(13)\n\t"
      "v_mfma_f32_32x32x16_bf16 v[96:111], v[244:247], v[72:75], v[96:111]\n\t"
      "ds_read_b128 v[252:255], v36 offset:32768\n\t"
      "ds_read_b64_tr_b16 v[60:61], v37 offset:21504\n\t"
      "ds_read_b64_tr_b16 v[62:63], v37 offset:22272\n\t"
      "buffer_load_dwordx4 v40, s[20:23], s48 offen offset:1024 lds\n\t"
      "ds_write_b32 v45, v42\n\t"
      "ds_write_b32 v45, v44 offset:1024\n\t"
      "v_mfma_f32_32x32x16_bf16 v[112:127], v[244:247], v[76:79], v[112:127]\n\t"
      "ds_read_b64_tr_b16 v[64:65], v37 offset:21568\n\t"
      "ds_read_b64_tr_b16 v[66:67], v37 offset:22336\n\t"
      "v_mul_f32 v22, 0x3b124925, v22\n\t"
      "v_fma_f32 v22, -v20, v20, v22\n\t"
      "v_cvt_pk_bf16_f32 v16, v20, v20\n\t"
      "v_max_f32 v22, 0, v22\n\t"
      "v_mfma_f32_32x32x16_bf16 v[128:143], v[244:247], v[80:83], v[128:143]\n\t"
      "ds_read_b64_tr_b16 v[68:69], v37 offset:21632\n\t"
      "ds_read_b64_tr_b16 v[70:71], v37 offset:22400\n\t"
      "buffer_load_dwordx4 v41, s[20:23], s48 offen offset:2048 lds\n\t"
      "v_and_b32 v17, 0xffff0000, v16\n\t"
      "s_waitcnt lgkmcnt(4)\n\t"
      "s_barrier\n\t"
      "s_waitcnt lgkmcnt(9)\n\t"
      "v_mfma_f32_32x32x16_bf16 v[96:111], v[248:251], v[84:87], v[96:111]\n\t"
      "ds_read_b128 v[4:7], v46 offset:0\n\t"
      "ds_read_b128 v[8:11], v46 offset:512\n\t"
      "ds_read_b128 v[12:15], v46 offset:1024\n\t"
      "v_add_f32 v22, 0x3727c5ac, v22\n\t"
      "v_sub_f32 v17, v20, v17\n\t"
      "v_rsq_f32 v24, v22\n\t"
      "v_mfma_f32_32x32x16_bf16 v[112:127], v[248:251], v[88:91], v[112:127]\n\t"
      "buffer_load_dwordx4 %[tid], s[20:23], s48 offen offset:3072 lds\n\t"
      "v_cvt_pk_bf16_f32 v17, v17, 0\n\t"
      "v_bfi_b32 v16, v27, 0, v16\n\t"
      "v_bfi_b32 v17, v27, 0, v17\n\t"
      "v_mov_b32 v22, 0\n\t"
      "v_mov_b32 v23, 0\n\t"
      "v_mfma_f32_32x32x16_bf16 v[128:143], v[248:251], v[92:95], v[128:143]\n\t"
      "v_accvgpr_write_b32 a168, v16\n\t"
      "v_accvgpr_write_b32 a169, v17\n\t"
      "s_waitcnt lgkmcnt(0)\n\t"
      "v_add_f32 v4, v4, v5\n\t"
      "v_add_f32 v6, v6, v7\n\t"
      "v_add_f32 v42, v4, v6\n\t"
      "s_waitcnt lgkmcnt(3)\n\t"
      "v_mfma_f32_32x32x16_bf16 v[96:111], v[252:255], v[60:63], v[96:111]\n\t"
      "s_bfe_u32 s48, s100, 262152\n\t"
      "s_add_u32 m0, s56, 139776\n\t"
      "s_mul_i32 s48, s48, 128\n\t"
      "s_add_u32 s48, s48, s53\n\t"
      "buffer_load_dwordx4 v48, s[24:27], s48 offen offset:0 lds\n\t"
      "v_cvt_pk_bf16_f32 v21, v42, 0\n\t"
      "v_mfma_f32_32x32x16_bf16 v[112:127], v[252:255], v[64:67], v[112:127]\n\t"
      "v_lshlrev_b32 v20, 16, v21\n\t"
      "v_sub_f32 v20, v42, v20\n\t"
      "v_cvt_pk_bf16_f32 v20, v42, v20\n\t"
      "v_add_f32 v8, v8, v9\n\t"
      "v_add_f32 v10, v10, v11\n\t"
      "v_add_f32 v43, v8, v10\n\t"
      "v_mfma_f32_32x32x16_bf16 v[128:143], v[252:255], v[68:71], v[128:143]\n\t"
      "v_cvt_pk_bf16_f32 v5, v43, 0\n\t"
      "v_lshlrev_b32 v4, 16, v5\n\t"
      "v_sub_f32 v4, v43, v4\n\t"
      "v_cvt_pk_bf16_f32 v4, v43, v4\n\t"
      "v_mov_b32 v6, 0\n\t"
      "v_mov_b32 v7, 0\n\t"
      "v_add_f32 v12, v12, v13\n\t"
      "v_add_f32 v14, v14, v15\n\t"
      "v_add_f32 v44, v12, v14\n\t"
      "v_cvt_pk_bf16_f32 v9, v44, 0\n\t"
      "v_lshlrev_b32 v8, 16, v9\n\t"
      "v_sub_f32 v8, v44, v8\n\t"
      "v_cvt_pk_bf16_f32 v8, v44, v8\n\t"
      "v_mov_b32 v10, 0\n\t"
      "v_mov_b32 v11, 0\n\t"
      "s_nop 1\n\t"
      "v_mfma_f32_32x32x16_bf16 v[96:111], v[16:19], v[20:23], v[96:111]\n\t"
      "v_cvt_pk_bf16_f32 v12, v24, v24\n\t"
      "v_and_b32 v12, 0xffff0000, v12\n\t"
      "v_sub_f32 v13, v24, v12\n\t"
      "v_cvt_pk_bf16_f32 v12, v24, v13\n\t"
      "v_and_b32 v14, 0xffff0000, v12\n\t"
      "v_mfma_f32_32x32x16_bf16 v[112:127], v[16:19], v[4:7], v[112:127]\n\t"
      "v_sub_f32 v13, v13, v14\n\t"
      "v_cvt_pk_bf16_f32 v13, v13, 0\n\t"
      "v_bfi_b32 v12, v27, 0, v12\n\t"
      "v_bfi_b32 v13, v27, 0, v13\n\t"
      "v_mov_b32 v14, 0\n\t"
      "v_mfma_f32_32x32x16_bf16 v[128:143], v[16:19], v[8:11], v[128:143]\n\t"
      "v_mov_b32 v15, 0\n\t"
      "v_mov_b32 v64, v39\n\t"
      "v_mov_b32 v65, v39\n\t"
      "v_mov_b32 v66, v39\n\t"
      "v_mov_b32 v67, v39\n\t"
      "s_nop 1\n\t"
      "v_mfma_f32_32x32x16_bf16 v[68:83], v[12:15], v[64:67], 0\n\t"
      "s_sub_u32 s50, 131072, s54\n\t"
      "v_add_u32 v33, s50, v33\n\t"
      "v_add_u32 v34, s50, v34\n\t"
      "v_add_u32 v35, s50, v35\n\t"
      "v_add_u32 v36, s50, v36\n\t"
      "s_mov_b32 exec_lo, 0xf0000000\n\t"
      "s_mov_b32 exec_hi, 0xf0000000\n\t"
      "v_mov_b32 v33, 135168\n\t"
      "v_mov_b32 v34, 135168\n\t"
      "v_mov_b32 v35, 135168\n\t"
      "v_mov_b32 v36, 135168\n\t"
      "s_mov_b64 exec, -1\n\t"
      "v_pk_mul_f32 v[96:97], v[96:97], v[68:69]\n\t"
      "v_pk_mul_f32 v[98:99], v[98:99], v[70:71]\n\t"
      "v_cvt_pk_bf16_f32 v96, v96, v97\n\t"
      "v_cvt_pk_bf16_f32 v97, v98, v99\n\t"
      "v_pk_mul_f32 v[100:101], v[100:101], v[72:73]\n\t"
      "v_pk_mul_f32 v[102:103], v[102:103], v[74:75]\n\t"
      "v_cvt_pk_bf16_f32 v98, v100, v101\n\t"
      "v_cvt_pk_bf16_f32 v99, v102, v103\n\t"
      "buffer_store_dwordx4 v[96:99], v50, s[28:31], s49 offen sc1\n\t"
      "v_pk_mul_f32 v[104:105], v[104:105], v[76:77]\n\t"
      "v_pk_mul_f32 v[106:107], v[106:107], v[78:79]\n\t"
      "v_cvt_pk_bf16_f32 v100, v104, v105\n\t"
      "v_cvt_pk_bf16_f32 v101, v106, v107\n\t"
      "v_pk_mul_f32 v[108:109], v[108:109], v[80:81]\n\t"
      "v_pk_mul_f32 v[110:111], v[110:111], v[82:83]\n\t"
      "v_cvt_pk_bf16_f32 v102, v108, v109\n\t"
      "v_cvt_pk_bf16_f32 v103, v110, v111\n\t"
      "buffer_store_dwordx4 v[100:103], v50, s[28:31], s49 offen offset:1024 sc1\n\t"
      "v_pk_mul_f32 v[112:113], v[112:113], v[68:69]\n\t"
      "v_pk_mul_f32 v[114:115], v[114:115], v[70:71]\n\t"
      "v_cvt_pk_bf16_f32 v112, v112, v113\n\t"
      "v_cvt_pk_bf16_f32 v113, v114, v115\n\t"
      "v_pk_mul_f32 v[116:117], v[116:117], v[72:73]\n\t"
      "v_pk_mul_f32 v[118:119], v[118:119], v[74:75]\n\t"
      "v_cvt_pk_bf16_f32 v114, v116, v117\n\t"
      "v_cvt_pk_bf16_f32 v115, v118, v119\n\t"
      "buffer_store_dwordx4 v[112:115], v53, s[28:31], s49 offen sc1\n\t"
      "v_pk_mul_f32 v[120:121], v[120:121], v[76:77]\n\t"
      "v_pk_mul_f32 v[122:123], v[122:123], v[78:79]\n\t"
      "v_cvt_pk_bf16_f32 v116, v120, v121\n\t"
      "v_cvt_pk_bf16_f32 v117, v122, v123\n\t"
      "v_pk_mul_f32 v[124:125], v[124:125], v[80:81]\n\t"
      "v_pk_mul_f32 v[126:127], v[126:127], v[82:83]\n\t"
      "v_cvt_pk_bf16_f32 v118, v124, v125\n\t"
      "v_cvt_pk_bf16_f32 v119, v126, v127\n\t"
      "buffer_store_dwordx4 v[116:119], v53, s[28:31], s49 offen offset:1024 sc1\n\t"
      "v_pk_mul_f32 v[128:129], v[128:129], v[68:69]\n\t"
      "v_pk_mul_f32 v[130:131], v[130:131], v[70:71]\n\t"
      "v_cvt_pk_bf16_f32 v128, v128, v129\n\t"
      "v_cvt_pk_bf16_f32 v129, v130, v131\n\t"
      "v_pk_mul_f32 v[132:133], v[132:133], v[72:73]\n\t"
      "v_pk_mul_f32 v[134:135], v[134:135], v[74:75]\n\t"
      "v_cvt_pk_bf16_f32 v130, v132, v133\n\t"
      "v_cvt_pk_bf16_f32 v131, v134, v135\n\t"
      "buffer_store_dwordx4 v[128:131], v56, s[28:31], s49 offen sc1\n\t"
      "v_pk_mul_f32 v[136:137], v[136:137], v[76:77]\n\t"
      "v_pk_mul_f32 v[138:139], v[138:139], v[78:79]\n\t"
      "v_cvt_pk_bf16_f32 v132, v136, v137\n\t"
      "v_cvt_pk_bf16_f32 v133, v138, v139\n\t"
      "v_pk_mul_f32 v[140:141], v[140:141], v[80:81]\n\t"
      "v_pk_mul_f32 v[142:143], v[142:143], v[82:83]\n\t"
      "v_cvt_pk_bf16_f32 v134, v140, v141\n\t"
      "v_cvt_pk_bf16_f32 v135, v142, v143\n\t"
      "buffer_store_dwordx4 v[132:135], v56, s[28:31], s49 offen offset:1024 sc1\n\t"
      "s_branch zph_%=\n\t"
      "mq_%=:\n\t"
      "s_bfe_u32 s48, s100, 262144\n\t"
      "s_add_u32 m0, s54, 0\n\t"
      "s_mul_i32 s48, s48, 128\n\t"
      "s_add_u32 s48, s48, s51\n\t"
      "buffer_load_dwordx4 v28, s[12:15], s48 offen offset:0 lds\n\t"
      "buffer_load_dwordx4 v29, s[12:15], s48 offen offset:1024 lds\n\t"
      "buffer_load_dwordx4 v40, s[12:15], s48 offen offset:2048 lds\n\t"
      "buffer_load_dwordx4 v41, s[12:15], s48 offen offset:3072 lds\n\t"
      "s_bfe_u32 s48, s100, 262144\n\t"
      "s_add_u32 m0, s55, 65536\n\t"
      "s_mul_i32 s48, s48, 172032\n\t"
      "s_add_u32 s48, s48, s93\n\t"
      "buffer_load_dwordx4 v30, s[16:19], s48 offen offset:0 lds\n\t"
      "buffer_load_dwordx4 v31, s[16:19], s48 offen offset:1024 lds\n\t"
      "buffer_load_dwordx4 v32, s[16:19], s48 offen offset:2048 lds\n\t"
      "s_bfe_u32 s48, s100, 262148\n\t"
      "s_add_u32 m0, s54, 16384\n\t"
      "s_mul_i32 s48, s48, 128\n\t"
      "s_add_u32 s48, s48, s51\n\t"
      "buffer_load_dwordx4 v28, s[12:15], s48 offen offset:0 lds\n\t"
      "v_bfe_u32 v24, v1, 1, 2\n\t"
      "v_bfe_u32 v25, v1, 4, 1\n\t"
      "v_lshl_or_b32 v24, v25, 2, v24\n\t"
      "v_lshrrev_b32 v26, 5, v1\n\t"
      "v_xor_b32 v24, v24, v26\n\t"
      "v_and_b32 v59, 31, v1\n\t"
      "v_lshlrev_b32 v24, 4, v24\n\t"
      "v_lshl_add_u32 v24, v59, 7, v24\n\t"
      "buffer_load_dwordx4 v29, s[12:15], s48 offen offset:1024 lds\n\t"
      "v_add_u32 v33, s54, v24\n\t"
      "v_xor_b32 v34, 32, v33\n\t"
      "v_xor_b32 v35, 64, v33\n\t"
      "v_xor_b32 v36, 0x60, v33\n\t"
      "v_lshlrev_b32 v59, 4, v59\n\t"
      "v_lshlrev_b32 v2, 4, v1\n\t"
      "v_bfe_u32 v24, v1, 2, 2\n\t"
      "v_lshrrev_b32 v25, 5, v1\n\t"
      "buffer_load_dwordx4 v40, s[12:15], s48 offen offset:2048 lds\n\t"
      "v_lshl_add_u32 v24, v25, 3, v24\n\t"
      "v_mul_u32_u24 v37, 192, v24\n\t"
      "v_bfe_u32 v25, v1, 4, 1\n\t"
      "v_lshl_add_u32 v37, v25, 5, v37\n\t"
      "v_bfrev_b32 v25, v1\n\t"
      "v_lshrrev_b32 v25, 30, v25\n\t"
      "v_lshl_add_u32 v37, v25, 3, v37\n\t"
      "v_add_u32 v37, 65536, v37\n\t"
      "buffer_load_dwordx4 v41, s[12:15], s48 offen offset:3072 lds\n\t"
      "v_add_u32 v38, s55, v37\n\t"
      "v_mov_b32 v39, 1065369472\n\t"
      "v_mov_b32 v42, 0\n\t"
      "v_mov_b32 v43, 0\n\t"
      "v_mov_b32 v44, 0\n\t"
      "s_mov_b32 s30, 27262976\n\t"
      "s_mov_b32 s31, 0x20000\n\t"
      "s_mov_b32 s34, 64512\n\t"
      "s_bfe_u32 s48, s100, 262148\n\t"
      "s_add_u32 m0, s55, 77824\n\t"
      "s_mul_i32 s48, s48, 172032\n\t"
      "s_add_u32 s48, s48, s93\n\t"
      "buffer_load_dwordx4 v30, s[16:19], s48 offen offset:0 lds\n\t"
      "s_mov_b32 s35, 0x20000\n\t"
      "s_lshr_b32 s50, %[bid], 3\n\t"
      "s_lshl_b32 s50, s50, 2\n\t"
      "s_add_u32 s50, s50, s44\n\t"
      "s_lshl_b32 s50, s50, 9\n\t"
      "v_mov_b32 v26, 2147483648\n\t"
      "buffer_load_dwordx4 v31, s[16:19], s48 offen offset:1024 lds\n\t"
      "s_mov_b64 exec, 15\n\t"
      "v_lshl_add_u32 v26, v1, 7, s50\n\t"
      "s_mov_b64 exec, -1\n\t"
      "buffer_load_dwordx4 v32, s[16:19], s48 offen offset:2048 lds\n\t"
      "buffer_load_dword a253, v26, s[32:35], 0 offen\n\t"
      "s_mov_b32 s22, 1605632\n\t"
      "s_mov_b32 s23, 0x20000\n\t"
      "s_mov_b32 s26, 401408\n\t"
      "s_mov_b32 s27, 0x20000\n\t"
      "s_mov_b32 s38, 1605632\n\t"
      "s_mov_b32 s39, 0x20000\n\t"
      "s_mul_i32 s52, s44, 57344\n\t"
      "s_lshl_b32 s50, s46, 8\n\t"
      "s_add_u32 s52, s52, s50\n\t"
      "s_mul_i32 s53, s45, 25088\n\t"
      "s_lshl_b32 s56, s44, 10\n\t"
      "v_lshrrev_b32 v24, 4, v1\n\t"
      "v_and_b32 v25, 15, v1\n\t"
      "v_lshlrev_b32 v26, 2, v24\n\t"
      "v_xor_b32 v25, v25, v26\n\t"
      "v_mul_u32_u24 v47, 3584, v24\n\t"
      "s_waitcnt vmcnt(8) lgkmcnt(0)\n\t"
      "s_barrier\n\t"
      "ds_read_b128 v[144:147], v33 offset:0\n\t"
      "ds_read_b64_tr_b16 v[60:61], v37 offset:0\n\t"
      "ds_read_b64_tr_b16 v[62:63], v37 offset:768\n\t"
      "ds_read_b64_tr_b16 v[64:65], v37 offset:64\n\t"
      "ds_read_b64_tr_b16 v[66:67], v37 offset:832\n\t"
      "ds_read_b64_tr_b16 v[68:69], v37 offset:128\n\t"
      "ds_read_b64_tr_b16 v[70:71], v37 offset:896\n\t"
      "ds_read_b128 v[148:151], v34 offset:0\n\t"
      "ds_read_b64_tr_b16 v[72:73], v37 offset:3072\n\t"
      "ds_read_b64_tr_b16 v[74:75], v37 offset:3840\n\t"
      "ds_read_b64_tr_b16 v[76:77], v37 offset:3136\n\t"
      "ds_read_b64_tr_b16 v[78:79], v37 offset:3904\n\t"
      "ds_read_b64_tr_b16 v[80:81], v37 offset:3200\n\t"
      "ds_read_b64_tr_b16 v[82:83], v37 offset:3968\n\t"
      "s_add_u32 s47, s47, 1\n\t"
      "s_bfe_u32 s48, s100, 262152\n\t"
      "s_add_u32 m0, s54, 32768\n\t"
      "s_mul_i32 s48, s48, 128\n\t"
      "s_add_u32 s48, s48, s51\n\t"
      "buffer_load_dwordx4 v28, s[12:15], s48 offen offset:0 lds\n\t"
      "buffer_load_dwordx4 v29, s[12:15], s48 offen offset:1024 lds\n\t"
      "v_lshl_add_u32 v47, v25, 4, v47\n\t"
      "s_lshl_b32 s50, s44, 3\n\t"
      "v_lshrrev_b32 v25, 3, v1\n\t"
      "v_add_u32 v25, s50, v25\n\t"
      "v_min_u32 v25, 27, v25\n\t"
      "v_mul_u32_u24 v25, 896, v25\n\t"
      "s_lshr_b32 s50, s44, 1\n\t"
      "s_lshl_b32 s50, s50, 6\n\t"
      "s_waitcnt lgkmcnt(7)\n\t"
      "v_mfma_f32_32x32x16_bf16 v[96:111], v[60:63], v[144:147], 0\n\t"
      "ds_read_b128 v[152:155], v35 offset:0\n\t"
      "ds_read_b64_tr_b16 v[84:85], v37 offset:6144\n\t"
      "ds_read_b64_tr_b16 v[86:87], v37 offset:6912\n\t"
      "v_and_b32 v26, 0x70, v28\n\t"
      "v_xor_b32 v26, s50, v26\n\t"
      "v_mfma_f32_32x32x16_bf16 v[112:127], v[144:147], v[64:67], 0\n\t"
      "ds_read_b64_tr_b16 v[88:89], v37 offset:6208\n\t"
      "ds_read_b64_tr_b16 v[90:91], v37 offset:6976\n\t"
      "ds_read_b64_tr_b16 v[4:5], v38 offset:0\n\t"
      "ds_read_b64_tr_b16 v[6:7], v38 offset:768\n\t"
      "buffer_load_dwordx4 v40, s[12:15], s48 offen offset:2048 lds\n\t"
      "v_add_u32 v48, v25, v26\n\t"
      "v_mfma_f32_32x32x16_bf16 v[128:143], v[144:147], v[68:71], 0\n\t"
      "ds_read_b64_tr_b16 v[92:93], v37 offset:6272\n\t"
      "ds_read_b64_tr_b16 v[94:95], v37 offset:7040\n\t"
      "buffer_load_dwordx4 v41, s[12:15], s48 offen offset:3072 lds\n\t"
      "v_lshrrev_b32 v27, 5, v1\n\t"
      "s_lshl_b32 s50, s44, 2\n\t"
      "s_waitcnt lgkmcnt(9)\n\t"
      "v_mfma_f32_32x32x16_bf16 v[96:111], v[72:75], v[148:151], v[96:111]\n\t"
      "ds_read_b128 v[156:159], v36 offset:0\n\t"
      "ds_read_b64_tr_b16 v[60:61], v37 offset:9216\n\t"
      "ds_read_b64_tr_b16 v[62:63], v37 offset:9984\n\t"
      "s_add_u32 s50, s50, 157696\n\t"
      "v_add_u32 v45, s50, v2\n\t"
      "v_mfma_f32_32x32x16_bf16 v[112:127], v[148:151], v[76:79], v[112:127]\n\t"
      "ds_read_b64_tr_b16 v[64:65], v37 offset:9280\n\t"
      "ds_read_b64_tr_b16 v[66:67], v37 offset:10048\n\t"
      "ds_read_b64_tr_b16 v[8:9], v38 offset:64\n\t"
      "ds_read_b64_tr_b16 v[10:11], v38 offset:832\n\t"
      "s_bfe_u32 s48, s100, 262152\n\t"
      "s_add_u32 m0, s55, 90112\n\t"
      "s_mul_i32 s48, s48, 172032\n\t"
      "s_add_u32 s48, s48, s93\n\t"
      "buffer_load_dwordx4 v30, s[16:19], s48 offen offset:0 lds\n\t"
      "v_mfma_f32_32x32x16_bf16 v[128:143], v[148:151], v[80:83], v[128:143]\n\t"
      "ds_read_b64_tr_b16 v[68:69], v37 offset:9344\n\t"
      "ds_read_b64_tr_b16 v[70:71], v37 offset:10112\n\t"
      "buffer_load_dwordx4 v31, s[16:19], s48 offen offset:1024 lds\n\t"
      "v_add_u32 v46, 157696, v59\n\t"
      "v_sub_u32 v27, 0, v27\n\t"
      "s_waitcnt vmcnt(7)\n\t"
      "s_barrier\n\t"
      "s_waitcnt lgkmcnt(9)\n\t"
      "v_mfma_f32_32x32x16_bf16 v[96:111], v[84:87], v[152:155], v[96:111]\n\t"
      "ds_read_b128 v[160:163], v33 offset:16384\n\t"
      "ds_read_b64_tr_b16 v[72:73], v37 offset:12288\n\t"
      "ds_read_b64_tr_b16 v[74:75], v37 offset:13056\n\t"
      "v_lshrrev_b32 v24, 2, v1\n\t"
      "v_min_u32 v24, 4, v24\n\t"
      "v_mfma_f32_32x32x16_bf16 v[112:127], v[152:155], v[88:91], v[112:127]\n\t"
      "ds_read_b64_tr_b16 v[76:77], v37 offset:12352\n\t"
      "ds_read_b64_tr_b16 v[78:79], v37 offset:13120\n\t"
      "ds_read_b64_tr_b16 v[12:13], v38 offset:128\n\t"
      "ds_read_b64_tr_b16 v[14:15], v38 offset:896\n\t"
      "buffer_load_dwordx4 v32, s[16:19], s48 offen offset:2048 lds\n\t"
      "v_mul_u32_u24 v24, 4352, v24\n\t"
      "v_mfma_f32_32x32x16_bf16 v[128:143], v[152:155], v[92:95], v[128:143]\n\t"
      "ds_read_b64_tr_b16 v[80:81], v37 offset:12416\n\t"
      "ds_read_b64_tr_b16 v[82:83], v37 offset:13184\n\t"
      "s_bfe_u32 s48, s100, 262156\n\t"
      "s_add_u32 m0, s54, 49152\n\t"
      "s_mul_i32 s48, s48, 128\n\t"
      "s_add_u32 s48, s48, s51\n\t"
      "buffer_load_dwordx4 v28, s[12:15], s48 offen offset:0 lds\n\t"
      "s_waitcnt lgkmcnt(9)\n\t"
      "v_mfma_f32_32x32x16_bf16 v[96:111], v[60:63], v[156:159], v[96:111]\n\t"
      "ds_read_b128 v[164:167], v34 offset:16384\n\t"
      "ds_read_b64_tr_b16 v[84:85], v37 offset:15360\n\t"
      "ds_read_b64_tr_b16 v[86:87], v37 offset:16128\n\t"
      "v_and_b32 v25, 3, v1\n\t"
      "v_lshl_add_u32 v24, v25, 2, v24\n\t"
      "v_mfma_f32_32x32x16_bf16 v[112:127], v[156:159], v[64:67], v[112:127]\n\t"
      "ds_read_b64_tr_b16 v[88:89], v37 offset:15424\n\t"
      "ds_read_b64_tr_b16 v[90:91], v37 offset:16192\n\t"
      "buffer_load_dwordx4 v29, s[12:15], s48 offen offset:1024 lds\n\t"
      "v_add_u32 v24, 131072, v24\n\t"
      "ds_write_b32 v24, v39 offset:4096\n\t"
      "s_lshl_b32 s50, s45, 4\n\t"
      "v_mfma_f32_32x32x16_bf16 v[128:143], v[156:159], v[68:71], v[128:143]\n\t"
      "ds_read_b64_tr_b16 v[92:93], v37 offset:15488\n\t"
      "ds_read_b64_tr_b16 v[94:95], v37 offset:16256\n\t"
      "buffer_load_dwordx4 v40, s[12:15], s48 offen offset:2048 lds\n\t"
      "s_add_u32 s50, s50, s46\n\t"
      "s_lshl_b32 s50, s50, 5\n\t"
      "buffer_load_dwordx4 v41, s[12:15], s48 offen offset:3072 lds\n\t"
      "s_waitcnt lgkmcnt(10)\n\t"
      "v_dot2c_f32_bf16 v42, v4, v39\n\t"
      "v_dot2c_f32_bf16 v43, v8, v39\n\t"
      "v_dot2c_f32_bf16 v44, v12, v39\n\t"
      "v_dot2c_f32_bf16 v42, v5, v39\n\t"
      "v_dot2c_f32_bf16 v43, v9, v39\n\t"
      "v_dot2c_f32_bf16 v44, v13, v39\n\t"
      "v_dot2c_f32_bf16 v42, v6, v39\n\t"
      "v_dot2c_f32_bf16 v43, v10, v39\n\t"
      "v_dot2c_f32_bf16 v44, v14, v39\n\t"
      "v_dot2c_f32_bf16 v42, v7, v39\n\t"
      "v_dot2c_f32_bf16 v43, v11, v39\n\t"
      "v_dot2c_f32_bf16 v44, v15, v39\n\t"
      "s_waitcnt lgkmcnt(8)\n\t"
      "v_mfma_f32_32x32x16_bf16 v[96:111], v[72:75], v[160:163], v[96:111]\n\t"
      "ds_read_b128 v[168:171], v35 offset:16384\n\t"
      "ds_read_b64_tr_b16 v[60:61], v37 offset:18432\n\t"
      "ds_read_b64_tr_b16 v[62:63], v37 offset:19200\n\t"
      "s_add_u32 s50, s50, s44\n\t"
      "s_lshl_b32 s50, s50, 2\n\t"
      "v_mfma_f32_32x32x16_bf16 v[112:127], v[160:163], v[76:79], v[112:127]\n\t"
      "ds_read_b64_tr_b16 v[64:65], v37 offset:18496\n\t"
      "ds_read_b64_tr_b16 v[66:67], v37 offset:19264\n\t"
      "ds_read_b64_tr_b16 v[4:5], v38 offset:12288\n\t"
      "ds_read_b64_tr_b16 v[6:7], v38 offset:13056\n\t"
      "s_bfe_u32 s48, s100, 262156\n\t"
      "s_add_u32 m0, s55, 102400\n\t"
      "s_mul_i32 s48, s48, 172032\n\t"
      "s_add_u32 s48, s48, s93\n\t"
      "buffer_load_dwordx4 v30, s[16:19], s48 offen offset:0 lds\n\t"
      "v_mfma_f32_32x32x16_bf16 v[128:143], v[160:163], v[80:83], v[128:143]\n\t"
      "ds_read_b64_tr_b16 v[68:69], v37 offset:18560\n\t"
      "ds_read_b64_tr_b16 v[70:71], v37 offset:19328\n\t"
      "buffer_load_dwordx4 v31, s[16:19], s48 offen offset:1024 lds\n\t"
      "s_add_u32 s50, s50, 262144\n\t"
      "v_lshl_add_u32 v49, v1, 15, s50\n\t"
      "s_waitcnt lgkmcnt(9)\n\t"
      "v_mfma_f32_32x32x16_bf16 v[96:111], v[84:87], v[164:167], v[96:111]\n\t"
      "ds_read_b128 v[172:175], v36 offset:16384\n\t"
      "ds_read_b64_tr_b16 v[72:73], v37 offset:21504\n\t"
      "ds_read_b64_tr_b16 v[74:75], v37 offset:22272\n\t"
      "s_lshl_b32 s57, s59, 10\n\t"
      "s_add_u32 s57, s57, 524288\n\t"
      "v_mfma_f32_32x32x16_bf16 v[112:127], v[164:167], v[88:91], v[112:127]\n\t"
      "ds_read_b64_tr_b16 v[76:77], v37 offset:21568\n\t"
      "ds_read_b64_tr_b16 v[78:79], v37 offset:22336\n\t"
      "ds_read_b64_tr_b16 v[8:9], v38 offset:12352\n\t"
      "ds_read_b64_tr_b16 v[10:11], v38 offset:13120\n\t"
      "buffer_load_dwordx4 v32, s[16:19], s48 offen offset:2048 lds\n\t"
      "s_lshl_b32 s49, s59, 12\n\t"
      "v_mfma_f32_32x32x16_bf16 v[128:143], v[164:167], v[92:95], v[128:143]\n\t"
      "ds_read_b64_tr_b16 v[80:81], v37 offset:21632\n\t"
      "ds_read_b64_tr_b16 v[82:83], v37 offset:22400\n\t"
      "s_bfe_u32 s48, s100, 262160\n\t"
      "s_add_u32 m0, s54, 0\n\t"
      "s_mul_i32 s48, s48, 128\n\t"
      "s_add_u32 s48, s48, s51\n\t"
      "buffer_load_dwordx4 v28, s[12:15], s48 offen offset:0 lds\n\t"
      "s_waitcnt vmcnt(8)\n\t"
      "s_barrier\n\t"
      "s_waitcnt lgkmcnt(9)\n\t"
      "v_mfma_f32_32x32x16_bf16 v[96:111], v[60:63], v[168:171], v[96:111]\n\t"
      "ds_read_b128 v[176:179], v33 offset:32768\n\t"
      "ds_read_b64_tr_b16 v[84:85], v37 offset:24576\n\t"
      "ds_read_b64_tr_b16 v[86:87], v37 offset:25344\n\t"
      "v_lshrrev_b32 v24, 1, v2\n\t"
      "v_xor_b32 v24, v24, v2\n\t"
      "v_mfma_f32_32x32x16_bf16 v[112:127], v[168:171], v[64:67], v[112:127]\n\t"
      "ds_read_b64_tr_b16 v[88:89], v37 offset:24640\n\t"
      "ds_read_b64_tr_b16 v[90:91], v37 offset:25408\n\t"
      "ds_read_b64_tr_b16 v[12:13], v38 offset:12416\n\t"
      "ds_read_b64_tr_b16 v[14:15], v38 offset:13184\n\t"
      "buffer_load_dwordx4 v29, s[12:15], s48 offen offset:1024 lds\n\t"
      "v_and_b32 v24, 64, v24\n\t"
      "v_mfma_f32_32x32x16_bf16 v[128:143], v[168:171], v[68:71], v[128:143]\n\t"
      "ds_read_b64_tr_b16 v[92:93], v37 offset:24704\n\t"
      "ds_read_b64_tr_b16 v[94:95], v37 offset:25472\n\t"
      "buffer_load_dwordx4 v40, s[12:15], s48 offen offset:2048 lds\n\t"
      "v_lshl_or_b32 v24, v24, 1, v24\n\t"
      "v_xor_b32 v26, v24, v2\n\t"
      "s_waitcnt lgkmcnt(9)\n\t"
      "v_mfma_f32_32x32x16_bf16 v[96:111], v[72:75], v[172:175], v[96:111]\n\t"
      "ds_read_b128 v[180:183], v34 offset:32768\n\t"
      "ds_read_b64_tr_b16 v[60:61], v37 offset:27648\n\t"
      "ds_read_b64_tr_b16 v[62:63], v37 offset:28416\n\t"
      "v_and_b32 v24, s65, v27\n\t"
      "v_add3_u32 v50, v24, v59, s64\n\t"
      "v_mfma_f32_32x32x16_bf16 v[112:127], v[172:175], v[76:79], v[112:127]\n\t"
      "ds_read_b64_tr_b16 v[64:65], v37 offset:27712\n\t"
      "ds_read_b64_tr_b16 v[66:67], v37 offset:28480\n\t"
      "buffer_load_dwordx4 v41, s[12:15], s48 offen offset:3072 lds\n\t"
      "v_and_b32 v24, s67, v27\n\t"
      "v_add3_u32 v51, v24, v59, s66\n\t"
      "v_and_b32 v24, s69, v27\n\t"
      "v_mfma_f32_32x32x16_bf16 v[128:143], v[172:175], v[80:83], v[128:143]\n\t"
      "ds_read_b64_tr_b16 v[68:69], v37 offset:27776\n\t"
      "ds_read_b64_tr_b16 v[70:71], v37 offset:28544\n\t"
      "s_bfe_u32 s48, s100, 262160\n\t"
      "s_add_u32 m0, s55, 114688\n\t"
      "s_mul_i32 s48, s48, 172032\n\t"
      "s_add_u32 s48, s48, s93\n\t"
      "buffer_load_dwordx4 v30, s[16:19], s48 offen offset:0 lds\n\t"
      "buffer_load_dwordx4 v31, s[16:19], s48 offen offset:1024 lds\n\t"
      "s_waitcnt lgkmcnt(9)\n\t"
      "v_dot2c_f32_bf16 v42, v4, v39\n\t"
      "v_dot2c_f32_bf16 v43, v8, v39\n\t"
      "v_dot2c_f32_bf16 v44, v12, v39\n\t"
      "v_dot2c_f32_bf16 v42, v5, v39\n\t"
      "v_dot2c_f32_bf16 v43, v9, v39\n\t"
      "v_dot2c_f32_bf16 v44, v13, v39\n\t"
      "v_dot2c_f32_bf16 v42, v6, v39\n\t"
      "v_dot2c_f32_bf16 v43, v10, v39\n\t"
      "v_dot2c_f32_bf16 v44, v14, v39\n\t"
      "v_dot2c_f32_bf16 v42, v7, v39\n\t"
      "v_dot2c_f32_bf16 v43, v11, v39\n\t"
      "v_dot2c_f32_bf16 v44, v15, v39\n\t"
      "s_waitcnt lgkmcnt(7)\n\t"
      "v_mfma_f32_32x32x16_bf16 v[96:111], v[84:87], v[176:179], v[96:111]\n\t"
      "ds_read_b128 v[184:187], v35 offset:32768\n\t"
      "ds_read_b64_tr_b16 v[72:73], v37 offset:30720\n\t"
      "ds_read_b64_tr_b16 v[74:75], v37 offset:31488\n\t"
      "v_add3_u32 v52, v24, v59, s68\n\t"
      "v_mfma_f32_32x32x16_bf16 v[112:127], v[176:179], v[88:91], v[112:127]\n\t"
      "ds_read_b64_tr_b16 v[76:77], v37 offset:30784\n\t"
      "ds_read_b64_tr_b16 v[78:79], v37 offset:31552\n\t"
      "ds_read_b64_tr_b16 v[4:5], v38 offset:24576\n\t"
      "ds_read_b64_tr_b16 v[6:7], v38 offset:25344\n\t"
      "buffer_load_dwordx4 v32, s[16:19], s48 offen offset:2048 lds\n\t"
      "v_mfma_f32_32x32x16_bf16 v[128:143], v[176:179], v[92:95], v[128:143]\n\t"
      "ds_read_b64_tr_b16 v[80:81], v37 offset:30848\n\t"
      "ds_read_b64_tr_b16 v[82:83], v37 offset:31616\n\t"
      "s_bfe_u32 s48, s100, 262164\n\t"
      "s_add_u32 m0, s54, 16384\n\t"
      "s_mul_i32 s48, s48, 128\n\t"
      "s_add_u32 s48, s48, s51\n\t"
      "buffer_load_dwordx4 v28, s[12:15], s48 offen offset:0 lds\n\t"
      "s_waitcnt lgkmcnt(9)\n\t"
      "v_mfma_f32_32x32x16_bf16 v[96:111], v[60:63], v[180:183], v[96:111]\n\t"
      "ds_read_b128 v[188:191], v36 offset:32768\n\t"
      "ds_read_b64_tr_b16 v[84:85], v37 offset:33792\n\t"
      "ds_read_b64_tr_b16 v[86:87], v37 offset:34560\n\t"
      "s_cmp_eq_u32 s70, 0\n\t"
      "s_cselect_b32 s65, 11, 12\n\t"
      "s_cselect_b32 s66, 0x3e8b97f6, 1.0\n\t"
      "v_mfma_f32_32x32x16_bf16 v[112:127], v[180:183], v[64:67], v[112:127]\n\t"
      "ds_read_b64_tr_b16 v[88:89], v37 offset:33856\n\t"
      "ds_read_b64_tr_b16 v[90:91], v37 offset:34624\n\t"
      "ds_read_b64_tr_b16 v[8:9], v38 offset:24640\n\t"
      "ds_read_b64_tr_b16 v[10:11], v38 offset:25408\n\t"
      "buffer_load_dwordx4 v29, s[12:15], s48 offen offset:1024 lds\n\t"
      "s_lshl_b32 s65, s59, s65\n\t"
      "v_mfma_f32_32x32x16_bf16 v[128:143], v[180:183], v[68:71], v[128:143]\n\t"
      "ds_read_b64_tr_b16 v[92:93], v37 offset:33920\n\t"
      "ds_read_b64_tr_b16 v[94:95], v37 offset:34688\n\t"
      "buffer_load_dwordx4 v40, s[12:15], s48 offen offset:2048 lds\n\t"
      "v_add_u32 v53, s71, v26\n\t"
      "s_waitcnt vmcnt(10)\n\t"
      "s_barrier\n\t"
      "s_waitcnt lgkmcnt(9)\n\t"
      "v_mfma_f32_32x32x16_bf16 v[96:111], v[72:75], v[184:187], v[96:111]\n\t"
      "ds_read_b128 v[192:195], v33 offset:49152\n\t"
      "ds_read_b64_tr_b16 v[60:61], v37 offset:36864\n\t"
      "ds_read_b64_tr_b16 v[62:63], v37 offset:37632\n\t"
      "s_mov_b64 exec, s[72:73]\n\t"
      "v_add_u32 v53, 261696, v53\n\t"
      "s_mov_b64 exec, -1\n\t"
      "v_mfma_f32_32x32x16_bf16 v[112:127], v[184:187], v[76:79], v[112:127]\n\t"
      "ds_read_b64_tr_b16 v[64:65], v37 offset:36928\n\t"
      "ds_read_b64_tr_b16 v[66:67], v37 offset:37696\n\t"
      "ds_read_b64_tr_b16 v[12:13], v38 offset:24704\n\t"
      "ds_read_b64_tr_b16 v[14:15], v38 offset:25472\n\t"
      "buffer_load_dwordx4 v41, s[12:15], s48 offen offset:3072 lds\n\t"
      "v_mfma_f32_32x32x16_bf16 v[128:143], v[184:187], v[80:83], v[128:143]\n\t"
      "ds_read_b64_tr_b16 v[68:69], v37 offset:36992\n\t"
      "ds_read_b64_tr_b16 v[70:71], v37 offset:37760\n\t"
      "s_bfe_u32 s48, s100, 262164\n\t"
      "s_add_u32 m0, s55, 65536\n\t"
      "s_mul_i32 s48, s48, 172032\n\t"
      "s_add_u32 s48, s48, s93\n\t"
      "buffer_load_dwordx4 v30, s[16:19], s48 offen offset:0 lds\n\t"
      "s_waitcnt lgkmcnt(9)\n\t"
      "v_mfma_f32_32x32x16_bf16 v[96:111], v[84:87], v[188:191], v[96:111]\n\t"
      "ds_read_b128 v[196:199], v34 offset:49152\n\t"
      "ds_read_b64_tr_b16 v[72:73], v37 offset:39936\n\t"
      "ds_read_b64_tr_b16 v[74:75], v37 offset:40704\n\t"
      "s_mov_b64 exec, s[74:75]\n\t"
      "v_add_u32 v53, 261696, v53\n\t"
      "s_mov_b64 exec, -1\n\t"
      "v_mfma_f32_32x32x16_bf16 v[112:127], v[188:191], v[88:91], v[112:127]\n\t"
      "ds_read_b64_tr_b16 v[76:77], v37 offset:40000\n\t"
      "ds_read_b64_tr_b16 v[78:79], v37 offset:40768\n\t"
      "buffer_load_dwordx4 v31, s[16:19], s48 offen offset:1024 lds\n\t"
      "v_cndmask_b32 v16, 0, v39, s[78:79]\n\t"
      "v_cndmask_b32 v17, 0, v39, s[78:79]\n\t"
      "v_cndmask_b32 v18, 0, v39, s[78:79]\n\t"
      "v_mfma_f32_32x32x16_bf16 v[128:143], v[188:191], v[92:95], v[128:143]\n\t"
      "ds_read_b64_tr_b16 v[80:81], v37 offset:40064\n\t"
      "ds_read_b64_tr_b16 v[82:83], v37 offset:40832\n\t"
      "buffer_load_dwordx4 v32, s[16:19], s48 offen offset:2048 lds\n\t"
      "v_cndmask_b32 v19, 0, v39, s[78:79]\n\t"
      "s_waitcnt lgkmcnt(9)\n\t"
      "v_dot2c_f32_bf16 v42, v4, v39\n\t"
      "v_dot2c_f32_bf16 v43, v8, v39\n\t"
      "v_dot2c_f32_bf16 v44, v12, v39\n\t"
      "v_dot2c_f32_bf16 v42, v5, v39\n\t"
      "v_dot2c_f32_bf16 v43, v9, v39\n\t"
      "v_dot2c_f32_bf16 v44, v13, v39\n\t"
      "v_dot2c_f32_bf16 v42, v6, v39\n\t"
      "v_dot2c_f32_bf16 v43, v10, v39\n\t"
      "v_dot2c_f32_bf16 v44, v14, v39\n\t"
      "v_dot2c_f32_bf16 v42, v7, v39\n\t"
      "v_dot2c_f32_bf16 v43, v11, v39\n\t"
      "v_dot2c_f32_bf16 v44, v15, v39\n\t"
      "s_waitcnt lgkmcnt(7)\n\t"
      "v_mfma_f32_32x32x16_bf16 v[96:111], v[60:63], v[192:195], v[96:111]\n\t"
      "ds_read_b128 v[200:203], v35 offset:49152\n\t"
      "ds_read_b64_tr_b16 v[84:85], v37 offset:43008\n\t"
      "ds_read_b64_tr_b16 v[86:87], v37 offset:43776\n\t"
      "s_mov_b64 exec, s[76:77]\n\t"
      "buffer_store_dwordx4 v[16:19], v53, s[28:31], s49 offen offset:64 sc1\n\t"
      "s_mov_b64 exec, -1\n\t"
      "v_mfma_f32_32x32x16_bf16 v[112:127], v[192:195], v[64:67], v[112:127]\n\t"
      "ds_read_b64_tr_b16 v[88:89], v37 offset:43072\n\t"
      "ds_read_b64_tr_b16 v[90:91], v37 offset:43840\n\t"
      "ds_read_b64_tr_b16 v[4:5], v38 offset:36864\n\t"
      "ds_read_b64_tr_b16 v[6:7], v38 offset:37632\n\t"
      "s_bfe_u32 s48, s100, 262168\n\t"
      "s_add_u32 m0, s54, 32768\n\t"
      "s_mul_i32 s48, s48, 128\n\t"
      "s_add_u32 s48, s48, s51\n\t"
      "buffer_load_dwordx4 v28, s[12:15], s48 offen offset:0 lds\n\t"
      "v_mfma_f32_32x32x16_bf16 v[128:143], v[192:195], v[68:71], v[128:143]\n\t"
      "ds_read_b64_tr_b16 v[92:93], v37 offset:43136\n\t"
      "ds_read_b64_tr_b16 v[94:95], v37 offset:43904\n\t"
      "buffer_load_dwordx4 v29, s[12:15], s48 offen offset:1024 lds\n\t"
      "s_mov_b64 exec, s[76:77]\n\t"
      "buffer_store_dwordx4 v[16:19], v53, s[28:31], s49 offen offset:1088 sc1\n\t"
      "s_mov_b64 exec, -1\n\t"
      "s_waitcnt lgkmcnt(9)\n\t"
      "v_mfma_f32_32x32x16_bf16 v[96:111], v[72:75], v[196:199], v[96:111]\n\t"
      "ds_read_b128 v[204:207], v36 offset:49152\n\t"
      "ds_read_b64_tr_b16 v[60:61], v37 offset:46080\n\t"
      "ds_read_b64_tr_b16 v[62:63], v37 offset:46848\n\t"
      "v_add_u32 v56, s90, v26\n\t"
      "v_mfma_f32_32x32x16_bf16 v[112:127], v[196:199], v[76:79], v[112:127]\n\t"
      "ds_read_b64_tr_b16 v[64:65], v37 offset:46144\n\t"
      "ds_read_b64_tr_b16 v[66:67], v37 offset:46912\n\t"
      "ds_read_b64_tr_b16 v[8:9], v38 offset:36928\n\t"
      "ds_read_b64_tr_b16 v[10:11], v38 offset:37696\n\t"
      "buffer_load_dwordx4 v40, s[12:15], s48 offen offset:2048 lds\n\t"
      "v_mfma_f32_32x32x16_bf16 v[128:143], v[196:199], v[80:83], v[128:143]\n\t"
      "ds_read_b64_tr_b16 v[68:69], v37 offset:46208\n\t"
      "ds_read_b64_tr_b16 v[70:71], v37 offset:46976\n\t"
      "buffer_load_dwordx4 v41, s[12:15], s48 offen offset:3072 lds\n\t"
      "s_mov_b64 exec, s[82:83]\n\t"
      "v_add_u32 v56, 261696, v56\n\t"
      "s_mov_b64 exec, -1\n\t"
      "s_waitcnt vmcnt(11)\n\t"
      "s_barrier\n\t"
      "s_waitcnt lgkmcnt(9)\n\t"
      "v_mfma_f32_32x32x16_bf16 v[96:111], v[84:87], v[200:203], v[96:111]\n\t"
      "ds_read_b128 v[208:211], v33 offset:0\n\t"
      "ds_read_b64_tr_b16 v[72:73], v37 offset:49152\n\t"
      "ds_read_b64_tr_b16 v[74:75], v37 offset:49920\n\t"
      "s_mov_b64 exec, s[84:85]\n\t"
      "v_add_u32 v56, 261696, v56\n\t"
      "s_mov_b64 exec, -1\n\t"
      "v_mfma_f32_32x32x16_bf16 v[112:127], v[200:203], v[88:91], v[112:127]\n\t"
      "ds_read_b64_tr_b16 v[76:77], v37 offset:49216\n\t"
      "ds_read_b64_tr_b16 v[78:79], v37 offset:49984\n\t"
      "ds_read_b64_tr_b16 v[12:13], v38 offset:36992\n\t"
      "ds_read_b64_tr_b16 v[14:15], v38 offset:37760\n\t"
      "s_bfe_u32 s48, s100, 262168\n\t"
      "s_add_u32 m0, s55, 77824\n\t"
      "s_mul_i32 s48, s48, 172032\n\t"
      "s_add_u32 s48, s48, s93\n\t"
      "buffer_load_dwordx4 v30, s[16:19], s48 offen offset:0 lds\n\t"
      "v_mfma_f32_32x32x16_bf16 v[128:143], v[200:203], v[92:95], v[128:143]\n\t"
      "ds_read_b64_tr_b16 v[80:81], v37 offset:49280\n\t"
      "ds_read_b64_tr_b16 v[82:83], v37 offset:50048\n\t"
      "buffer_load_dwordx4 v31, s[16:19], s48 offen offset:1024 lds\n\t"
      "v_cndmask_b32 v16, 0, v39, s[88:89]\n\t"
      "v_cndmask_b32 v17, 0, v39, s[88:89]\n\t"
      "s_waitcnt lgkmcnt(9)\n\t"
      "v_mfma_f32_32x32x16_bf16 v[96:111], v[60:63], v[204:207], v[96:111]\n\t"
      "ds_read_b128 v[212:215], v34 offset:0\n\t"
      "ds_read_b64_tr_b16 v[84:85], v37 offset:52224\n\t"
      "ds_read_b64_tr_b16 v[86:87], v37 offset:52992\n\t"
      "v_cndmask_b32 v18, 0, v39, s[88:89]\n\t"
      "v_cndmask_b32 v19, 0, v39, s[88:89]\n\t"
      "v_mfma_f32_32x32x16_bf16 v[112:127], v[204:207], v[64:67], v[112:127]\n\t"
      "ds_read_b64_tr_b16 v[88:89], v37 offset:52288\n\t"
      "ds_read_b64_tr_b16 v[90:91], v37 offset:53056\n\t"
      "buffer_load_dwordx4 v32, s[16:19], s48 offen offset:2048 lds\n\t"
      "s_mov_b64 exec, s[86:87]\n\t"
      "buffer_store_dwordx4 v[16:19], v56, s[28:31], s49 offen offset:64 sc1\n\t"
      "s_mov_b64 exec, -1\n\t"
      "v_mfma_f32_32x32x16_bf16 v[128:143], v[204:207], v[68:71], v[128:143]\n\t"
      "ds_read_b64_tr_b16 v[92:93], v37 offset:52352\n\t"
      "ds_read_b64_tr_b16 v[94:95], v37 offset:53120\n\t"
      "s_mov_b64 exec, s[86:87]\n\t"
      "buffer_store_dwordx4 v[16:19], v56, s[28:31], s49 offen offset:1088 sc1\n\t"
      "s_mov_b64 exec, -1\n\t"
      "s_waitcnt lgkmcnt(9)\n\t"
      "v_dot2c_f32_bf16 v42, v4, v39\n\t"
      "v_dot2c_f32_bf16 v43, v8, v39\n\t"
      "v_dot2c_f32_bf16 v44, v12, v39\n\t"
      "v_dot2c_f32_bf16 v42, v5, v39\n\t"
      "v_dot2c_f32_bf16 v43, v9, v39\n\t"
      "v_dot2c_f32_bf16 v44, v13, v39\n\t"
      "v_dot2c_f32_bf16 v42, v6, v39\n\t"
      "v_dot2c_f32_bf16 v43, v10, v39\n\t"
      "v_dot2c_f32_bf16 v44, v14, v39\n\t"
      "v_dot2c_f32_bf16 v42, v7, v39\n\t"
      "v_dot2c_f32_bf16 v43, v11, v39\n\t"
      "v_dot2c_f32_bf16 v44, v15, v39\n\t"
      "s_waitcnt lgkmcnt(7)\n\t"
      "v_mfma_f32_32x32x16_bf16 v[96:111], v[72:75], v[208:211], v[96:111]\n\t"
      "ds_read_b128 v[216:219], v35 offset:0\n\t"
      "ds_read_b64_tr_b16 v[60:61], v37 offset:55296\n\t"
      "ds_read_b64_tr_b16 v[62:63], v37 offset:56064\n\t"
      "v_bfe_u32 v24, v1, 2, 2\n\t"
      "v_mul_u32_u24 v28, 320, v24\n\t"
      "v_mfma_f32_32x32x16_bf16 v[112:127], v[208:211], v[76:79], v[112:127]\n\t"
      "ds_read_b64_tr_b16 v[64:65], v37 offset:55360\n\t"
      "ds_read_b64_tr_b16 v[66:67], v37 offset:56128\n\t"
      "ds_read_b64_tr_b16 v[4:5], v38 offset:49152\n\t"
      "ds_read_b64_tr_b16 v[6:7], v38 offset:49920\n\t"
      "v_add_u32 v40, 13312, v47\n\t"
      "v_add_u32 v41, 26624, v47\n\t"
      "v_add_u32 %[tid], 39936, v47\n\t"
      "s_bfe_u32 s48, s100, 262144\n\t"
      "s_add_u32 m0, s54, 49152\n\t"
      "s_mul_i32 s48, s48, 229376\n\t"
      "s_add_u32 s48, s48, s52\n\t"
      "buffer_load_dwordx4 v47, s[20:23], s48 offen offset:0 lds\n\t"
      "v_mfma_f32_32x32x16_bf16 v[128:143], v[208:211], v[80:83], v[128:143]\n\t"
      "ds_read_b64_tr_b16 v[68:69], v37 offset:55424\n\t"
      "ds_read_b64_tr_b16 v[70:71], v37 offset:56192\n\t"
      "buffer_load_dwordx4 v40, s[20:23], s48 offen offset:1024 lds\n\t"
      "v_and_b32 v24, 0x800, v27\n\t"
      "v_bfe_u32 v25, v1, 4, 1\n\t"
      "s_waitcnt lgkmcnt(9)\n\t"
      "v_mfma_f32_32x32x16_bf16 v[96:111], v[84:87], v[212:215], v[96:111]\n\t"
      "ds_read_b128 v[220:223], v36 offset:0\n\t"
      "ds_read_b64_tr_b16 v[72:73], v37 offset:58368\n\t"
      "ds_read_b64_tr_b16 v[74:75], v37 offset:59136\n\t"
      "v_lshl_add_u32 v24, v25, 5, v24\n\t"
      "v_and_b32 v25, 3, v1\n\t"
      "v_mfma_f32_32x32x16_bf16 v[112:127], v[212:215], v[88:91], v[112:127]\n\t"
      "ds_read_b64_tr_b16 v[76:77], v37 offset:58432\n\t"
      "ds_read_b64_tr_b16 v[78:79], v37 offset:59200\n\t"
      "ds_read_b64_tr_b16 v[8:9], v38 offset:49216\n\t"
      "ds_read_b64_tr_b16 v[10:11], v38 offset:49984\n\t"
      "buffer_load_dwordx4 v41, s[20:23], s48 offen offset:2048 lds\n\t"
      "v_lshl_add_u32 v24, v25, 3, v24\n\t"
      "v_mfma_f32_32x32x16_bf16 v[128:143], v[212:215], v[92:95], v[128:143]\n\t"
      "ds_read_b64_tr_b16 v[80:81], v37 offset:58496\n\t"
      "ds_read_b64_tr_b16 v[82:83], v37 offset:59264\n\t"
      "buffer_load_dwordx4 %[tid], s[20:23], s48 offen offset:3072 lds\n\t"
      "v_add_u32 v28, v28, v24\n\t"
      "v_xor_b32 v29, 64, v28\n\t"
      "s_waitcnt vmcnt(11)\n\t"
      "s_barrier\n\t"
      "s_waitcnt lgkmcnt(9)\n\t"
      "v_mfma_f32_32x32x16_bf16 v[96:111], v[60:63], v[216:219], v[96:111]\n\t"
      "ds_read_b128 v[224:227], v33 offset:16384\n\t"
      "ds_read_b64_tr_b16 v[84:85], v37 offset:0\n\t"
      "ds_read_b64_tr_b16 v[86:87], v37 offset:768\n\t"
      "v_xor_b32 v30, 0x80, v28\n\t"
      "v_xor_b32 v31, 0xc0, v28\n\t"
      "v_mfma_f32_32x32x16_bf16 v[112:127], v[216:219], v[64:67], v[112:127]\n\t"
      "ds_read_b64_tr_b16 v[88:89], v37 offset:64\n\t"
      "ds_read_b64_tr_b16 v[90:91], v37 offset:832\n\t"
      "ds_read_b64_tr_b16 v[12:13], v38 offset:49280\n\t"
      "ds_read_b64_tr_b16 v[14:15], v38 offset:50048\n\t"
      "s_bfe_u32 s48, s100, 262144\n\t"
      "s_add_u32 m0, s56, 131072\n\t"
      "s_mul_i32 s48, s48, 128\n\t"
      "s_add_u32 s48, s48, s53\n\t"
      "buffer_load_dwordx4 v48, s[24:27], s48 offen offset:0 lds\n\t"
      "v_mfma_f32_32x32x16_bf16 v[128:143], v[216:219], v[68:71], v[128:143]\n\t"
      "ds_read_b64_tr_b16 v[92:93], v37 offset:128\n\t"
      "ds_read_b64_tr_b16 v[94:95], v37 offset:896\n\t"
      "s_lshl_b32 s50, s44, 6\n\t"
      "v_xor_b32 v32, s50, v28\n\t"
      "v_accvgpr_write_b32 a170, 0\n\t"
      "v_accvgpr_write_b32 a171, 0\n\t"
      "s_waitcnt lgkmcnt(9)\n\t"
      "v_mfma_f32_32x32x16_bf16 v[96:111], v[72:75], v[220:223], v[96:111]\n\t"
      "ds_read_b128 v[228:231], v34 offset:16384\n\t"
      "ds_read_b64_tr_b16 v[60:61], v37 offset:3072\n\t"
      "ds_read_b64_tr_b16 v[62:63], v37 offset:3840\n\t"
      "v_mfma_f32_32x32x16_bf16 v[112:127], v[220:223], v[76:79], v[112:127]\n\t"
      "ds_read_b64_tr_b16 v[64:65], v37 offset:3136\n\t"
      "ds_read_b64_tr_b16 v[66:67], v37 offset:3904\n\t"
      "v_mfma_f32_32x32x16_bf16 v[128:143], v[220:223], v[80:83], v[128:143]\n\t"
      "ds_read_b64_tr_b16 v[68:69], v37 offset:3200\n\t"
      "ds_read_b64_tr_b16 v[70:71], v37 offset:3968\n\t"
      "buffer_load_dwordx2 v[20:21], v2, s[28:31], s57 offen sc1\n\t"
      "buffer_load_dwordx2 v[22:23], v2, s[28:31], s57 offen offset:8 sc1\n\t"
      "s_waitcnt lgkmcnt(9)\n\t"
      "v_dot2c_f32_bf16 v42, v4, v39\n\t"
      "v_dot2c_f32_bf16 v43, v8, v39\n\t"
      "v_dot2c_f32_bf16 v44, v12, v39\n\t"
      "v_dot2c_f32_bf16 v42, v5, v39\n\t"
      "v_dot2c_f32_bf16 v43, v9, v39\n\t"
      "v_dot2c_f32_bf16 v44, v13, v39\n\t"
      "v_dot2c_f32_bf16 v42, v6, v39\n\t"
      "v_dot2c_f32_bf16 v43, v10, v39\n\t"
      "v_dot2c_f32_bf16 v44, v14, v39\n\t"
      "v_dot2c_f32_bf16 v42, v7, v39\n\t"
      "v_dot2c_f32_bf16 v43, v11, v39\n\t"
      "v_dot2c_f32_bf16 v44, v15, v39\n\t"
      "s_waitcnt lgkmcnt(7)\n\t"
      "v_mfma_f32_32x32x16_bf16 v[96:111], v[84:87], v[224:227], v[96:111]\n\t"
      "ds_read_b128 v[232:235], v35 offset:16384\n\t"
      "ds_read_b64_tr_b16 v[72:73], v37 offset:6144\n\t"
      "ds_read_b64_tr_b16 v[74:75], v37 offset:6912\n\t"
      "v_mfma_f32_32x32x16_bf16 v[112:127], v[224:227], v[88:91], v[112:127]\n\t"
      "ds_read_b64_tr_b16 v[76:77], v37 offset:6208\n\t"
      "ds_read_b64_tr_b16 v[78:79], v37 offset:6976\n\t"
      "ds_read_b64_tr_b16 v[4:5], v38 offset:0\n\t"
      "ds_read_b64_tr_b16 v[6:7], v38 offset:768\n\t"
      "s_bfe_u32 s48, s100, 262148\n\t"
      "s_add_u32 m0, s54, 0\n\t"
      "s_mul_i32 s48, s48, 229376\n\t"
      "s_add_u32 s48, s48, s52\n\t"
      "buffer_load_dwordx4 v47, s[20:23], s48 offen offset:0 lds\n\t"
      "v_mfma_f32_32x32x16_bf16 v[128:143], v[224:227], v[92:95], v[128:143]\n\t"
      "ds_read_b64_tr_b16 v[80:81], v37 offset:6272\n\t"
      "ds_read_b64_tr_b16 v[82:83], v37 offset:7040\n\t"
      "buffer_load_dwordx4 v40, s[20:23], s48 offen offset:1024 lds\n\t"
      "s_waitcnt lgkmcnt(9)\n\t"
      "v_mfma_f32_32x32x16_bf16 v[96:111], v[60:63], v[228:231], v[96:111]\n\t"
      "ds_read_b128 v[236:239], v36 offset:16384\n\t"
      "ds_read_b64_tr_b16 v[84:85], v37 offset:9216\n\t"
      "ds_read_b64_tr_b16 v[86:87], v37 offset:9984\n\t"
      "v_mfma_f32_32x32x16_bf16 v[112:127], v[228:231], v[64:67], v[112:127]\n\t"
      "ds_read_b64_tr_b16 v[88:89], v37 offset:9280\n\t"
      "ds_read_b64_tr_b16 v[90:91], v37 offset:10048\n\t"
      "ds_read_b64_tr_b16 v[8:9], v38 offset:64\n\t"
      "ds_read_b64_tr_b16 v[10:11], v38 offset:832\n\t"
      "buffer_load_dwordx4 v41, s[20:23], s48 offen offset:2048 lds\n\t"
      "v_mfma_f32_32x32x16_bf16 v[128:143], v[228:231], v[68:71], v[128:143]\n\t"
      "ds_read_b64_tr_b16 v[92:93], v37 offset:9344\n\t"
      "ds_read_b64_tr_b16 v[94:95], v37 offset:10112\n\t"
      "buffer_load_dwordx4 %[tid], s[20:23], s48 offen offset:3072 lds\n\t"
      "s_waitcnt vmcnt(11)\n\t"
      "s_barrier\n\t"
      "s_waitcnt lgkmcnt(9)\n\t"
      "v_mfma_f32_32x32x16_bf16 v[96:111], v[72:75], v[232:235], v[96:111]\n\t"
      "ds_read_b128 v[240:243], v33 offset:32768\n\t"
      "ds_read_b64_tr_b16 v[60:61], v37 offset:12288\n\t"
      "ds_read_b64_tr_b16 v[62:63], v37 offset:13056\n\t"
      "v_mfma_f32_32x32x16_bf16 v[112:127], v[232:235], v[76:79], v[112:127]\n\t"
      "ds_read_b64_tr_b16 v[64:65], v37 offset:12352\n\t"
      "ds_read_b64_tr_b16 v[66:67], v37 offset:13120\n\t"
      "ds_read_b64_tr_b16 v[12:13], v38 offset:128\n\t"
      "ds_read_b64_tr_b16 v[14:15], v38 offset:896\n\t"
      "s_bfe_u32 s48, s100, 262148\n\t"
      "s_add_u32 m0, s56, 135424\n\t"
      "s_mul_i32 s48, s48, 128\n\t"
      "s_add_u32 s48, s48, s53\n\t"
      "buffer_load_dwordx4 v48, s[24:27], s48 offen offset:0 lds\n\t"
      "v_mfma_f32_32x32x16_bf16 v[128:143], v[232:235], v[80:83], v[128:143]\n\t"
      "ds_read_b64_tr_b16 v[68:69], v37 offset:12416\n\t"
      "ds_read_b64_tr_b16 v[70:71], v37 offset:13184\n\t"
      "s_waitcnt lgkmcnt(9)\n\t"
      "v_mfma_f32_32x32x16_bf16 v[96:111], v[84:87], v[236:239], v[96:111]\n\t"
      "ds_read_b128 v[244:247], v34 offset:32768\n\t"
      "ds_read_b64_tr_b16 v[72:73], v37 offset:15360\n\t"
      "ds_read_b64_tr_b16 v[74:75], v37 offset:16128\n\t"
      "v_mfma_f32_32x32x16_bf16 v[112:127], v[236:239], v[88:91], v[112:127]\n\t"
      "ds_read_b64_tr_b16 v[76:77], v37 offset:15424\n\t"
      "ds_read_b64_tr_b16 v[78:79], v37 offset:16192\n\t"
      "v_mfma_f32_32x32x16_bf16 v[128:143], v[236:239], v[92:95], v[128:143]\n\t"
      "ds_read_b64_tr_b16 v[80:81], v37 offset:15488\n\t"
      "ds_read_b64_tr_b16 v[82:83], v37 offset:16256\n\t"
      "s_waitcnt lgkmcnt(9)\n\t"
      "v_dot2c_f32_bf16 v42, v4, v39\n\t"
      "v_dot2c_f32_bf16 v43, v8, v39\n\t"
      "v_dot2c_f32_bf16 v44, v12, v39\n\t"
      "v_dot2c_f32_bf16 v42, v5, v39\n\t"
      "v_dot2c_f32_bf16 v43, v9, v39\n\t"
      "v_dot2c_f32_bf16 v44, v13, v39\n\t"
      "v_dot2c_f32_bf16 v42, v6, v39\n\t"
      "v_dot2c_f32_bf16 v43, v10, v39\n\t"
      "v_dot2c_f32_bf16 v44, v14, v39\n\t"
      "v_dot2c_f32_bf16 v42, v7, v39\n\t"
      "v_dot2c_f32_bf16 v43, v11, v39\n\t"
      "v_dot2c_f32_bf16 v44, v15, v39\n\t"
      "ds_read_b64_tr_b16 v[4:5], v38 offset:12288\n\t"
      "ds_read_b64_tr_b16 v[6:7], v38 offset:13056\n\t"
      "ds_read_b64_tr_b16 v[8:9], v38 offset:12352\n\t"
      "ds_read_b64_tr_b16 v[10:11], v38 offset:13120\n\t"
      "ds_read_b64_tr_b16 v[12:13], v38 offset:12416\n\t"
      "ds_read_b64_tr_b16 v[14:15], v38 offset:13184\n\t"
      "s_waitcnt vmcnt(5)\n\t"
      "v_xor_b32 v21, s47, v21\n\t"
      "v_xor_b32 v23, s47, v23\n\t"
      "v_or_b32 v21, v21, v23\n\t"
      "v_cmp_ne_u32 vcc, 0, v21\n\t"
      "v_permlane32_swap_b32 v20, v22\n\t"
      "s_cbranch_vccnz xpc_%=\n\t"
      "xrc_%=:\n\t"
      "v_mov_b32 v25, 0\n\t"
      "v_add_f32 v20, v20, v22\n\t"
      "v_mov_b32 v18, 0\n\t"
      "v_mov_b32 v22, v20\n\t"
      "s_waitcnt lgkmcnt(0)\n\t"
      "v_dot2c_f32_bf16 v42, v4, v39\n\t"
      "v_dot2c_f32_bf16 v43, v8, v39\n\t"
      "v_dot2c_f32_bf16 v44, v12, v39\n\t"
      "v_dot2c_f32_bf16 v42, v5, v39\n\t"
      "v_dot2c_f32_bf16 v43, v9, v39\n\t"
      "v_dot2c_f32_bf16 v44, v13, v39\n\t"
      "v_dot2c_f32_bf16 v42, v6, v39\n\t"
      "v_dot2c_f32_bf16 v43, v10, v39\n\t"
      "v_dot2c_f32_bf16 v44, v14, v39\n\t"
      "v_dot2c_f32_bf16 v42, v7, v39\n\t"
      "v_dot2c_f32_bf16 v43, v11, v39\n\t"
      "v_dot2c_f32_bf16 v44, v15, v39\n\t"
      "s_waitcnt lgkmcnt(13)\n\t"
      "v_mfma_f32_32x32x16_bf16 v[96:111], v[60:63], v[240:243], v[96:111]\n\t"
      "ds_read_b128 v[248:251], v35 offset:32768\n\t"
      "ds_read_b64_tr_b16 v[84:85], v37 offset:18432\n\t"
      "ds_read_b64_tr_b16 v[86:87], v37 offset:19200\n\t"
      "v_permlane32_swap_b32 v20, v22\n\t"
      "v_mov_b32 v19, 0\n\t"
      "v_permlane32_swap_b32 v42, v43\n\t"
      "v_mfma_f32_32x32x16_bf16 v[112:127], v[240:243], v[64:67], v[112:127]\n\t"
      "ds_read_b64_tr_b16 v[88:89], v37 offset:18496\n\t"
      "ds_read_b64_tr_b16 v[90:91], v37 offset:19264\n\t"
      "s_bfe_u32 s48, s100, 262152\n\t"
      "s_add_u32 m0, s54, 16384\n\t"
      "s_mul_i32 s48, s48, 229376\n\t"
      "s_add_u32 s48, s48, s52\n\t"
      "buffer_load_dwordx4 v47, s[20:23], s48 offen offset:0 lds\n\t"
      "v_mfma_f32_32x32x16_bf16 v[128:143], v[240:243], v[68:71], v[128:143]\n\t"
      "ds_read_b64_tr_b16 v[92:93], v37 offset:18560\n\t"
      "ds_read_b64_tr_b16 v[94:95], v37 offset:19328\n\t"
      "v_permlane32_swap_b32 v44, v25\n\t"
      "v_mul_f32 v20, 0xbb124925, v20\n\t"
      "v_add_f32 v42, v42, v43\n\t"
      "v_add_f32 v44, v44, v25\n\t"
      "s_waitcnt lgkmcnt(13)\n\t"
      "v_mfma_f32_32x32x16_bf16 v[96:111], v[72:75], v[244:247], v[96:111]\n\t"
      "ds_read_b128 v[252:255], v36 offset:32768\n\t"
      "ds_read_b64_tr_b16 v[60:61], v37 offset:21504\n\t"
      "ds_read_b64_tr_b16 v[62:63], v37 offset:22272\n\t"
      "buffer_load_dwordx4 v40, s[20:23], s48 offen offset:1024 lds\n\t"
      "ds_write_b32 v45, v42\n\t"
      "ds_write_b32 v45, v44 offset:1024\n\t"
      "v_mfma_f32_32x32x16_bf16 v[112:127], v[244:247], v[76:79], v[112:127]\n\t"
      "ds_read_b64_tr_b16 v[64:65], v37 offset:21568\n\t"
      "ds_read_b64_tr_b16 v[66:67], v37 offset:22336\n\t"
      "v_mul_f32 v22, 0x3b124925, v22\n\t"
      "v_fma_f32 v22, -v20, v20, v22\n\t"
      "v_cvt_pk_bf16_f32 v16, v20, v20\n\t"
      "v_max_f32 v22, 0, v22\n\t"
      "v_mfma_f32_32x32x16_bf16 v[128:143], v[244:247], v[80:83], v[128:143]\n\t"
      "ds_read_b64_tr_b16 v[68:69], v37 offset:21632\n\t"
      "ds_read_b64_tr_b16 v[70:71], v37 offset:22400\n\t"
      "buffer_load_dwordx4 v41, s[20:23], s48 offen offset:2048 lds\n\t"
      "v_and_b32 v17, 0xffff0000, v16\n\t"
      "s_waitcnt lgkmcnt(4)\n\t"
      "s_barrier\n\t"
      "s_waitcnt lgkmcnt(9)\n\t"
      "v_mfma_f32_32x32x16_bf16 v[96:111], v[84:87], v[248:251], v[96:111]\n\t"
      "ds_read_b128 v[4:7], v46 offset:0\n\t"
      "ds_read_b128 v[8:11], v46 offset:512\n\t"
      "ds_read_b128 v[12:15], v46 offset:1024\n\t"
      "v_add_f32 v22, 0x3727c5ac, v22\n\t"
      "v_sub_f32 v17, v20, v17\n\t"
      "v_rsq_f32 v24, v22\n\t"
      "v_mfma_f32_32x32x16_bf16 v[112:127], v[248:251], v[88:91], v[112:127]\n\t"
      "buffer_load_dwordx4 %[tid], s[20:23], s48 offen offset:3072 lds\n\t"
      "v_cvt_pk_bf16_f32 v17, v17, 0\n\t"
      "v_bfi_b32 v16, v27, 0, v16\n\t"
      "v_bfi_b32 v17, v27, 0, v17\n\t"
      "v_mov_b32 v22, 0\n\t"
      "v_mov_b32 v23, 0\n\t"
      "v_mfma_f32_32x32x16_bf16 v[128:143], v[248:251], v[92:95], v[128:143]\n\t"
      "v_accvgpr_write_b32 a168, v16\n\t"
      "v_accvgpr_write_b32 a169, v17\n\t"
      "s_waitcnt lgkmcnt(0)\n\t"
      "v_add_f32 v4, v4, v5\n\t"
      "v_add_f32 v6, v6, v7\n\t"
      "v_add_f32 v42, v4, v6\n\t"
      "s_waitcnt lgkmcnt(3)\n\t"
      "v_mfma_f32_32x32x16_bf16 v[96:111], v[60:63], v[252:255], v[96:111]\n\t"
      "s_bfe_u32 s48, s100, 262152\n\t"
      "s_add_u32 m0, s56, 139776\n\t"
      "s_mul_i32 s48, s48, 128\n\t"
      "s_add_u32 s48, s48, s53\n\t"
      "buffer_load_dwordx4 v48, s[24:27], s48 offen offset:0 lds\n\t"
      "v_cvt_pk_bf16_f32 v21, v42, 0\n\t"
      "v_mfma_f32_32x32x16_bf16 v[112:127], v[252:255], v[64:67], v[112:127]\n\t"
      "v_lshlrev_b32 v20, 16, v21\n\t"
      "v_sub_f32 v20, v42, v20\n\t"
      "v_cvt_pk_bf16_f32 v20, v42, v20\n\t"
      "v_add_f32 v8, v8, v9\n\t"
      "v_add_f32 v10, v10, v11\n\t"
      "v_add_f32 v43, v8, v10\n\t"
      "v_mfma_f32_32x32x16_bf16 v[128:143], v[252:255], v[68:71], v[128:143]\n\t"
      "v_cvt_pk_bf16_f32 v5, v43, 0\n\t"
      "v_lshlrev_b32 v4, 16, v5\n\t"
      "v_sub_f32 v4, v43, v4\n\t"
      "v_cvt_pk_bf16_f32 v4, v43, v4\n\t"
      "v_mov_b32 v6, 0\n\t"
      "v_mov_b32 v7, 0\n\t"
      "v_add_f32 v12, v12, v13\n\t"
      "v_add_f32 v14, v14, v15\n\t"
      "v_add_f32 v44, v12, v14\n\t"
      "v_cvt_pk_bf16_f32 v9, v44, 0\n\t"
      "v_lshlrev_b32 v8, 16, v9\n\t"
      "v_sub_f32 v8, v44, v8\n\t"
      "v_cvt_pk_bf16_f32 v8, v44, v8\n\t"
      "v_mov_b32 v10, 0\n\t"
      "v_mov_b32 v11, 0\n\t"
      "s_nop 1\n\t"
      "v_mfma_f32_32x32x16_bf16 v[96:111], v[20:23], v[16:19], v[96:111]\n\t"
      "v_cvt_pk_bf16_f32 v12, v24, v24\n\t"
      "v_and_b32 v12, 0xffff0000, v12\n\t"
      "v_sub_f32 v13, v24, v12\n\t"
      "v_cvt_pk_bf16_f32 v12, v24, v13\n\t"
      "v_and_b32 v14, 0xffff0000, v12\n\t"
      "v_and_b32 v84, s70, v39\n\t"
      "v_mul_f32 v86, s66, v24\n\t"
      "v_lshrrev_b32 v85, 16, v84\n\t"
      "v_mfma_f32_32x32x16_bf16 v[112:127], v[16:19], v[4:7], v[112:127]\n\t"
      "v_sub_f32 v13, v13, v14\n\t"
      "v_cvt_pk_bf16_f32 v13, v13, 0\n\t"
      "v_bfi_b32 v12, v27, 0, v12\n\t"
      "v_bfi_b32 v13, v27, 0, v13\n\t"
      "v_mov_b32 v14, 0\n\t"
      "v_mfma_f32_32x32x16_bf16 v[128:143], v[16:19], v[8:11], v[128:143]\n\t"
      "v_mov_b32 v15, 0\n\t"
      "v_mov_b32 v64, v39\n\t"
      "v_mov_b32 v65, v39\n\t"
      "v_mov_b32 v66, v39\n\t"
      "v_mov_b32 v67, v39\n\t"
      "s_nop 1\n\t"
      "v_mfma_f32_32x32x16_bf16 v[68:83], v[12:15], v[64:67], 0\n\t"
      "s_sub_u32 s50, 131072, s54\n\t"
      "v_add_u32 v33, s50, v33\n\t"
      "v_add_u32 v34, s50, v34\n\t"
      "v_add_u32 v35, s50, v35\n\t"
      "v_add_u32 v36, s50, v36\n\t"
      "s_mov_b32 exec_lo, 0xf0000000\n\t"
      "s_mov_b32 exec_hi, 0xf0000000\n\t"
      "v_mov_b32 v33, 135168\n\t"
      "v_mov_b32 v34, 135168\n\t"
      "v_mov_b32 v35, 135168\n\t"
      "v_mov_b32 v36, 135168\n\t"
      "s_mov_b64 exec, -1\n\t"
      "v_pk_mul_f32 v[96:97], v[96:97], v[86:87] op_sel_hi:[1,0]\n\t"
      "v_pk_mul_f32 v[98:99], v[98:99], v[86:87] op_sel_hi:[1,0]\n\t"
      "v_cvt_pk_bf16_f32 v96, v96, v97\n\t"
      "v_cvt_pk_bf16_f32 v97, v98, v99\n\t"
      "v_pk_mul_f32 v[100:101], v[100:101], v[86:87] op_sel_hi:[1,0]\n\t"
      "v_pk_mul_f32 v[102:103], v[102:103], v[86:87] op_sel_hi:[1,0]\n\t"
      "v_cvt_pk_bf16_f32 v98, v100, v101\n\t"
      "v_cvt_pk_bf16_f32 v99, v102, v103\n\t"
      "v_pk_mul_f32 v[104:105], v[104:105], v[86:87] op_sel_hi:[1,0]\n\t"
      "v_pk_mul_f32 v[106:107], v[106:107], v[86:87] op_sel_hi:[1,0]\n\t"
      "v_cvt_pk_bf16_f32 v100, v104, v105\n\t"
      "v_cvt_pk_bf16_f32 v101, v106, v107\n\t"
      "v_pk_mul_f32 v[108:109], v[108:109], v[86:87] op_sel_hi:[1,0]\n\t"
      "v_pk_mul_f32 v[110:111], v[110:111], v[86:87] op_sel_hi:[1,0]\n\t"
      "v_cvt_pk_bf16_f32 v102, v108, v109\n\t"
      "v_cvt_pk_bf16_f32 v103, v110, v111\n\t"
      "v_cndmask_b32 v86, v102, v98, s[60:61]\n\t"
      "v_cndmask_b32 v87, v103, v99, s[60:61]\n\t"
      "v_cndmask_b32 v98, v98, v84, s[60:61]\n\t"
      "v_cndmask_b32 v99, v99, v85, s[60:61]\n\t"
      "buffer_store_dwordx4 v[96:99], v50, s[28:31], s65 offen sc1\n\t"
      "v_cndmask_b32 v102, v102, v84, s[62:63]\n\t"
      "v_cndmask_b32 v103, v103, v85, s[62:63]\n\t"
      "buffer_store_dwordx4 v[100:103], v51, s[28:31], s65 offen sc1\n\t"
      "buffer_store_dwordx4 v[84:87], v52, s[28:31], s65 offen sc1\n\t"
      "v_pk_mul_f32 v[112:113], v[112:113], v[68:69]\n\t"
      "v_pk_mul_f32 v[114:115], v[114:115], v[70:71]\n\t"
      "v_cvt_pk_bf16_f32 v112, v112, v113\n\t"
      "v_cvt_pk_bf16_f32 v113, v114, v115\n\t"
      "v_pk_mul_f32 v[116:117], v[116:117], v[72:73]\n\t"
      "v_pk_mul_f32 v[118:119], v[118:119], v[74:75]\n\t"
      "v_cvt_pk_bf16_f32 v114, v116, v117\n\t"
      "v_cvt_pk_bf16_f32 v115, v118, v119\n\t"
      "buffer_store_dwordx4 v[112:115], v53, s[28:31], s49 offen sc1\n\t"
      "v_pk_mul_f32 v[120:121], v[120:121], v[76:77]\n\t"
      "v_pk_mul_f32 v[122:123], v[122:123], v[78:79]\n\t"
      "v_cvt_pk_bf16_f32 v116, v120, v121\n\t"
      "v_cvt_pk_bf16_f32 v117, v122, v123\n\t"
      "v_pk_mul_f32 v[124:125], v[124:125], v[80:81]\n\t"
      "v_pk_mul_f32 v[126:127], v[126:127], v[82:83]\n\t"
      "v_cvt_pk_bf16_f32 v118, v124, v125\n\t"
      "v_cvt_pk_bf16_f32 v119, v126, v127\n\t"
      "buffer_store_dwordx4 v[116:119], v53, s[28:31], s49 offen offset:1024 sc1\n\t"
      "v_pk_mul_f32 v[128:129], v[128:129], v[68:69]\n\t"
      "v_pk_mul_f32 v[130:131], v[130:131], v[70:71]\n\t"
      "v_cvt_pk_bf16_f32 v128, v128, v129\n\t"
      "v_cvt_pk_bf16_f32 v129, v130, v131\n\t"
      "v_pk_mul_f32 v[132:133], v[132:133], v[72:73]\n\t"
      "v_pk_mul_f32 v[134:135], v[134:135], v[74:75]\n\t"
      "v_cvt_pk_bf16_f32 v130, v132, v133\n\t"
      "v_cvt_pk_bf16_f32 v131, v134, v135\n\t"
      "buffer_store_dwordx4 v[128:131], v56, s[28:31], s49 offen sc1\n\t"
      "v_pk_mul_f32 v[136:137], v[136:137], v[76:77]\n\t"
      "v_pk_mul_f32 v[138:139], v[138:139], v[78:79]\n\t"
      "v_cvt_pk_bf16_f32 v132, v136, v137\n\t"
      "v_cvt_pk_bf16_f32 v133, v138, v139\n\t"
      "v_pk_mul_f32 v[140:141], v[140:141], v[80:81]\n\t"
      "v_pk_mul_f32 v[142:143], v[142:143], v[82:83]\n\t"
      "v_cvt_pk_bf16_f32 v134, v140, v141\n\t"
      "v_cvt_pk_bf16_f32 v135, v142, v143\n\t"
      "buffer_store_dwordx4 v[132:135], v56, s[28:31], s49 offen offset:1024 sc1\n\t"
      "s_branch zph_%=\n\t"
      "side_%=:\n\t"
      "s_mov_b32 s100, 0x06543210\n\t"
      "s_sub_u32 s49, s46, 14\n\t"
      "s_mul_i32 s50, s49, 448\n\t"
      "s_add_u32 s51, s51, s50\n\t"
      "s_bfe_u32 s48, s100, 262144\n\t"
      "s_add_u32 m0, s54, 0\n\t"
      "s_mul_i32 s48, s48, 128\n\t"
      "s_add_u32 s48, s48, s51\n\t"
      "buffer_load_dwordx4 v28, s[12:15], s48 offen offset:0 lds\n\t"
      "buffer_load_dwordx4 v29, s[12:15], s48 offen offset:1024 lds\n\t"
      "buffer_load_dwordx4 v40, s[12:15], s48 offen offset:2048 lds\n\t"
      "buffer_load_dwordx4 v41, s[12:15], s48 offen offset:3072 lds\n\t"
      "s_bfe_u32 s48, s100, 262148\n\t"
      "s_add_u32 m0, s54, 16384\n\t"
      "s_mul_i32 s48, s48, 128\n\t"
      "s_add_u32 s48, s48, s51\n\t"
      "buffer_load_dwordx4 v28, s[12:15], s48 offen offset:0 lds\n\t"
      "buffer_load_dwordx4 v29, s[12:15], s48 offen offset:1024 lds\n\t"
      "buffer_load_dwordx4 v40, s[12:15], s48 offen offset:2048 lds\n\t"
      "buffer_load_dwordx4 v41, s[12:15], s48 offen offset:3072 lds\n\t"
      "s_bfe_u32 s48, s100, 262152\n\t"
      "s_add_u32 m0, s54, 32768\n\t"
      "s_mul_i32 s48, s48, 128\n\t"
      "s_add_u32 s48, s48, s51\n\t"
      "buffer_load_dwordx4 v28, s[12:15], s48 offen offset:0 lds\n\t"
      "buffer_load_dwordx4 v29, s[12:15], s48 offen offset:1024 lds\n\t"
      "buffer_load_dwordx4 v40, s[12:15], s48 offen offset:2048 lds\n\t"
      "buffer_load_dwordx4 v41, s[12:15], s48 offen offset:3072 lds\n\t"
      "s_bfe_u32 s48, s100, 262156\n\t"
      "s_add_u32 m0, s54, 49152\n\t"
      "s_mul_i32 s48, s48, 128\n\t"
      "s_add_u32 s48, s48, s51\n\t"
      "buffer_load_dwordx4 v28, s[12:15], s48 offen offset:0 lds\n\t"
      "buffer_load_dwordx4 v29, s[12:15], s48 offen offset:1024 lds\n\t"
      "buffer_load_dwordx4 v40, s[12:15], s48 offen offset:2048 lds\n\t"
      "buffer_load_dwordx4 v41, s[12:15], s48 offen offset:3072 lds\n\t"
      "v_bfe_u32 v24, v1, 1, 2\n\t"
      "v_bfe_u32 v25, v1, 4, 1\n\t"
      "v_lshl_or_b32 v24, v25, 2, v24\n\t"
      "v_lshrrev_b32 v26, 5, v1\n\t"
      "v_xor_b32 v24, v24, v26\n\t"
      "v_and_b32 v59, 31, v1\n\t"
      "v_lshlrev_b32 v24, 4, v24\n\t"
      "v_lshl_add_u32 v24, v59, 7, v24\n\t"
      "v_add_u32 v33, s54, v24\n\t"
      "v_xor_b32 v34, 32, v33\n\t"
      "v_xor_b32 v35, 64, v33\n\t"
      "v_xor_b32 v36, 0x60, v33\n\t"
      "v_lshlrev_b32 v59, 4, v59\n\t"
      "v_lshlrev_b32 v2, 4, v1\n\t"
      "s_mov_b32 s30, 27262976\n\t"
      "s_mov_b32 s31, 0x20000\n\t"
      "s_mov_b32 s34, 64512\n\t"
      "s_mov_b32 s35, 0x20000\n\t"
      "s_mov_b32 s26, 401408\n\t"
      "s_mov_b32 s27, 0x20000\n\t"
      "s_lshl_b32 s50, s45, 4\n\t"
      "s_add_u32 s50, s50, s46\n\t"
      "s_lshl_b32 s50, s50, 5\n\t"
      "s_add_u32 s50, s50, s44\n\t"
      "s_lshl_b32 s50, s50, 2\n\t"
      "s_add_u32 s50, s50, 262144\n\t"
      "v_lshl_add_u32 v49, v1, 15, s50\n\t"
      "s_lshl_b32 s56, s44, 10\n\t"
      "v_lshrrev_b32 v24, 3, v1\n\t"
      "v_mul_u32_u24 v25, 896, v24\n\t"
      "v_and_b32 v26, 7, v1\n\t"
      "v_lshl_add_u32 v25, v26, 4, v25\n\t"
      "s_mul_i32 s48, s59, 6272\n\t"
      "buffer_load_dwordx4 v[60:63], v25, s[24:27], s48 offen offset:0\n\t"
      "buffer_load_dwordx4 v[64:67], v25, s[24:27], s48 offen offset:128\n\t"
      "buffer_load_dwordx4 v[68:71], v25, s[24:27], s48 offen offset:256\n\t"
      "buffer_load_dwordx4 v[72:75], v25, s[24:27], s48 offen offset:384\n\t"
      "buffer_load_dwordx4 v[76:79], v25, s[24:27], s48 offen offset:512\n\t"
      "buffer_load_dwordx4 v[80:83], v25, s[24:27], s48 offen offset:640\n\t"
      "buffer_load_dwordx4 v[84:87], v25, s[24:27], s48 offen offset:768\n\t"
      "v_mov_b32 v39, 1065369472\n\t"
      "v_mov_b32 v20, 0\n\t"
      "v_mov_b32 v22, 0\n\t"
      "v_mov_b32 v42, 0\n\t"
      "s_and_b32 s48, %[bid], 7\n\t"
      "s_lshl_b32 s94, 1, s48\n\t"
      "s_mov_b32 s95, 0\n\t"
      "s_lshl_b32 s57, s59, 1\n\t"
      "s_add_u32 s57, s57, s49\n\t"
      "s_lshl_b32 s57, s57, 9\n\t"
      "s_add_u32 s57, s57, 524288\n\t"
      "v_lshrrev_b32 v26, 5, v1\n\t"
      "v_lshl_add_u32 v26, v26, 3, v59\n\t"
      "s_waitcnt vmcnt(7) lgkmcnt(0)\n\t"
      "s_add_u32 s47, s47, 1\n\t"
      "ds_read_b128 v[144:147], v33 offset:0\n\t"
      "ds_read_b128 v[148:151], v34 offset:0\n\t"
      "ds_read_b128 v[152:155], v35 offset:0\n\t"
      "ds_read_b128 v[156:159], v36 offset:0\n\t"
      "ds_read_b128 v[160:163], v33 offset:16384\n\t"
      "ds_read_b128 v[164:167], v34 offset:16384\n\t"
      "ds_read_b128 v[168:171], v35 offset:16384\n\t"
      "ds_read_b128 v[172:175], v36 offset:16384\n\t"
      "ds_read_b128 v[176:179], v33 offset:32768\n\t"
      "ds_read_b128 v[180:183], v34 offset:32768\n\t"
      "ds_read_b128 v[184:187], v35 offset:32768\n\t"
      "ds_read_b128 v[188:191], v36 offset:32768\n\t"
      "ds_read_b128 v[192:195], v33 offset:49152\n\t"
      "ds_read_b128 v[196:199], v34 offset:49152\n\t"
      "v_mov_b32 v21, s47\n\t"
      "s_waitcnt lgkmcnt(13)\n\t"
      "v_dot2c_f32_bf16 v20, v144, v39\n\t"
      "v_dot2c_f32_bf16 v22, v144, v144\n\t"
      "v_dot2c_f32_bf16 v20, v145, v39\n\t"
      "v_dot2c_f32_bf16 v22, v145, v145\n\t"
      "v_dot2c_f32_bf16 v20, v146, v39\n\t"
      "v_dot2c_f32_bf16 v22, v146, v146\n\t"
      "v_dot2c_f32_bf16 v20, v147, v39\n\t"
      "v_dot2c_f32_bf16 v22, v147, v147\n\t"
      "s_waitcnt lgkmcnt(12)\n\t"
      "v_dot2c_f32_bf16 v20, v148, v39\n\t"
      "v_dot2c_f32_bf16 v22, v148, v148\n\t"
      "v_dot2c_f32_bf16 v20, v149, v39\n\t"
      "v_dot2c_f32_bf16 v22, v149, v149\n\t"
      "v_dot2c_f32_bf16 v20, v150, v39\n\t"
      "v_dot2c_f32_bf16 v22, v150, v150\n\t"
      "v_dot2c_f32_bf16 v20, v151, v39\n\t"
      "v_dot2c_f32_bf16 v22, v151, v151\n\t"
      "s_waitcnt lgkmcnt(11)\n\t"
      "v_dot2c_f32_bf16 v20, v152, v39\n\t"
      "v_dot2c_f32_bf16 v22, v152, v152\n\t"
      "v_dot2c_f32_bf16 v20, v153, v39\n\t"
      "v_dot2c_f32_bf16 v22, v153, v153\n\t"
      "v_dot2c_f32_bf16 v20, v154, v39\n\t"
      "v_dot2c_f32_bf16 v22, v154, v154\n\t"
      "v_dot2c_f32_bf16 v20, v155, v39\n\t"
      "v_dot2c_f32_bf16 v22, v155, v155\n\t"
      "s_waitcnt lgkmcnt(10)\n\t"
      "v_dot2c_f32_bf16 v20, v156, v39\n\t"
      "v_dot2c_f32_bf16 v22, v156, v156\n\t"
      "v_dot2c_f32_bf16 v20, v157, v39\n\t"
      "v_dot2c_f32_bf16 v22, v157, v157\n\t"
      "v_dot2c_f32_bf16 v20, v158, v39\n\t"
      "v_dot2c_f32_bf16 v22, v158, v158\n\t"
      "v_dot2c_f32_bf16 v20, v159, v39\n\t"
      "v_dot2c_f32_bf16 v22, v159, v159\n\t"
      "s_waitcnt lgkmcnt(9)\n\t"
      "v_dot2c_f32_bf16 v20, v160, v39\n\t"
      "v_dot2c_f32_bf16 v22, v160, v160\n\t"
      "v_dot2c_f32_bf16 v20, v161, v39\n\t"
      "v_dot2c_f32_bf16 v22, v161, v161\n\t"
      "v_dot2c_f32_bf16 v20, v162, v39\n\t"
      "v_dot2c_f32_bf16 v22, v162, v162\n\t"
      "v_dot2c_f32_bf16 v20, v163, v39\n\t"
      "v_dot2c_f32_bf16 v22, v163, v163\n\t"
      "s_waitcnt lgkmcnt(8)\n\t"
      "v_dot2c_f32_bf16 v20, v164, v39\n\t"
      "v_dot2c_f32_bf16 v22, v164, v164\n\t"
      "v_dot2c_f32_bf16 v20, v165, v39\n\t"
      "v_dot2c_f32_bf16 v22, v165, v165\n\t"
      "v_dot2c_f32_bf16 v20, v166, v39\n\t"
      "v_dot2c_f32_bf16 v22, v166, v166\n\t"
      "v_dot2c_f32_bf16 v20, v167, v39\n\t"
      "v_dot2c_f32_bf16 v22, v167, v167\n\t"
      "s_waitcnt lgkmcnt(7)\n\t"
      "v_dot2c_f32_bf16 v20, v168, v39\n\t"
      "v_dot2c_f32_bf16 v22, v168, v168\n\t"
      "v_dot2c_f32_bf16 v20, v169, v39\n\t"
      "v_dot2c_f32_bf16 v22, v169, v169\n\t"
      "v_dot2c_f32_bf16 v20, v170, v39\n\t"
      "v_dot2c_f32_bf16 v22, v170, v170\n\t"
      "v_dot2c_f32_bf16 v20, v171, v39\n\t"
      "v_dot2c_f32_bf16 v22, v171, v171\n\t"
      "s_waitcnt lgkmcnt(6)\n\t"
      "v_dot2c_f32_bf16 v20, v172, v39\n\t"
      "v_dot2c_f32_bf16 v22, v172, v172\n\t"
      "v_dot2c_f32_bf16 v20, v173, v39\n\t"
      "v_dot2c_f32_bf16 v22, v173, v173\n\t"
      "v_dot2c_f32_bf16 v20, v174, v39\n\t"
      "v_dot2c_f32_bf16 v22, v174, v174\n\t"
      "v_dot2c_f32_bf16 v20, v175, v39\n\t"
      "v_dot2c_f32_bf16 v22, v175, v175\n\t"
      "s_waitcnt lgkmcnt(5)\n\t"
      "v_dot2c_f32_bf16 v20, v176, v39\n\t"
      "v_dot2c_f32_bf16 v22, v176, v176\n\t"
      "v_dot2c_f32_bf16 v20, v177, v39\n\t"
      "v_dot2c_f32_bf16 v22, v177, v177\n\t"
      "v_dot2c_f32_bf16 v20, v178, v39\n\t"
      "v_dot2c_f32_bf16 v22, v178, v178\n\t"
      "v_dot2c_f32_bf16 v20, v179, v39\n\t"
      "v_dot2c_f32_bf16 v22, v179, v179\n\t"
      "s_waitcnt lgkmcnt(4)\n\t"
      "v_dot2c_f32_bf16 v20, v180, v39\n\t"
      "v_dot2c_f32_bf16 v22, v180, v180\n\t"
      "v_dot2c_f32_bf16 v20, v181, v39\n\t"
      "v_dot2c_f32_bf16 v22, v181, v181\n\t"
      "v_dot2c_f32_bf16 v20, v182, v39\n\t"
      "v_dot2c_f32_bf16 v22, v182, v182\n\t"
      "v_dot2c_f32_bf16 v20, v183, v39\n\t"
      "v_dot2c_f32_bf16 v22, v183, v183\n\t"
      "s_waitcnt lgkmcnt(3)\n\t"
      "v_dot2c_f32_bf16 v20, v184, v39\n\t"
      "v_dot2c_f32_bf16 v22, v184, v184\n\t"
      "v_dot2c_f32_bf16 v20, v185, v39\n\t"
      "v_dot2c_f32_bf16 v22, v185, v185\n\t"
      "v_dot2c_f32_bf16 v20, v186, v39\n\t"
      "v_dot2c_f32_bf16 v22, v186, v186\n\t"
      "v_dot2c_f32_bf16 v20, v187, v39\n\t"
      "v_dot2c_f32_bf16 v22, v187, v187\n\t"
      "s_waitcnt lgkmcnt(2)\n\t"
      "v_dot2c_f32_bf16 v20, v188, v39\n\t"
      "v_dot2c_f32_bf16 v22, v188, v188\n\t"
      "v_dot2c_f32_bf16 v20, v189, v39\n\t"
      "v_dot2c_f32_bf16 v22, v189, v189\n\t"
      "v_dot2c_f32_bf16 v20, v190, v39\n\t"
      "v_dot2c_f32_bf16 v22, v190, v190\n\t"
      "v_dot2c_f32_bf16 v20, v191, v39\n\t"
      "v_dot2c_f32_bf16 v22, v191, v191\n\t"
      "s_waitcnt lgkmcnt(1)\n\t"
      "v_dot2c_f32_bf16 v20, v192, v39\n\t"
      "v_dot2c_f32_bf16 v22, v192, v192\n\t"
      "v_dot2c_f32_bf16 v20, v193, v39\n\t"
      "v_dot2c_f32_bf16 v22, v193, v193\n\t"
      "v_dot2c_f32_bf16 v20, v194, v39\n\t"
      "v_dot2c_f32_bf16 v22, v194, v194\n\t"
      "v_dot2c_f32_bf16 v20, v195, v39\n\t"
      "v_dot2c_f32_bf16 v22, v195, v195\n\t"
      "s_waitcnt lgkmcnt(0)\n\t"
      "v_dot2c_f32_bf16 v20, v196, v39\n\t"
      "v_dot2c_f32_bf16 v22, v196, v196\n\t"
      "v_dot2c_f32_bf16 v20, v197, v39\n\t"
      "v_dot2c_f32_bf16 v22, v197, v197\n\t"
      "v_dot2c_f32_bf16 v20, v198, v39\n\t"
      "v_dot2c_f32_bf16 v22, v198, v198\n\t"
      "v_dot2c_f32_bf16 v20, v199, v39\n\t"
      "v_dot2c_f32_bf16 v22, v199, v199\n\t"
      "s_nop 3\n\t"
      "v_permlane32_swap_b32 v20, v22\n\t"
      "s_nop 1\n\t"
      "v_add_f32 v20, v20, v22\n\t"
      "buffer_store_dwordx2 v[20:21], v26, s[28:31], s57 offen sc1\n\t"
      "s_waitcnt vmcnt(1)\n\t"
      "v_dot2c_f32_bf16 v42, v60, v39\n\t"
      "v_dot2c_f32_bf16 v42, v61, v39\n\t"
      "v_dot2c_f32_bf16 v42, v62, v39\n\t"
      "v_dot2c_f32_bf16 v42, v63, v39\n\t"
      "v_dot2c_f32_bf16 v42, v64, v39\n\t"
      "v_dot2c_f32_bf16 v42, v65, v39\n\t"
      "v_dot2c_f32_bf16 v42, v66, v39\n\t"
      "v_dot2c_f32_bf16 v42, v67, v39\n\t"
      "v_dot2c_f32_bf16 v42, v68, v39\n\t"
      "v_dot2c_f32_bf16 v42, v69, v39\n\t"
      "v_dot2c_f32_bf16 v42, v70, v39\n\t"
      "v_dot2c_f32_bf16 v42, v71, v39\n\t"
      "v_dot2c_f32_bf16 v42, v72, v39\n\t"
      "v_dot2c_f32_bf16 v42, v73, v39\n\t"
      "v_dot2c_f32_bf16 v42, v74, v39\n\t"
      "v_dot2c_f32_bf16 v42, v75, v39\n\t"
      "v_dot2c_f32_bf16 v42, v76, v39\n\t"
      "v_dot2c_f32_bf16 v42, v77, v39\n\t"
      "v_dot2c_f32_bf16 v42, v78, v39\n\t"
      "v_dot2c_f32_bf16 v42, v79, v39\n\t"
      "v_dot2c_f32_bf16 v42, v80, v39\n\t"
      "v_dot2c_f32_bf16 v42, v81, v39\n\t"
      "v_dot2c_f32_bf16 v42, v82, v39\n\t"
      "v_dot2c_f32_bf16 v42, v83, v39\n\t"
      "v_dot2c_f32_bf16 v42, v84, v39\n\t"
      "v_dot2c_f32_bf16 v42, v85, v39\n\t"
      "v_dot2c_f32_bf16 v42, v86, v39\n\t"
      "v_dot2c_f32_bf16 v42, v87, v39\n\t"
      "s_nop 3\n\t"
      "v_add_f32_dpp v42, v42, v42 quad_perm:[1,0,3,2] row_mask:0xf bank_mask:0xf\n\t"
      "s_nop 1\n\t"
      "v_add_f32_dpp v42, v42, v42 quad_perm:[2,3,0,1] row_mask:0xf bank_mask:0xf\n\t"
      "s_nop 1\n\t"
      "v_add_f32_dpp v42, v42, v42 row_half_mirror row_mask:0xf bank_mask:0xf\n\t"
      "s_nop 1\n\t"
      "v_mul_f32 v42, 0xbb124925, v42\n\t"
      "s_lshl_b32 s48, s45, 5\n\t"
      "s_mul_i32 s49, s44, 7\n\t"
      "s_add_u32 s48, s48, s49\n\t"
      "s_lshl_b32 s48, s48, 2\n\t"
      "s_add_u32 s48, s48, 147456\n\t"
      "v_lshlrev_b32 v24, 2, v24\n\t"
      "s_mov_b32 exec_lo, 0x01010101\n\t"
      "s_mov_b32 exec_hi, 0x00010101\n\t"
      "buffer_store_dword v42, v24, s[28:31], s48 offen sc1\n\t"
      "s_mov_b64 exec, -1\n\t"
      "s_lshl_b32 s48, s45, 2\n\t"
      "s_add_u32 s48, s48, gtab_%=-pc0_%=\n\t"
      "s_load_dword s79, s[32:33], s48\n\t"
      "s_waitcnt vmcnt(0) lgkmcnt(0)\n\t"
      "v_mov_b32 v60, s47\n\t"
      "s_mov_b64 exec, s[94:95]\n\t"
      "buffer_store_dword v60, v49, s[28:31], 0 offen sc1\n\t"
      "s_mov_b64 exec, -1\n\t"
      "s_and_b32 s93, %[bid], 7\n\t"
      "s_cmp_ge_u32 s46, 7\n\t"
      "s_cselect_b32 s58, 1, 0\n\t"
      "s_mul_i32 s57, s58, 7\n\t"
      "s_sub_u32 s57, s46, s57\n\t"
      "s_lshl_b32 s91, s59, 16\n\t"
      "s_add_u32 s91, s91, 3145728\n\t"
      "s_add_u32 s12, s28, s91\n\t"
      "s_addc_u32 s13, s29, 0\n\t"
      "s_mov_b32 s14, 0x10000\n\t"
      "s_lshl_b32 s92, s46, 2\n\t"
      "s_add_u32 s92, s92, s44\n\t"
      "s_lshl_b32 s91, s45, 6\n\t"
      "s_add_u32 s91, s91, s92\n\t"
      "s_lshl_b32 s91, s91, 11\n\t"
      "s_add_u32 s68, s91, 1048576\n\t"
      "s_mul_i32 s67, s92, 147456\n\t"
      "s_mul_i32 s91, s45, 9216\n\t"
      "s_add_u32 s67, s67, s91\n\t"
      "s_add_u32 s67, s67, 13631488\n\t"
      "s_lshl_b32 s78, s92, 15\n\t"
      "s_lshr_b32 s91, s45, 2\n\t"
      "s_lshl_b32 s91, s91, 13\n\t"
      "s_add_u32 s78, s78, s91\n\t"
      "s_add_u32 s78, s78, 7340032\n\t"
      "v_lshrrev_b32 v27, 5, v1\n\t"
      "v_and_b32 v24, 1, v1\n\t"
      "v_lshl_add_u32 v24, v27, 1, v24\n\t"
      "v_add_u32 v24, 4, v24\n\t"
      "v_and_b32 v25, 3, v1\n\t"
      "v_cmp_gt_u32 vcc, 16, v1\n\t"
      "v_lshrrev_b32 v26, 2, v1\n\t"
      "s_lshl_b32 s66, s44, 2\n\t"
      "v_cndmask_b32 v24, v24, v25, vcc\n\t"
      "v_lshlrev_b32 v24, 2, v24\n\t"
      "v_lshrrev_b32 v24, v24, s79\n\t"
      "v_and_b32 v24, 15, v24\n\t"
      "v_add_u32 v26, s66, v26\n\t"
      "v_mov_b32 v25, s46\n\t"
      "v_mov_b32 v23, s45\n\t"
      "v_cmp_gt_u32 vcc, 32, v1\n\t"
      "s_nop 1\n\t"
      "v_cndmask_b32 v25, v23, v25, vcc\n\t"
      "v_cmp_gt_u32 vcc, 16, v1\n\t"
      "s_nop 1\n\t"
      "v_cndmask_b32 v25, v25, v26, vcc\n\t"
      "v_lshl_add_u32 v24, v25, 4, v24\n\t"
      "s_lshl_b32 s91, s93, 15\n\t"
      "s_add_u32 s91, s91, 262144\n\t"
      "v_lshl_add_u32 v23, v24, 7, s91\n\t"
      "s_lshl_b32 s80, s44, 4\n\t"
      "s_lshl_b32 s91, s44, 9\n\t"
      "s_add_u32 s91, s91, 153600\n\t"
      "v_add_u32 v4, s91, v59\n\t"
      "v_lshl_add_u32 v5, v27, 11, s66\n\t"
      "v_sub_u32 v27, 0, v27\n\t"
      "s_mov_b32 s66, 0\n\t"
      "s_mov_b32 s79, 0x4f800000\n\t"
      "s_bitcmp1_b32 s45, 0\n\t"
      "s_cselect_b32 s64, -1, 0\n\t"
      "s_cselect_b32 s63, 0, -1\n\t"
      "s_mov_b32 s65, 0\n\t"
      "s_mov_b32 s62, 0\n\t"
      "s_and_b32 s91, s45, 3\n\t"
      "s_lshl_b32 s91, s91, 10\n\t"
      "v_add_u32 v12, s91, v2\n\t"
      "s_bfe_u32 s91, s45, 0x10001\n\t"
      "s_add_u32 s91, s91, 4\n\t"
      "s_lshl_b32 s91, s91, 10\n\t"
      "s_and_b32 s92, s45, 1\n\t"
      "s_lshl_b32 s92, s92, 9\n\t"
      "s_add_u32 s91, s91, s92\n\t"
      "v_and_b32 v24, 2147483648, v27\n\t"
      "v_add3_u32 v13, v24, v59, s91\n\t"
      "s_bfe_u32 s91, s45, 0x10001\n\t"
      "s_lshl_b32 s91, s91, 9\n\t"
      "s_lshr_b32 s92, s92, 6\n\t"
      "s_add_u32 s91, s91, s92\n\t"
      "s_add_u32 s91, s91, 6144\n\t"
      "v_xor_b32 v24, 2147483648, v24\n\t"
      "v_add3_u32 v14, v24, v59, s91\n\t"
      "s_lshl_b32 s91, s45, 7\n\t"
      "s_add_u32 s91, s91, 4096\n\t"
      "v_and_b32 v24, 15, v1\n\t"
      "v_lshl_add_u32 v6, v24, 2, s91\n\t"
      "s_lshl_b32 s91, s93, 3\n\t"
      "s_add_u32 s91, s91, s57\n\t"
      "s_lshl_b32 s91, s91, 7\n\t"
      "s_add_u32 s91, s91, 65536\n\t"
      "s_cmp_lt_u32 s46, 14\n\t"
      "s_cselect_b32 exec_lo, -1, 0\n\t"
      "s_mov_b32 exec_hi, 0\n\t"
      "v_lshl_add_u32 v6, v1, 2, s91\n\t"
      "s_mov_b64 exec, -1\n\t"
      "s_lshl_b32 s91, s57, 4\n\t"
      "s_add_u32 s91, s91, s45\n\t"
      "s_lshl_b32 s91, s91, 1\n\t"
      "s_add_u32 s91, s91, s58\n\t"
      "s_lshl_b32 s91, s91, 2\n\t"
      "s_add_u32 s91, s91, 65536\n\t"
      "v_mov_b32 v7, 2147483648\n\t"
      "s_cmp_lt_u32 s46, 14\n\t"
      "s_cselect_b32 s70, 0xff, 0\n\t"
      "s_cmp_eq_u32 s44, 0\n\t"
      "s_cselect_b32 s70, s70, 0\n\t"
      "s_cselect_b32 s69, 0x100, 0\n\t"
      "s_mov_b32 exec_lo, s70\n\t"
      "s_mov_b32 exec_hi, 0\n\t"
      "v_lshl_add_u32 v7, v1, 10, s91\n\t"
      "s_mov_b64 exec, -1\n\t"
      "s_lshl_b32 s91, s46, 5\n\t"
      "s_add_u32 s91, s91, s45\n\t"
      "s_lshl_b32 s91, s91, 2\n\t"
      "s_add_u32 s91, s91, 4096\n\t"
      "s_mov_b32 exec_lo, s69\n\t"
      "s_mov_b32 exec_hi, 0\n\t"
      "v_mov_b32 v7, s91\n\t"
      "s_mov_b64 exec, -1\n\t"
      "s_cmp_lt_u32 s46, 14\n\t"
      "s_cselect_b32 s69, s56, 0x30000\n\t"
      "s_cselect_b32 s70, 0x1000, 0\n\t"
      "s_cselect_b32 s18, 27262976, 0\n\t"
      "s_mov_b32 s16, s28\n\t"
      "s_mov_b32 s17, s29\n\t"
      "s_lshl_b32 s71, s46, 17\n\t"
      "s_add_u32 s71, s71, s56\n\t"
      "s_add_u32 s71, s71, 9437184\n\t"
      "s_and_b32 s91, s46, 3\n\t"
      "s_lshl_b32 s92, s91, 15\n\t"
      "s_xor_b32 s71, s71, s92\n\t"
      "s_lshl_b32 s81, s59, 15\n\t"
      "s_add_u32 s81, s81, 7340032\n\t"
      "v_bfe_u32 v24, v1, 2, 2\n\t"
      "v_and_b32 v25, 3, v1\n\t"
      "v_lshl_add_u32 v24, v24, 3, v25\n\t"
      "s_add_u32 s92, s81, 1008\n\t"
      "v_lshl_add_u32 v18, v24, 10, s92\n\t"
      "s_lshl_b32 s91, s91, 13\n\t"
      "s_xor_b32 s81, s81, s91\n\t"
      "v_lshlrev_b32 v25, 4, v25\n\t"
      "v_mov_b32 v15, 155140\n\t"
      "v_add_u32 v16, v25, v15\n\t"
      "v_add_u32 v17, 0x10000, v2\n\t"
      "s_mul_i32 s72, s57, 229376\n\t"
      "s_mul_i32 s91, s44, 14336\n\t"
      "s_add_u32 s72, s72, s91\n\t"
      "s_add_u32 s72, s72, 11533312\n\t"
      "s_bitcmp1_b32 s44, 0\n\t"
      "s_cselect_b32 s92, 0, 0x400\n\t"
      "s_sub_u32 s91, 0x800, s92\n\t"
      "v_add_u32 v8, s91, v2\n\t"
      "v_add_u32 v9, s92, v2\n\t"
      "v_lshrrev_b32 v24, 1, v2\n\t"
      "v_and_b32 v25, 2147483648, v27\n\t"
      "v_or_b32 v10, v24, v25\n\t"
      "s_lshl_b32 s73, s46, 10\n\t"
      "s_add_u32 s73, s73, 131072\n\t"
      "s_xor_b32 s91, s44, 3\n\t"
      "s_mul_i32 s91, s91, 12288\n\t"
      "s_add_u32 s74, s91, 114688\n\t"
      "s_xor_b32 s91, s44, 2\n\t"
      "s_mul_i32 s91, s91, 12288\n\t"
      "s_add_u32 s75, s91, 118784\n\t"
      "s_xor_b32 s91, s44, 1\n\t"
      "s_mul_i32 s91, s91, 12288\n\t"
      "s_add_u32 s76, s91, 122880\n\t"
      "s_mul_i32 s91, s44, 12288\n\t"
      "s_add_u32 s77, s91, 114688\n\t"
      "s_mul_i32 s82, s59, 147456\n\t"
      "s_lshl_b32 s91, s46, 11\n\t"
      "s_add_u32 s82, s82, s91\n\t"
      "s_add_u32 s82, s82, 13631488\n\t"
      "s_lshl_b32 s92, s45, 1\n\t"
      "s_add_u32 s92, s92, s58\n\t"
      "s_lshl_b32 s92, s92, 1\n\t"
      "s_lshr_b32 s91, s44, 1\n\t"
      "s_add_u32 s92, s92, s91\n\t"
      "s_mul_i32 s91, s92, 147456\n\t"
      "v_add_u32 v19, s91, v2\n\t"
      "s_add_u32 s91, s91, 13774848\n\t"
      "v_add_u32 v21, s91, v59\n\t"
      "s_xor_b32 s91, s92, 1\n\t"
      "s_mul_i32 s91, s91, 147456\n\t"
      "v_add_u32 v20, s91, v2\n\t"
      "s_and_b32 s91, s44, 1\n\t"
      "s_lshl_b32 s83, s57, 1\n\t"
      "s_add_u32 s91, s91, s83\n\t"
      "s_lshl_b32 s83, s91, 11\n\t"
      "s_add_u32 s83, s83, 13746176\n\t"
      "s_mul_i32 s92, s92, 28672\n\t"
      "s_lshl_b32 s91, s91, 6\n\t"
      "s_add_u32 s91, s91, s92\n\t"
      "v_mul_u32_u24 v24, 56, v59\n\t"
      "v_and_b32 v25, 16, v27\n\t"
      "v_add3_u32 v22, v24, v25, s91\n\t"
      "s_mov_b32 s42, 1835008\n\t"
      "s_mov_b32 s43, 0x20000\n\t"
      "s_mul_i32 s91, s44, 7168\n\t"
      "s_add_u32 s91, s91, 13631488\n\t"
      "s_lshl_b32 s92, s57, 10\n\t"
      "s_add_u32 s84, s91, s92\n\t"
      "s_cmp_ge_u32 s57, 6\n\t"
      "s_cselect_b32 s92, -6, 1\n\t"
      "s_lshl_b32 s92, s92, 10\n\t"
      "s_add_u32 s85, s84, s92\n\t"
      "s_cmp_ge_u32 s57, 5\n\t"
      "s_cselect_b32 s92, -5, 2\n\t"
      "s_lshl_b32 s92, s92, 10\n\t"
      "s_add_u32 s86, s84, s92\n\t"
      "s_cmp_ge_u32 s57, 4\n\t"
      "s_cselect_b32 s92, -4, 3\n\t"
      "s_lshl_b32 s92, s92, 10\n\t"
      "s_add_u32 s87, s84, s92\n\t"
      "s_cmp_ge_u32 s57, 3\n\t"
      "s_cselect_b32 s92, -3, 4\n\t"
      "s_lshl_b32 s92, s92, 10\n\t"
      "s_add_u32 s88, s84, s92\n\t"
      "s_cmp_ge_u32 s57, 2\n\t"
      "s_cselect_b32 s92, -2, 5\n\t"
      "s_lshl_b32 s92, s92, 10\n\t"
      "s_add_u32 s89, s84, s92\n\t"
      "s_cmp_ge_u32 s57, 1\n\t"
      "s_cselect_b32 s92, -1, 6\n\t"
      "s_lshl_b32 s92, s92, 10\n\t"
      "s_add_u32 s90, s84, s92\n\t"
      "s_lshr_b32 s50, %[bid], 3\n\t"
      "s_lshl_b32 s50, s50, 2\n\t"
      "s_add_u32 s50, s50, s44\n\t"
      "s_lshl_b32 s50, s50, 9\n\t"
      "v_mov_b32 v11, 2147483648\n\t"
      "s_mov_b64 exec, 15\n\t"
      "v_lshl_add_u32 v11, v1, 7, s50\n\t"
      "s_mov_b64 exec, -1\n\t"
      "s_lshl_b32 s48, s45, 1\n\t"
      "s_add_u32 s48, s48, s46\n\t"
      "s_sub_u32 s48, s48, 14\n\t"
      "s_lshl_b32 s48, s48, 2\n\t"
      "s_add_u32 s48, s48, s44\n\t"
      "s_lshl_b32 s48, s48, 14\n\t"
      "s_add_u32 s48, s48, 23068672\n\t"
      "s_add_u32 s16, s28, s48\n\t"
      "s_addc_u32 s17, s29, 0\n\t"
      "s_movk_i32 s18, 0x3800\n\t"
      "s_mov_b32 s72, 4294852608\n\t"
      "s_mov_b32 s71, 2147483648\n\t"
      "s_mov_b32 s73, 2147483648\n\t"
      "v_mov_b32 v8, v2\n\t"
      "v_mov_b32 v9, v2\n\t"
      "buffer_store_dwordx4 v[144:147], v2, s[28:31], s48 offen offset:0 sc1\n\t"
      "buffer_store_dwordx4 v[148:151], v2, s[28:31], s48 offen offset:1024 sc1\n\t"
      "buffer_store_dwordx4 v[152:155], v2, s[28:31], s48 offen offset:2048 sc1\n\t"
      "buffer_store_dwordx4 v[156:159], v2, s[28:31], s48 offen offset:3072 sc1\n\t"
      "s_add_u32 s48, s48, 0x1000\n\t"
      "buffer_store_dwordx4 v[160:163], v2, s[28:31], s48 offen offset:0 sc1\n\t"
      "buffer_store_dwordx4 v[164:167], v2, s[28:31], s48 offen offset:1024 sc1\n\t"
      "buffer_store_dwordx4 v[168:171], v2, s[28:31], s48 offen offset:2048 sc1\n\t"
      "buffer_store_dwordx4 v[172:175], v2, s[28:31], s48 offen offset:3072 sc1\n\t"
      "s_add_u32 s48, s48, 0x1000\n\t"
      "buffer_store_dwordx4 v[176:179], v2, s[28:31], s48 offen offset:0 sc1\n\t"
      "buffer_store_dwordx4 v[180:183], v2, s[28:31], s48 offen offset:1024 sc1\n\t"
      "buffer_store_dwordx4 v[184:187], v2, s[28:31], s48 offen offset:2048 sc1\n\t"
      "buffer_store_dwordx4 v[188:191], v2, s[28:31], s48 offen offset:3072 sc1\n\t"
      "s_add_u32 s48, s48, 0x1000\n\t"
      "buffer_store_dwordx4 v[192:195], v2, s[28:31], s48 offen offset:0 sc1\n\t"
      "buffer_store_dwordx4 v[196:199], v2, s[28:31], s48 offen offset:1024 sc1\n\t"
      "v_mov_b32 v64, -1\n\t"
      "v_mov_b32 v65, -1\n\t"
      "v_mov_b32 v66, -1\n\t"
      "v_mov_b32 v67, -1\n\t"
      "buffer_store_dwordx4 v[64:67], v12, s[28:31], s78 offen sc1\n\t"
      "buffer_store_dwordx4 v[64:67], v13, s[28:31], s78 offen sc1\n\t"
      "buffer_store_dwordx2 v[64:65], v14, s[28:31], s78 offen sc1\n\t"
      "v_accvgpr_write_b32 a168, 0\n\t"
      "v_accvgpr_write_b32 a169, 0\n\t"
      "v_accvgpr_write_b32 a170, 0\n\t"
      "v_accvgpr_write_b32 a171, 0\n\t"
      "s_barrier\n\t"
      "s_sub_u32 s49, s46, 14\n\t"
      "s_mul_i32 s49, s49, 448\n\t"
      "s_mov_b32 s50, s56\n\t"
      "s_movk_i32 s48, 448\n\t"
      "s_lshl_b32 s91, s44, 6\n\t"
      "v_add_u32 v24, s91, v1\n\t"
      "wol_%=:\n\t"
      "v_mul_u32_u24 v25, 9363, v24\n\t"
      "v_lshlrev_b32 v26, 4, v24\n\t"
      "v_lshrrev_b32 v25, 18, v25\n\t"
      "s_mov_b32 m0, s50\n\t"
      "v_mad_u32_u24 v25, v25, s48, v26\n\t"
      "v_add_u32 v24, 0x100, v24\n\t"
      "buffer_load_dwordx4 v25, s[24:27], s49 offen lds\n\t"
      "s_add_u32 s50, s50, 0x1000\n\t"
      "s_cmp_lt_u32 s50, 100352\n\t"
      "s_cbranch_scc1 wol_%=\n\t"
      "s_mov_b32 s87, s49\n\t"
      "s_lshl_b32 s84, s59, 15\n\t"
      "s_add_u32 s84, s84, 7340032\n\t"
      "s_lshl_b32 s85, s44, 11\n\t"
      "s_add_u32 s86, s85, 1024\n\t"
      "s_min_u32 s86, s86, 6144\n\t"
      "s_mul_i32 s91, s59, 147456\n\t"
      "s_lshl_b32 s89, s87, 5\n\t"
      "s_add_u32 s89, s89, s91\n\t"
      "s_add_u32 s89, s89, 13746176\n\t"
      "s_sub_u32 s50, s46, 14\n\t"
      "s_lshl_b32 s50, s50, 3\n\t"
      "s_add_u32 s91, s91, s50\n\t"
      "s_add_u32 s91, s91, 13774848\n\t"
      "v_and_b32 v60, 4, v27\n\t"
      "v_add3_u32 v21, v60, v59, s91\n\t"
      "v_and_b32 v60, 31, v1\n\t"
      "v_and_b32 v61, 8, v27\n\t"
      "v_sub_u32 v60, v60, v61\n\t"
      "v_and_b32 v61, 1, v60\n\t"
      "v_lshlrev_b32 v61, 4, v61\n\t"
      "v_mov_b32 v62, 0x3f80\n\t"
      "v_lshlrev_b32 v61, v61, v62\n\t"
      "v_ashrrev_i32 v60, 1, v60\n\t"
      "v_cmp_eq_u32 vcc, 0, v60\n\t"
      "s_nop 1\n\t"
      "v_cndmask_b32 v28, 0, v61, vcc\n\t"
      "v_cmp_eq_u32 vcc, 1, v60\n\t"
      "s_nop 1\n\t"
      "v_cndmask_b32 v29, 0, v61, vcc\n\t"
      "v_cmp_eq_u32 vcc, 2, v60\n\t"
      "s_nop 1\n\t"
      "v_cndmask_b32 v30, 0, v61, vcc\n\t"
      "v_cmp_eq_u32 vcc, 3, v60\n\t"
      "s_nop 1\n\t"
      "v_cndmask_b32 v31, 0, v61, vcc\n\t"
      "v_cmp_eq_u32 vcc, 8, v60\n\t"
      "s_nop 1\n\t"
      "v_cndmask_b32 v32, 0, v61, vcc\n\t"
      "v_cmp_eq_u32 vcc, 9, v60\n\t"
      "s_nop 1\n\t"
      "v_cndmask_b32 v33, 0, v61, vcc\n\t"
      "v_cmp_eq_u32 vcc, 10, v60\n\t"
      "s_nop 1\n\t"
      "v_cndmask_b32 v34, 0, v61, vcc\n\t"
      "v_cmp_eq_u32 vcc, 11, v60\n\t"
      "s_nop 1\n\t"
      "v_cndmask_b32 v35, 0, v61, vcc\n\t"
      "v_bfe_u32 v60, v1, 2, 2\n\t"
      "v_mul_u32_u24 v60, 448, v60\n\t"
      "v_bfe_u32 v61, v1, 4, 1\n\t"
      "v_lshl_add_u32 v60, v61, 5, v60\n\t"
      "v_and_b32 v61, 3, v1\n\t"
      "v_lshl_add_u32 v60, v61, 3, v60\n\t"
      "v_mov_b32 v62, v27\n\t"
      "v_and_b32 v61, 1792, v62\n\t"
      "v_add_u32 v10, v60, v61\n\t"
      "v_add_u32 v17, 50176, v10\n\t"
      "v_add_u32 v19, 100352, v10\n\t"
      "v_and_b32 v61, 12544, v62\n\t"
      "v_add_u32 v20, v60, v61\n\t"
      "v_add_u32 v22, 50176, v20\n\t"
      "v_add_u32 v24, 100352, v20\n\t"
      "v_and_b32 v61, 25088, v62\n\t"
      "v_add_u32 v25, v60, v61\n\t"
      "v_add_u32 v26, 50176, v25\n\t"
      "v_add_u32 v27, 100352, v25\n\t"
      "s_sleep 40\n\t"
      "sp_%=:\n\t"
      "s_mov_b32 s50, 0x10000\n\t"
      "spl_%=:\n\t"
      "s_sub_u32 s50, s50, 1\n\t"
      "s_cmp_eq_u32 s50, 0\n\t"
      "s_cbranch_scc1 sr_%=\n\t"
      "buffer_load_dwordx4 v[60:63], v23, s[28:31], 0 offen sc1\n\t"
      "s_waitcnt vmcnt(0)\n\t"
      "v_xor_b32 v60, s47, v60\n\t"
      "v_xor_b32 v61, s47, v61\n\t"
      "v_xor_b32 v62, s47, v62\n\t"
      "v_xor_b32 v63, s47, v63\n\t"
      "v_or3_b32 v60, v60, v61, v62\n\t"
      "v_or_b32 v60, v60, v63\n\t"
      "v_cmp_ne_u32 vcc, 0, v60\n\t"
      "s_nop 1\n\t"
      "s_cbranch_vccz sr_%=\n\t"
      "s_sleep 3\n\t"
      "s_branch spl_%=\n\t"
      "sr_%=:\n\t"
      "buffer_load_dwordx4 a[96:99], v2, s[12:15], 0 offen offset:0 sc1\n\t"
      "buffer_load_dwordx4 a[100:103], v2, s[12:15], 0 offen offset:1024 sc1\n\t"
      "s_xor_b32 s49, s68, 6144\n\t"
      "buffer_load_dwordx4 a[64:67], v2, s[28:31], s49 offen offset:0 sc1\n\t"
      "buffer_load_dwordx4 a[68:71], v2, s[28:31], s49 offen offset:1024 sc1\n\t"
      "s_xor_b32 s49, s68, 4096\n\t"
      "buffer_load_dwordx4 a[72:75], v2, s[28:31], s49 offen offset:0 sc1\n\t"
      "buffer_load_dwordx4 a[76:79], v2, s[28:31], s49 offen offset:1024 sc1\n\t"
      "s_xor_b32 s49, s68, 2048\n\t"
      "buffer_load_dwordx4 a[80:83], v2, s[28:31], s49 offen offset:0 sc1\n\t"
      "buffer_load_dwordx4 a[84:87], v2, s[28:31], s49 offen offset:1024 sc1\n\t"
      "s_xor_b32 s49, s68, 0\n\t"
      "buffer_load_dwordx4 a[88:91], v2, s[28:31], s49 offen offset:0 sc1\n\t"
      "buffer_load_dwordx4 a[92:95], v2, s[28:31], s49 offen offset:1024 sc1\n\t"
      "buffer_load_dwordx4 a[104:107], v2, s[12:15], 0 offen offset:2048 sc1\n\t"
      "buffer_load_dwordx4 a[108:111], v2, s[12:15], 0 offen offset:3072 sc1\n\t"
      "s_movk_i32 s49, 4096\n\t"
      "buffer_load_dwordx4 a[112:115], v2, s[12:15], s49 offen offset:0 sc1\n\t"
      "buffer_load_dwordx4 a[116:119], v2, s[12:15], s49 offen offset:1024 sc1\n\t"
      "buffer_load_dwordx4 a[120:123], v2, s[12:15], s49 offen offset:2048 sc1\n\t"
      "buffer_load_dwordx4 a[124:127], v2, s[12:15], s49 offen offset:3072 sc1\n\t"
      "s_movk_i32 s49, 8192\n\t"
      "buffer_load_dwordx4 a[128:131], v2, s[12:15], s49 offen offset:0 sc1\n\t"
      "buffer_load_dwordx4 a[132:135], v2, s[12:15], s49 offen offset:1024 sc1\n\t"
      "buffer_load_dwordx4 a[136:139], v2, s[12:15], s49 offen offset:2048 sc1\n\t"
      "buffer_load_dwordx4 a[140:143], v2, s[12:15], s49 offen offset:3072 sc1\n\t"
      "s_movk_i32 s49, 12288\n\t"
      "buffer_load_dwordx4 a[144:147], v2, s[12:15], s49 offen offset:0 sc1\n\t"
      "buffer_load_dwordx4 a[148:151], v2, s[12:15], s49 offen offset:1024 sc1\n\t"
      "buffer_load_dwordx4 a[152:155], v2, s[12:15], s49 offen offset:2048 sc1\n\t"
      "s_lshr_b32 s91, s85, 4\n\t"
      "v_add_u32 v60, s91, v1\n\t"
      "v_mul_u32_u24 v61, 9363, v60\n\t"
      "v_lshlrev_b32 v62, 4, v60\n\t"
      "v_lshrrev_b32 v61, 18, v61\n\t"
      "v_mad_u32_u24 v23, v61, s48, v62\n\t"
      "s_lshr_b32 s91, s86, 4\n\t"
      "v_add_u32 v60, s91, v1\n\t"
      "v_mul_u32_u24 v61, 9363, v60\n\t"
      "v_lshlrev_b32 v62, 4, v60\n\t"
      "v_lshrrev_b32 v61, 18, v61\n\t"
      "v_mad_u32_u24 %[tid], v61, s48, v62\n\t"
      "s_branch att_%=\n\t"
      "opj_%=:\n\t"
      "s_waitcnt vmcnt(3)\n\t"
      "ds_read_b64_tr_b16 v[56:57], v10 offset:0\n\t"
      "ds_read_b64_tr_b16 v[58:59], v10 offset:3584\n\t"
      "ds_read_b64_tr_b16 v[60:61], v10 offset:64\n\t"
      "ds_read_b64_tr_b16 v[62:63], v10 offset:3648\n\t"
      "ds_read_b64_tr_b16 v[64:65], v10 offset:128\n\t"
      "ds_read_b64_tr_b16 v[66:67], v10 offset:3712\n\t"
      "ds_read_b64_tr_b16 v[68:69], v10 offset:192\n\t"
      "ds_read_b64_tr_b16 v[70:71], v10 offset:3776\n\t"
      "ds_read_b64_tr_b16 v[72:73], v10 offset:256\n\t"
      "ds_read_b64_tr_b16 v[74:75], v10 offset:3840\n\t"
      "ds_read_b64_tr_b16 v[76:77], v10 offset:320\n\t"
      "ds_read_b64_tr_b16 v[78:79], v10 offset:3904\n\t"
      "ds_read_b64_tr_b16 v[80:81], v10 offset:384\n\t"
      "ds_read_b64_tr_b16 v[82:83], v10 offset:3968\n\t"
      "v_mov_b32 v42, 0\n\t"
      "v_mov_b32 v43, 0\n\t"
      "v_mov_b32 v44, 0\n\t"
      "v_mov_b32 v45, 0\n\t"
      "v_mov_b32 v41, 0x3f803f80\n\t"
      "v_mfma_f32_32x32x16_bf16 v[144:159], v[28:31], a[176:179], 0\n\t"
      "v_mfma_f32_32x32x16_bf16 v[144:159], v[32:35], a[180:183], v[144:159]\n\t"
      "v_mfma_f32_32x32x16_bf16 v[160:175], v[28:31], a[184:187], 0\n\t"
      "v_mfma_f32_32x32x16_bf16 v[160:175], v[32:35], a[188:191], v[160:175]\n\t"
      "v_mfma_f32_32x32x16_bf16 v[176:191], v[28:31], a[192:195], 0\n\t"
      "v_mfma_f32_32x32x16_bf16 v[176:191], v[32:35], a[196:199], v[176:191]\n\t"
      "v_mfma_f32_32x32x16_bf16 v[192:207], v[28:31], a[200:203], 0\n\t"
      "v_mfma_f32_32x32x16_bf16 v[192:207], v[32:35], a[204:207], v[192:207]\n\t"
      "v_mfma_f32_32x32x16_bf16 v[208:223], v[28:31], a[208:211], 0\n\t"
      "v_mfma_f32_32x32x16_bf16 v[208:223], v[32:35], a[212:215], v[208:223]\n\t"
      "v_mfma_f32_32x32x16_bf16 v[224:239], v[28:31], a[216:219], 0\n\t"
      "v_mfma_f32_32x32x16_bf16 v[224:239], v[32:35], a[220:223], v[224:239]\n\t"
      "v_mfma_f32_32x32x16_bf16 v[240:255], v[28:31], a[224:227], 0\n\t"
      "v_mfma_f32_32x32x16_bf16 v[240:255], v[32:35], a[228:231], v[240:255]\n\t"
      "s_mov_b32 s50, 0x10000\n\t"
      "ppr_%=:\n\t"
      "buffer_load_dword v40, v18, s[28:31], 0 offen sc1\n\t"
      "s_sub_u32 s50, s50, 1\n\t"
      "s_waitcnt vmcnt(0)\n\t"
      "v_cmp_eq_u32 vcc, -1, v40\n\t"
      "s_cmp_eq_u32 s50, 0\n\t"
      "s_cbranch_scc1 pprd_%=\n\t"
      "s_cbranch_vccnz ppr_%=\n\t"
      "pprd_%=:\n\t"
      "s_add_u32 s48, s84, 0\n\t"
      "buffer_load_dwordx4 v[88:91], v2, s[28:31], s48 offen sc1\n\t"
      "s_add_u32 s48, s84, 1024\n\t"
      "buffer_load_dwordx4 v[92:95], v2, s[28:31], s48 offen sc1\n\t"
      "s_add_u32 s48, s84, 2048\n\t"
      "buffer_load_dwordx4 v[96:99], v2, s[28:31], s48 offen sc1\n\t"
      "s_add_u32 s48, s84, 3072\n\t"
      "buffer_load_dwordx4 v[100:103], v2, s[28:31], s48 offen sc1\n\t"
      "s_add_u32 s48, s84, 4096\n\t"
      "buffer_load_dwordx4 v[104:107], v2, s[28:31], s48 offen sc1\n\t"
      "s_barrier\n\t"
      "s_waitcnt vmcnt(4)\n\t"
      "v_max3_u32 v40, v88, v89, v90\n\t"
      "v_max_u32 v40, v40, v91\n\t"
      "v_cmp_eq_u32 vcc, -1, v40\n\t"
      "s_nop 0\n\t"
      "s_cbranch_vccnz qo0_%=\n\t"
      "qr0_%=:\n\t"
      "s_waitcnt lgkmcnt(10)\n\t"
      "v_mfma_f32_32x32x16_bf16 v[144:159], v[56:59], v[88:91], v[144:159]\n\t"
      "ds_read_b64_tr_b16 v[84:85], v10 offset:12544\n\t"
      "ds_read_b64_tr_b16 v[86:87], v10 offset:16128\n\t"
      "s_add_u32 s48, s84, 5120\n\t"
      "buffer_load_dwordx4 v[108:111], v2, s[28:31], s48 offen sc1\n\t"
      "v_mfma_f32_32x32x16_bf16 v[160:175], v[60:63], v[88:91], v[160:175]\n\t"
      "ds_read_b64_tr_b16 v[56:57], v10 offset:12608\n\t"
      "ds_read_b64_tr_b16 v[58:59], v10 offset:16192\n\t"
      "s_waitcnt lgkmcnt(10)\n\t"
      "v_mfma_f32_32x32x16_bf16 v[176:191], v[64:67], v[88:91], v[176:191]\n\t"
      "ds_read_b64_tr_b16 v[60:61], v10 offset:12672\n\t"
      "ds_read_b64_tr_b16 v[62:63], v10 offset:16256\n\t"
      "s_add_u32 m0, s85, 100352\n\t"
      "s_add_u32 s48, s87, 200704\n\t"
      "buffer_load_dwordx4 v23, s[24:27], s48 offen lds\n\t"
      "v_mfma_f32_32x32x16_bf16 v[192:207], v[68:71], v[88:91], v[192:207]\n\t"
      "ds_read_b64_tr_b16 v[64:65], v10 offset:12736\n\t"
      "ds_read_b64_tr_b16 v[66:67], v10 offset:16320\n\t"
      "s_waitcnt lgkmcnt(10)\n\t"
      "v_mfma_f32_32x32x16_bf16 v[208:223], v[72:75], v[88:91], v[208:223]\n\t"
      "ds_read_b64_tr_b16 v[68:69], v10 offset:12800\n\t"
      "ds_read_b64_tr_b16 v[70:71], v10 offset:16384\n\t"
      "s_waitcnt vmcnt(5)\n\t"
      "v_max3_u32 v40, v92, v93, v94\n\t"
      "v_max_u32 v40, v40, v95\n\t"
      "v_mfma_f32_32x32x16_bf16 v[224:239], v[76:79], v[88:91], v[224:239]\n\t"
      "ds_read_b64_tr_b16 v[72:73], v10 offset:12864\n\t"
      "ds_read_b64_tr_b16 v[74:75], v10 offset:16448\n\t"
      "v_cmp_eq_u32 vcc, -1, v40\n\t"
      "s_nop 0\n\t"
      "s_cbranch_vccnz qo1_%=\n\t"
      "qr1_%=:\n\t"
      "s_waitcnt lgkmcnt(10)\n\t"
      "v_mfma_f32_32x32x16_bf16 v[240:255], v[80:83], v[88:91], v[240:255]\n\t"
      "ds_read_b64_tr_b16 v[76:77], v10 offset:12928\n\t"
      "ds_read_b64_tr_b16 v[78:79], v10 offset:16512\n\t"
      "s_add_u32 m0, s85, 107520\n\t"
      "s_add_u32 s48, s87, 215040\n\t"
      "buffer_load_dwordx4 v23, s[24:27], s48 offen lds\n\t"
      "v_mfma_f32_32x32x16_bf16 v[144:159], v[84:87], v[92:95], v[144:159]\n\t"
      "ds_read_b64_tr_b16 v[80:81], v10 offset:25088\n\t"
      "ds_read_b64_tr_b16 v[82:83], v10 offset:28672\n\t"
      "s_add_u32 s48, s84, 6144\n\t"
      "buffer_load_dwordx4 v[112:115], v2, s[28:31], s48 offen sc1\n\t"
      "s_waitcnt lgkmcnt(10)\n\t"
      "v_mfma_f32_32x32x16_bf16 v[160:175], v[56:59], v[92:95], v[160:175]\n\t"
      "ds_read_b64_tr_b16 v[84:85], v10 offset:25152\n\t"
      "ds_read_b64_tr_b16 v[86:87], v10 offset:28736\n\t"
      "v_mfma_f32_32x32x16_bf16 v[176:191], v[60:63], v[92:95], v[176:191]\n\t"
      "ds_read_b64_tr_b16 v[56:57], v10 offset:25216\n\t"
      "ds_read_b64_tr_b16 v[58:59], v10 offset:28800\n\t"
      "s_add_u32 m0, s85, 114688\n\t"
      "s_add_u32 s48, s87, 229376\n\t"
      "buffer_load_dwordx4 v23, s[24:27], s48 offen lds\n\t"
      "s_waitcnt lgkmcnt(10)\n\t"
      "v_mfma_f32_32x32x16_bf16 v[192:207], v[64:67], v[92:95], v[192:207]\n\t"
      "ds_read_b64_tr_b16 v[60:61], v10 offset:25280\n\t"
      "ds_read_b64_tr_b16 v[62:63], v10 offset:28864\n\t"
      "v_mfma_f32_32x32x16_bf16 v[208:223], v[68:71], v[92:95], v[208:223]\n\t"
      "ds_read_b64_tr_b16 v[64:65], v10 offset:25344\n\t"
      "ds_read_b64_tr_b16 v[66:67], v10 offset:28928\n\t"
      "s_waitcnt vmcnt(7)\n\t"
      "v_max3_u32 v40, v96, v97, v98\n\t"
      "v_max_u32 v40, v40, v99\n\t"
      "s_waitcnt lgkmcnt(10)\n\t"
      "v_mfma_f32_32x32x16_bf16 v[224:239], v[72:75], v[92:95], v[224:239]\n\t"
      "ds_read_b64_tr_b16 v[68:69], v10 offset:25408\n\t"
      "ds_read_b64_tr_b16 v[70:71], v10 offset:28992\n\t"
      "v_cmp_eq_u32 vcc, -1, v40\n\t"
      "s_nop 0\n\t"
      "s_cbranch_vccnz qo2_%=\n\t"
      "qr2_%=:\n\t"
      "v_mfma_f32_32x32x16_bf16 v[240:255], v[76:79], v[92:95], v[240:255]\n\t"
      "ds_read_b64_tr_b16 v[72:73], v10 offset:25472\n\t"
      "ds_read_b64_tr_b16 v[74:75], v10 offset:29056\n\t"
      "s_add_u32 m0, s85, 121856\n\t"
      "s_add_u32 s48, s87, 243712\n\t"
      "buffer_load_dwordx4 v23, s[24:27], s48 offen lds\n\t"
      "s_waitcnt lgkmcnt(10)\n\t"
      "v_mfma_f32_32x32x16_bf16 v[144:159], v[80:83], v[96:99], v[144:159]\n\t"
      "ds_read_b64_tr_b16 v[76:77], v10 offset:37632\n\t"
      "ds_read_b64_tr_b16 v[78:79], v10 offset:41216\n\t"
      "s_add_u32 s48, s84, 8192\n\t"
      "buffer_load_dwordx4 v[116:119], v2, s[28:31], s48 offen sc1\n\t"
      "v_mfma_f32_32x32x16_bf16 v[160:175], v[84:87], v[96:99], v[160:175]\n\t"
      "ds_read_b64_tr_b16 v[80:81], v10 offset:37696\n\t"
      "ds_read_b64_tr_b16 v[82:83], v10 offset:41280\n\t"
      "s_waitcnt lgkmcnt(10)\n\t"
      "v_mfma_f32_32x32x16_bf16 v[176:191], v[56:59], v[96:99], v[176:191]\n\t"
      "ds_read_b64_tr_b16 v[84:85], v10 offset:37760\n\t"
      "ds_read_b64_tr_b16 v[86:87], v10 offset:41344\n\t"
      "s_add_u32 m0, s85, 129024\n\t"
      "s_add_u32 s48, s87, 258048\n\t"
      "buffer_load_dwordx4 v23, s[24:27], s48 offen lds\n\t"
      "v_mfma_f32_32x32x16_bf16 v[192:207], v[60:63], v[96:99], v[192:207]\n\t"
      "ds_read_b64_tr_b16 v[56:57], v10 offset:37824\n\t"
      "ds_read_b64_tr_b16 v[58:59], v10 offset:41408\n\t"
      "s_waitcnt lgkmcnt(10)\n\t"
      "v_mfma_f32_32x32x16_bf16 v[208:223], v[64:67], v[96:99], v[208:223]\n\t"
      "ds_read_b64_tr_b16 v[60:61], v10 offset:37888\n\t"
      "ds_read_b64_tr_b16 v[62:63], v10 offset:41472\n\t"
      "s_waitcnt vmcnt(9)\n\t"
      "v_max3_u32 v40, v100, v101, v102\n\t"
      "v_max_u32 v40, v40, v103\n\t"
      "v_mfma_f32_32x32x16_bf16 v[224:239], v[68:71], v[96:99], v[224:239]\n\t"
      "ds_read_b64_tr_b16 v[64:65], v10 offset:37952\n\t"
      "ds_read_b64_tr_b16 v[66:67], v10 offset:41536\n\t"
      "v_cmp_eq_u32 vcc, -1, v40\n\t"
      "s_nop 0\n\t"
      "s_cbranch_vccnz qo3_%=\n\t"
      "qr3_%=:\n\t"
      "s_waitcnt lgkmcnt(10)\n\t"
      "v_mfma_f32_32x32x16_bf16 v[240:255], v[72:75], v[96:99], v[240:255]\n\t"
      "ds_read_b64_tr_b16 v[68:69], v10 offset:38016\n\t"
      "ds_read_b64_tr_b16 v[70:71], v10 offset:41600\n\t"
      "s_add_u32 m0, s85, 136192\n\t"
      "s_add_u32 s48, s87, 272384\n\t"
      "buffer_load_dwordx4 v23, s[24:27], s48 offen lds\n\t"
      "v_mfma_f32_32x32x16_bf16 v[144:159], v[76:79], v[100:103], v[144:159]\n\t"
      "ds_read_b64_tr_b16 v[72:73], v20 offset:7168\n\t"
      "ds_read_b64_tr_b16 v[74:75], v20 offset:10752\n\t"
      "s_add_u32 s48, s84, 9216\n\t"
      "buffer_load_dwordx4 v[120:123], v2, s[28:31], s48 offen sc1\n\t"
      "s_waitcnt lgkmcnt(10)\n\t"
      "v_mfma_f32_32x32x16_bf16 v[160:175], v[80:83], v[100:103], v[160:175]\n\t"
      "ds_read_b64_tr_b16 v[76:77], v20 offset:7232\n\t"
      "ds_read_b64_tr_b16 v[78:79], v20 offset:10816\n\t"
      "v_mfma_f32_32x32x16_bf16 v[176:191], v[84:87], v[100:103], v[176:191]\n\t"
      "ds_read_b64_tr_b16 v[80:81], v20 offset:7296\n\t"
      "ds_read_b64_tr_b16 v[82:83], v20 offset:10880\n\t"
      "s_add_u32 m0, s85, 143360\n\t"
      "s_add_u32 s48, s87, 286720\n\t"
      "buffer_load_dwordx4 v23, s[24:27], s48 offen lds\n\t"
      "s_waitcnt lgkmcnt(10)\n\t"
      "v_mfma_f32_32x32x16_bf16 v[192:207], v[56:59], v[100:103], v[192:207]\n\t"
      "ds_read_b64_tr_b16 v[84:85], v20 offset:7360\n\t"
      "ds_read_b64_tr_b16 v[86:87], v20 offset:10944\n\t"
      "v_mfma_f32_32x32x16_bf16 v[208:223], v[60:63], v[100:103], v[208:223]\n\t"
      "ds_read_b64_tr_b16 v[56:57], v20 offset:7424\n\t"
      "ds_read_b64_tr_b16 v[58:59], v20 offset:11008\n\t"
      "s_waitcnt vmcnt(11)\n\t"
      "v_max3_u32 v40, v104, v105, v106\n\t"
      "v_max_u32 v40, v40, v107\n\t"
      "s_waitcnt lgkmcnt(10)\n\t"
      "v_mfma_f32_32x32x16_bf16 v[224:239], v[64:67], v[100:103], v[224:239]\n\t"
      "ds_read_b64_tr_b16 v[60:61], v20 offset:7488\n\t"
      "ds_read_b64_tr_b16 v[62:63], v20 offset:11072\n\t"
      "v_cmp_eq_u32 vcc, -1, v40\n\t"
      "s_nop 0\n\t"
      "s_cbranch_vccnz qo4_%=\n\t"
      "qr4_%=:\n\t"
      "v_mfma_f32_32x32x16_bf16 v[240:255], v[68:71], v[100:103], v[240:255]\n\t"
      "ds_read_b64_tr_b16 v[64:65], v20 offset:7552\n\t"
      "ds_read_b64_tr_b16 v[66:67], v20 offset:11136\n\t"
      "s_add_u32 m0, s86, 100352\n\t"
      "s_add_u32 s48, s87, 200704\n\t"
      "buffer_load_dwordx4 %[tid], s[24:27], s48 offen lds\n\t"
      "s_waitcnt lgkmcnt(10)\n\t"
      "v_mfma_f32_32x32x16_bf16 v[144:159], v[72:75], v[104:107], v[144:159]\n\t"
      "ds_read_b64_tr_b16 v[68:69], v20 offset:32256\n\t"
      "ds_read_b64_tr_b16 v[70:71], v20 offset:35840\n\t"
      "s_add_u32 s48, s84, 10240\n\t"
      "buffer_load_dwordx4 v[124:127], v2, s[28:31], s48 offen sc1\n\t"
      "v_mfma_f32_32x32x16_bf16 v[160:175], v[76:79], v[104:107], v[160:175]\n\t"
      "ds_read_b64_tr_b16 v[72:73], v20 offset:32320\n\t"
      "ds_read_b64_tr_b16 v[74:75], v20 offset:35904\n\t"
      "s_waitcnt lgkmcnt(10)\n\t"
      "v_mfma_f32_32x32x16_bf16 v[176:191], v[80:83], v[104:107], v[176:191]\n\t"
      "ds_read_b64_tr_b16 v[76:77], v20 offset:32384\n\t"
      "ds_read_b64_tr_b16 v[78:79], v20 offset:35968\n\t"
      "s_add_u32 m0, s86, 107520\n\t"
      "s_add_u32 s48, s87, 215040\n\t"
      "buffer_load_dwordx4 %[tid], s[24:27], s48 offen lds\n\t"
      "v_mfma_f32_32x32x16_bf16 v[192:207], v[84:87], v[104:107], v[192:207]\n\t"
      "ds_read_b64_tr_b16 v[80:81], v20 offset:32448\n\t"
      "ds_read_b64_tr_b16 v[82:83], v20 offset:36032\n\t"
      "s_waitcnt lgkmcnt(10)\n\t"
      "v_mfma_f32_32x32x16_bf16 v[208:223], v[56:59], v[104:107], v[208:223]\n\t"
      "ds_read_b64_tr_b16 v[84:85], v20 offset:32512\n\t"
      "ds_read_b64_tr_b16 v[86:87], v20 offset:36096\n\t"
      "s_waitcnt vmcnt(13)\n\t"
      "v_max3_u32 v40, v108, v109, v110\n\t"
      "v_max_u32 v40, v40, v111\n\t"
      "v_mfma_f32_32x32x16_bf16 v[224:239], v[60:63], v[104:107], v[224:239]\n\t"
      "ds_read_b64_tr_b16 v[56:57], v20 offset:32576\n\t"
      "ds_read_b64_tr_b16 v[58:59], v20 offset:36160\n\t"
      "v_cmp_eq_u32 vcc, -1, v40\n\t"
      "s_nop 0\n\t"
      "s_cbranch_vccnz qo5_%=\n\t"
      "qr5_%=:\n\t"
      "s_waitcnt lgkmcnt(10)\n\t"
      "v_mfma_f32_32x32x16_bf16 v[240:255], v[64:67], v[104:107], v[240:255]\n\t"
      "ds_read_b64_tr_b16 v[60:61], v20 offset:32640\n\t"
      "ds_read_b64_tr_b16 v[62:63], v20 offset:36224\n\t"
      "s_add_u32 m0, s86, 114688\n\t"
      "s_add_u32 s48, s87, 229376\n\t"
      "buffer_load_dwordx4 %[tid], s[24:27], s48 offen lds\n\t"
      "v_mfma_f32_32x32x16_bf16 v[144:159], v[68:71], v[108:111], v[144:159]\n\t"
      "ds_read_b64_tr_b16 v[64:65], v25 offset:8960\n\t"
      "ds_read_b64_tr_b16 v[66:67], v25 offset:21504\n\t"
      "s_add_u32 s48, s84, 11264\n\t"
      "buffer_load_dwordx4 v[128:131], v2, s[28:31], s48 offen sc1\n\t"
      "s_waitcnt lgkmcnt(10)\n\t"
      "v_mfma_f32_32x32x16_bf16 v[160:175], v[72:75], v[108:111], v[160:175]\n\t"
      "ds_read_b64_tr_b16 v[68:69], v25 offset:9024\n\t"
      "ds_read_b64_tr_b16 v[70:71], v25 offset:21568\n\t"
      "v_mfma_f32_32x32x16_bf16 v[176:191], v[76:79], v[108:111], v[176:191]\n\t"
      "ds_read_b64_tr_b16 v[72:73], v25 offset:9088\n\t"
      "ds_read_b64_tr_b16 v[74:75], v25 offset:21632\n\t"
      "s_add_u32 m0, s86, 121856\n\t"
      "s_add_u32 s48, s87, 243712\n\t"
      "buffer_load_dwordx4 %[tid], s[24:27], s48 offen lds\n\t"
      "s_waitcnt lgkmcnt(10)\n\t"
      "v_mfma_f32_32x32x16_bf16 v[192:207], v[80:83], v[108:111], v[192:207]\n\t"
      "ds_read_b64_tr_b16 v[76:77], v25 offset:9152\n\t"
      "ds_read_b64_tr_b16 v[78:79], v25 offset:21696\n\t"
      "v_mfma_f32_32x32x16_bf16 v[208:223], v[84:87], v[108:111], v[208:223]\n\t"
      "ds_read_b64_tr_b16 v[80:81], v25 offset:9216\n\t"
      "ds_read_b64_tr_b16 v[82:83], v25 offset:21760\n\t"
      "s_waitcnt vmcnt(13)\n\t"
      "v_max3_u32 v40, v112, v113, v114\n\t"
      "v_max_u32 v40, v40, v115\n\t"
      "s_waitcnt lgkmcnt(10)\n\t"
      "v_mfma_f32_32x32x16_bf16 v[224:239], v[56:59], v[108:111], v[224:239]\n\t"
      "ds_read_b64_tr_b16 v[84:85], v25 offset:9280\n\t"
      "ds_read_b64_tr_b16 v[86:87], v25 offset:21824\n\t"
      "v_cmp_eq_u32 vcc, -1, v40\n\t"
      "s_nop 0\n\t"
      "s_cbranch_vccnz qo6_%=\n\t"
      "qr6_%=:\n\t"
      "v_mfma_f32_32x32x16_bf16 v[240:255], v[60:63], v[108:111], v[240:255]\n\t"
      "ds_read_b64_tr_b16 v[56:57], v25 offset:9344\n\t"
      "ds_read_b64_tr_b16 v[58:59], v25 offset:21888\n\t"
      "s_add_u32 m0, s86, 129024\n\t"
      "s_add_u32 s48, s87, 258048\n\t"
      "buffer_load_dwordx4 %[tid], s[24:27], s48 offen lds\n\t"
      "s_waitcnt lgkmcnt(10)\n\t"
      "v_mfma_f32_32x32x16_bf16 v[144:159], v[64:67], v[112:115], v[144:159]\n\t"
      "ds_read_b64_tr_b16 v[60:61], v17 offset:0\n\t"
      "ds_read_b64_tr_b16 v[62:63], v17 offset:3584\n\t"
      "s_add_u32 s48, s84, 12288\n\t"
      "buffer_load_dwordx4 v[132:135], v2, s[28:31], s48 offen sc1\n\t"
      "v_mfma_f32_32x32x16_bf16 v[160:175], v[68:71], v[112:115], v[160:175]\n\t"
      "ds_read_b64_tr_b16 v[64:65], v17 offset:64\n\t"
      "ds_read_b64_tr_b16 v[66:67], v17 offset:3648\n\t"
      "s_waitcnt lgkmcnt(10)\n\t"
      "v_mfma_f32_32x32x16_bf16 v[176:191], v[72:75], v[112:115], v[176:191]\n\t"
      "ds_read_b64_tr_b16 v[68:69], v17 offset:128\n\t"
      "ds_read_b64_tr_b16 v[70:71], v17 offset:3712\n\t"
      "s_add_u32 m0, s86, 136192\n\t"
      "s_add_u32 s48, s87, 272384\n\t"
      "buffer_load_dwordx4 %[tid], s[24:27], s48 offen lds\n\t"
      "v_mfma_f32_32x32x16_bf16 v[192:207], v[76:79], v[112:115], v[192:207]\n\t"
      "ds_read_b64_tr_b16 v[72:73], v17 offset:192\n\t"
      "ds_read_b64_tr_b16 v[74:75], v17 offset:3776\n\t"
      "s_waitcnt lgkmcnt(10)\n\t"
      "v_mfma_f32_32x32x16_bf16 v[208:223], v[80:83], v[112:115], v[208:223]\n\t"
      "ds_read_b64_tr_b16 v[76:77], v17 offset:256\n\t"
      "ds_read_b64_tr_b16 v[78:79], v17 offset:3840\n\t"
      "s_waitcnt vmcnt(13)\n\t"
      "v_max3_u32 v40, v116, v117, v118\n\t"
      "v_max_u32 v40, v40, v119\n\t"
      "v_mfma_f32_32x32x16_bf16 v[224:239], v[84:87], v[112:115], v[224:239]\n\t"
      "ds_read_b64_tr_b16 v[80:81], v17 offset:320\n\t"
      "ds_read_b64_tr_b16 v[82:83], v17 offset:3904\n\t"
      "v_cmp_eq_u32 vcc, -1, v40\n\t"
      "s_nop 0\n\t"
      "s_cbranch_vccnz qo7_%=\n\t"
      "qr7_%=:\n\t"
      "s_waitcnt lgkmcnt(10)\n\t"
      "v_mfma_f32_32x32x16_bf16 v[240:255], v[56:59], v[112:115], v[240:255]\n\t"
      "ds_read_b64_tr_b16 v[84:85], v17 offset:384\n\t"
      "ds_read_b64_tr_b16 v[86:87], v17 offset:3968\n\t"
      "s_add_u32 m0, s86, 143360\n\t"
      "s_add_u32 s48, s87, 286720\n\t"
      "buffer_load_dwordx4 %[tid], s[24:27], s48 offen lds\n\t"
      "v_mfma_f32_32x32x16_bf16 v[144:159], v[60:63], v[116:119], v[144:159]\n\t"
      "ds_read_b64_tr_b16 v[56:57], v17 offset:12544\n\t"
      "ds_read_b64_tr_b16 v[58:59], v17 offset:16128\n\t"
      "s_add_u32 s48, s84, 13312\n\t"
      "buffer_load_dwordx4 v[136:139], v2, s[28:31], s48 offen sc1\n\t"
      "s_waitcnt lgkmcnt(10)\n\t"
      "v_mfma_f32_32x32x16_bf16 v[160:175], v[64:67], v[116:119], v[160:175]\n\t"
      "ds_read_b64_tr_b16 v[60:61], v17 offset:12608\n\t"
      "ds_read_b64_tr_b16 v[62:63], v17 offset:16192\n\t"
      "v_mfma_f32_32x32x16_bf16 v[176:191], v[68:71], v[116:119], v[176:191]\n\t"
      "ds_read_b64_tr_b16 v[64:65], v17 offset:12672\n\t"
      "ds_read_b64_tr_b16 v[66:67], v17 offset:16256\n\t"
      "s_add_u32 s48, s84, 14336\n\t"
      "buffer_load_dwordx4 v[140:143], v2, s[28:31], s48 offen sc1\n\t"
      "s_waitcnt lgkmcnt(10)\n\t"
      "v_mfma_f32_32x32x16_bf16 v[192:207], v[72:75], v[116:119], v[192:207]\n\t"
      "ds_read_b64_tr_b16 v[68:69], v17 offset:12736\n\t"
      "ds_read_b64_tr_b16 v[70:71], v17 offset:16320\n\t"
      "v_mfma_f32_32x32x16_bf16 v[208:223], v[76:79], v[116:119], v[208:223]\n\t"
      "ds_read_b64_tr_b16 v[72:73], v17 offset:12800\n\t"
      "ds_read_b64_tr_b16 v[74:75], v17 offset:16384\n\t"
      "s_waitcnt vmcnt(13)\n\t"
      "v_max3_u32 v40, v120, v121, v122\n\t"
      "v_max_u32 v40, v40, v123\n\t"
      "s_waitcnt lgkmcnt(10)\n\t"
      "v_mfma_f32_32x32x16_bf16 v[224:239], v[80:83], v[116:119], v[224:239]\n\t"
      "ds_read_b64_tr_b16 v[76:77], v17 offset:12864\n\t"
      "ds_read_b64_tr_b16 v[78:79], v17 offset:16448\n\t"
      "v_cmp_eq_u32 vcc, -1, v40\n\t"
      "s_nop 0\n\t"
      "s_cbranch_vccnz qo8_%=\n\t"
      "qr8_%=:\n\t"
      "v_mfma_f32_32x32x16_bf16 v[240:255], v[84:87], v[116:119], v[240:255]\n\t"
      "ds_read_b64_tr_b16 v[80:81], v17 offset:12928\n\t"
      "ds_read_b64_tr_b16 v[82:83], v17 offset:16512\n\t"
      "s_barrier\n\t"
      "s_waitcnt lgkmcnt(10)\n\t"
      "v_mfma_f32_32x32x16_bf16 v[144:159], v[56:59], v[120:123], v[144:159]\n\t"
      "ds_read_b64_tr_b16 v[84:85], v17 offset:25088\n\t"
      "ds_read_b64_tr_b16 v[86:87], v17 offset:28672\n\t"
      "s_add_u32 s48, s84, 16384\n\t"
      "buffer_load_dwordx4 v[88:91], v2, s[28:31], s48 offen sc1\n\t"
      "v_mfma_f32_32x32x16_bf16 v[160:175], v[60:63], v[120:123], v[160:175]\n\t"
      "ds_read_b64_tr_b16 v[56:57], v17 offset:25152\n\t"
      "ds_read_b64_tr_b16 v[58:59], v17 offset:28736\n\t"
      "s_waitcnt lgkmcnt(10)\n\t"
      "v_mfma_f32_32x32x16_bf16 v[176:191], v[64:67], v[120:123], v[176:191]\n\t"
      "ds_read_b64_tr_b16 v[60:61], v17 offset:25216\n\t"
      "ds_read_b64_tr_b16 v[62:63], v17 offset:28800\n\t"
      "v_mfma_f32_32x32x16_bf16 v[192:207], v[68:71], v[120:123], v[192:207]\n\t"
      "ds_read_b64_tr_b16 v[64:65], v17 offset:25280\n\t"
      "ds_read_b64_tr_b16 v[66:67], v17 offset:28864\n\t"
      "s_add_u32 m0, s85, 7168\n\t"
      "s_add_u32 s48, s87, 315392\n\t"
      "buffer_load_dwordx4 v23, s[24:27], s48 offen lds\n\t"
      "s_waitcnt lgkmcnt(10)\n\t"
      "v_mfma_f32_32x32x16_bf16 v[208:223], v[72:75], v[120:123], v[208:223]\n\t"
      "ds_read_b64_tr_b16 v[68:69], v17 offset:25344\n\t"
      "ds_read_b64_tr_b16 v[70:71], v17 offset:28928\n\t"
      "s_waitcnt vmcnt(12)\n\t"
      "v_max3_u32 v40, v124, v125, v126\n\t"
      "v_max_u32 v40, v40, v127\n\t"
      "v_mfma_f32_32x32x16_bf16 v[224:239], v[76:79], v[120:123], v[224:239]\n\t"
      "ds_read_b64_tr_b16 v[72:73], v17 offset:25408\n\t"
      "ds_read_b64_tr_b16 v[74:75], v17 offset:28992\n\t"
      "v_cmp_eq_u32 vcc, -1, v40\n\t"
      "s_nop 0\n\t"
      "s_cbranch_vccnz qo9_%=\n\t"
      "qr9_%=:\n\t"
      "s_waitcnt lgkmcnt(10)\n\t"
      "v_mfma_f32_32x32x16_bf16 v[240:255], v[80:83], v[120:123], v[240:255]\n\t"
      "ds_read_b64_tr_b16 v[76:77], v17 offset:25472\n\t"
      "ds_read_b64_tr_b16 v[78:79], v17 offset:29056\n\t"
      "s_add_u32 m0, s85, 0\n\t"
      "s_add_u32 s48, s87, 301056\n\t"
      "buffer_load_dwordx4 v23, s[24:27], s48 offen lds\n\t"
      "v_mfma_f32_32x32x16_bf16 v[144:159], v[84:87], v[124:127], v[144:159]\n\t"
      "ds_read_b64_tr_b16 v[80:81], v17 offset:37632\n\t"
      "ds_read_b64_tr_b16 v[82:83], v17 offset:41216\n\t"
      "s_add_u32 s48, s84, 17408\n\t"
      "buffer_load_dwordx4 v[92:95], v2, s[28:31], s48 offen sc1\n\t"
      "s_waitcnt lgkmcnt(10)\n\t"
      "v_mfma_f32_32x32x16_bf16 v[160:175], v[56:59], v[124:127], v[160:175]\n\t"
      "ds_read_b64_tr_b16 v[84:85], v17 offset:37696\n\t"
      "ds_read_b64_tr_b16 v[86:87], v17 offset:41280\n\t"
      "v_mfma_f32_32x32x16_bf16 v[176:191], v[60:63], v[124:127], v[176:191]\n\t"
      "ds_read_b64_tr_b16 v[56:57], v17 offset:37760\n\t"
      "ds_read_b64_tr_b16 v[58:59], v17 offset:41344\n\t"
      "s_add_u32 s48, s84, 18432\n\t"
      "buffer_load_dwordx4 v[96:99], v2, s[28:31], s48 offen sc1\n\t"
      "s_waitcnt lgkmcnt(10)\n\t"
      "v_mfma_f32_32x32x16_bf16 v[192:207], v[64:67], v[124:127], v[192:207]\n\t"
      "ds_read_b64_tr_b16 v[60:61], v17 offset:37824\n\t"
      "ds_read_b64_tr_b16 v[62:63], v17 offset:41408\n\t"
      "s_add_u32 m0, s85, 21504\n\t"
      "s_add_u32 s48, s87, 344064\n\t"
      "buffer_load_dwordx4 v23, s[24:27], s48 offen lds\n\t"
      "v_mfma_f32_32x32x16_bf16 v[208:223], v[68:71], v[124:127], v[208:223]\n\t"
      "ds_read_b64_tr_b16 v[64:65], v17 offset:37888\n\t"
      "ds_read_b64_tr_b16 v[66:67], v17 offset:41472\n\t"
      "s_waitcnt vmcnt(13)\n\t"
      "v_max3_u32 v40, v128, v129, v130\n\t"
      "v_max_u32 v40, v40, v131\n\t"
      "s_waitcnt lgkmcnt(10)\n\t"
      "v_mfma_f32_32x32x16_bf16 v[224:239], v[72:75], v[124:127], v[224:239]\n\t"
      "ds_read_b64_tr_b16 v[68:69], v17 offset:37952\n\t"
      "ds_read_b64_tr_b16 v[70:71], v17 offset:41536\n\t"
      "v_cmp_eq_u32 vcc, -1, v40\n\t"
      "s_nop 0\n\t"
      "s_cbranch_vccnz qo10_%=\n\t"
      "qr10_%=:\n\t"
      "v_mfma_f32_32x32x16_bf16 v[240:255], v[76:79], v[124:127], v[240:255]\n\t"
      "ds_read_b64_tr_b16 v[72:73], v17 offset:38016\n\t"
      "ds_read_b64_tr_b16 v[74:75], v17 offset:41600\n\t"
      "s_add_u32 m0, s85, 14336\n\t"
      "s_add_u32 s48, s87, 329728\n\t"
      "buffer_load_dwordx4 v23, s[24:27], s48 offen lds\n\t"
      "s_waitcnt lgkmcnt(10)\n\t"
      "v_mfma_f32_32x32x16_bf16 v[144:159], v[80:83], v[128:131], v[144:159]\n\t"
      "ds_read_b64_tr_b16 v[76:77], v22 offset:7168\n\t"
      "ds_read_b64_tr_b16 v[78:79], v22 offset:10752\n\t"
      "s_add_u32 s48, s84, 19456\n\t"
      "buffer_load_dwordx4 v[100:103], v2, s[28:31], s48 offen sc1\n\t"
      "v_mfma_f32_32x32x16_bf16 v[160:175], v[84:87], v[128:131], v[160:175]\n\t"
      "ds_read_b64_tr_b16 v[80:81], v22 offset:7232\n\t"
      "ds_read_b64_tr_b16 v[82:83], v22 offset:10816\n\t"
      "s_waitcnt lgkmcnt(10)\n\t"
      "v_mfma_f32_32x32x16_bf16 v[176:191], v[56:59], v[128:131], v[176:191]\n\t"
      "ds_read_b64_tr_b16 v[84:85], v22 offset:7296\n\t"
      "ds_read_b64_tr_b16 v[86:87], v22 offset:10880\n\t"
      "s_add_u32 s48, s84, 20480\n\t"
      "buffer_load_dwordx4 v[104:107], v2, s[28:31], s48 offen sc1\n\t"
      "v_mfma_f32_32x32x16_bf16 v[192:207], v[60:63], v[128:131], v[192:207]\n\t"
      "ds_read_b64_tr_b16 v[56:57], v22 offset:7360\n\t"
      "ds_read_b64_tr_b16 v[58:59], v22 offset:10944\n\t"
      "s_add_u32 m0, s85, 35840\n\t"
      "s_add_u32 s48, s87, 372736\n\t"
      "buffer_load_dwordx4 v23, s[24:27], s48 offen lds\n\t"
      "s_waitcnt lgkmcnt(10)\n\t"
      "v_mfma_f32_32x32x16_bf16 v[208:223], v[64:67], v[128:131], v[208:223]\n\t"
      "ds_read_b64_tr_b16 v[60:61], v22 offset:7424\n\t"
      "ds_read_b64_tr_b16 v[62:63], v22 offset:11008\n\t"
      "s_waitcnt vmcnt(14)\n\t"
      "v_max3_u32 v40, v132, v133, v134\n\t"
      "v_max_u32 v40, v40, v135\n\t"
      "v_mfma_f32_32x32x16_bf16 v[224:239], v[68:71], v[128:131], v[224:239]\n\t"
      "ds_read_b64_tr_b16 v[64:65], v22 offset:7488\n\t"
      "ds_read_b64_tr_b16 v[66:67], v22 offset:11072\n\t"
      "v_cmp_eq_u32 vcc, -1, v40\n\t"
      "s_nop 0\n\t"
      "s_cbranch_vccnz qo11_%=\n\t"
      "qr11_%=:\n\t"
      "s_waitcnt lgkmcnt(10)\n\t"
      "v_mfma_f32_32x32x16_bf16 v[240:255], v[72:75], v[128:131], v[240:255]\n\t"
      "ds_read_b64_tr_b16 v[68:69], v22 offset:7552\n\t"
      "ds_read_b64_tr_b16 v[70:71], v22 offset:11136\n\t"
      "s_add_u32 m0, s85, 28672\n\t"
      "s_add_u32 s48, s87, 358400\n\t"
      "buffer_load_dwordx4 v23, s[24:27], s48 offen lds\n\t"
      "v_mfma_f32_32x32x16_bf16 v[144:159], v[76:79], v[132:135], v[144:159]\n\t"
      "ds_read_b64_tr_b16 v[72:73], v22 offset:32256\n\t"
      "ds_read_b64_tr_b16 v[74:75], v22 offset:35840\n\t"
      "s_add_u32 s48, s84, 21504\n\t"
      "buffer_load_dwordx4 v[108:111], v2, s[28:31], s48 offen sc1\n\t"
      "s_waitcnt lgkmcnt(10)\n\t"
      "v_mfma_f32_32x32x16_bf16 v[160:175], v[80:83], v[132:135], v[160:175]\n\t"
      "ds_read_b64_tr_b16 v[76:77], v22 offset:32320\n\t"
      "ds_read_b64_tr_b16 v[78:79], v22 offset:35904\n\t"
      "v_mfma_f32_32x32x16_bf16 v[176:191], v[84:87], v[132:135], v[176:191]\n\t"
      "ds_read_b64_tr_b16 v[80:81], v22 offset:32384\n\t"
      "ds_read_b64_tr_b16 v[82:83], v22 offset:35968\n\t"
      "s_waitcnt lgkmcnt(10)\n\t"
      "v_mfma_f32_32x32x16_bf16 v[192:207], v[56:59], v[132:135], v[192:207]\n\t"
      "ds_read_b64_tr_b16 v[84:85], v22 offset:32448\n\t"
      "ds_read_b64_tr_b16 v[86:87], v22 offset:36032\n\t"
      "s_add_u32 m0, s86, 0\n\t"
      "s_add_u32 s48, s87, 301056\n\t"
      "buffer_load_dwordx4 %[tid], s[24:27], s48 offen lds\n\t"
      "v_mfma_f32_32x32x16_bf16 v[208:223], v[60:63], v[132:135], v[208:223]\n\t"
      "ds_read_b64_tr_b16 v[56:57], v22 offset:32512\n\t"
      "ds_read_b64_tr_b16 v[58:59], v22 offset:36096\n\t"
      "s_waitcnt vmcnt(14)\n\t"
      "v_max3_u32 v40, v136, v137, v138\n\t"
      "v_max_u32 v40, v40, v139\n\t"
      "s_waitcnt lgkmcnt(10)\n\t"
      "v_mfma_f32_32x32x16_bf16 v[224:239], v[64:67], v[132:135], v[224:239]\n\t"
      "ds_read_b64_tr_b16 v[60:61], v22 offset:32576\n\t"
      "ds_read_b64_tr_b16 v[62:63], v22 offset:36160\n\t"
      "v_cmp_eq_u32 vcc, -1, v40\n\t"
      "s_nop 0\n\t"
      "s_cbranch_vccnz qo12_%=\n\t"
      "qr12_%=:\n\t"
      "v_mfma_f32_32x32x16_bf16 v[240:255], v[68:71], v[132:135], v[240:255]\n\t"
      "ds_read_b64_tr_b16 v[64:65], v22 offset:32640\n\t"
      "ds_read_b64_tr_b16 v[66:67], v22 offset:36224\n\t"
      "s_add_u32 m0, s85, 43008\n\t"
      "s_add_u32 s48, s87, 387072\n\t"
      "buffer_load_dwordx4 v23, s[24:27], s48 offen lds\n\t"
      "s_waitcnt lgkmcnt(10)\n\t"
      "v_mfma_f32_32x32x16_bf16 v[144:159], v[72:75], v[136:139], v[144:159]\n\t"
      "ds_read_b64_tr_b16 v[68:69], v26 offset:8960\n\t"
      "ds_read_b64_tr_b16 v[70:71], v26 offset:21504\n\t"
      "s_add_u32 s48, s84, 22528\n\t"
      "buffer_load_dwordx4 v[112:115], v2, s[28:31], s48 offen sc1\n\t"
      "v_mfma_f32_32x32x16_bf16 v[160:175], v[76:79], v[136:139], v[160:175]\n\t"
      "ds_read_b64_tr_b16 v[72:73], v26 offset:9024\n\t"
      "ds_read_b64_tr_b16 v[74:75], v26 offset:21568\n\t"
      "s_waitcnt lgkmcnt(10)\n\t"
      "v_mfma_f32_32x32x16_bf16 v[176:191], v[80:83], v[136:139], v[176:191]\n\t"
      "ds_read_b64_tr_b16 v[76:77], v26 offset:9088\n\t"
      "ds_read_b64_tr_b16 v[78:79], v26 offset:21632\n\t"
      "s_add_u32 s48, s84, 24576\n\t"
      "buffer_load_dwordx4 v[116:119], v2, s[28:31], s48 offen sc1\n\t"
      "v_mfma_f32_32x32x16_bf16 v[192:207], v[84:87], v[136:139], v[192:207]\n\t"
      "ds_read_b64_tr_b16 v[80:81], v26 offset:9152\n\t"
      "ds_read_b64_tr_b16 v[82:83], v26 offset:21696\n\t"
      "s_waitcnt lgkmcnt(10)\n\t"
      "v_mfma_f32_32x32x16_bf16 v[208:223], v[56:59], v[136:139], v[208:223]\n\t"
      "ds_read_b64_tr_b16 v[84:85], v26 offset:9216\n\t"
      "ds_read_b64_tr_b16 v[86:87], v26 offset:21760\n\t"
      "s_waitcnt vmcnt(16)\n\t"
      "v_max3_u32 v40, v140, v141, v142\n\t"
      "v_max_u32 v40, v40, v143\n\t"
      "v_mfma_f32_32x32x16_bf16 v[224:239], v[60:63], v[136:139], v[224:239]\n\t"
      "ds_read_b64_tr_b16 v[56:57], v26 offset:9280\n\t"
      "ds_read_b64_tr_b16 v[58:59], v26 offset:21824\n\t"
      "v_cmp_eq_u32 vcc, -1, v40\n\t"
      "s_nop 0\n\t"
      "s_cbranch_vccnz qo13_%=\n\t"
      "qr13_%=:\n\t"
      "s_waitcnt lgkmcnt(10)\n\t"
      "v_mfma_f32_32x32x16_bf16 v[240:255], v[64:67], v[136:139], v[240:255]\n\t"
      "ds_read_b64_tr_b16 v[60:61], v26 offset:9344\n\t"
      "ds_read_b64_tr_b16 v[62:63], v26 offset:21888\n\t"
      "s_waitcnt vmcnt(18)\n\t"
      "s_barrier\n\t"
      "s_add_u32 m0, s86, 7168\n\t"
      "s_add_u32 s48, s87, 315392\n\t"
      "buffer_load_dwordx4 %[tid], s[24:27], s48 offen lds\n\t"
      "v_mfma_f32_32x32x16_bf16 v[144:159], v[68:71], v[140:143], v[144:159]\n\t"
      "ds_read_b64_tr_b16 v[64:65], v19 offset:0\n\t"
      "ds_read_b64_tr_b16 v[66:67], v19 offset:3584\n\t"
      "s_add_u32 s48, s84, 25600\n\t"
      "buffer_load_dwordx4 v[120:123], v2, s[28:31], s48 offen sc1\n\t"
      "s_waitcnt lgkmcnt(10)\n\t"
      "v_mfma_f32_32x32x16_bf16 v[160:175], v[72:75], v[140:143], v[160:175]\n\t"
      "ds_read_b64_tr_b16 v[68:69], v19 offset:64\n\t"
      "ds_read_b64_tr_b16 v[70:71], v19 offset:3648\n\t"
      "v_mfma_f32_32x32x16_bf16 v[176:191], v[76:79], v[140:143], v[176:191]\n\t"
      "ds_read_b64_tr_b16 v[72:73], v19 offset:128\n\t"
      "ds_read_b64_tr_b16 v[74:75], v19 offset:3712\n\t"
      "s_add_u32 s48, s84, 26624\n\t"
      "buffer_load_dwordx4 v[124:127], v2, s[28:31], s48 offen sc1\n\t"
      "s_waitcnt lgkmcnt(10)\n\t"
      "v_mfma_f32_32x32x16_bf16 v[192:207], v[80:83], v[140:143], v[192:207]\n\t"
      "ds_read_b64_tr_b16 v[76:77], v19 offset:192\n\t"
      "ds_read_b64_tr_b16 v[78:79], v19 offset:3776\n\t"
      "v_mfma_f32_32x32x16_bf16 v[208:223], v[84:87], v[140:143], v[208:223]\n\t"
      "ds_read_b64_tr_b16 v[80:81], v19 offset:256\n\t"
      "ds_read_b64_tr_b16 v[82:83], v19 offset:3840\n\t"
      "s_waitcnt vmcnt(18)\n\t"
      "v_max3_u32 v40, v88, v89, v90\n\t"
      "v_max_u32 v40, v40, v91\n\t"
      "s_waitcnt lgkmcnt(10)\n\t"
      "v_mfma_f32_32x32x16_bf16 v[224:239], v[56:59], v[140:143], v[224:239]\n\t"
      "ds_read_b64_tr_b16 v[84:85], v19 offset:320\n\t"
      "ds_read_b64_tr_b16 v[86:87], v19 offset:3904\n\t"
      "v_cmp_eq_u32 vcc, -1, v40\n\t"
      "s_nop 0\n\t"
      "s_cbranch_vccnz qo14_%=\n\t"
      "qr14_%=:\n\t"
      "v_mfma_f32_32x32x16_bf16 v[240:255], v[60:63], v[140:143], v[240:255]\n\t"
      "ds_read_b64_tr_b16 v[56:57], v19 offset:384\n\t"
      "ds_read_b64_tr_b16 v[58:59], v19 offset:3968\n\t"
      "s_add_u32 m0, s86, 14336\n\t"
      "s_add_u32 s48, s87, 329728\n\t"
      "buffer_load_dwordx4 %[tid], s[24:27], s48 offen lds\n\t"
      "s_waitcnt lgkmcnt(10)\n\t"
      "v_mfma_f32_32x32x16_bf16 v[144:159], v[64:67], v[88:91], v[144:159]\n\t"
      "ds_read_b64_tr_b16 v[60:61], v19 offset:12544\n\t"
      "ds_read_b64_tr_b16 v[62:63], v19 offset:16128\n\t"
      "s_add_u32 s48, s84, 27648\n\t"
      "buffer_load_dwordx4 v[128:131], v2, s[28:31], s48 offen sc1\n\t"
      "v_mfma_f32_32x32x16_bf16 v[160:175], v[68:71], v[88:91], v[160:175]\n\t"
      "ds_read_b64_tr_b16 v[64:65], v19 offset:12608\n\t"
      "ds_read_b64_tr_b16 v[66:67], v19 offset:16192\n\t"
      "s_add_u32 m0, s86, 21504\n\t"
      "s_add_u32 s48, s87, 344064\n\t"
      "buffer_load_dwordx4 %[tid], s[24:27], s48 offen lds\n\t"
      "s_waitcnt lgkmcnt(10)\n\t"
      "v_mfma_f32_32x32x16_bf16 v[176:191], v[72:75], v[88:91], v[176:191]\n\t"
      "ds_read_b64_tr_b16 v[68:69], v19 offset:12672\n\t"
      "ds_read_b64_tr_b16 v[70:71], v19 offset:16256\n\t"
      "v_mfma_f32_32x32x16_bf16 v[192:207], v[76:79], v[88:91], v[192:207]\n\t"
      "ds_read_b64_tr_b16 v[72:73], v19 offset:12736\n\t"
      "ds_read_b64_tr_b16 v[74:75], v19 offset:16320\n\t"
      "s_waitcnt vmcnt(18)\n\t"
      "v_max3_u32 v40, v92, v93, v94\n\t"
      "v_max_u32 v40, v40, v95\n\t"
      "s_waitcnt lgkmcnt(10)\n\t"
      "v_mfma_f32_32x32x16_bf16 v[208:223], v[80:83], v[88:91], v[208:223]\n\t"
      "ds_read_b64_tr_b16 v[76:77], v19 offset:12800\n\t"
      "ds_read_b64_tr_b16 v[78:79], v19 offset:16384\n\t"
      "v_cmp_eq_u32 vcc, -1, v40\n\t"
      "s_nop 0\n\t"
      "s_cbranch_vccnz qo15_%=\n\t"
      "qr15_%=:\n\t"
      "v_mfma_f32_32x32x16_bf16 v[224:239], v[84:87], v[88:91], v[224:239]\n\t"
      "ds_read_b64_tr_b16 v[80:81], v19 offset:12864\n\t"
      "ds_read_b64_tr_b16 v[82:83], v19 offset:16448\n\t"
      "s_waitcnt vmcnt(17)\n\t"
      "v_max3_u32 v40, v96, v97, v98\n\t"
      "v_max_u32 v40, v40, v99\n\t"
      "s_waitcnt lgkmcnt(10)\n\t"
      "v_mfma_f32_32x32x16_bf16 v[240:255], v[56:59], v[88:91], v[240:255]\n\t"
      "ds_read_b64_tr_b16 v[84:85], v19 offset:12928\n\t"
      "ds_read_b64_tr_b16 v[86:87], v19 offset:16512\n\t"
      "v_cmp_eq_u32 vcc, -1, v40\n\t"
      "s_nop 0\n\t"
      "s_cbranch_vccnz qo16_%=\n\t"
      "qr16_%=:\n\t"
      "v_mfma_f32_32x32x16_bf16 v[144:159], v[60:63], v[92:95], v[144:159]\n\t"
      "ds_read_b64_tr_b16 v[56:57], v19 offset:25088\n\t"
      "ds_read_b64_tr_b16 v[58:59], v19 offset:28672\n\t"
      "s_add_u32 s48, s84, 28672\n\t"
      "buffer_load_dwordx4 v[132:135], v2, s[28:31], s48 offen sc1\n\t"
      "s_waitcnt lgkmcnt(10)\n\t"
      "v_mfma_f32_32x32x16_bf16 v[160:175], v[64:67], v[92:95], v[160:175]\n\t"
      "ds_read_b64_tr_b16 v[60:61], v19 offset:25152\n\t"
      "ds_read_b64_tr_b16 v[62:63], v19 offset:28736\n\t"
      "s_add_u32 m0, s86, 28672\n\t"
      "s_add_u32 s48, s87, 358400\n\t"
      "buffer_load_dwordx4 %[tid], s[24:27], s48 offen lds\n\t"
      "v_mfma_f32_32x32x16_bf16 v[176:191], v[68:71], v[92:95], v[176:191]\n\t"
      "ds_read_b64_tr_b16 v[64:65], v19 offset:25216\n\t"
      "ds_read_b64_tr_b16 v[66:67], v19 offset:28800\n\t"
      "s_add_u32 s48, s84, 29696\n\t"
      "buffer_load_dwordx4 v[136:139], v2, s[28:31], s48 offen sc1\n\t"
      "s_waitcnt lgkmcnt(10)\n\t"
      "v_mfma_f32_32x32x16_bf16 v[192:207], v[72:75], v[92:95], v[192:207]\n\t"
      "ds_read_b64_tr_b16 v[68:69], v19 offset:25280\n\t"
      "ds_read_b64_tr_b16 v[70:71], v19 offset:28864\n\t"
      "s_waitcnt vmcnt(17)\n\t"
      "v_max3_u32 v40, v100, v101, v102\n\t"
      "v_max_u32 v40, v40, v103\n\t"
      "v_mfma_f32_32x32x16_bf16 v[208:223], v[76:79], v[92:95], v[208:223]\n\t"
      "ds_read_b64_tr_b16 v[72:73], v19 offset:25344\n\t"
      "ds_read_b64_tr_b16 v[74:75], v19 offset:28928\n\t"
      "v_cmp_eq_u32 vcc, -1, v40\n\t"
      "s_nop 0\n\t"
      "s_cbranch_vccnz qo17_%=\n\t"
      "qr17_%=:\n\t"
      "s_waitcnt lgkmcnt(10)\n\t"
      "v_mfma_f32_32x32x16_bf16 v[224:239], v[80:83], v[92:95], v[224:239]\n\t"
      "ds_read_b64_tr_b16 v[76:77], v19 offset:25408\n\t"
      "ds_read_b64_tr_b16 v[78:79], v19 offset:28992\n\t"
      "s_waitcnt vmcnt(16)\n\t"
      "v_max3_u32 v40, v104, v105, v106\n\t"
      "v_max_u32 v40, v40, v107\n\t"
      "v_mfma_f32_32x32x16_bf16 v[240:255], v[84:87], v[92:95], v[240:255]\n\t"
      "ds_read_b64_tr_b16 v[80:81], v19 offset:25472\n\t"
      "ds_read_b64_tr_b16 v[82:83], v19 offset:29056\n\t"
      "v_cmp_eq_u32 vcc, -1, v40\n\t"
      "s_nop 0\n\t"
      "s_cbranch_vccnz qo18_%=\n\t"
      "qr18_%=:\n\t"
      "s_waitcnt lgkmcnt(10)\n\t"
      "v_mfma_f32_32x32x16_bf16 v[144:159], v[56:59], v[96:99], v[144:159]\n\t"
      "ds_read_b64_tr_b16 v[84:85], v19 offset:37632\n\t"
      "ds_read_b64_tr_b16 v[86:87], v19 offset:41216\n\t"
      "s_add_u32 s48, s84, 30720\n\t"
      "buffer_load_dwordx4 v[140:143], v2, s[28:31], s48 offen sc1\n\t"
      "v_mfma_f32_32x32x16_bf16 v[160:175], v[60:63], v[96:99], v[160:175]\n\t"
      "ds_read_b64_tr_b16 v[56:57], v19 offset:37696\n\t"
      "ds_read_b64_tr_b16 v[58:59], v19 offset:41280\n\t"
      "s_add_u32 m0, s86, 35840\n\t"
      "s_add_u32 s48, s87, 372736\n\t"
      "buffer_load_dwordx4 %[tid], s[24:27], s48 offen lds\n\t"
      "s_waitcnt lgkmcnt(10)\n\t"
      "v_mfma_f32_32x32x16_bf16 v[176:191], v[64:67], v[96:99], v[176:191]\n\t"
      "ds_read_b64_tr_b16 v[60:61], v19 offset:37760\n\t"
      "ds_read_b64_tr_b16 v[62:63], v19 offset:41344\n\t"
      "v_mfma_f32_32x32x16_bf16 v[192:207], v[68:71], v[96:99], v[192:207]\n\t"
      "ds_read_b64_tr_b16 v[64:65], v19 offset:37824\n\t"
      "ds_read_b64_tr_b16 v[66:67], v19 offset:41408\n\t"
      "s_waitcnt vmcnt(15)\n\t"
      "v_max3_u32 v40, v108, v109, v110\n\t"
      "v_max_u32 v40, v40, v111\n\t"
      "s_waitcnt lgkmcnt(10)\n\t"
      "v_mfma_f32_32x32x16_bf16 v[208:223], v[72:75], v[96:99], v[208:223]\n\t"
      "ds_read_b64_tr_b16 v[68:69], v19 offset:37888\n\t"
      "ds_read_b64_tr_b16 v[70:71], v19 offset:41472\n\t"
      "v_cmp_eq_u32 vcc, -1, v40\n\t"
      "s_nop 0\n\t"
      "s_cbranch_vccnz qo19_%=\n\t"
      "qr19_%=:\n\t"
      "v_mfma_f32_32x32x16_bf16 v[224:239], v[76:79], v[96:99], v[224:239]\n\t"
      "ds_read_b64_tr_b16 v[72:73], v19 offset:37952\n\t"
      "ds_read_b64_tr_b16 v[74:75], v19 offset:41536\n\t"
      "s_waitcnt vmcnt(12)\n\t"
      "v_max3_u32 v40, v112, v113, v114\n\t"
      "v_max_u32 v40, v40, v115\n\t"
      "s_waitcnt lgkmcnt(10)\n\t"
      "v_mfma_f32_32x32x16_bf16 v[240:255], v[80:83], v[96:99], v[240:255]\n\t"
      "ds_read_b64_tr_b16 v[76:77], v19 offset:38016\n\t"
      "ds_read_b64_tr_b16 v[78:79], v19 offset:41600\n\t"
      "v_cmp_eq_u32 vcc, -1, v40\n\t"
      "s_nop 0\n\t"
      "s_cbranch_vccnz qo20_%=\n\t"
      "qr20_%=:\n\t"
      "v_mfma_f32_32x32x16_bf16 v[144:159], v[84:87], v[100:103], v[144:159]\n\t"
      "ds_read_b64_tr_b16 v[80:81], v24 offset:7168\n\t"
      "ds_read_b64_tr_b16 v[82:83], v24 offset:10752\n\t"
      "s_waitcnt lgkmcnt(10)\n\t"
      "v_mfma_f32_32x32x16_bf16 v[160:175], v[56:59], v[100:103], v[160:175]\n\t"
      "ds_read_b64_tr_b16 v[84:85], v24 offset:7232\n\t"
      "ds_read_b64_tr_b16 v[86:87], v24 offset:10816\n\t"
      "s_add_u32 m0, s86, 43008\n\t"
      "s_add_u32 s48, s87, 387072\n\t"
      "buffer_load_dwordx4 %[tid], s[24:27], s48 offen lds\n\t"
      "v_mfma_f32_32x32x16_bf16 v[176:191], v[60:63], v[100:103], v[176:191]\n\t"
      "ds_read_b64_tr_b16 v[56:57], v24 offset:7296\n\t"
      "ds_read_b64_tr_b16 v[58:59], v24 offset:10880\n\t"
      "s_waitcnt lgkmcnt(10)\n\t"
      "v_mfma_f32_32x32x16_bf16 v[192:207], v[64:67], v[100:103], v[192:207]\n\t"
      "ds_read_b64_tr_b16 v[60:61], v24 offset:7360\n\t"
      "ds_read_b64_tr_b16 v[62:63], v24 offset:10944\n\t"
      "s_waitcnt vmcnt(12)\n\t"
      "v_max3_u32 v40, v116, v117, v118\n\t"
      "v_max_u32 v40, v40, v119\n\t"
      "v_mfma_f32_32x32x16_bf16 v[208:223], v[68:71], v[100:103], v[208:223]\n\t"
      "ds_read_b64_tr_b16 v[64:65], v24 offset:7424\n\t"
      "ds_read_b64_tr_b16 v[66:67], v24 offset:11008\n\t"
      "v_cmp_eq_u32 vcc, -1, v40\n\t"
      "s_nop 0\n\t"
      "s_cbranch_vccnz qo21_%=\n\t"
      "qr21_%=:\n\t"
      "s_waitcnt lgkmcnt(10)\n\t"
      "v_mfma_f32_32x32x16_bf16 v[224:239], v[72:75], v[100:103], v[224:239]\n\t"
      "ds_read_b64_tr_b16 v[68:69], v24 offset:7488\n\t"
      "ds_read_b64_tr_b16 v[70:71], v24 offset:11072\n\t"
      "s_waitcnt vmcnt(10)\n\t"
      "v_max3_u32 v40, v120, v121, v122\n\t"
      "v_max_u32 v40, v40, v123\n\t"
      "v_mfma_f32_32x32x16_bf16 v[240:255], v[76:79], v[100:103], v[240:255]\n\t"
      "ds_read_b64_tr_b16 v[72:73], v24 offset:7552\n\t"
      "ds_read_b64_tr_b16 v[74:75], v24 offset:11136\n\t"
      "v_cmp_eq_u32 vcc, -1, v40\n\t"
      "s_nop 0\n\t"
      "s_cbranch_vccnz qo22_%=\n\t"
      "qr22_%=:\n\t"
      "s_waitcnt lgkmcnt(10)\n\t"
      "v_mfma_f32_32x32x16_bf16 v[144:159], v[80:83], v[104:107], v[144:159]\n\t"
      "ds_read_b64_tr_b16 v[76:77], v24 offset:32256\n\t"
      "ds_read_b64_tr_b16 v[78:79], v24 offset:35840\n\t"
      "v_mfma_f32_32x32x16_bf16 v[160:175], v[84:87], v[104:107], v[160:175]\n\t"
      "ds_read_b64_tr_b16 v[80:81], v24 offset:32320\n\t"
      "ds_read_b64_tr_b16 v[82:83], v24 offset:35904\n\t"
      "s_waitcnt lgkmcnt(10)\n\t"
      "v_mfma_f32_32x32x16_bf16 v[176:191], v[56:59], v[104:107], v[176:191]\n\t"
      "ds_read_b64_tr_b16 v[84:85], v24 offset:32384\n\t"
      "ds_read_b64_tr_b16 v[86:87], v24 offset:35968\n\t"
      "v_mfma_f32_32x32x16_bf16 v[192:207], v[60:63], v[104:107], v[192:207]\n\t"
      "ds_read_b64_tr_b16 v[56:57], v24 offset:32448\n\t"
      "ds_read_b64_tr_b16 v[58:59], v24 offset:36032\n\t"
      "s_waitcnt vmcnt(9)\n\t"
      "v_max3_u32 v40, v124, v125, v126\n\t"
      "v_max_u32 v40, v40, v127\n\t"
      "s_waitcnt lgkmcnt(10)\n\t"
      "v_mfma_f32_32x32x16_bf16 v[208:223], v[64:67], v[104:107], v[208:223]\n\t"
      "ds_read_b64_tr_b16 v[60:61], v24 offset:32512\n\t"
      "ds_read_b64_tr_b16 v[62:63], v24 offset:36096\n\t"
      "v_cmp_eq_u32 vcc, -1, v40\n\t"
      "s_nop 0\n\t"
      "s_cbranch_vccnz qo23_%=\n\t"
      "qr23_%=:\n\t"
      "v_mfma_f32_32x32x16_bf16 v[224:239], v[68:71], v[104:107], v[224:239]\n\t"
      "ds_read_b64_tr_b16 v[64:65], v24 offset:32576\n\t"
      "ds_read_b64_tr_b16 v[66:67], v24 offset:36160\n\t"
      "s_waitcnt vmcnt(7)\n\t"
      "v_max3_u32 v40, v128, v129, v130\n\t"
      "v_max_u32 v40, v40, v131\n\t"
      "s_waitcnt lgkmcnt(10)\n\t"
      "v_mfma_f32_32x32x16_bf16 v[240:255], v[72:75], v[104:107], v[240:255]\n\t"
      "ds_read_b64_tr_b16 v[68:69], v24 offset:32640\n\t"
      "ds_read_b64_tr_b16 v[70:71], v24 offset:36224\n\t"
      "v_cmp_eq_u32 vcc, -1, v40\n\t"
      "s_nop 0\n\t"
      "s_cbranch_vccnz qo24_%=\n\t"
      "qr24_%=:\n\t"
      "v_mfma_f32_32x32x16_bf16 v[144:159], v[76:79], v[108:111], v[144:159]\n\t"
      "ds_read_b64_tr_b16 v[72:73], v27 offset:8960\n\t"
      "ds_read_b64_tr_b16 v[74:75], v27 offset:21504\n\t"
      "s_waitcnt lgkmcnt(10)\n\t"
      "v_mfma_f32_32x32x16_bf16 v[160:175], v[80:83], v[108:111], v[160:175]\n\t"
      "ds_read_b64_tr_b16 v[76:77], v27 offset:9024\n\t"
      "ds_read_b64_tr_b16 v[78:79], v27 offset:21568\n\t"
      "v_mfma_f32_32x32x16_bf16 v[176:191], v[84:87], v[108:111], v[176:191]\n\t"
      "ds_read_b64_tr_b16 v[80:81], v27 offset:9088\n\t"
      "ds_read_b64_tr_b16 v[82:83], v27 offset:21632\n\t"
      "s_waitcnt lgkmcnt(10)\n\t"
      "v_mfma_f32_32x32x16_bf16 v[192:207], v[56:59], v[108:111], v[192:207]\n\t"
      "ds_read_b64_tr_b16 v[84:85], v27 offset:9152\n\t"
      "ds_read_b64_tr_b16 v[86:87], v27 offset:21696\n\t"
      "s_waitcnt vmcnt(5)\n\t"
      "v_max3_u32 v40, v132, v133, v134\n\t"
      "v_max_u32 v40, v40, v135\n\t"
      "v_mfma_f32_32x32x16_bf16 v[208:223], v[60:63], v[108:111], v[208:223]\n\t"
      "ds_read_b64_tr_b16 v[56:57], v27 offset:9216\n\t"
      "ds_read_b64_tr_b16 v[58:59], v27 offset:21760\n\t"
      "v_cmp_eq_u32 vcc, -1, v40\n\t"
      "s_nop 0\n\t"
      "s_cbranch_vccnz qo25_%=\n\t"
      "qr25_%=:\n\t"
      "s_waitcnt lgkmcnt(10)\n\t"
      "v_mfma_f32_32x32x16_bf16 v[224:239], v[64:67], v[108:111], v[224:239]\n\t"
      "ds_read_b64_tr_b16 v[60:61], v27 offset:9280\n\t"
      "ds_read_b64_tr_b16 v[62:63], v27 offset:21824\n\t"
      "s_waitcnt vmcnt(3)\n\t"
      "v_max3_u32 v40, v136, v137, v138\n\t"
      "v_max_u32 v40, v40, v139\n\t"
      "v_mfma_f32_32x32x16_bf16 v[240:255], v[68:71], v[108:111], v[240:255]\n\t"
      "ds_read_b64_tr_b16 v[64:65], v27 offset:9344\n\t"
      "ds_read_b64_tr_b16 v[66:67], v27 offset:21888\n\t"
      "v_cmp_eq_u32 vcc, -1, v40\n\t"
      "s_nop 0\n\t"
      "s_cbranch_vccnz qo26_%=\n\t"
      "qr26_%=:\n\t"
      "s_waitcnt vmcnt(0)\n\t"
      "s_barrier\n\t"
      "s_waitcnt lgkmcnt(10)\n\t"
      "v_mfma_f32_32x32x16_bf16 v[144:159], v[72:75], v[112:115], v[144:159]\n\t"
      "ds_read_b64_tr_b16 v[68:69], v10 offset:0\n\t"
      "ds_read_b64_tr_b16 v[70:71], v10 offset:3584\n\t"
      "v_mfma_f32_32x32x16_bf16 v[160:175], v[76:79], v[112:115], v[160:175]\n\t"
      "ds_read_b64_tr_b16 v[72:73], v10 offset:12544\n\t"
      "ds_read_b64_tr_b16 v[74:75], v10 offset:16128\n\t"
      "s_waitcnt lgkmcnt(10)\n\t"
      "v_mfma_f32_32x32x16_bf16 v[176:191], v[80:83], v[112:115], v[176:191]\n\t"
      "ds_read_b64_tr_b16 v[76:77], v10 offset:25088\n\t"
      "ds_read_b64_tr_b16 v[78:79], v10 offset:28672\n\t"
      "v_mfma_f32_32x32x16_bf16 v[192:207], v[84:87], v[112:115], v[192:207]\n\t"
      "ds_read_b64_tr_b16 v[80:81], v10 offset:37632\n\t"
      "ds_read_b64_tr_b16 v[82:83], v10 offset:41216\n\t"
      "s_waitcnt vmcnt(2)\n\t"
      "v_max3_u32 v40, v140, v141, v142\n\t"
      "v_max_u32 v40, v40, v143\n\t"
      "s_waitcnt lgkmcnt(10)\n\t"
      "v_mfma_f32_32x32x16_bf16 v[208:223], v[56:59], v[112:115], v[208:223]\n\t"
      "ds_read_b64_tr_b16 v[84:85], v20 offset:7168\n\t"
      "ds_read_b64_tr_b16 v[86:87], v20 offset:10752\n\t"
      "v_cmp_eq_u32 vcc, -1, v40\n\t"
      "s_nop 0\n\t"
      "s_cbranch_vccnz qo27_%=\n\t"
      "qr27_%=:\n\t"
      "v_mfma_f32_32x32x16_bf16 v[224:239], v[60:63], v[112:115], v[224:239]\n\t"
      "ds_read_b64_tr_b16 v[56:57], v20 offset:32256\n\t"
      "ds_read_b64_tr_b16 v[58:59], v20 offset:35840\n\t"
      "s_waitcnt lgkmcnt(10)\n\t"
      "v_mfma_f32_32x32x16_bf16 v[240:255], v[64:67], v[112:115], v[240:255]\n\t"
      "ds_read_b64_tr_b16 v[60:61], v25 offset:8960\n\t"
      "ds_read_b64_tr_b16 v[62:63], v25 offset:21504\n\t"
      "v_mfma_f32_32x32x16_bf16 v[144:159], v[68:71], v[116:119], v[144:159]\n\t"
      "ds_read_b64_tr_b16 v[64:65], v10 offset:64\n\t"
      "ds_read_b64_tr_b16 v[66:67], v10 offset:3648\n\t"
      "s_waitcnt lgkmcnt(10)\n\t"
      "v_mfma_f32_32x32x16_bf16 v[144:159], v[72:75], v[120:123], v[144:159]\n\t"
      "ds_read_b64_tr_b16 v[68:69], v10 offset:12608\n\t"
      "ds_read_b64_tr_b16 v[70:71], v10 offset:16192\n\t"
      "v_mfma_f32_32x32x16_bf16 v[144:159], v[76:79], v[124:127], v[144:159]\n\t"
      "ds_read_b64_tr_b16 v[72:73], v10 offset:25152\n\t"
      "ds_read_b64_tr_b16 v[74:75], v10 offset:28736\n\t"
      "s_waitcnt lgkmcnt(10)\n\t"
      "v_mfma_f32_32x32x16_bf16 v[144:159], v[80:83], v[128:131], v[144:159]\n\t"
      "ds_read_b64_tr_b16 v[76:77], v10 offset:37696\n\t"
      "ds_read_b64_tr_b16 v[78:79], v10 offset:41280\n\t"
      "v_mfma_f32_32x32x16_bf16 v[144:159], v[84:87], v[132:135], v[144:159]\n\t"
      "ds_read_b64_tr_b16 v[80:81], v20 offset:7232\n\t"
      "ds_read_b64_tr_b16 v[82:83], v20 offset:10816\n\t"
      "s_waitcnt lgkmcnt(10)\n\t"
      "v_mfma_f32_32x32x16_bf16 v[144:159], v[56:59], v[136:139], v[144:159]\n\t"
      "ds_read_b64_tr_b16 v[84:85], v20 offset:32320\n\t"
      "ds_read_b64_tr_b16 v[86:87], v20 offset:35904\n\t"
      "v_mfma_f32_32x32x16_bf16 v[144:159], v[60:63], v[140:143], v[144:159]\n\t"
      "ds_read_b64_tr_b16 v[56:57], v25 offset:9024\n\t"
      "ds_read_b64_tr_b16 v[58:59], v25 offset:21568\n\t"
      "s_waitcnt lgkmcnt(10)\n\t"
      "v_mfma_f32_32x32x16_bf16 v[160:175], v[64:67], v[116:119], v[160:175]\n\t"
      "ds_read_b64_tr_b16 v[60:61], v10 offset:128\n\t"
      "ds_read_b64_tr_b16 v[62:63], v10 offset:3712\n\t"
      "v_mfma_f32_32x32x16_bf16 v[160:175], v[68:71], v[120:123], v[160:175]\n\t"
      "ds_read_b64_tr_b16 v[64:65], v10 offset:12672\n\t"
      "ds_read_b64_tr_b16 v[66:67], v10 offset:16256\n\t"
      "s_waitcnt lgkmcnt(10)\n\t"
      "v_mfma_f32_32x32x16_bf16 v[160:175], v[72:75], v[124:127], v[160:175]\n\t"
      "ds_read_b64_tr_b16 v[68:69], v10 offset:25216\n\t"
      "ds_read_b64_tr_b16 v[70:71], v10 offset:28800\n\t"
      "v_mfma_f32_32x32x16_bf16 v[160:175], v[76:79], v[128:131], v[160:175]\n\t"
      "ds_read_b64_tr_b16 v[72:73], v10 offset:37760\n\t"
      "ds_read_b64_tr_b16 v[74:75], v10 offset:41344\n\t"
      "v_cvt_pk_bf16_f32 v144, v144, v145\n\t"
      "v_cvt_pk_bf16_f32 v145, v146, v147\n\t"
      "s_waitcnt lgkmcnt(10)\n\t"
      "v_mfma_f32_32x32x16_bf16 v[160:175], v[80:83], v[132:135], v[160:175]\n\t"
      "ds_read_b64_tr_b16 v[76:77], v20 offset:7296\n\t"
      "ds_read_b64_tr_b16 v[78:79], v20 offset:10880\n\t"
      "v_cvt_pk_bf16_f32 v146, v148, v149\n\t"
      "v_cvt_pk_bf16_f32 v147, v150, v151\n\t"
      "v_mfma_f32_32x32x16_bf16 v[160:175], v[84:87], v[136:139], v[160:175]\n\t"
      "ds_read_b64_tr_b16 v[80:81], v20 offset:32384\n\t"
      "ds_read_b64_tr_b16 v[82:83], v20 offset:35968\n\t"
      "s_add_u32 s48, s89, 0\n\t"
      "buffer_store_dwordx4 v[144:147], v2, s[28:31], s48 offen sc1\n\t"
      "s_waitcnt lgkmcnt(10)\n\t"
      "v_mfma_f32_32x32x16_bf16 v[160:175], v[56:59], v[140:143], v[160:175]\n\t"
      "ds_read_b64_tr_b16 v[84:85], v25 offset:9088\n\t"
      "ds_read_b64_tr_b16 v[86:87], v25 offset:21632\n\t"
      "v_cvt_pk_bf16_f32 v148, v152, v153\n\t"
      "v_cvt_pk_bf16_f32 v149, v154, v155\n\t"
      "v_mfma_f32_32x32x16_bf16 v[176:191], v[60:63], v[116:119], v[176:191]\n\t"
      "ds_read_b64_tr_b16 v[56:57], v10 offset:192\n\t"
      "ds_read_b64_tr_b16 v[58:59], v10 offset:3776\n\t"
      "v_cvt_pk_bf16_f32 v150, v156, v157\n\t"
      "v_cvt_pk_bf16_f32 v151, v158, v159\n\t"
      "s_waitcnt lgkmcnt(10)\n\t"
      "v_mfma_f32_32x32x16_bf16 v[176:191], v[64:67], v[120:123], v[176:191]\n\t"
      "ds_read_b64_tr_b16 v[60:61], v10 offset:12736\n\t"
      "ds_read_b64_tr_b16 v[62:63], v10 offset:16320\n\t"
      "buffer_store_dwordx4 v[148:151], v2, s[28:31], s48 offen offset:1024 sc1\n\t"
      "v_mfma_f32_32x32x16_bf16 v[176:191], v[68:71], v[124:127], v[176:191]\n\t"
      "ds_read_b64_tr_b16 v[64:65], v10 offset:25280\n\t"
      "ds_read_b64_tr_b16 v[66:67], v10 offset:28864\n\t"
      "s_waitcnt lgkmcnt(10)\n\t"
      "v_mfma_f32_32x32x16_bf16 v[176:191], v[72:75], v[128:131], v[176:191]\n\t"
      "ds_read_b64_tr_b16 v[68:69], v10 offset:37824\n\t"
      "ds_read_b64_tr_b16 v[70:71], v10 offset:41408\n\t"
      "v_cvt_pk_bf16_f32 v160, v160, v161\n\t"
      "v_cvt_pk_bf16_f32 v161, v162, v163\n\t"
      "v_mfma_f32_32x32x16_bf16 v[176:191], v[76:79], v[132:135], v[176:191]\n\t"
      "ds_read_b64_tr_b16 v[72:73], v20 offset:7360\n\t"
      "ds_read_b64_tr_b16 v[74:75], v20 offset:10944\n\t"
      "v_cvt_pk_bf16_f32 v162, v164, v165\n\t"
      "v_cvt_pk_bf16_f32 v163, v166, v167\n\t"
      "s_waitcnt lgkmcnt(10)\n\t"
      "v_mfma_f32_32x32x16_bf16 v[176:191], v[80:83], v[136:139], v[176:191]\n\t"
      "ds_read_b64_tr_b16 v[76:77], v20 offset:32448\n\t"
      "ds_read_b64_tr_b16 v[78:79], v20 offset:36032\n\t"
      "s_add_u32 s48, s89, 2048\n\t"
      "buffer_store_dwordx4 v[160:163], v2, s[28:31], s48 offen sc1\n\t"
      "v_mfma_f32_32x32x16_bf16 v[176:191], v[84:87], v[140:143], v[176:191]\n\t"
      "ds_read_b64_tr_b16 v[80:81], v25 offset:9152\n\t"
      "ds_read_b64_tr_b16 v[82:83], v25 offset:21696\n\t"
      "v_cvt_pk_bf16_f32 v164, v168, v169\n\t"
      "v_cvt_pk_bf16_f32 v165, v170, v171\n\t"
      "s_waitcnt lgkmcnt(10)\n\t"
      "v_mfma_f32_32x32x16_bf16 v[192:207], v[56:59], v[116:119], v[192:207]\n\t"
      "ds_read_b64_tr_b16 v[84:85], v10 offset:256\n\t"
      "ds_read_b64_tr_b16 v[86:87], v10 offset:3840\n\t"
      "v_cvt_pk_bf16_f32 v166, v172, v173\n\t"
      "v_cvt_pk_bf16_f32 v167, v174, v175\n\t"
      "v_mfma_f32_32x32x16_bf16 v[192:207], v[60:63], v[120:123], v[192:207]\n\t"
      "ds_read_b64_tr_b16 v[56:57], v10 offset:12800\n\t"
      "ds_read_b64_tr_b16 v[58:59], v10 offset:16384\n\t"
      "buffer_store_dwordx4 v[164:167], v2, s[28:31], s48 offen offset:1024 sc1\n\t"
      "s_waitcnt lgkmcnt(10)\n\t"
      "v_mfma_f32_32x32x16_bf16 v[192:207], v[64:67], v[124:127], v[192:207]\n\t"
      "ds_read_b64_tr_b16 v[60:61], v10 offset:25344\n\t"
      "ds_read_b64_tr_b16 v[62:63], v10 offset:28928\n\t"
      "v_mfma_f32_32x32x16_bf16 v[192:207], v[68:71], v[128:131], v[192:207]\n\t"
      "ds_read_b64_tr_b16 v[64:65], v10 offset:37888\n\t"
      "ds_read_b64_tr_b16 v[66:67], v10 offset:41472\n\t"
      "v_cvt_pk_bf16_f32 v176, v176, v177\n\t"
      "v_cvt_pk_bf16_f32 v177, v178, v179\n\t"
      "s_waitcnt lgkmcnt(10)\n\t"
      "v_mfma_f32_32x32x16_bf16 v[192:207], v[72:75], v[132:135], v[192:207]\n\t"
      "ds_read_b64_tr_b16 v[68:69], v20 offset:7424\n\t"
      "ds_read_b64_tr_b16 v[70:71], v20 offset:11008\n\t"
      "v_cvt_pk_bf16_f32 v178, v180, v181\n\t"
      "v_cvt_pk_bf16_f32 v179, v182, v183\n\t"
      "v_mfma_f32_32x32x16_bf16 v[192:207], v[76:79], v[136:139], v[192:207]\n\t"
      "ds_read_b64_tr_b16 v[72:73], v20 offset:32512\n\t"
      "ds_read_b64_tr_b16 v[74:75], v20 offset:36096\n\t"
      "s_add_u32 s48, s89, 4096\n\t"
      "buffer_store_dwordx4 v[176:179], v2, s[28:31], s48 offen sc1\n\t"
      "s_waitcnt lgkmcnt(10)\n\t"
      "v_mfma_f32_32x32x16_bf16 v[192:207], v[80:83], v[140:143], v[192:207]\n\t"
      "ds_read_b64_tr_b16 v[76:77], v25 offset:9216\n\t"
      "ds_read_b64_tr_b16 v[78:79], v25 offset:21760\n\t"
      "v_cvt_pk_bf16_f32 v180, v184, v185\n\t"
      "v_cvt_pk_bf16_f32 v181, v186, v187\n\t"
      "v_mfma_f32_32x32x16_bf16 v[208:223], v[84:87], v[116:119], v[208:223]\n\t"
      "ds_read_b64_tr_b16 v[80:81], v10 offset:320\n\t"
      "ds_read_b64_tr_b16 v[82:83], v10 offset:3904\n\t"
      "v_cvt_pk_bf16_f32 v182, v188, v189\n\t"
      "v_cvt_pk_bf16_f32 v183, v190, v191\n\t"
      "s_waitcnt lgkmcnt(10)\n\t"
      "v_mfma_f32_32x32x16_bf16 v[208:223], v[56:59], v[120:123], v[208:223]\n\t"
      "ds_read_b64_tr_b16 v[84:85], v10 offset:12864\n\t"
      "ds_read_b64_tr_b16 v[86:87], v10 offset:16448\n\t"
      "buffer_store_dwordx4 v[180:183], v2, s[28:31], s48 offen offset:1024 sc1\n\t"
      "v_mfma_f32_32x32x16_bf16 v[208:223], v[60:63], v[124:127], v[208:223]\n\t"
      "ds_read_b64_tr_b16 v[56:57], v10 offset:25408\n\t"
      "ds_read_b64_tr_b16 v[58:59], v10 offset:28992\n\t"
      "s_waitcnt lgkmcnt(10)\n\t"
      "v_mfma_f32_32x32x16_bf16 v[208:223], v[64:67], v[128:131], v[208:223]\n\t"
      "ds_read_b64_tr_b16 v[60:61], v10 offset:37952\n\t"
      "ds_read_b64_tr_b16 v[62:63], v10 offset:41536\n\t"
      "v_cvt_pk_bf16_f32 v192, v192, v193\n\t"
      "v_cvt_pk_bf16_f32 v193, v194, v195\n\t"
      "v_mfma_f32_32x32x16_bf16 v[208:223], v[68:71], v[132:135], v[208:223]\n\t"
      "ds_read_b64_tr_b16 v[64:65], v20 offset:7488\n\t"
      "ds_read_b64_tr_b16 v[66:67], v20 offset:11072\n\t"
      "v_cvt_pk_bf16_f32 v194, v196, v197\n\t"
      "v_cvt_pk_bf16_f32 v195, v198, v199\n\t"
      "s_waitcnt lgkmcnt(10)\n\t"
      "v_mfma_f32_32x32x16_bf16 v[208:223], v[72:75], v[136:139], v[208:223]\n\t"
      "ds_read_b64_tr_b16 v[68:69], v20 offset:32576\n\t"
      "ds_read_b64_tr_b16 v[70:71], v20 offset:36160\n\t"
      "s_add_u32 s48, s89, 6144\n\t"
      "buffer_store_dwordx4 v[192:195], v2, s[28:31], s48 offen sc1\n\t"
      "v_mfma_f32_32x32x16_bf16 v[208:223], v[76:79], v[140:143], v[208:223]\n\t"
      "ds_read_b64_tr_b16 v[72:73], v25 offset:9280\n\t"
      "ds_read_b64_tr_b16 v[74:75], v25 offset:21824\n\t"
      "v_cvt_pk_bf16_f32 v196, v200, v201\n\t"
      "v_cvt_pk_bf16_f32 v197, v202, v203\n\t"
      "s_waitcnt lgkmcnt(10)\n\t"
      "v_mfma_f32_32x32x16_bf16 v[224:239], v[80:83], v[116:119], v[224:239]\n\t"
      "ds_read_b64_tr_b16 v[76:77], v10 offset:384\n\t"
      "ds_read_b64_tr_b16 v[78:79], v10 offset:3968\n\t"
      "v_cvt_pk_bf16_f32 v198, v204, v205\n\t"
      "v_cvt_pk_bf16_f32 v199, v206, v207\n\t"
      "v_mfma_f32_32x32x16_bf16 v[224:239], v[84:87], v[120:123], v[224:239]\n\t"
      "ds_read_b64_tr_b16 v[80:81], v10 offset:12928\n\t"
      "ds_read_b64_tr_b16 v[82:83], v10 offset:16512\n\t"
      "buffer_store_dwordx4 v[196:199], v2, s[28:31], s48 offen offset:1024 sc1\n\t"
      "s_waitcnt lgkmcnt(10)\n\t"
      "v_mfma_f32_32x32x16_bf16 v[224:239], v[56:59], v[124:127], v[224:239]\n\t"
      "ds_read_b64_tr_b16 v[84:85], v10 offset:25472\n\t"
      "ds_read_b64_tr_b16 v[86:87], v10 offset:29056\n\t"
      "v_mfma_f32_32x32x16_bf16 v[224:239], v[60:63], v[128:131], v[224:239]\n\t"
      "ds_read_b64_tr_b16 v[56:57], v10 offset:38016\n\t"
      "ds_read_b64_tr_b16 v[58:59], v10 offset:41600\n\t"
      "v_cvt_pk_bf16_f32 v208, v208, v209\n\t"
      "v_cvt_pk_bf16_f32 v209, v210, v211\n\t"
      "s_waitcnt lgkmcnt(10)\n\t"
      "v_mfma_f32_32x32x16_bf16 v[224:239], v[64:67], v[132:135], v[224:239]\n\t"
      "ds_read_b64_tr_b16 v[60:61], v20 offset:7552\n\t"
      "ds_read_b64_tr_b16 v[62:63], v20 offset:11136\n\t"
      "v_cvt_pk_bf16_f32 v210, v212, v213\n\t"
      "v_cvt_pk_bf16_f32 v211, v214, v215\n\t"
      "v_mfma_f32_32x32x16_bf16 v[224:239], v[68:71], v[136:139], v[224:239]\n\t"
      "ds_read_b64_tr_b16 v[64:65], v20 offset:32640\n\t"
      "ds_read_b64_tr_b16 v[66:67], v20 offset:36224\n\t"
      "s_add_u32 s48, s89, 8192\n\t"
      "buffer_store_dwordx4 v[208:211], v2, s[28:31], s48 offen sc1\n\t"
      "s_waitcnt lgkmcnt(10)\n\t"
      "v_mfma_f32_32x32x16_bf16 v[224:239], v[72:75], v[140:143], v[224:239]\n\t"
      "ds_read_b64_tr_b16 v[68:69], v25 offset:9344\n\t"
      "ds_read_b64_tr_b16 v[70:71], v25 offset:21888\n\t"
      "v_cvt_pk_bf16_f32 v212, v216, v217\n\t"
      "v_cvt_pk_bf16_f32 v213, v218, v219\n\t"
      "v_mfma_f32_32x32x16_bf16 v[240:255], v[76:79], v[116:119], v[240:255]\n\t"
      "v_cvt_pk_bf16_f32 v214, v220, v221\n\t"
      "v_cvt_pk_bf16_f32 v215, v222, v223\n\t"
      "buffer_store_dwordx4 v[212:215], v2, s[28:31], s48 offen offset:1024 sc1\n\t"
      "s_waitcnt lgkmcnt(8)\n\t"
      "v_mfma_f32_32x32x16_bf16 v[240:255], v[80:83], v[120:123], v[240:255]\n\t"
      "v_mfma_f32_32x32x16_bf16 v[240:255], v[84:87], v[124:127], v[240:255]\n\t"
      "s_waitcnt lgkmcnt(4)\n\t"
      "v_mfma_f32_32x32x16_bf16 v[240:255], v[56:59], v[128:131], v[240:255]\n\t"
      "s_nop 5\n\t"
      "v_cvt_pk_bf16_f32 v224, v224, v225\n\t"
      "v_cvt_pk_bf16_f32 v225, v226, v227\n\t"
      "v_cvt_pk_bf16_f32 v226, v228, v229\n\t"
      "v_mfma_f32_32x32x16_bf16 v[240:255], v[60:63], v[132:135], v[240:255]\n\t"
      "v_cvt_pk_bf16_f32 v227, v230, v231\n\t"
      "s_add_u32 s48, s89, 10240\n\t"
      "buffer_store_dwordx4 v[224:227], v2, s[28:31], s48 offen sc1\n\t"
      "v_cvt_pk_bf16_f32 v228, v232, v233\n\t"
      "s_waitcnt lgkmcnt(0)\n\t"
      "v_mfma_f32_32x32x16_bf16 v[240:255], v[64:67], v[136:139], v[240:255]\n\t"
      "v_cvt_pk_bf16_f32 v229, v234, v235\n\t"
      "v_cvt_pk_bf16_f32 v230, v236, v237\n\t"
      "v_cvt_pk_bf16_f32 v231, v238, v239\n\t"
      "buffer_store_dwordx4 v[228:231], v2, s[28:31], s48 offen offset:1024 sc1\n\t"
      "v_mfma_f32_32x32x16_bf16 v[240:255], v[68:71], v[140:143], v[240:255]\n\t"
      "v_dot2c_f32_bf16 v44, v144, v144\n\t"
      "v_dot2c_f32_bf16 v42, v144, v41\n\t"
      "v_dot2c_f32_bf16 v45, v145, v145\n\t"
      "v_dot2c_f32_bf16 v43, v145, v41\n\t"
      "v_dot2c_f32_bf16 v44, v146, v146\n\t"
      "v_dot2c_f32_bf16 v42, v146, v41\n\t"
      "v_dot2c_f32_bf16 v45, v147, v147\n\t"
      "v_dot2c_f32_bf16 v43, v147, v41\n\t"
      "v_dot2c_f32_bf16 v44, v148, v148\n\t"
      "v_dot2c_f32_bf16 v42, v148, v41\n\t"
      "v_dot2c_f32_bf16 v45, v149, v149\n\t"
      "v_dot2c_f32_bf16 v43, v149, v41\n\t"
      "v_dot2c_f32_bf16 v44, v150, v150\n\t"
      "v_dot2c_f32_bf16 v42, v150, v41\n\t"
      "v_dot2c_f32_bf16 v45, v151, v151\n\t"
      "v_dot2c_f32_bf16 v43, v151, v41\n\t"
      "v_cvt_pk_bf16_f32 v240, v240, v241\n\t"
      "v_cvt_pk_bf16_f32 v241, v242, v243\n\t"
      "v_cvt_pk_bf16_f32 v242, v244, v245\n\t"
      "v_cvt_pk_bf16_f32 v243, v246, v247\n\t"
      "s_add_u32 s48, s89, 12288\n\t"
      "buffer_store_dwordx4 v[240:243], v2, s[28:31], s48 offen sc1\n\t"
      "v_cvt_pk_bf16_f32 v244, v248, v249\n\t"
      "v_cvt_pk_bf16_f32 v245, v250, v251\n\t"
      "v_cvt_pk_bf16_f32 v246, v252, v253\n\t"
      "v_cvt_pk_bf16_f32 v247, v254, v255\n\t"
      "buffer_store_dwordx4 v[244:247], v2, s[28:31], s48 offen offset:1024 sc1\n\t"
      "v_dot2c_f32_bf16 v44, v160, v160\n\t"
      "v_dot2c_f32_bf16 v42, v160, v41\n\t"
      "v_dot2c_f32_bf16 v45, v161, v161\n\t"
      "v_dot2c_f32_bf16 v43, v161, v41\n\t"
      "v_dot2c_f32_bf16 v44, v162, v162\n\t"
      "v_dot2c_f32_bf16 v42, v162, v41\n\t"
      "v_dot2c_f32_bf16 v45, v163, v163\n\t"
      "v_dot2c_f32_bf16 v43, v163, v41\n\t"
      "v_dot2c_f32_bf16 v44, v164, v164\n\t"
      "v_dot2c_f32_bf16 v42, v164, v41\n\t"
      "v_dot2c_f32_bf16 v45, v165, v165\n\t"
      "v_dot2c_f32_bf16 v43, v165, v41\n\t"
      "v_dot2c_f32_bf16 v44, v166, v166\n\t"
      "v_dot2c_f32_bf16 v42, v166, v41\n\t"
      "v_dot2c_f32_bf16 v45, v167, v167\n\t"
      "v_dot2c_f32_bf16 v43, v167, v41\n\t"
      "v_dot2c_f32_bf16 v44, v176, v176\n\t"
      "v_dot2c_f32_bf16 v42, v176, v41\n\t"
      "v_dot2c_f32_bf16 v45, v177, v177\n\t"
      "v_dot2c_f32_bf16 v43, v177, v41\n\t"
      "v_dot2c_f32_bf16 v44, v178, v178\n\t"
      "v_dot2c_f32_bf16 v42, v178, v41\n\t"
      "v_dot2c_f32_bf16 v45, v179, v179\n\t"
      "v_dot2c_f32_bf16 v43, v179, v41\n\t"
      "v_dot2c_f32_bf16 v44, v180, v180\n\t"
      "v_dot2c_f32_bf16 v42, v180, v41\n\t"
      "v_dot2c_f32_bf16 v45, v181, v181\n\t"
      "v_dot2c_f32_bf16 v43, v181, v41\n\t"
      "v_dot2c_f32_bf16 v44, v182, v182\n\t"
      "v_dot2c_f32_bf16 v42, v182, v41\n\t"
      "v_dot2c_f32_bf16 v45, v183, v183\n\t"
      "v_dot2c_f32_bf16 v43, v183, v41\n\t"
      "v_dot2c_f32_bf16 v44, v192, v192\n\t"
      "v_dot2c_f32_bf16 v42, v192, v41\n\t"
      "v_dot2c_f32_bf16 v45, v193, v193\n\t"
      "v_dot2c_f32_bf16 v43, v193, v41\n\t"
      "v_dot2c_f32_bf16 v44, v194, v194\n\t"
      "v_dot2c_f32_bf16 v42, v194, v41\n\t"
      "v_dot2c_f32_bf16 v45, v195, v195\n\t"
      "v_dot2c_f32_bf16 v43, v195, v41\n\t"
      "v_dot2c_f32_bf16 v44, v196, v196\n\t"
      "v_dot2c_f32_bf16 v42, v196, v41\n\t"
      "v_dot2c_f32_bf16 v45, v197, v197\n\t"
      "v_dot2c_f32_bf16 v43, v197, v41\n\t"
      "v_dot2c_f32_bf16 v44, v198, v198\n\t"
      "v_dot2c_f32_bf16 v42, v198, v41\n\t"
      "v_dot2c_f32_bf16 v45, v199, v199\n\t"
      "v_dot2c_f32_bf16 v43, v199, v41\n\t"
      "v_dot2c_f32_bf16 v44, v208, v208\n\t"
      "v_dot2c_f32_bf16 v42, v208, v41\n\t"
      "v_dot2c_f32_bf16 v45, v209, v209\n\t"
      "v_dot2c_f32_bf16 v43, v209, v41\n\t"
      "v_dot2c_f32_bf16 v44, v210, v210\n\t"
      "v_dot2c_f32_bf16 v42, v210, v41\n\t"
      "v_dot2c_f32_bf16 v45, v211, v211\n\t"
      "v_dot2c_f32_bf16 v43, v211, v41\n\t"
      "v_dot2c_f32_bf16 v44, v212, v212\n\t"
      "v_dot2c_f32_bf16 v42, v212, v41\n\t"
      "v_dot2c_f32_bf16 v45, v213, v213\n\t"
      "v_dot2c_f32_bf16 v43, v213, v41\n\t"
      "v_dot2c_f32_bf16 v44, v214, v214\n\t"
      "v_dot2c_f32_bf16 v42, v214, v41\n\t"
      "v_dot2c_f32_bf16 v45, v215, v215\n\t"
      "v_dot2c_f32_bf16 v43, v215, v41\n\t"
      "v_dot2c_f32_bf16 v44, v224, v224\n\t"
      "v_dot2c_f32_bf16 v42, v224, v41\n\t"
      "v_dot2c_f32_bf16 v45, v225, v225\n\t"
      "v_dot2c_f32_bf16 v43, v225, v41\n\t"
      "v_dot2c_f32_bf16 v44, v226, v226\n\t"
      "v_dot2c_f32_bf16 v42, v226, v41\n\t"
      "v_dot2c_f32_bf16 v45, v227, v227\n\t"
      "v_dot2c_f32_bf16 v43, v227, v41\n\t"
      "v_dot2c_f32_bf16 v44, v228, v228\n\t"
      "v_dot2c_f32_bf16 v42, v228, v41\n\t"
      "v_dot2c_f32_bf16 v45, v229, v229\n\t"
      "v_dot2c_f32_bf16 v43, v229, v41\n\t"
      "v_dot2c_f32_bf16 v44, v230, v230\n\t"
      "v_dot2c_f32_bf16 v42, v230, v41\n\t"
      "v_dot2c_f32_bf16 v45, v231, v231\n\t"
      "v_dot2c_f32_bf16 v43, v231, v41\n\t"
      "v_dot2c_f32_bf16 v44, v240, v240\n\t"
      "v_dot2c_f32_bf16 v42, v240, v41\n\t"
      "v_dot2c_f32_bf16 v45, v241, v241\n\t"
      "v_dot2c_f32_bf16 v43, v241, v41\n\t"
      "v_dot2c_f32_bf16 v44, v242, v242\n\t"
      "v_dot2c_f32_bf16 v42, v242, v41\n\t"
      "v_dot2c_f32_bf16 v45, v243, v243\n\t"
      "v_dot2c_f32_bf16 v43, v243, v41\n\t"
      "v_dot2c_f32_bf16 v44, v244, v244\n\t"
      "v_dot2c_f32_bf16 v42, v244, v41\n\t"
      "v_dot2c_f32_bf16 v45, v245, v245\n\t"
      "v_dot2c_f32_bf16 v43, v245, v41\n\t"
      "v_dot2c_f32_bf16 v44, v246, v246\n\t"
      "v_dot2c_f32_bf16 v42, v246, v41\n\t"
      "v_dot2c_f32_bf16 v45, v247, v247\n\t"
      "v_dot2c_f32_bf16 v43, v247, v41\n\t"
      "s_nop 3\n\t"
      "v_add_f32 v42, v42, v43\n\t"
      "v_add_f32 v44, v44, v45\n\t"
      "s_nop 1\n\t"
      "v_permlane32_swap_b32 v42, v44\n\t"
      "s_nop 1\n\t"
      "v_add_f32 v42, v42, v44\n\t"
      "buffer_store_dword v42, v21, s[28:31], 0 offen sc1\n\t"
      "s_cmp_lg_u32 %[bid], 255\n\t"
      "s_cbranch_scc1 out_%=\n\t"
      "v_lshrrev_b32 v40, 4, v1\n\t"
      "v_and_b32 v41, 15, v1\n\t"
      "v_lshlrev_b32 v40, 7, v40\n\t"
      "v_lshl_add_u32 v40, v41, 2, v40\n\t"
      "s_lshl_b32 s49, s44, 9\n\t"
      "s_add_u32 s49, s49, 4096\n\t"
      "s_mov_b32 s50, 0x10000\n\t"
      "gl_%=:\n\t"
      "buffer_load_dword v41, v40, s[28:31], s49 offen sc1\n\t"
      "s_sub_u32 s50, s50, 1\n\t"
      "s_waitcnt vmcnt(0)\n\t"
      "v_cmp_ne_u32 vcc, s47, v41\n\t"
      "s_cmp_eq_u32 s50, 0\n\t"
      "s_cbranch_scc1 gd_%=\n\t"
      "s_cbranch_vccnz gl_%=\n\t"
      "gd_%=:\n\t"
      "s_barrier\n\t"
      "s_cmp_lg_u32 s44, 0\n\t"
      "s_cbranch_scc1 out_%=\n\t"
      "v_mov_b32 v40, s47\n\t"
      "v_mov_b32 v41, 0\n\t"
      "s_mov_b64 exec, 1\n\t"
      "buffer_store_dword v40, v41, s[28:31], 0 offen sc1\n\t"
      "s_mov_b64 exec, -1\n\t"
      "s_branch out_%=\n\t"
      "tab_%=:\n\t"
      ".long 0x0, 0x0, 0x0, 0xffffffff, 0x100000, 0x200, 0x100400, 0x200, 0x80000000, 0x80120000, 0x0, 0x120200, 0x0, 0x0, 0x0, 0x0\n\t"
      ".long 0x200, 0x120600, 0x1fa00, 0x80000000, 0x0, 0x0, 0x0, 0x0, 0xffffffff, 0x0, 0x140200, 0x200, 0x140600, 0x1fc00, 0x160000, 0x7fea0000\n\t"
      ".long 0x0, 0x0, 0x3, 0x0, 0x0, 0x0, 0x0, 0x0, 0x0, 0x0, 0x0, 0x0, 0x0, 0x0, 0x0, 0x0\n\t"
      ".long 0x0, 0x0, 0x0, 0x0, 0x160400, 0x200, 0x180000, 0x200, 0x80000000, 0x0, 0x0, 0x180400, 0x0, 0xffffffff, 0x0, 0x0\n\t"
      ".long 0x200, 0x1a0200, 0x200, 0x80000000, 0x801a0000, 0x0, 0x0, 0x0, 0x0, 0x0, 0x1a0600, 0x1fa00, 0x1c0200, 0x200, 0x80000000, 0x0\n\t"
      ".long 0x0, 0xc0, 0xe, 0x0, 0x0, 0x0, 0x0, 0x0, 0x0, 0x0, 0x0, 0x0, 0x0, 0x0, 0x0, 0x0\n\t"
      ".long 0xffffffff, 0x0, 0x0, 0x0, 0x1c0600, 0x1fc00, 0x1e0400, 0x200, 0x1e0000, 0x7fe20000, 0x0, 0x200000, 0x0, 0x0, 0x0, 0xffffffff\n\t"
      ".long 0x200, 0x200400, 0x200, 0x80000000, 0x80220000, 0x0, 0x0, 0x0, 0x0, 0x0, 0x220200, 0x200, 0x220600, 0x1fa00, 0x80000000, 0x0\n\t"
      ".long 0x0, 0x180, 0x38, 0x0, 0x0, 0x0, 0x0, 0x0, 0x0, 0x0, 0x0, 0x0, 0x0, 0x0, 0x0, 0x0\n\t"
      ".long 0x0, 0x0, 0xffffffff, 0x0, 0x240200, 0x200, 0x240600, 0x1fc00, 0x260000, 0x7fda0000, 0x0, 0x260400, 0x0, 0x0, 0x0, 0x0\n\t"
      ".long 0x200, 0x280000, 0x200, 0x80000000, 0x0, 0x0, 0x0, 0xffffffff, 0x0, 0x0, 0x280400, 0x200, 0x2a0200, 0x200, 0x80000000, 0x802a0000\n\t"
      ".long 0x0, 0x240, 0x60, 0x0, 0x0, 0x0, 0x0, 0x0, 0x0, 0x0, 0x0, 0x0, 0x0, 0x0, 0x0, 0x0\n\t"
      ".long 0x0, 0x0, 0x0, 0x0, 0x2a0600, 0x1fa00, 0x2c0200, 0x200, 0x80000000, 0x0, 0x0, 0x2c0600, 0xffffffff, 0x0, 0x0, 0x0\n\t"
      ".long 0x1fc00, 0x2e0400, 0x200, 0x2e0000, 0x7fd20000, 0x0, 0x0, 0x0, 0x0, 0xffffffff, 0x300000, 0x200, 0x300400, 0x200, 0x80000000, 0x80340000\n\t"
      ".long 0xffffffff, 0x300, 0xc1, 0x0, 0x0, 0x0, 0x0, 0x0, 0x0, 0x0, 0x0, 0x0, 0x0, 0x0, 0x0, 0x0\n\t"
      ".long 0x0, 0x0, 0x0, 0x0, 0x340200, 0x200, 0x340600, 0x3fa00, 0x80000000, 0x0, 0xffffffff, 0x380200, 0x0, 0x0, 0xffffffff, 0x0\n\t"
      ".long 0x200, 0x380600, 0x3fc00, 0x3c0000, 0x7fc40000, 0xffffffff, 0x0, 0x0, 0x0, 0x0, 0x3c0400, 0x200, 0x400000, 0x200, 0x80000000, 0x0\n\t"
      ".long 0xffffffff, 0x3c0, 0x7, 0x0, 0x0, 0x0, 0x0, 0x0, 0x0, 0x0, 0x0, 0x0, 0x0, 0x0, 0x0, 0x0\n\t"
      ".long 0x0, 0xffffffff, 0x0, 0x0, 0x400400, 0x200, 0x440200, 0x200, 0x80000000, 0x80440000, 0xffffffff, 0x440600, 0x0, 0x0, 0x0, 0x0\n\t"
      ".long 0x3fa00, 0x480200, 0x200, 0x80000000, 0x0, 0xffffffff, 0xffffffff, 0x0, 0x0, 0x0, 0x480600, 0x3fc00, 0x4c0400, 0x200, 0x4c0000, 0x7fb40000\n\t"
      ".long 0xffffffff, 0x480, 0xc, 0x0, 0x0, 0x0, 0x0, 0x0, 0x0, 0x0, 0x0, 0x0, 0x0, 0x0, 0x0, 0x0\n\t"
      ".long 0x0, 0x0, 0x0, 0xffffffff, 0x500000, 0x200, 0x500400, 0x200, 0x80000000, 0x80540000, 0xffffffff, 0x540200, 0x0, 0x0, 0x0, 0x0\n\t"
      ".long 0x200, 0x540600, 0x3fa00, 0x80000000, 0x0, 0xffffffff, 0x0, 0x0, 0xffffffff, 0x0, 0x580200, 0x200, 0x580600, 0x3fc00, 0x5c0000, 0x7fa40000\n\t"
      ".long 0xffffffff, 0x540, 0x30, 0x0, 0x0, 0x0, 0x0, 0x0, 0x0, 0x0, 0x0, 0x0, 0x0, 0x0, 0x0, 0x0\n\t"
      ".long 0x0, 0x0, 0x0, 0x0, 0x5c0400, 0x200, 0x600000, 0x200, 0x80000000, 0x0, 0xffffffff, 0x600400, 0x0, 0xffffffff, 0x0, 0x0\n\t"
      ".long 0x200, 0x640200, 0x200, 0x80000000, 0x80640000, 0xffffffff, 0x0, 0x0, 0x0, 0x0, 0x640600, 0x3fa00, 0x680200, 0x200, 0x80000000, 0x0\n\t"
      ".long 0xffffffff, 0x600, 0xe0, 0x0, 0x0, 0x0, 0x0, 0x0, 0x0, 0x0, 0x0, 0x0, 0x0, 0x0, 0x0, 0x0\n\t"
      ".long 0xffffffff, 0x0, 0x0, 0x0, 0x680600, 0x3fc00, 0x6c0400, 0x200, 0x6c0000, 0x7f940000, 0xffffffff, 0x300800, 0xf0000000, 0xf0000000, 0x0, 0x0\n\t"
      ".long 0xf00000, 0xf00000, 0x100000, 0x100000, 0x0, 0x0, 0xf0f00000, 0xf0f00000, 0x0, 0x0, 0xf000000, 0xf000000, 0x1000000, 0x1000000, 0x340840, 0x0\n\t"
      ".long 0x0, 0x6c0, 0x83, 0x0, 0x0, 0x0, 0x0, 0x0, 0x0, 0x0, 0x0, 0x0, 0x0, 0x0, 0x0, 0x0\n\t"
      ".long 0xfff00000, 0xfff00000, 0x0, 0x0, 0xf0000, 0xf0000, 0x10000, 0x10000, 0x380880, 0x0, 0x0, 0x3c08c0, 0xffff0000, 0xffff0000, 0x0, 0x0\n\t"
      ".long 0xf000, 0xf000, 0x1000, 0x1000, 0x0, 0x0, 0xfffff000, 0xfffff000, 0x0, 0x0, 0xf0, 0xf0, 0x10, 0x10, 0x400900, 0x0\n\t"
      ".long 0x0, 0x780, 0x6, 0x0, 0x0, 0x0, 0x0, 0x0, 0x0, 0x0, 0x0, 0x0, 0x0, 0x0, 0x0, 0x0\n\t"
      ".long 0xfffff0f0, 0xfffff0f0, 0x0, 0x0, 0xf00, 0xf00, 0x100, 0x100, 0x440940, 0x0, 0x0, 0x480980, 0xfffffff0, 0xfffffff0, 0x0, 0x0\n\t"
      ".long 0xf000000f, 0xf000000f, 0x10000001, 0x10000001, 0x0, 0x0, 0xf0000000, 0xf0000000, 0x0, 0x0, 0xf00000, 0xf00000, 0x100000, 0x100000, 0x500800, 0x0\n\t"
      ".long 0x0, 0x840, 0x1c, 0x0, 0x0, 0x0, 0x0, 0x0, 0x0, 0x0, 0x0, 0x0, 0x0, 0x0, 0x0, 0x0\n\t"
      ".long 0xf0f00000, 0xf0f00000, 0x0, 0x0, 0xf000000, 0xf000000, 0x1000000, 0x1000000, 0x540840, 0x0, 0x0, 0x580880, 0xfff00000, 0xfff00000, 0x0, 0x0\n\t"
      ".long 0xf0000, 0xf0000, 0x10000, 0x10000, 0x0, 0x0, 0xffff0000, 0xffff0000, 0x0, 0x0, 0xf000, 0xf000, 0x1000, 0x1000, 0x5c08c0, 0x0\n\t"
      ".long 0x0, 0x900, 0x70, 0x0, 0x0, 0x0, 0x0, 0x0, 0x0, 0x0, 0x0, 0x0, 0x0, 0x0, 0x0, 0x0\n\t"
      ".long 0xfffff000, 0xfffff000, 0x0, 0x0, 0xf0, 0xf0, 0x10, 0x10, 0x600900, 0x0, 0x0, 0x640940, 0xfffff0f0, 0xfffff0f0, 0x0, 0x0\n\t"
      ".long 0xf00, 0xf00, 0x100, 0x100, 0x0, 0x0, 0xfffffff0, 0xfffffff0, 0x0, 0x0, 0xf000000f, 0xf000000f, 0x10000001, 0x10000001, 0x680980, 0x0\n\t"
      ".long 0x0, 0x9c0, 0xc0, 0x0, 0x0, 0x0, 0x0, 0x0, 0x0, 0x0, 0x0, 0x0, 0x0, 0x0, 0x0, 0x0\n\t"
      ".long 0xfffff000, 0xfffff000, 0x0, 0x0, 0xf0, 0xf0, 0x10, 0x10, 0x600900, 0x0, 0x0, 0x640940, 0xfffff0f0, 0xfffff0f0, 0x0, 0x0\n\t"
      ".long 0xf00, 0xf00, 0x100, 0x100, 0x0, 0x0, 0xfffffff0, 0xfffffff0, 0x0, 0x0, 0xf000000f, 0xf000000f, 0x10000001, 0x10000001, 0x680980, 0x0\n\t"
      ".long 0x0, 0x9c0, 0xc0, 0x0, 0x0, 0x0, 0x0, 0x0, 0x0, 0x0, 0x0, 0x0, 0x0, 0x0, 0x0, 0x0\n\t"
      ".long 0xfffff000, 0xfffff000, 0x0, 0x0, 0xf0, 0xf0, 0x10, 0x10, 0x600900, 0x0, 0x0, 0x640940, 0xfffff0f0, 0xfffff0f0, 0x0, 0x0\n\t"
      ".long 0xf00, 0xf00, 0x100, 0x100, 0x0, 0x0, 0xfffffff0, 0xfffffff0, 0x0, 0x0, 0xf000000f, 0xf000000f, 0x10000001, 0x10000001, 0x680980, 0x0\n\t"
      ".long 0x0, 0x9c0, 0xc0, 0x0, 0x0, 0x0, 0x0, 0x0, 0x0, 0x0, 0x0, 0x0, 0x0, 0x0, 0x0, 0x0\n\t"
      "gtab_%=:\n\t"
      ".long 0xff009944, 0xff009954, 0xff00a955, 0xff10aa55, 0xff11aa65, 0xff11ba66, 0xff21bb66, 0xff22bb66, 0xff22bb77, 0xff22cb77, 0xff32cc77, 0xff33cc87, 0xff33dc88, 0xff43dd88, 0xff44dd98, 0xff44dd99\n\t"
      "so0_%=:\n\t"
      "s_mov_b32 s50, 0x4000\n\t"
      "lo0_%=:\n\t"
      "s_sub_u32 s50, s50, 1\n\t"
      "s_cmp_eq_u32 s50, 0\n\t"
      "s_cbranch_scc1 ro0_%=\n\t"
      "s_sleep 2\n\t"
      "s_xor_b32 s48, s81, 0\n\t"
      "buffer_load_dwordx4 v[128:131], v2, s[28:31], s48 offen offset:0 sc1\n\t"
      "buffer_load_dwordx4 v[132:135], v2, s[28:31], s48 offen offset:1024 sc1\n\t"
      "buffer_load_dwordx4 v[136:139], v2, s[28:31], s48 offen offset:2048 sc1\n\t"
      "buffer_load_dwordx4 v[140:143], v2, s[28:31], s48 offen offset:3072 sc1\n\t"
      "s_waitcnt vmcnt(0)\n\t"
      "v_max3_u32 v28, v128, v129, v130\n\t"
      "v_max3_u32 v28, v28, v131, v132\n\t"
      "v_max3_u32 v28, v28, v133, v134\n\t"
      "v_max3_u32 v28, v28, v135, v136\n\t"
      "v_max3_u32 v28, v28, v137, v138\n\t"
      "v_max3_u32 v28, v28, v139, v140\n\t"
      "v_max3_u32 v28, v28, v141, v142\n\t"
      "v_max_u32 v28, v28, v143\n\t"
      "v_cmp_eq_u32 vcc, -1, v28\n\t"
      "s_nop 1\n\t"
      "s_cbranch_vccnz lo0_%=\n\t"
      "s_branch ro0_%=\n\t"
      "so1_%=:\n\t"
      "s_mov_b32 s50, 0x4000\n\t"
      "lo1_%=:\n\t"
      "s_sub_u32 s50, s50, 1\n\t"
      "s_cmp_eq_u32 s50, 0\n\t"
      "s_cbranch_scc1 ro1_%=\n\t"
      "s_sleep 2\n\t"
      "s_xor_b32 s48, s81, 4096\n\t"
      "buffer_load_dwordx4 v[144:147], v2, s[28:31], s48 offen offset:0 sc1\n\t"
      "buffer_load_dwordx4 v[148:151], v2, s[28:31], s48 offen offset:1024 sc1\n\t"
      "buffer_load_dwordx4 v[152:155], v2, s[28:31], s48 offen offset:2048 sc1\n\t"
      "s_waitcnt vmcnt(0)\n\t"
      "v_max3_u32 v28, v144, v145, v146\n\t"
      "v_max3_u32 v28, v28, v147, v148\n\t"
      "v_max3_u32 v28, v28, v149, v150\n\t"
      "v_max3_u32 v28, v28, v151, v152\n\t"
      "v_max3_u32 v28, v28, v153, v154\n\t"
      "v_max_u32 v28, v28, v155\n\t"
      "v_cmp_eq_u32 vcc, -1, v28\n\t"
      "s_nop 1\n\t"
      "s_cbranch_vccnz lo1_%=\n\t"
      "s_branch ro1_%=\n\t"
      "so2_%=:\n\t"
      "s_mov_b32 s50, 0x4000\n\t"
      "lo2_%=:\n\t"
      "s_sub_u32 s50, s50, 1\n\t"
      "s_cmp_eq_u32 s50, 0\n\t"
      "s_cbranch_scc1 ro2_%=\n\t"
      "s_sleep 2\n\t"
      "s_xor_b32 s48, s81, 8192\n\t"
      "buffer_load_dwordx4 v[156:159], v2, s[28:31], s48 offen offset:0 sc1\n\t"
      "buffer_load_dwordx4 v[160:163], v2, s[28:31], s48 offen offset:1024 sc1\n\t"
      "buffer_load_dwordx4 v[164:167], v2, s[28:31], s48 offen offset:2048 sc1\n\t"
      "buffer_load_dwordx4 v[168:171], v2, s[28:31], s48 offen offset:3072 sc1\n\t"
      "s_waitcnt vmcnt(0)\n\t"
      "v_max3_u32 v28, v156, v157, v158\n\t"
      "v_max3_u32 v28, v28, v159, v160\n\t"
      "v_max3_u32 v28, v28, v161, v162\n\t"
      "v_max3_u32 v28, v28, v163, v164\n\t"
      "v_max3_u32 v28, v28, v165, v166\n\t"
      "v_max3_u32 v28, v28, v167, v168\n\t"
      "v_max3_u32 v28, v28, v169, v170\n\t"
      "v_max_u32 v28, v28, v171\n\t"
      "v_cmp_eq_u32 vcc, -1, v28\n\t"
      "s_nop 1\n\t"
      "s_cbranch_vccnz lo2_%=\n\t"
      "s_branch ro2_%=\n\t"
      "so3_%=:\n\t"
      "s_mov_b32 s50, 0x4000\n\t"
      "lo3_%=:\n\t"
      "s_sub_u32 s50, s50, 1\n\t"
      "s_cmp_eq_u32 s50, 0\n\t"
      "s_cbranch_scc1 ro3_%=\n\t"
      "s_sleep 2\n\t"
      "s_xor_b32 s48, s81, 12288\n\t"
      "buffer_load_dwordx4 v[172:175], v2, s[28:31], s48 offen offset:0 sc1\n\t"
      "buffer_load_dwordx4 v[176:179], v2, s[28:31], s48 offen offset:1024 sc1\n\t"
      "buffer_load_dwordx4 v[180:183], v2, s[28:31], s48 offen offset:2048 sc1\n\t"
      "s_waitcnt vmcnt(0)\n\t"
      "v_max3_u32 v28, v172, v173, v174\n\t"
      "v_max3_u32 v28, v28, v175, v176\n\t"
      "v_max3_u32 v28, v28, v177, v178\n\t"
      "v_max3_u32 v28, v28, v179, v180\n\t"
      "v_max3_u32 v28, v28, v181, v182\n\t"
      "v_max_u32 v28, v28, v183\n\t"
      "v_cmp_eq_u32 vcc, -1, v28\n\t"
      "s_nop 1\n\t"
      "s_cbranch_vccnz lo3_%=\n\t"
      "s_branch ro3_%=\n\t"
      "so4_%=:\n\t"
      "s_mov_b32 s50, 0x4000\n\t"
      "lo4_%=:\n\t"
      "s_sub_u32 s50, s50, 1\n\t"
      "s_cmp_eq_u32 s50, 0\n\t"
      "s_cbranch_scc1 ro4_%=\n\t"
      "s_sleep 2\n\t"
      "s_xor_b32 s48, s81, 16384\n\t"
      "buffer_load_dwordx4 v[184:187], v2, s[28:31], s48 offen offset:0 sc1\n\t"
      "buffer_load_dwordx4 v[188:191], v2, s[28:31], s48 offen offset:1024 sc1\n\t"
      "buffer_load_dwordx4 v[192:195], v2, s[28:31], s48 offen offset:2048 sc1\n\t"
      "buffer_load_dwordx4 v[196:199], v2, s[28:31], s48 offen offset:3072 sc1\n\t"
      "s_waitcnt vmcnt(0)\n\t"
      "v_max3_u32 v28, v184, v185, v186\n\t"
      "v_max3_u32 v28, v28, v187, v188\n\t"
      "v_max3_u32 v28, v28, v189, v190\n\t"
      "v_max3_u32 v28, v28, v191, v192\n\t"
      "v_max3_u32 v28, v28, v193, v194\n\t"
      "v_max3_u32 v28, v28, v195, v196\n\t"
      "v_max3_u32 v28, v28, v197, v198\n\t"
      "v_max_u32 v28, v28, v199\n\t"
      "v_cmp_eq_u32 vcc, -1, v28\n\t"
      "s_nop 1\n\t"
      "s_cbranch_vccnz lo4_%=\n\t"
      "s_branch ro4_%=\n\t"
      "so5_%=:\n\t"
      "s_mov_b32 s50, 0x4000\n\t"
      "lo5_%=:\n\t"
      "s_sub_u32 s50, s50, 1\n\t"
      "s_cmp_eq_u32 s50, 0\n\t"
      "s_cbranch_scc1 ro5_%=\n\t"
      "s_sleep 2\n\t"
      "s_xor_b32 s48, s81, 20480\n\t"
      "buffer_load_dwordx4 v[200:203], v2, s[28:31], s48 offen offset:0 sc1\n\t"
      "buffer_load_dwordx4 v[204:207], v2, s[28:31], s48 offen offset:1024 sc1\n\t"
      "buffer_load_dwordx4 v[208:211], v2, s[28:31], s48 offen offset:2048 sc1\n\t"
      "s_waitcnt vmcnt(0)\n\t"
      "v_max3_u32 v28, v200, v201, v202\n\t"
      "v_max3_u32 v28, v28, v203, v204\n\t"
      "v_max3_u32 v28, v28, v205, v206\n\t"
      "v_max3_u32 v28, v28, v207, v208\n\t"
      "v_max3_u32 v28, v28, v209, v210\n\t"
      "v_max_u32 v28, v28, v211\n\t"
      "v_cmp_eq_u32 vcc, -1, v28\n\t"
      "s_nop 1\n\t"
      "s_cbranch_vccnz lo5_%=\n\t"
      "s_branch ro5_%=\n\t"
      "so6_%=:\n\t"
      "s_mov_b32 s50, 0x4000\n\t"
      "lo6_%=:\n\t"
      "s_sub_u32 s50, s50, 1\n\t"
      "s_cmp_eq_u32 s50, 0\n\t"
      "s_cbranch_scc1 ro6_%=\n\t"
      "s_sleep 2\n\t"
      "s_xor_b32 s48, s81, 24576\n\t"
      "buffer_load_dwordx4 v[212:215], v2, s[28:31], s48 offen offset:0 sc1\n\t"
      "buffer_load_dwordx4 v[216:219], v2, s[28:31], s48 offen offset:1024 sc1\n\t"
      "buffer_load_dwordx4 v[220:223], v2, s[28:31], s48 offen offset:2048 sc1\n\t"
      "buffer_load_dwordx4 v[224:227], v2, s[28:31], s48 offen offset:3072 sc1\n\t"
      "s_waitcnt vmcnt(0)\n\t"
      "v_max3_u32 v28, v212, v213, v214\n\t"
      "v_max3_u32 v28, v28, v215, v216\n\t"
      "v_max3_u32 v28, v28, v217, v218\n\t"
      "v_max3_u32 v28, v28, v219, v220\n\t"
      "v_max3_u32 v28, v28, v221, v222\n\t"
      "v_max3_u32 v28, v28, v223, v224\n\t"
      "v_max3_u32 v28, v28, v225, v226\n\t"
      "v_max_u32 v28, v28, v227\n\t"
      "v_cmp_eq_u32 vcc, -1, v28\n\t"
      "s_nop 1\n\t"
      "s_cbranch_vccnz lo6_%=\n\t"
      "s_branch ro6_%=\n\t"
      "so7_%=:\n\t"
      "s_mov_b32 s50, 0x4000\n\t"
      "lo7_%=:\n\t"
      "s_sub_u32 s50, s50, 1\n\t"
      "s_cmp_eq_u32 s50, 0\n\t"
      "s_cbranch_scc1 ro7_%=\n\t"
      "s_sleep 2\n\t"
      "s_xor_b32 s48, s81, 28672\n\t"
      "buffer_load_dwordx4 v[228:231], v2, s[28:31], s48 offen offset:0 sc1\n\t"
      "buffer_load_dwordx4 v[232:235], v2, s[28:31], s48 offen offset:1024 sc1\n\t"
      "buffer_load_dwordx4 v[236:239], v2, s[28:31], s48 offen offset:2048 sc1\n\t"
      "s_waitcnt vmcnt(0)\n\t"
      "v_max3_u32 v28, v228, v229, v230\n\t"
      "v_max3_u32 v28, v28, v231, v232\n\t"
      "v_max3_u32 v28, v28, v233, v234\n\t"
      "v_max3_u32 v28, v28, v235, v236\n\t"
      "v_max3_u32 v28, v28, v237, v238\n\t"
      "v_max_u32 v28, v28, v239\n\t"
      "v_cmp_eq_u32 vcc, -1, v28\n\t"
      "s_nop 1\n\t"
      "s_cbranch_vccnz lo7_%=\n\t"
      "s_branch ro7_%=\n\t"
      "sh0_%=:\n\t"
      "s_mov_b32 s50, 0x4000\n\t"
      "lh0_%=:\n\t"
      "s_sub_u32 s50, s50, 1\n\t"
      "s_cmp_eq_u32 s50, 0\n\t"
      "s_cbranch_scc1 rh0_%=\n\t"
      "s_sleep 2\n\t"
      "s_add_u32 s48, s84, 0\n\t"
      "buffer_load_dwordx4 v[128:131], v19, s[28:31], s48 offen sc1\n\t"
      "buffer_load_dwordx4 v[132:135], v20, s[28:31], s48 offen sc1\n\t"
      "s_add_u32 s48, s85, 0\n\t"
      "buffer_load_dwordx4 v[136:139], v19, s[28:31], s48 offen sc1\n\t"
      "buffer_load_dwordx4 v[140:143], v20, s[28:31], s48 offen sc1\n\t"
      "s_add_u32 s48, s86, 0\n\t"
      "buffer_load_dwordx4 v[144:147], v19, s[28:31], s48 offen sc1\n\t"
      "buffer_load_dwordx4 v[148:151], v20, s[28:31], s48 offen sc1\n\t"
      "s_add_u32 s48, s87, 0\n\t"
      "buffer_load_dwordx4 v[152:155], v19, s[28:31], s48 offen sc1\n\t"
      "buffer_load_dwordx4 v[156:159], v20, s[28:31], s48 offen sc1\n\t"
      "s_add_u32 s48, s88, 0\n\t"
      "buffer_load_dwordx4 v[160:163], v19, s[28:31], s48 offen sc1\n\t"
      "buffer_load_dwordx4 v[164:167], v20, s[28:31], s48 offen sc1\n\t"
      "s_add_u32 s48, s89, 0\n\t"
      "buffer_load_dwordx4 v[168:171], v19, s[28:31], s48 offen sc1\n\t"
      "buffer_load_dwordx4 v[172:175], v20, s[28:31], s48 offen sc1\n\t"
      "s_add_u32 s48, s90, 0\n\t"
      "buffer_load_dwordx4 v[176:179], v19, s[28:31], s48 offen sc1\n\t"
      "buffer_load_dwordx4 v[180:183], v20, s[28:31], s48 offen sc1\n\t"
      "s_waitcnt vmcnt(0)\n\t"
      "v_max3_u32 v28, v128, v129, v130\n\t"
      "v_max3_u32 v28, v28, v131, v132\n\t"
      "v_max3_u32 v28, v28, v133, v134\n\t"
      "v_max3_u32 v28, v28, v135, v136\n\t"
      "v_max3_u32 v28, v28, v137, v138\n\t"
      "v_max3_u32 v28, v28, v139, v140\n\t"
      "v_max3_u32 v28, v28, v141, v142\n\t"
      "v_max3_u32 v28, v28, v143, v144\n\t"
      "v_max3_u32 v28, v28, v145, v146\n\t"
      "v_max3_u32 v28, v28, v147, v148\n\t"
      "v_max3_u32 v28, v28, v149, v150\n\t"
      "v_max3_u32 v28, v28, v151, v152\n\t"
      "v_max3_u32 v28, v28, v153, v154\n\t"
      "v_max3_u32 v28, v28, v155, v156\n\t"
      "v_max3_u32 v28, v28, v157, v158\n\t"
      "v_max_u32 v28, v28, v159\n\t"
      "v_cmp_eq_u32 vcc, -1, v28\n\t"
      "s_nop 1\n\t"
      "s_cbranch_vccnz lh0_%=\n\t"
      "s_branch rh0_%=\n\t"
      "sh1_%=:\n\t"
      "s_mov_b32 s50, 0x4000\n\t"
      "lh1_%=:\n\t"
      "s_sub_u32 s50, s50, 1\n\t"
      "s_cmp_eq_u32 s50, 0\n\t"
      "s_cbranch_scc1 rh1_%=\n\t"
      "s_sleep 2\n\t"
      "s_add_u32 s48, s88, 0\n\t"
      "buffer_load_dwordx4 v[160:163], v19, s[28:31], s48 offen sc1\n\t"
      "buffer_load_dwordx4 v[164:167], v20, s[28:31], s48 offen sc1\n\t"
      "s_add_u32 s48, s89, 0\n\t"
      "buffer_load_dwordx4 v[168:171], v19, s[28:31], s48 offen sc1\n\t"
      "buffer_load_dwordx4 v[172:175], v20, s[28:31], s48 offen sc1\n\t"
      "s_add_u32 s48, s90, 0\n\t"
      "buffer_load_dwordx4 v[176:179], v19, s[28:31], s48 offen sc1\n\t"
      "buffer_load_dwordx4 v[180:183], v20, s[28:31], s48 offen sc1\n\t"
      "s_add_u32 s48, s84, 28672\n\t"
      "buffer_load_dwordx4 v[192:195], v19, s[28:31], s48 offen sc1\n\t"
      "buffer_load_dwordx4 v[196:199], v20, s[28:31], s48 offen sc1\n\t"
      "s_add_u32 s48, s85, 28672\n\t"
      "buffer_load_dwordx4 v[200:203], v19, s[28:31], s48 offen sc1\n\t"
      "buffer_load_dwordx4 v[204:207], v20, s[28:31], s48 offen sc1\n\t"
      "s_add_u32 s48, s86, 28672\n\t"
      "buffer_load_dwordx4 v[208:211], v19, s[28:31], s48 offen sc1\n\t"
      "buffer_load_dwordx4 v[212:215], v20, s[28:31], s48 offen sc1\n\t"
      "s_add_u32 s48, s87, 28672\n\t"
      "buffer_load_dwordx4 v[216:219], v19, s[28:31], s48 offen sc1\n\t"
      "buffer_load_dwordx4 v[220:223], v20, s[28:31], s48 offen sc1\n\t"
      "s_waitcnt vmcnt(0)\n\t"
      "v_max3_u32 v28, v160, v161, v162\n\t"
      "v_max3_u32 v28, v28, v163, v164\n\t"
      "v_max3_u32 v28, v28, v165, v166\n\t"
      "v_max3_u32 v28, v28, v167, v168\n\t"
      "v_max3_u32 v28, v28, v169, v170\n\t"
      "v_max3_u32 v28, v28, v171, v172\n\t"
      "v_max3_u32 v28, v28, v173, v174\n\t"
      "v_max3_u32 v28, v28, v175, v176\n\t"
      "v_max3_u32 v28, v28, v177, v178\n\t"
      "v_max3_u32 v28, v28, v179, v180\n\t"
      "v_max3_u32 v28, v28, v181, v182\n\t"
      "v_max_u32 v28, v28, v183\n\t"
      "v_cmp_eq_u32 vcc, -1, v28\n\t"
      "s_nop 1\n\t"
      "s_cbranch_vccnz lh1_%=\n\t"
      "s_branch rh1_%=\n\t"
      "sh2_%=:\n\t"
      "s_mov_b32 s50, 0x4000\n\t"
      "lh2_%=:\n\t"
      "s_sub_u32 s50, s50, 1\n\t"
      "s_cmp_eq_u32 s50, 0\n\t"
      "s_cbranch_scc1 rh2_%=\n\t"
      "s_sleep 2\n\t"
      "s_add_u32 s48, s84, 28672\n\t"
      "buffer_load_dwordx4 v[192:195], v19, s[28:31], s48 offen sc1\n\t"
      "buffer_load_dwordx4 v[196:199], v20, s[28:31], s48 offen sc1\n\t"
      "s_add_u32 s48, s85, 28672\n\t"
      "buffer_load_dwordx4 v[200:203], v19, s[28:31], s48 offen sc1\n\t"
      "buffer_load_dwordx4 v[204:207], v20, s[28:31], s48 offen sc1\n\t"
      "s_add_u32 s48, s86, 28672\n\t"
      "buffer_load_dwordx4 v[208:211], v19, s[28:31], s48 offen sc1\n\t"
      "buffer_load_dwordx4 v[212:215], v20, s[28:31], s48 offen sc1\n\t"
      "s_add_u32 s48, s87, 28672\n\t"
      "buffer_load_dwordx4 v[216:219], v19, s[28:31], s48 offen sc1\n\t"
      "buffer_load_dwordx4 v[220:223], v20, s[28:31], s48 offen sc1\n\t"
      "s_add_u32 s48, s88, 28672\n\t"
      "buffer_load_dwordx4 v[224:227], v19, s[28:31], s48 offen sc1\n\t"
      "buffer_load_dwordx4 v[228:231], v20, s[28:31], s48 offen sc1\n\t"
      "s_add_u32 s48, s89, 28672\n\t"
      "buffer_load_dwordx4 v[232:235], v19, s[28:31], s48 offen sc1\n\t"
      "buffer_load_dwordx4 v[236:239], v20, s[28:31], s48 offen sc1\n\t"
      "s_add_u32 s48, s90, 28672\n\t"
      "buffer_load_dwordx4 v[240:243], v19, s[28:31], s48 offen sc1\n\t"
      "buffer_load_dwordx4 v[244:247], v20, s[28:31], s48 offen sc1\n\t"
      "s_waitcnt vmcnt(0)\n\t"
      "v_max3_u32 v28, v192, v193, v194\n\t"
      "v_max3_u32 v28, v28, v195, v196\n\t"
      "v_max3_u32 v28, v28, v197, v198\n\t"
      "v_max3_u32 v28, v28, v199, v200\n\t"
      "v_max3_u32 v28, v28, v201, v202\n\t"
      "v_max3_u32 v28, v28, v203, v204\n\t"
      "v_max3_u32 v28, v28, v205, v206\n\t"
      "v_max3_u32 v28, v28, v207, v208\n\t"
      "v_max3_u32 v28, v28, v209, v210\n\t"
      "v_max3_u32 v28, v28, v211, v212\n\t"
      "v_max3_u32 v28, v28, v213, v214\n\t"
      "v_max3_u32 v28, v28, v215, v216\n\t"
      "v_max3_u32 v28, v28, v217, v218\n\t"
      "v_max3_u32 v28, v28, v219, v220\n\t"
      "v_max3_u32 v28, v28, v221, v222\n\t"
      "v_max_u32 v28, v28, v223\n\t"
      "v_cmp_eq_u32 vcc, -1, v28\n\t"
      "s_nop 1\n\t"
      "s_cbranch_vccnz lh2_%=\n\t"
      "s_branch rh2_%=\n\t"
      "sh3_%=:\n\t"
      "s_mov_b32 s50, 0x4000\n\t"
      "lh3_%=:\n\t"
      "s_sub_u32 s50, s50, 1\n\t"
      "s_cmp_eq_u32 s50, 0\n\t"
      "s_cbranch_scc1 rh3_%=\n\t"
      "s_sleep 2\n\t"
      "s_add_u32 s48, s88, 28672\n\t"
      "buffer_load_dwordx4 v[224:227], v19, s[28:31], s48 offen sc1\n\t"
      "buffer_load_dwordx4 v[228:231], v20, s[28:31], s48 offen sc1\n\t"
      "s_add_u32 s48, s89, 28672\n\t"
      "buffer_load_dwordx4 v[232:235], v19, s[28:31], s48 offen sc1\n\t"
      "buffer_load_dwordx4 v[236:239], v20, s[28:31], s48 offen sc1\n\t"
      "s_add_u32 s48, s90, 28672\n\t"
      "buffer_load_dwordx4 v[240:243], v19, s[28:31], s48 offen sc1\n\t"
      "buffer_load_dwordx4 v[244:247], v20, s[28:31], s48 offen sc1\n\t"
      "s_add_u32 s48, s84, 57344\n\t"
      "buffer_load_dwordx4 v[128:131], v19, s[28:31], s48 offen sc1\n\t"
      "buffer_load_dwordx4 v[132:135], v20, s[28:31], s48 offen sc1\n\t"
      "s_add_u32 s48, s85, 57344\n\t"
      "buffer_load_dwordx4 v[136:139], v19, s[28:31], s48 offen sc1\n\t"
      "buffer_load_dwordx4 v[140:143], v20, s[28:31], s48 offen sc1\n\t"
      "s_add_u32 s48, s86, 57344\n\t"
      "buffer_load_dwordx4 v[144:147], v19, s[28:31], s48 offen sc1\n\t"
      "buffer_load_dwordx4 v[148:151], v20, s[28:31], s48 offen sc1\n\t"
      "s_add_u32 s48, s87, 57344\n\t"
      "buffer_load_dwordx4 v[152:155], v19, s[28:31], s48 offen sc1\n\t"
      "buffer_load_dwordx4 v[156:159], v20, s[28:31], s48 offen sc1\n\t"
      "s_waitcnt vmcnt(0)\n\t"
      "v_max3_u32 v28, v224, v225, v226\n\t"
      "v_max3_u32 v28, v28, v227, v228\n\t"
      "v_max3_u32 v28, v28, v229, v230\n\t"
      "v_max3_u32 v28, v28, v231, v232\n\t"
      "v_max3_u32 v28, v28, v233, v234\n\t"
      "v_max3_u32 v28, v28, v235, v236\n\t"
      "v_max3_u32 v28, v28, v237, v238\n\t"
      "v_max3_u32 v28, v28, v239, v240\n\t"
      "v_max3_u32 v28, v28, v241, v242\n\t"
      "v_max3_u32 v28, v28, v243, v244\n\t"
      "v_max3_u32 v28, v28, v245, v246\n\t"
      "v_max_u32 v28, v28, v247\n\t"
      "v_cmp_eq_u32 vcc, -1, v28\n\t"
      "s_nop 1\n\t"
      "s_cbranch_vccnz lh3_%=\n\t"
      "s_branch rh3_%=\n\t"
      "sh4_%=:\n\t"
      "s_mov_b32 s50, 0x4000\n\t"
      "lh4_%=:\n\t"
      "s_sub_u32 s50, s50, 1\n\t"
      "s_cmp_eq_u32 s50, 0\n\t"
      "s_cbranch_scc1 rh4_%=\n\t"
      "s_sleep 2\n\t"
      "s_add_u32 s48, s84, 57344\n\t"
      "buffer_load_dwordx4 v[128:131], v19, s[28:31], s48 offen sc1\n\t"
      "buffer_load_dwordx4 v[132:135], v20, s[28:31], s48 offen sc1\n\t"
      "s_add_u32 s48, s85, 57344\n\t"
      "buffer_load_dwordx4 v[136:139], v19, s[28:31], s48 offen sc1\n\t"
      "buffer_load_dwordx4 v[140:143], v20, s[28:31], s48 offen sc1\n\t"
      "s_add_u32 s48, s86, 57344\n\t"
      "buffer_load_dwordx4 v[144:147], v19, s[28:31], s48 offen sc1\n\t"
      "buffer_load_dwordx4 v[148:151], v20, s[28:31], s48 offen sc1\n\t"
      "s_add_u32 s48, s87, 57344\n\t"
      "buffer_load_dwordx4 v[152:155], v19, s[28:31], s48 offen sc1\n\t"
      "buffer_load_dwordx4 v[156:159], v20, s[28:31], s48 offen sc1\n\t"
      "s_add_u32 s48, s88, 57344\n\t"
      "buffer_load_dwordx4 v[160:163], v19, s[28:31], s48 offen sc1\n\t"
      "buffer_load_dwordx4 v[164:167], v20, s[28:31], s48 offen sc1\n\t"
      "s_add_u32 s48, s89, 57344\n\t"
      "buffer_load_dwordx4 v[168:171], v19, s[28:31], s48 offen sc1\n\t"
      "buffer_load_dwordx4 v[172:175], v20, s[28:31], s48 offen sc1\n\t"
      "s_add_u32 s48, s90, 57344\n\t"
      "buffer_load_dwordx4 v[176:179], v19, s[28:31], s48 offen sc1\n\t"
      "buffer_load_dwordx4 v[180:183], v20, s[28:31], s48 offen sc1\n\t"
      "s_waitcnt vmcnt(0)\n\t"
      "v_max3_u32 v28, v128, v129, v130\n\t"
      "v_max3_u32 v28, v28, v131, v132\n\t"
      "v_max3_u32 v28, v28, v133, v134\n\t"
      "v_max3_u32 v28, v28, v135, v136\n\t"
      "v_max3_u32 v28, v28, v137, v138\n\t"
      "v_max3_u32 v28, v28, v139, v140\n\t"
      "v_max3_u32 v28, v28, v141, v142\n\t"
      "v_max3_u32 v28, v28, v143, v144\n\t"
      "v_max3_u32 v28, v28, v145, v146\n\t"
      "v_max3_u32 v28, v28, v147, v148\n\t"
      "v_max3_u32 v28, v28, v149, v150\n\t"
      "v_max3_u32 v28, v28, v151, v152\n\t"
      "v_max3_u32 v28, v28, v153, v154\n\t"
      "v_max3_u32 v28, v28, v155, v156\n\t"
      "v_max3_u32 v28, v28, v157, v158\n\t"
      "v_max_u32 v28, v28, v159\n\t"
      "v_cmp_eq_u32 vcc, -1, v28\n\t"
      "s_nop 1\n\t"
      "s_cbranch_vccnz lh4_%=\n\t"
      "s_branch rh4_%=\n\t"
      "sh5_%=:\n\t"
      "s_mov_b32 s50, 0x4000\n\t"
      "lh5_%=:\n\t"
      "s_sub_u32 s50, s50, 1\n\t"
      "s_cmp_eq_u32 s50, 0\n\t"
      "s_cbranch_scc1 rh5_%=\n\t"
      "s_sleep 2\n\t"
      "s_add_u32 s48, s88, 57344\n\t"
      "buffer_load_dwordx4 v[160:163], v19, s[28:31], s48 offen sc1\n\t"
      "buffer_load_dwordx4 v[164:167], v20, s[28:31], s48 offen sc1\n\t"
      "s_add_u32 s48, s89, 57344\n\t"
      "buffer_load_dwordx4 v[168:171], v19, s[28:31], s48 offen sc1\n\t"
      "buffer_load_dwordx4 v[172:175], v20, s[28:31], s48 offen sc1\n\t"
      "s_add_u32 s48, s90, 57344\n\t"
      "buffer_load_dwordx4 v[176:179], v19, s[28:31], s48 offen sc1\n\t"
      "buffer_load_dwordx4 v[180:183], v20, s[28:31], s48 offen sc1\n\t"
      "s_add_u32 s48, s84, 86016\n\t"
      "buffer_load_dwordx4 v[192:195], v19, s[28:31], s48 offen sc1\n\t"
      "buffer_load_dwordx4 v[196:199], v20, s[28:31], s48 offen sc1\n\t"
      "s_add_u32 s48, s85, 86016\n\t"
      "buffer_load_dwordx4 v[200:203], v19, s[28:31], s48 offen sc1\n\t"
      "buffer_load_dwordx4 v[204:207], v20, s[28:31], s48 offen sc1\n\t"
      "s_add_u32 s48, s86, 86016\n\t"
      "buffer_load_dwordx4 v[208:211], v19, s[28:31], s48 offen sc1\n\t"
      "buffer_load_dwordx4 v[212:215], v20, s[28:31], s48 offen sc1\n\t"
      "s_add_u32 s48, s87, 86016\n\t"
      "buffer_load_dwordx4 v[216:219], v19, s[28:31], s48 offen sc1\n\t"
      "buffer_load_dwordx4 v[220:223], v20, s[28:31], s48 offen sc1\n\t"
      "s_waitcnt vmcnt(0)\n\t"
      "v_max3_u32 v28, v160, v161, v162\n\t"
      "v_max3_u32 v28, v28, v163, v164\n\t"
      "v_max3_u32 v28, v28, v165, v166\n\t"
      "v_max3_u32 v28, v28, v167, v168\n\t"
      "v_max3_u32 v28, v28, v169, v170\n\t"
      "v_max3_u32 v28, v28, v171, v172\n\t"
      "v_max3_u32 v28, v28, v173, v174\n\t"
      "v_max3_u32 v28, v28, v175, v176\n\t"
      "v_max3_u32 v28, v28, v177, v178\n\t"
      "v_max3_u32 v28, v28, v179, v180\n\t"
      "v_max3_u32 v28, v28, v181, v182\n\t"
      "v_max_u32 v28, v28, v183\n\t"
      "v_cmp_eq_u32 vcc, -1, v28\n\t"
      "s_nop 1\n\t"
      "s_cbranch_vccnz lh5_%=\n\t"
      "s_branch rh5_%=\n\t"
      "sh6_%=:\n\t"
      "s_mov_b32 s50, 0x4000\n\t"
      "lh6_%=:\n\t"
      "s_sub_u32 s50, s50, 1\n\t"
      "s_cmp_eq_u32 s50, 0\n\t"
      "s_cbranch_scc1 rh6_%=\n\t"
      "s_sleep 2\n\t"
      "s_add_u32 s48, s84, 86016\n\t"
      "buffer_load_dwordx4 v[192:195], v19, s[28:31], s48 offen sc1\n\t"
      "buffer_load_dwordx4 v[196:199], v20, s[28:31], s48 offen sc1\n\t"
      "s_add_u32 s48, s85, 86016\n\t"
      "buffer_load_dwordx4 v[200:203], v19, s[28:31], s48 offen sc1\n\t"
      "buffer_load_dwordx4 v[204:207], v20, s[28:31], s48 offen sc1\n\t"
      "s_add_u32 s48, s86, 86016\n\t"
      "buffer_load_dwordx4 v[208:211], v19, s[28:31], s48 offen sc1\n\t"
      "buffer_load_dwordx4 v[212:215], v20, s[28:31], s48 offen sc1\n\t"
      "s_add_u32 s48, s87, 86016\n\t"
      "buffer_load_dwordx4 v[216:219], v19, s[28:31], s48 offen sc1\n\t"
      "buffer_load_dwordx4 v[220:223], v20, s[28:31], s48 offen sc1\n\t"
      "s_add_u32 s48, s88, 86016\n\t"
      "buffer_load_dwordx4 v[224:227], v19, s[28:31], s48 offen sc1\n\t"
      "buffer_load_dwordx4 v[228:231], v20, s[28:31], s48 offen sc1\n\t"
      "s_add_u32 s48, s89, 86016\n\t"
      "buffer_load_dwordx4 v[232:235], v19, s[28:31], s48 offen sc1\n\t"
      "buffer_load_dwordx4 v[236:239], v20, s[28:31], s48 offen sc1\n\t"
      "s_add_u32 s48, s90, 86016\n\t"
      "buffer_load_dwordx4 v[240:243], v19, s[28:31], s48 offen sc1\n\t"
      "buffer_load_dwordx4 v[244:247], v20, s[28:31], s48 offen sc1\n\t"
      "s_waitcnt vmcnt(0)\n\t"
      "v_max3_u32 v28, v192, v193, v194\n\t"
      "v_max3_u32 v28, v28, v195, v196\n\t"
      "v_max3_u32 v28, v28, v197, v198\n\t"
      "v_max3_u32 v28, v28, v199, v200\n\t"
      "v_max3_u32 v28, v28, v201, v202\n\t"
      "v_max3_u32 v28, v28, v203, v204\n\t"
      "v_max3_u32 v28, v28, v205, v206\n\t"
      "v_max3_u32 v28, v28, v207, v208\n\t"
      "v_max3_u32 v28, v28, v209, v210\n\t"
      "v_max3_u32 v28, v28, v211, v212\n\t"
      "v_max3_u32 v28, v28, v213, v214\n\t"
      "v_max3_u32 v28, v28, v215, v216\n\t"
      "v_max3_u32 v28, v28, v217, v218\n\t"
      "v_max3_u32 v28, v28, v219, v220\n\t"
      "v_max3_u32 v28, v28, v221, v222\n\t"
      "v_max_u32 v28, v28, v223\n\t"
      "v_cmp_eq_u32 vcc, -1, v28\n\t"
      "s_nop 1\n\t"
      "s_cbranch_vccnz lh6_%=\n\t"
      "s_branch rh6_%=\n\t"
      "sh7_%=:\n\t"
      "s_mov_b32 s50, 0x4000\n\t"
      "lh7_%=:\n\t"
      "s_sub_u32 s50, s50, 1\n\t"
      "s_cmp_eq_u32 s50, 0\n\t"
      "s_cbranch_scc1 rh7_%=\n\t"
      "s_sleep 2\n\t"
      "s_add_u32 s48, s88, 86016\n\t"
      "buffer_load_dwordx4 v[224:227], v19, s[28:31], s48 offen sc1\n\t"
      "buffer_load_dwordx4 v[228:231], v20, s[28:31], s48 offen sc1\n\t"
      "s_add_u32 s48, s89, 86016\n\t"
      "buffer_load_dwordx4 v[232:235], v19, s[28:31], s48 offen sc1\n\t"
      "buffer_load_dwordx4 v[236:239], v20, s[28:31], s48 offen sc1\n\t"
      "s_add_u32 s48, s90, 86016\n\t"
      "buffer_load_dwordx4 v[240:243], v19, s[28:31], s48 offen sc1\n\t"
      "buffer_load_dwordx4 v[244:247], v20, s[28:31], s48 offen sc1\n\t"
      "s_waitcnt vmcnt(0)\n\t"
      "v_max3_u32 v28, v224, v225, v226\n\t"
      "v_max3_u32 v28, v28, v227, v228\n\t"
      "v_max3_u32 v28, v28, v229, v230\n\t"
      "v_max3_u32 v28, v28, v231, v232\n\t"
      "v_max3_u32 v28, v28, v233, v234\n\t"
      "v_max3_u32 v28, v28, v235, v236\n\t"
      "v_max3_u32 v28, v28, v237, v238\n\t"
      "v_max3_u32 v28, v28, v239, v240\n\t"
      "v_max3_u32 v28, v28, v241, v242\n\t"
      "v_max3_u32 v28, v28, v243, v244\n\t"
      "v_max3_u32 v28, v28, v245, v246\n\t"
      "v_max_u32 v28, v28, v247\n\t"
      "v_cmp_eq_u32 vcc, -1, v28\n\t"
      "s_nop 1\n\t"
      "s_cbranch_vccnz lh7_%=\n\t"
      "s_branch rh7_%=\n\t"
      "shv_%=:\n\t"
      "s_mov_b32 s50, 0x4000\n\t"
      "lhv_%=:\n\t"
      "s_sub_u32 s50, s50, 1\n\t"
      "s_cmp_eq_u32 s50, 0\n\t"
      "s_cbranch_scc1 rhv_%=\n\t"
      "s_sleep 2\n\t"
      "buffer_load_dwordx4 v[36:39], v19, s[28:31], s83 offen sc1\n\t"
      "buffer_load_dwordx4 v[40:43], v19, s[28:31], s83 offen offset:1024 sc1\n\t"
      "buffer_load_dwordx4 v[32:35], v21, s[28:31], 0 offen sc1\n\t"
      "s_waitcnt vmcnt(0)\n\t"
      "v_max3_u32 v28, v32, v33, v34\n\t"
      "v_max3_u32 v28, v28, v35, v36\n\t"
      "v_max3_u32 v28, v28, v37, v38\n\t"
      "v_max3_u32 v28, v28, v39, v40\n\t"
      "v_max3_u32 v28, v28, v41, v42\n\t"
      "v_max_u32 v28, v28, v43\n\t"
      "v_cmp_eq_u32 vcc, -1, v28\n\t"
      "s_nop 1\n\t"
      "s_cbranch_vccnz lhv_%=\n\t"
      "s_branch rhv_%=\n\t"
      "fw_%=:\n\t"
      "s_mov_b32 s48, 0x10000\n\t"
      "fwl_%=:\n\t"
      "s_sub_u32 s48, s48, 1\n\t"
      "s_cmp_eq_u32 s48, 0\n\t"
      "s_cbranch_scc1 fok_%=\n\t"
      "s_sleep 4\n\t"
      "buffer_load_dword v132, v6, s[28:31], 0 offen sc1\n\t"
      "s_waitcnt vmcnt(0)\n\t"
      "v_cmp_ne_u32 vcc, s47, v132\n\t"
      "s_nop 4\n\t"
      "s_cbranch_vccnz fwl_%=\n\t"
      "s_branch fok_%=\n\t"
      "mxm_%=:\n\t"
      "s_add_u32 s48, s48, 0x1000\n\t"
      "s_and_b32 s48, s48, 0xffff\n\t"
      "buffer_load_dwordx4 a[96:99], v2, s[12:15], s48 offen offset:0 sc1\n\t"
      "buffer_load_dwordx4 a[100:103], v2, s[12:15], s48 offen offset:1024 sc1\n\t"
      "s_waitcnt vmcnt(0)\n\t"
      "s_branch mx_%=\n\t"
      "redo_%=:\n\t"
      "s_mov_b32 s66, 1\n\t"
      "s_mov_b32 s50, 17\n\t"
      "s_mov_b32 s67, 0x7fff0000\n\t"
      "v_accvgpr_write_b32 a168, 0\n\t"
      "v_accvgpr_write_b32 a169, 0\n\t"
      "s_waitcnt vmcnt(0)\n\t"
      "s_barrier\n\t"
      "s_mov_b32 s49, 0\n\t"
      "buffer_load_dwordx4 a[96:99], v2, s[12:15], s49 offen offset:0 sc1\n\t"
      "buffer_load_dwordx4 a[100:103], v2, s[12:15], s49 offen offset:1024 sc1\n\t"
      "s_xor_b32 s48, s68, 6144\n\t"
      "buffer_load_dwordx4 a[64:67], v2, s[28:31], s48 offen offset:0 sc1\n\t"
      "buffer_load_dwordx4 a[68:71], v2, s[28:31], s48 offen offset:1024 sc1\n\t"
      "s_xor_b32 s48, s68, 4096\n\t"
      "buffer_load_dwordx4 a[72:75], v2, s[28:31], s48 offen offset:0 sc1\n\t"
      "buffer_load_dwordx4 a[76:79], v2, s[28:31], s48 offen offset:1024 sc1\n\t"
      "s_xor_b32 s48, s68, 2048\n\t"
      "buffer_load_dwordx4 a[80:83], v2, s[28:31], s48 offen offset:0 sc1\n\t"
      "buffer_load_dwordx4 a[84:87], v2, s[28:31], s48 offen offset:1024 sc1\n\t"
      "s_xor_b32 s48, s68, 0\n\t"
      "buffer_load_dwordx4 a[88:91], v2, s[28:31], s48 offen offset:0 sc1\n\t"
      "buffer_load_dwordx4 a[92:95], v2, s[28:31], s48 offen offset:1024 sc1\n\t"
      "buffer_load_dwordx4 a[104:107], v2, s[12:15], s49 offen offset:2048 sc1\n\t"
      "buffer_load_dwordx4 a[108:111], v2, s[12:15], s49 offen offset:3072 sc1\n\t"
      "s_add_u32 s49, s49, 0x1000\n\t"
      "buffer_load_dwordx4 a[112:115], v2, s[12:15], s49 offen offset:0 sc1\n\t"
      "buffer_load_dwordx4 a[116:119], v2, s[12:15], s49 offen offset:1024 sc1\n\t"
      "buffer_load_dwordx4 a[120:123], v2, s[12:15], s49 offen offset:2048 sc1\n\t"
      "buffer_load_dwordx4 a[124:127], v2, s[12:15], s49 offen offset:3072 sc1\n\t"
      "s_add_u32 s49, s49, 0x1000\n\t"
      "buffer_load_dwordx4 a[128:131], v2, s[12:15], s49 offen offset:0 sc1\n\t"
      "buffer_load_dwordx4 a[132:135], v2, s[12:15], s49 offen offset:1024 sc1\n\t"
      "buffer_load_dwordx4 a[136:139], v2, s[12:15], s49 offen offset:2048 sc1\n\t"
      "buffer_load_dwordx4 a[140:143], v2, s[12:15], s49 offen offset:3072 sc1\n\t"
      "s_add_u32 s49, s49, 0x1000\n\t"
      "buffer_load_dwordx4 a[144:147], v2, s[12:15], s49 offen offset:0 sc1\n\t"
      "buffer_load_dwordx4 a[148:151], v2, s[12:15], s49 offen offset:1024 sc1\n\t"
      "buffer_load_dwordx4 a[152:155], v2, s[12:15], s49 offen offset:2048 sc1\n\t"
      "s_branch top_%=\n\t"
      "qo0_%=:\n\t"
      "s_mov_b32 s50, 0x4000\n\t"
      "ql0_%=:\n\t"
      "s_sub_u32 s50, s50, 1\n\t"
      "s_cmp_eq_u32 s50, 0\n\t"
      "s_cbranch_scc1 qr0_%=\n\t"
      "s_add_u32 s48, s84, 0\n\t"
      "buffer_load_dwordx4 v[88:91], v2, s[28:31], s48 offen sc1\n\t"
      "s_waitcnt vmcnt(0)\n\t"
      "v_max3_u32 v40, v88, v89, v90\n\t"
      "v_max_u32 v40, v40, v91\n\t"
      "v_cmp_eq_u32 vcc, -1, v40\n\t"
      "s_nop 1\n\t"
      "s_cbranch_vccnz ql0_%=\n\t"
      "s_branch qr0_%=\n\t"
      "qo1_%=:\n\t"
      "s_mov_b32 s50, 0x4000\n\t"
      "ql1_%=:\n\t"
      "s_sub_u32 s50, s50, 1\n\t"
      "s_cmp_eq_u32 s50, 0\n\t"
      "s_cbranch_scc1 qr1_%=\n\t"
      "s_add_u32 s48, s84, 1024\n\t"
      "buffer_load_dwordx4 v[92:95], v2, s[28:31], s48 offen sc1\n\t"
      "s_waitcnt vmcnt(0)\n\t"
      "v_max3_u32 v40, v92, v93, v94\n\t"
      "v_max_u32 v40, v40, v95\n\t"
      "v_cmp_eq_u32 vcc, -1, v40\n\t"
      "s_nop 1\n\t"
      "s_cbranch_vccnz ql1_%=\n\t"
      "s_branch qr1_%=\n\t"
      "qo2_%=:\n\t"
      "s_mov_b32 s50, 0x4000\n\t"
      "ql2_%=:\n\t"
      "s_sub_u32 s50, s50, 1\n\t"
      "s_cmp_eq_u32 s50, 0\n\t"
      "s_cbranch_scc1 qr2_%=\n\t"
      "s_add_u32 s48, s84, 2048\n\t"
      "buffer_load_dwordx4 v[96:99], v2, s[28:31], s48 offen sc1\n\t"
      "s_waitcnt vmcnt(0)\n\t"
      "v_max3_u32 v40, v96, v97, v98\n\t"
      "v_max_u32 v40, v40, v99\n\t"
      "v_cmp_eq_u32 vcc, -1, v40\n\t"
      "s_nop 1\n\t"
      "s_cbranch_vccnz ql2_%=\n\t"
      "s_branch qr2_%=\n\t"
      "qo3_%=:\n\t"
      "s_mov_b32 s50, 0x4000\n\t"
      "ql3_%=:\n\t"
      "s_sub_u32 s50, s50, 1\n\t"
      "s_cmp_eq_u32 s50, 0\n\t"
      "s_cbranch_scc1 qr3_%=\n\t"
      "s_add_u32 s48, s84, 3072\n\t"
      "buffer_load_dwordx4 v[100:103], v2, s[28:31], s48 offen sc1\n\t"
      "s_waitcnt vmcnt(0)\n\t"
      "v_max3_u32 v40, v100, v101, v102\n\t"
      "v_max_u32 v40, v40, v103\n\t"
      "v_cmp_eq_u32 vcc, -1, v40\n\t"
      "s_nop 1\n\t"
      "s_cbranch_vccnz ql3_%=\n\t"
      "s_branch qr3_%=\n\t"
      "qo4_%=:\n\t"
      "s_mov_b32 s50, 0x4000\n\t"
      "ql4_%=:\n\t"
      "s_sub_u32 s50, s50, 1\n\t"
      "s_cmp_eq_u32 s50, 0\n\t"
      "s_cbranch_scc1 qr4_%=\n\t"
      "s_add_u32 s48, s84, 4096\n\t"
      "buffer_load_dwordx4 v[104:107], v2, s[28:31], s48 offen sc1\n\t"
      "s_waitcnt vmcnt(0)\n\t"
      "v_max3_u32 v40, v104, v105, v106\n\t"
      "v_max_u32 v40, v40, v107\n\t"
      "v_cmp_eq_u32 vcc, -1, v40\n\t"
      "s_nop 1\n\t"
      "s_cbranch_vccnz ql4_%=\n\t"
      "s_branch qr4_%=\n\t"
      "qo5_%=:\n\t"
      "s_mov_b32 s50, 0x4000\n\t"
      "ql5_%=:\n\t"
      "s_sub_u32 s50, s50, 1\n\t"
      "s_cmp_eq_u32 s50, 0\n\t"
      "s_cbranch_scc1 qr5_%=\n\t"
      "s_add_u32 s48, s84, 5120\n\t"
      "buffer_load_dwordx4 v[108:111], v2, s[28:31], s48 offen sc1\n\t"
      "s_waitcnt vmcnt(0)\n\t"
      "v_max3_u32 v40, v108, v109, v110\n\t"
      "v_max_u32 v40, v40, v111\n\t"
      "v_cmp_eq_u32 vcc, -1, v40\n\t"
      "s_nop 1\n\t"
      "s_cbranch_vccnz ql5_%=\n\t"
      "s_branch qr5_%=\n\t"
      "qo6_%=:\n\t"
      "s_mov_b32 s50, 0x4000\n\t"
      "ql6_%=:\n\t"
      "s_sub_u32 s50, s50, 1\n\t"
      "s_cmp_eq_u32 s50, 0\n\t"
      "s_cbranch_scc1 qr6_%=\n\t"
      "s_add_u32 s48, s84, 6144\n\t"
      "buffer_load_dwordx4 v[112:115], v2, s[28:31], s48 offen sc1\n\t"
      "s_waitcnt vmcnt(0)\n\t"
      "v_max3_u32 v40, v112, v113, v114\n\t"
      "v_max_u32 v40, v40, v115\n\t"
      "v_cmp_eq_u32 vcc, -1, v40\n\t"
      "s_nop 1\n\t"
      "s_cbranch_vccnz ql6_%=\n\t"
      "s_branch qr6_%=\n\t"
      "qo7_%=:\n\t"
      "s_mov_b32 s50, 0x4000\n\t"
      "ql7_%=:\n\t"
      "s_sub_u32 s50, s50, 1\n\t"
      "s_cmp_eq_u32 s50, 0\n\t"
      "s_cbranch_scc1 qr7_%=\n\t"
      "s_add_u32 s48, s84, 8192\n\t"
      "buffer_load_dwordx4 v[116:119], v2, s[28:31], s48 offen sc1\n\t"
      "s_waitcnt vmcnt(0)\n\t"
      "v_max3_u32 v40, v116, v117, v118\n\t"
      "v_max_u32 v40, v40, v119\n\t"
      "v_cmp_eq_u32 vcc, -1, v40\n\t"
      "s_nop 1\n\t"
      "s_cbranch_vccnz ql7_%=\n\t"
      "s_branch qr7_%=\n\t"
      "qo8_%=:\n\t"
      "s_mov_b32 s50, 0x4000\n\t"
      "ql8_%=:\n\t"
      "s_sub_u32 s50, s50, 1\n\t"
      "s_cmp_eq_u32 s50, 0\n\t"
      "s_cbranch_scc1 qr8_%=\n\t"
      "s_add_u32 s48, s84, 9216\n\t"
      "buffer_load_dwordx4 v[120:123], v2, s[28:31], s48 offen sc1\n\t"
      "s_waitcnt vmcnt(0)\n\t"
      "v_max3_u32 v40, v120, v121, v122\n\t"
      "v_max_u32 v40, v40, v123\n\t"
      "v_cmp_eq_u32 vcc, -1, v40\n\t"
      "s_nop 1\n\t"
      "s_cbranch_vccnz ql8_%=\n\t"
      "s_branch qr8_%=\n\t"
      "qo9_%=:\n\t"
      "s_mov_b32 s50, 0x4000\n\t"
      "ql9_%=:\n\t"
      "s_sub_u32 s50, s50, 1\n\t"
      "s_cmp_eq_u32 s50, 0\n\t"
      "s_cbranch_scc1 qr9_%=\n\t"
      "s_add_u32 s48, s84, 10240\n\t"
      "buffer_load_dwordx4 v[124:127], v2, s[28:31], s48 offen sc1\n\t"
      "s_waitcnt vmcnt(0)\n\t"
      "v_max3_u32 v40, v124, v125, v126\n\t"
      "v_max_u32 v40, v40, v127\n\t"
      "v_cmp_eq_u32 vcc, -1, v40\n\t"
      "s_nop 1\n\t"
      "s_cbranch_vccnz ql9_%=\n\t"
      "s_branch qr9_%=\n\t"
      "qo10_%=:\n\t"
      "s_mov_b32 s50, 0x4000\n\t"
      "ql10_%=:\n\t"
      "s_sub_u32 s50, s50, 1\n\t"
      "s_cmp_eq_u32 s50, 0\n\t"
      "s_cbranch_scc1 qr10_%=\n\t"
      "s_add_u32 s48, s84, 11264\n\t"
      "buffer_load_dwordx4 v[128:131], v2, s[28:31], s48 offen sc1\n\t"
      "s_waitcnt vmcnt(0)\n\t"
      "v_max3_u32 v40, v128, v129, v130\n\t"
      "v_max_u32 v40, v40, v131\n\t"
      "v_cmp_eq_u32 vcc, -1, v40\n\t"
      "s_nop 1\n\t"
      "s_cbranch_vccnz ql10_%=\n\t"
      "s_branch qr10_%=\n\t"
      "qo11_%=:\n\t"
      "s_mov_b32 s50, 0x4000\n\t"
      "ql11_%=:\n\t"
      "s_sub_u32 s50, s50, 1\n\t"
      "s_cmp_eq_u32 s50, 0\n\t"
      "s_cbranch_scc1 qr11_%=\n\t"
      "s_add_u32 s48, s84, 12288\n\t"
      "buffer_load_dwordx4 v[132:135], v2, s[28:31], s48 offen sc1\n\t"
      "s_waitcnt vmcnt(0)\n\t"
      "v_max3_u32 v40, v132, v133, v134\n\t"
      "v_max_u32 v40, v40, v135\n\t"
      "v_cmp_eq_u32 vcc, -1, v40\n\t"
      "s_nop 1\n\t"
      "s_cbranch_vccnz ql11_%=\n\t"
      "s_branch qr11_%=\n\t"
      "qo12_%=:\n\t"
      "s_mov_b32 s50, 0x4000\n\t"
      "ql12_%=:\n\t"
      "s_sub_u32 s50, s50, 1\n\t"
      "s_cmp_eq_u32 s50, 0\n\t"
      "s_cbranch_scc1 qr12_%=\n\t"
      "s_add_u32 s48, s84, 13312\n\t"
      "buffer_load_dwordx4 v[136:139], v2, s[28:31], s48 offen sc1\n\t"
      "s_waitcnt vmcnt(0)\n\t"
      "v_max3_u32 v40, v136, v137, v138\n\t"
      "v_max_u32 v40, v40, v139\n\t"
      "v_cmp_eq_u32 vcc, -1, v40\n\t"
      "s_nop 1\n\t"
      "s_cbranch_vccnz ql12_%=\n\t"
      "s_branch qr12_%=\n\t"
      "qo13_%=:\n\t"
      "s_mov_b32 s50, 0x4000\n\t"
      "ql13_%=:\n\t"
      "s_sub_u32 s50, s50, 1\n\t"
      "s_cmp_eq_u32 s50, 0\n\t"
      "s_cbranch_scc1 qr13_%=\n\t"
      "s_add_u32 s48, s84, 14336\n\t"
      "buffer_load_dwordx4 v[140:143], v2, s[28:31], s48 offen sc1\n\t"
      "s_waitcnt vmcnt(0)\n\t"
      "v_max3_u32 v40, v140, v141, v142\n\t"
      "v_max_u32 v40, v40, v143\n\t"
      "v_cmp_eq_u32 vcc, -1, v40\n\t"
      "s_nop 1\n\t"
      "s_cbranch_vccnz ql13_%=\n\t"
      "s_branch qr13_%=\n\t"
      "qo14_%=:\n\t"
      "s_mov_b32 s50, 0x4000\n\t"
      "ql14_%=:\n\t"
      "s_sub_u32 s50, s50, 1\n\t"
      "s_cmp_eq_u32 s50, 0\n\t"
      "s_cbranch_scc1 qr14_%=\n\t"
      "s_add_u32 s48, s84, 16384\n\t"
      "buffer_load_dwordx4 v[88:91], v2, s[28:31], s48 offen sc1\n\t"
      "s_waitcnt vmcnt(0)\n\t"
      "v_max3_u32 v40, v88, v89, v90\n\t"
      "v_max_u32 v40, v40, v91\n\t"
      "v_cmp_eq_u32 vcc, -1, v40\n\t"
      "s_nop 1\n\t"
      "s_cbranch_vccnz ql14_%=\n\t"
      "s_branch qr14_%=\n\t"
      "qo15_%=:\n\t"
      "s_mov_b32 s50, 0x4000\n\t"
      "ql15_%=:\n\t"
      "s_sub_u32 s50, s50, 1\n\t"
      "s_cmp_eq_u32 s50, 0\n\t"
      "s_cbranch_scc1 qr15_%=\n\t"
      "s_add_u32 s48, s84, 17408\n\t"
      "buffer_load_dwordx4 v[92:95], v2, s[28:31], s48 offen sc1\n\t"
      "s_waitcnt vmcnt(0)\n\t"
      "v_max3_u32 v40, v92, v93, v94\n\t"
      "v_max_u32 v40, v40, v95\n\t"
      "v_cmp_eq_u32 vcc, -1, v40\n\t"
      "s_nop 1\n\t"
      "s_cbranch_vccnz ql15_%=\n\t"
      "s_branch qr15_%=\n\t"
      "qo16_%=:\n\t"
      "s_mov_b32 s50, 0x4000\n\t"
      "ql16_%=:\n\t"
      "s_sub_u32 s50, s50, 1\n\t"
      "s_cmp_eq_u32 s50, 0\n\t"
      "s_cbranch_scc1 qr16_%=\n\t"
      "s_add_u32 s48, s84, 18432\n\t"
      "buffer_load_dwordx4 v[96:99], v2, s[28:31], s48 offen sc1\n\t"
      "s_waitcnt vmcnt(0)\n\t"
      "v_max3_u32 v40, v96, v97, v98\n\t"
      "v_max_u32 v40, v40, v99\n\t"
      "v_cmp_eq_u32 vcc, -1, v40\n\t"
      "s_nop 1\n\t"
      "s_cbranch_vccnz ql16_%=\n\t"
      "s_branch qr16_%=\n\t"
      "qo17_%=:\n\t"
      "s_mov_b32 s50, 0x4000\n\t"
      "ql17_%=:\n\t"
      "s_sub_u32 s50, s50, 1\n\t"
      "s_cmp_eq_u32 s50, 0\n\t"
      "s_cbranch_scc1 qr17_%=\n\t"
      "s_add_u32 s48, s84, 19456\n\t"
      "buffer_load_dwordx4 v[100:103], v2, s[28:31], s48 offen sc1\n\t"
      "s_waitcnt vmcnt(0)\n\t"
      "v_max3_u32 v40, v100, v101, v102\n\t"
      "v_max_u32 v40, v40, v103\n\t"
      "v_cmp_eq_u32 vcc, -1, v40\n\t"
      "s_nop 1\n\t"
      "s_cbranch_vccnz ql17_%=\n\t"
      "s_branch qr17_%=\n\t"
      "qo18_%=:\n\t"
      "s_mov_b32 s50, 0x4000\n\t"
      "ql18_%=:\n\t"
      "s_sub_u32 s50, s50, 1\n\t"
      "s_cmp_eq_u32 s50, 0\n\t"
      "s_cbranch_scc1 qr18_%=\n\t"
      "s_add_u32 s48, s84, 20480\n\t"
      "buffer_load_dwordx4 v[104:107], v2, s[28:31], s48 offen sc1\n\t"
      "s_waitcnt vmcnt(0)\n\t"
      "v_max3_u32 v40, v104, v105, v106\n\t"
      "v_max_u32 v40, v40, v107\n\t"
      "v_cmp_eq_u32 vcc, -1, v40\n\t"
      "s_nop 1\n\t"
      "s_cbranch_vccnz ql18_%=\n\t"
      "s_branch qr18_%=\n\t"
      "qo19_%=:\n\t"
      "s_mov_b32 s50, 0x4000\n\t"
      "ql19_%=:\n\t"
      "s_sub_u32 s50, s50, 1\n\t"
      "s_cmp_eq_u32 s50, 0\n\t"
      "s_cbranch_scc1 qr19_%=\n\t"
      "s_add_u32 s48, s84, 21504\n\t"
      "buffer_load_dwordx4 v[108:111], v2, s[28:31], s48 offen sc1\n\t"
      "s_waitcnt vmcnt(0)\n\t"
      "v_max3_u32 v40, v108, v109, v110\n\t"
      "v_max_u32 v40, v40, v111\n\t"
      "v_cmp_eq_u32 vcc, -1, v40\n\t"
      "s_nop 1\n\t"
      "s_cbranch_vccnz ql19_%=\n\t"
      "s_branch qr19_%=\n\t"
      "qo20_%=:\n\t"
      "s_mov_b32 s50, 0x4000\n\t"
      "ql20_%=:\n\t"
      "s_sub_u32 s50, s50, 1\n\t"
      "s_cmp_eq_u32 s50, 0\n\t"
      "s_cbranch_scc1 qr20_%=\n\t"
      "s_add_u32 s48, s84, 22528\n\t"
      "buffer_load_dwordx4 v[112:115], v2, s[28:31], s48 offen sc1\n\t"
      "s_waitcnt vmcnt(0)\n\t"
      "v_max3_u32 v40, v112, v113, v114\n\t"
      "v_max_u32 v40, v40, v115\n\t"
      "v_cmp_eq_u32 vcc, -1, v40\n\t"
      "s_nop 1\n\t"
      "s_cbranch_vccnz ql20_%=\n\t"
      "s_branch qr20_%=\n\t"
      "qo21_%=:\n\t"
      "s_mov_b32 s50, 0x4000\n\t"
      "ql21_%=:\n\t"
      "s_sub_u32 s50, s50, 1\n\t"
      "s_cmp_eq_u32 s50, 0\n\t"
      "s_cbranch_scc1 qr21_%=\n\t"
      "s_add_u32 s48, s84, 24576\n\t"
      "buffer_load_dwordx4 v[116:119], v2, s[28:31], s48 offen sc1\n\t"
      "s_waitcnt vmcnt(0)\n\t"
      "v_max3_u32 v40, v116, v117, v118\n\t"
      "v_max_u32 v40, v40, v119\n\t"
      "v_cmp_eq_u32 vcc, -1, v40\n\t"
      "s_nop 1\n\t"
      "s_cbranch_vccnz ql21_%=\n\t"
      "s_branch qr21_%=\n\t"
      "qo22_%=:\n\t"
      "s_mov_b32 s50, 0x4000\n\t"
      "ql22_%=:\n\t"
      "s_sub_u32 s50, s50, 1\n\t"
      "s_cmp_eq_u32 s50, 0\n\t"
      "s_cbranch_scc1 qr22_%=\n\t"
      "s_add_u32 s48, s84, 25600\n\t"
      "buffer_load_dwordx4 v[120:123], v2, s[28:31], s48 offen sc1\n\t"
      "s_waitcnt vmcnt(0)\n\t"
      "v_max3_u32 v40, v120, v121, v122\n\t"
      "v_max_u32 v40, v40, v123\n\t"
      "v_cmp_eq_u32 vcc, -1, v40\n\t"
      "s_nop 1\n\t"
      "s_cbranch_vccnz ql22_%=\n\t"
      "s_branch qr22_%=\n\t"
      "qo23_%=:\n\t"
      "s_mov_b32 s50, 0x4000\n\t"
      "ql23_%=:\n\t"
      "s_sub_u32 s50, s50, 1\n\t"
      "s_cmp_eq_u32 s50, 0\n\t"
      "s_cbranch_scc1 qr23_%=\n\t"
      "s_add_u32 s48, s84, 26624\n\t"
      "buffer_load_dwordx4 v[124:127], v2, s[28:31], s48 offen sc1\n\t"
      "s_waitcnt vmcnt(0)\n\t"
      "v_max3_u32 v40, v124, v125, v126\n\t"
      "v_max_u32 v40, v40, v127\n\t"
      "v_cmp_eq_u32 vcc, -1, v40\n\t"
      "s_nop 1\n\t"
      "s_cbranch_vccnz ql23_%=\n\t"
      "s_branch qr23_%=\n\t"
      "qo24_%=:\n\t"
      "s_mov_b32 s50, 0x4000\n\t"
      "ql24_%=:\n\t"
      "s_sub_u32 s50, s50, 1\n\t"
      "s_cmp_eq_u32 s50, 0\n\t"
      "s_cbranch_scc1 qr24_%=\n\t"
      "s_add_u32 s48, s84, 27648\n\t"
      "buffer_load_dwordx4 v[128:131], v2, s[28:31], s48 offen sc1\n\t"
      "s_waitcnt vmcnt(0)\n\t"
      "v_max3_u32 v40, v128, v129, v130\n\t"
      "v_max_u32 v40, v40, v131\n\t"
      "v_cmp_eq_u32 vcc, -1, v40\n\t"
      "s_nop 1\n\t"
      "s_cbranch_vccnz ql24_%=\n\t"
      "s_branch qr24_%=\n\t"
      "qo25_%=:\n\t"
      "s_mov_b32 s50, 0x4000\n\t"
      "ql25_%=:\n\t"
      "s_sub_u32 s50, s50, 1\n\t"
      "s_cmp_eq_u32 s50, 0\n\t"
      "s_cbranch_scc1 qr25_%=\n\t"
      "s_add_u32 s48, s84, 28672\n\t"
      "buffer_load_dwordx4 v[132:135], v2, s[28:31], s48 offen sc1\n\t"
      "s_waitcnt vmcnt(0)\n\t"
      "v_max3_u32 v40, v132, v133, v134\n\t"
      "v_max_u32 v40, v40, v135\n\t"
      "v_cmp_eq_u32 vcc, -1, v40\n\t"
      "s_nop 1\n\t"
      "s_cbranch_vccnz ql25_%=\n\t"
      "s_branch qr25_%=\n\t"
      "qo26_%=:\n\t"
      "s_mov_b32 s50, 0x4000\n\t"
      "ql26_%=:\n\t"
      "s_sub_u32 s50, s50, 1\n\t"
      "s_cmp_eq_u32 s50, 0\n\t"
      "s_cbranch_scc1 qr26_%=\n\t"
      "s_add_u32 s48, s84, 29696\n\t"
      "buffer_load_dwordx4 v[136:139], v2, s[28:31], s48 offen sc1\n\t"
      "s_waitcnt vmcnt(0)\n\t"
      "v_max3_u32 v40, v136, v137, v138\n\t"
      "v_max_u32 v40, v40, v139\n\t"
      "v_cmp_eq_u32 vcc, -1, v40\n\t"
      "s_nop 1\n\t"
      "s_cbranch_vccnz ql26_%=\n\t"
      "s_branch qr26_%=\n\t"
      "qo27_%=:\n\t"
      "s_mov_b32 s50, 0x4000\n\t"
      "ql27_%=:\n\t"
      "s_sub_u32 s50, s50, 1\n\t"
      "s_cmp_eq_u32 s50, 0\n\t"
      "s_cbranch_scc1 qr27_%=\n\t"
      "s_add_u32 s48, s84, 30720\n\t"
      "buffer_load_dwordx4 v[140:143], v2, s[28:31], s48 offen sc1\n\t"
      "s_waitcnt vmcnt(0)\n\t"
      "v_max3_u32 v40, v140, v141, v142\n\t"
      "v_max_u32 v40, v40, v143\n\t"
      "v_cmp_eq_u32 vcc, -1, v40\n\t"
      "s_nop 1\n\t"
      "s_cbranch_vccnz ql27_%=\n\t"
      "s_branch qr27_%=\n\t"
      "zp_%=:\n\t"
      "s_mov_b32 s50, 0x10000\n\t"
      "zpl_%=:\n\t"
      "s_sub_u32 s50, s50, 1\n\t"
      "s_cmp_eq_u32 s50, 0\n\t"
      "s_cbranch_scc1 zr_%=\n\t"
      "buffer_load_dwordx4 v[92:95], v23, s[28:31], 0 offen sc1\n\t"
      "s_waitcnt vmcnt(0)\n\t"
      "v_xor_b32 v92, s47, v92\n\t"
      "v_xor_b32 v93, s47, v93\n\t"
      "v_xor_b32 v94, s47, v94\n\t"
      "v_xor_b32 v95, s47, v95\n\t"
      "v_or3_b32 v92, v92, v93, v94\n\t"
      "v_or_b32 v92, v92, v95\n\t"
      "v_cmp_ne_u32 vcc, 0, v92\n\t"
      "s_nop 1\n\t"
      "s_cbranch_vccz zr_%=\n\t"
      "s_sleep 3\n\t"
      "s_branch zpl_%=\n\t"
      "xpa_%=:\n\t"
      "s_mov_b32 s50, 0x10000\n\t"
      "xla_%=:\n\t"
      "buffer_load_dwordx2 v[20:21], v2, s[28:31], s57 offen sc1\n\t"
      "buffer_load_dwordx2 v[22:23], v2, s[28:31], s57 offen offset:8 sc1\n\t"
      "s_sub_u32 s50, s50, 1\n\t"
      "s_waitcnt vmcnt(0)\n\t"
      "v_xor_b32 v21, s47, v21\n\t"
      "v_xor_b32 v23, s47, v23\n\t"
      "v_or_b32 v21, v21, v23\n\t"
      "v_cmp_ne_u32 vcc, 0, v21\n\t"
      "v_permlane32_swap_b32 v20, v22\n\t"
      "s_cmp_eq_u32 s50, 0\n\t"
      "s_cbranch_scc1 xra_%=\n\t"
      "s_cbranch_vccnz xla_%=\n\t"
      "s_branch xra_%=\n\t"
      "xpb_%=:\n\t"
      "s_mov_b32 s50, 0x10000\n\t"
      "xlb_%=:\n\t"
      "buffer_load_dwordx2 v[20:21], v2, s[28:31], s57 offen sc1\n\t"
      "buffer_load_dwordx2 v[22:23], v2, s[28:31], s57 offen offset:8 sc1\n\t"
      "s_sub_u32 s50, s50, 1\n\t"
      "s_waitcnt vmcnt(0)\n\t"
      "v_xor_b32 v21, s47, v21\n\t"
      "v_xor_b32 v23, s47, v23\n\t"
      "v_or_b32 v21, v21, v23\n\t"
      "v_cmp_ne_u32 vcc, 0, v21\n\t"
      "v_permlane32_swap_b32 v20, v22\n\t"
      "s_cmp_eq_u32 s50, 0\n\t"
      "s_cbranch_scc1 xrb_%=\n\t"
      "s_cbranch_vccnz xlb_%=\n\t"
      "s_branch xrb_%=\n\t"
      "xpc_%=:\n\t"
      "s_mov_b32 s50, 0x10000\n\t"
      "xlc_%=:\n\t"
      "buffer_load_dwordx2 v[20:21], v2, s[28:31], s57 offen sc1\n\t"
      "buffer_load_dwordx2 v[22:23], v2, s[28:31], s57 offen offset:8 sc1\n\t"
      "s_sub_u32 s50, s50, 1\n\t"
      "s_waitcnt vmcnt(0)\n\t"
      "v_xor_b32 v21, s47, v21\n\t"
      "v_xor_b32 v23, s47, v23\n\t"
      "v_or_b32 v21, v21, v23\n\t"
      "v_cmp_ne_u32 vcc, 0, v21\n\t"
      "v_permlane32_swap_b32 v20, v22\n\t"
      "s_cmp_eq_u32 s50, 0\n\t"
      "s_cbranch_scc1 xrc_%=\n\t"
      "s_cbranch_vccnz xlc_%=\n\t"
      "s_branch xrc_%=\n\t"
      "out_%=:\n\t"
      "v_mov_b32 %[tid], v3\n\t"
      "v_writelane_b32 %[tid], s47, 30\n\t"
      "v_writelane_b32 %[tid], s44, 31\n\t"
      "s_nop 1\n\t"
      : [ka] "+&s"(ka), [tid] "+&v"(sv)
      : [bid] "s"(b)
      : "memory", "scc", "vcc", ACLOB,
        "v1", "v2", "v3", "v4", "v5", "v6", "v7", "v8", "v9", "v10", "v11", "v12", "v13", "v14", "v15", "v16", "v17", "v18", "v19", "v20", "v21", "v22", "v23", "v24",
        "v25", "v26", "v27", "v28", "v29", "v30", "v31", "v32", "v33", "v34", "v35", "v36", "v37", "v38", "v39", "v40", "v41", "v42", "v43", "v44", "v45", "v46", "v47", "v48",
        "v49", "v50", "v51", "v52", "v53", "v54", "v55", "v56", "v57", "v58", "v59", "v60", "v61", "v62", "v63", "v64", "v65", "v66", "v67", "v68", "v69", "v70", "v71", "v72",
        "v73", "v74", "v75", "v76", "v77", "v78", "v79", "v80", "v81", "v82", "v83", "v84", "v85", "v86", "v87", "v88", "v89", "v90", "v91", "v92", "v93", "v94", "v95", "v96",
        "v97", "v98", "v99", "v100", "v101", "v102", "v103", "v104", "v105", "v106", "v107", "v108", "v109", "v110", "v111", "v112", "v113", "v114", "v115", "v116", "v117", "v118", "v119", "v120",
        "v121", "v122", "v123", "v124", "v125", "v126", "v127", "v128", "v129", "v130", "v131", "v132", "v133", "v134", "v135", "v136", "v137", "v138", "v139", "v140", "v141", "v142", "v143", "v144",
        "v145", "v146", "v147", "v148", "v149", "v150", "v151", "v152", "v153", "v154", "v155", "v156", "v157", "v158", "v159", "v160", "v161", "v162", "v163", "v164", "v165", "v166", "v167", "v168",
        "v169", "v170", "v171", "v172", "v173", "v174", "v175", "v176", "v177", "v178", "v179", "v180", "v181", "v182", "v183", "v184", "v185", "v186", "v187", "v188", "v189", "v190", "v191", "v192",
        "v193", "v194", "v195", "v196", "v197", "v198", "v199", "v200", "v201", "v202", "v203", "v204", "v205", "v206", "v207", "v208", "v209", "v210", "v211", "v212", "v213", "v214", "v215", "v216",
        "v217", "v218", "v219", "v220", "v221", "v222", "v223", "v224", "v225", "v226", "v227", "v228", "v229", "v230", "v231", "v232", "v233", "v234", "v235", "v236", "v237", "v238", "v239", "v240",
        "v241", "v242", "v243", "v244", "v245", "v246", "v247", "v248", "v249", "v250", "v251", "v252", "v253", "v254", "v255", "s12", "s13", "s14", "s15", "s16", "s17", "s18", "s19", "s20",
        "s21", "s22", "s23", "s24", "s25", "s26", "s27", "s28", "s29", "s30", "s31", "s32", "s33", "s34", "s35", "s36", "s37", "s38", "s39", "s40", "s41", "s42", "s43", "s44",
        "s45", "s46", "s47", "s48", "s49", "s50", "s51", "s52", "s53", "s54", "s55", "s56", "s57", "s58", "s59", "s60", "s61", "s62", "s63", "s64", "s65", "s66", "s67", "s68",
        "s69", "s70", "s71", "s72", "s73", "s74", "s75", "s76", "s77", "s78", "s79", "s80", "s81", "s82", "s83", "s84", "s85", "s86", "s87", "s88", "s89", "s90", "s91", "s92",
        "s93", "s94", "s95", "s96", "s97", "s98", "s99", "s100", "s101");
}

extern "C" void kernel_launch(void* const* d_in, const int* in_sizes, int n_in, void* d_out, int out_size, void* d_ws, size_t ws_size, hipStream_t stream) {
  (void)hipFuncSetAttribute((const void*)fused_block, hipFuncAttributeMaxDynamicSharedMemorySize, LDS_BYTES);
  fused_block<<<256, 256, LDS_BYTES, stream>>>((const char*)d_in[0], (const char*)d_in[1], (const char*)d_in[2], (const char*)d_in[4], (const char*)d_in[5], (char*)d_out, (char*)d_ws);
}
```
